```python
import torch

def _softmax(x, axis=-1):
    return torch.softmax(x, dim=axis)

def _gelu(x):
    return torch.nn.functional.gelu(x, approximate='tanh')

def _silu(x):
    return torch.nn.functional.silu(x)

def _sigmoid(x):
    return torch.sigmoid(x)

def _layernorm(x, axis=-1, eps=1e-05):
    m = x.mean(dim=axis, keepdim=True)
    v = x.var(dim=axis, keepdim=True, unbiased=False)
    return (x - m) / torch.sqrt(v + eps)

def _scatter_add(tgt, idx, src, axis=0):
    return tgt.clone().index_add_(axis, idx, src)

def _segment_sum(src, seg, n):
    out = torch.zeros((n,) + src.shape[1:], dtype=src.dtype)
    return out.index_add_(0, seg, src)

def _pad(x, pw):
    flat = tuple((v for pair in reversed(pw) for v in pair))
    return torch.nn.functional.pad(x, flat)
kernel_name = 'adaLN_cond__spectral_fnet__mixer_gmlp__prompt'

def setup_inputs():

    _w = lambda a: (lambda t: t.to(torch.bfloat16) if t.is_floating_point() else t)(torch.as_tensor(a))

    def _A_setup():
        B, S, D = (1, 2048, 640)
        ns = torch.arange(S)[:, None] * torch.arange(S)[None, :]
        nd = torch.arange(D)[:, None] * torch.arange(D)[None, :]
        Ws_re = torch.cos(-2.0 * torch.pi * ns / S).to(float)
        Ws_im = torch.sin(-2.0 * torch.pi * ns / S).to(float)
        Wd_re = torch.cos(-2.0 * torch.pi * nd / D).to(float)
        Wd_im = torch.sin(-2.0 * torch.pi * nd / D).to(float)
        return {'x': _w((lambda *s: torch.randn(*s, dtype=torch.bfloat16) * 0.1)(B, S, D)), 'dft_seq_re': _w(Ws_re), 'dft_seq_im': _w(Ws_im), 'dft_hid_re': _w(Wd_re), 'dft_hid_im': _w(Wd_im)}
    _A_in = _A_setup()

    def _B_setup():
        B, S, D, F = (1, 2048, 640, 1728)
        Fh = F // 2
        return {'x': _w((lambda *s: torch.randn(*s, dtype=torch.bfloat16) * 0.1)(B, S, D)), 'w_in': _w((lambda *s: torch.randn(*s, dtype=torch.bfloat16) * 3.2)(D, 2 * Fh)), 'w_sp': _w(0.01 * (lambda *s: torch.randn(*s, dtype=torch.bfloat16) * 3.2)(S, S)), 'b_sp': _w(1.0 + 0.01 * (lambda *s: torch.randn(*s, dtype=torch.bfloat16) * 1.6)(S)), 'w_out': _w((lambda *s: torch.randn(*s, dtype=torch.bfloat16) * 1.6)(Fh, D))}
    _B_in = _B_setup()
    return {'A_x': _A_in['x'], 'A_dft_seq_re': _A_in['dft_seq_re'], 'A_dft_seq_im': _A_in['dft_seq_im'], 'A_dft_hid_re': _A_in['dft_hid_re'], 'A_dft_hid_im': _A_in['dft_hid_im'], 'B_x': _B_in['x'], 'B_w_in': _B_in['w_in'], 'B_w_sp': _B_in['w_sp'], 'B_b_sp': _B_in['b_sp'], 'B_w_out': _B_in['w_out'], 'c': _w((lambda *s: torch.randn(*s, dtype=torch.bfloat16) * 0.2)(1, 640) * 8.0), 'w_mod': _w((lambda *s: torch.randn(*s, dtype=torch.bfloat16) * 0.2)(640, 6 * 640) * 640 ** (-0.5) * 8.0)}

def reference(A_x, A_dft_seq_re, A_dft_seq_im, A_dft_hid_re, A_dft_hid_im, B_x, B_w_in, B_w_sp, B_b_sp, B_w_out, c, w_mod):

    def _A_ref(x, dft_seq_re, dft_seq_im, dft_hid_re, dft_hid_im):
        a = torch.einsum('bsd,ed->bse', x, dft_hid_re)
        b = torch.einsum('bsd,ed->bse', x, dft_hid_im)
        y = torch.einsum('ts,bse->bte', dft_seq_re, a) - torch.einsum('ts,bse->bte', dft_seq_im, b)
        return y

    def _B_ref(x, w_in, w_sp, b_sp, w_out):
        B, S, D = x.shape
        F = w_in.shape[-1]
        Fh = F // 2
        z = _gelu(torch.einsum('bsd,df->bsf', x, w_in))
        z1 = z[..., :Fh].clone()
        z2 = _layernorm(z[..., Fh:], axis=-1)
        g = torch.einsum('ts,bsf->btf', w_sp, z2) + b_sp[None, :, None]
        return torch.einsum('bsf,fd->bsd', z1 * g, w_out)
    x = A_x
    mod = (_silu(c) @ w_mod).reshape(c.shape[0], 6, 1, -1)
    sa, ca, ga, sb, cb, gb = (mod[:, 0].clone(), mod[:, 1].clone(), mod[:, 2].clone(), mod[:, 3].clone(), mod[:, 4].clone(), mod[:, 5].clone())
    h = _layernorm(x, axis=-1) * (1 + ca) + sa
    x = x + ga * _A_ref(h, A_dft_seq_re, A_dft_seq_im, A_dft_hid_re, A_dft_hid_im)
    h = _layernorm(x, axis=-1) * (1 + cb) + sb
    x = x + gb * _B_ref(h, B_w_in, B_w_sp, B_b_sp, B_w_out)
    return x
```

```cpp
#include <hip/hip_runtime.h>
#include <hip/hip_bf16.h>
#include <stdint.h>
typedef unsigned short bf16_t;
__device__ __forceinline__ float b2f(bf16_t v){ return __uint_as_float(((unsigned)v)<<16); }
__device__ __forceinline__ bf16_t f2b(float f){ __hip_bfloat16 h = __float2bfloat16(f); return *(bf16_t*)&h; }
__device__ __forceinline__ float rb(float f){ return b2f(f2b(f)); }
constexpr int S=2048, D=640, F=1728, FH=864;

__global__ void k_mod(const bf16_t* c, const bf16_t* w, float* mod){
  int n = blockIdx.x*blockDim.x+threadIdx.x; if(n>=6*D) return;
  float acc=0; for(int k=0;k<D;k++){ float x=b2f(c[k]); float s=rb(x/(1.f+__expf(-x))); acc+=s*b2f(w[(size_t)k*6*D+n]); }
  mod[n]=rb(acc);
}
__global__ void k_ln(const bf16_t* x, const float* sh, const float* sc, bf16_t* h, int ld, int n, int coloff, int ldo){
  int r=blockIdx.x; __shared__ float red[256];
  const bf16_t* p=x+(size_t)r*ld+coloff;
  float s=0; for(int i=threadIdx.x;i<n;i+=256) s+=b2f(p[i]);
  red[threadIdx.x]=s; __syncthreads();
  for(int o=128;o>0;o>>=1){ if(threadIdx.x<o) red[threadIdx.x]+=red[threadIdx.x+o]; __syncthreads(); }
  float m=red[0]/n; __syncthreads();
  float q=0; for(int i=threadIdx.x;i<n;i+=256){ float d=b2f(p[i])-m; q+=d*d; }
  red[threadIdx.x]=q; __syncthreads();
  for(int o=128;o>0;o>>=1){ if(threadIdx.x<o) red[threadIdx.x]+=red[threadIdx.x+o]; __syncthreads(); }
  float v=red[0]/n; float rs=1.f/sqrtf(v+1e-5f);
  for(int i=threadIdx.x;i<n;i+=256){ float y=(b2f(p[i])-m)*rs; if(sc){ y=rb(y); y=rb(rb(y*rb(1.f+sc[i]))+sh[i]); } h[(size_t)r*ldo+i]=f2b(y); }
}
__global__ void k_gemm(const bf16_t* A,int lda,const bf16_t* B,int sbk,int sbn,float* C,int ldc,int M,int N,int K){
  __shared__ float As[16][65], Bs[16][65];
  int tx=threadIdx.x&15, ty=threadIdx.x>>4; int m0=blockIdx.y*64, n0=blockIdx.x*64;
  float acc[4][4]={};
  for(int k0=0;k0<K;k0+=16){
    for(int i=threadIdx.x;i<1024;i+=256){ int mm=i>>4,kk=i&15; As[kk][mm]=(m0+mm<M)?b2f(A[(size_t)(m0+mm)*lda+k0+kk]):0.f; }
    for(int i=threadIdx.x;i<1024;i+=256){ int kk,nn; if(sbn==1){nn=i&63;kk=i>>6;}else{kk=i&15;nn=i>>4;} Bs[kk][nn]=(n0+nn<N)?b2f(B[(size_t)(k0+kk)*sbk+(size_t)(n0+nn)*sbn]):0.f; }
    __syncthreads();
    for(int kk=0;kk<16;kk++){ float a[4],b[4]; for(int i=0;i<4;i++){a[i]=As[kk][ty*4+i]; b[i]=Bs[kk][tx*4+i];}
      for(int i=0;i<4;i++)for(int j=0;j<4;j++)acc[i][j]+=a[i]*b[j]; }
    __syncthreads();
  }
  for(int i=0;i<4;i++)for(int j=0;j<4;j++){ int m=m0+ty*4+i,n=n0+tx*4+j; if(m<M&&n<N) C[(size_t)m*ldc+n]=acc[i][j]; }
}
__global__ void k_cvt(const float* a, bf16_t* o, int n, int gelu){ int i=blockIdx.x*blockDim.x+threadIdx.x; if(i<n){ float x=rb(a[i]); if(gelu){ float u=0.7978845608028654f*(x+0.044715f*x*x*x); x=0.5f*x*(1.f+tanhf(u)); } o[i]=f2b(x);} }
__global__ void k_x1(const bf16_t* x,const float* yr,const float* yi,const float* g,bf16_t* x1){ int i=blockIdx.x*blockDim.x+threadIdx.x; if(i<S*D){ float y=rb(rb(yr[i])-rb(yi[i])); x1[i]=f2b(b2f(x[i])+rb(g[i%D]*y)); } }
__global__ void k_p(const float* gg,const bf16_t* bsp,const bf16_t* z,bf16_t* p){ int i=blockIdx.x*blockDim.x+threadIdx.x; if(i<S*FH){ int r=i/FH,c=i%FH; float g=rb(rb(gg[i])+b2f(bsp[r])); p[i]=f2b(b2f(z[(size_t)r*F+c])*g);} }
__global__ void k_out(const bf16_t* x1,const float* o,const float* g,bf16_t* out){ int i=blockIdx.x*blockDim.x+threadIdx.x; if(i<S*D){ out[i]=f2b(b2f(x1[i])+rb(g[i%D]*rb(o[i]))); } }

extern "C" void kernel_launch(void* const* d_in, const int* in_sizes, int n_in, void* d_out, int out_size, void* d_ws, size_t ws_size, hipStream_t stream){
  const bf16_t* x=(const bf16_t*)d_in[0]; const bf16_t* wsr=(const bf16_t*)d_in[1]; const bf16_t* wsi=(const bf16_t*)d_in[2];
  const bf16_t* wdr=(const bf16_t*)d_in[3]; const bf16_t* wdi=(const bf16_t*)d_in[4];
  const bf16_t* win=(const bf16_t*)d_in[6]; const bf16_t* wsp=(const bf16_t*)d_in[7]; const bf16_t* bsp=(const bf16_t*)d_in[8]; const bf16_t* wout=(const bf16_t*)d_in[9];
  const bf16_t* c=(const bf16_t*)d_in[10]; const bf16_t* wmod=(const bf16_t*)d_in[11];
  char* ws=(char*)d_ws; size_t o=0; auto al=[&](size_t b){ void* p=ws+o; o+=(b+255)&~(size_t)255; return p; };
  float* mod=(float*)al(6*D*4); bf16_t* h=(bf16_t*)al(S*D*2); float* t0=(float*)al((size_t)S*F*4); float* t1=(float*)al((size_t)S*F*4);
  bf16_t* a=(bf16_t*)al(S*D*2); bf16_t* b=(bf16_t*)al(S*D*2); bf16_t* x1=(bf16_t*)al(S*D*2); bf16_t* z=(bf16_t*)al((size_t)S*F*2); bf16_t* z2=(bf16_t*)al((size_t)S*FH*2); bf16_t* p=(bf16_t*)al((size_t)S*FH*2);
  k_mod<<<15,256,0,stream>>>(c,wmod,mod);
  k_ln<<<S,256,0,stream>>>(x,mod+0,mod+D,h,D,D,0,D);
  k_gemm<<<dim3(10,32),256,0,stream>>>(h,D,wdr,1,D,t0,D,S,D,D); k_cvt<<<S*D/256,256,0,stream>>>(t0,a,S*D,0);
  k_gemm<<<dim3(10,32),256,0,stream>>>(h,D,wdi,1,D,t0,D,S,D,D); k_cvt<<<S*D/256,256,0,stream>>>(t0,b,S*D,0);
  k_gemm<<<dim3(10,32),256,0,stream>>>(wsr,S,a,D,1,t0,D,S,D,S);
  k_gemm<<<dim3(10,32),256,0,stream>>>(wsi,S,b,D,1,t1,D,S,D,S);
  k_x1<<<S*D/256,256,0,stream>>>(x,t0,t1,mod+2*D,x1);
  k_ln<<<S,256,0,stream>>>(x1,mod+3*D,mod+4*D,h,D,D,0,D);
  k_gemm<<<dim3(27,32),256,0,stream>>>(h,D,win,F,1,t0,F,S,F,D); k_cvt<<<S*F/256,256,0,stream>>>(t0,z,S*F,1);
  k_ln<<<S,256,0,stream>>>(z,nullptr,nullptr,z2,F,FH,FH,FH);
  k_gemm<<<dim3(14,32),256,0,stream>>>(wsp,S,z2,FH,1,t0,FH,S,FH,S);
  k_p<<<S*FH/256,256,0,stream>>>(t0,bsp,z,p);
  k_gemm<<<dim3(10,32),256,0,stream>>>(p,FH,wout,D,1,t0,D,S,D,FH);
  k_out<<<S*D/256,256,0,stream>>>(x1,t0,mod+5*D,(bf16_t*)d_out);
}
```

```cpp
#include <hip/hip_runtime.h>
#include <stdint.h>
#include <type_traits>

typedef unsigned short bf16_t;
typedef short bf16x8 __attribute__((ext_vector_type(8)));
typedef short s16x4 __attribute__((ext_vector_type(4)));
typedef float f32x4 __attribute__((ext_vector_type(4)));
typedef float f32x2 __attribute__((ext_vector_type(2)));
typedef unsigned u32x4 __attribute__((ext_vector_type(4)));
typedef unsigned u32x2 __attribute__((ext_vector_type(2)));
typedef __bf16 bf2_t __attribute__((ext_vector_type(2)));
#define LAS __attribute__((address_space(3)))
#define GAS __attribute__((address_space(1)))
typedef GAS unsigned gu32;
typedef GAS unsigned long long gu64;
#define RLX_AGENT __ATOMIC_RELAXED, __HIP_MEMORY_SCOPE_AGENT
#define DEV __device__ __forceinline__

constexpr int S = 2048, D = 640, F = 1728, FH = 864;
constexpr int NT = 512;

constexpr size_t WS_FLAGS = 0;
constexpr size_t WS_MOD   = 65536;
constexpr size_t WS_XN    = 131072;
constexpr size_t WS_WIN   = WS_XN + 64 * 40 * 1024;
constexpr size_t WS_WOUT  = WS_WIN + 20 * 109 * 1024;
constexpr size_t WS_UV    = WS_WOUT + 27 * 40 * 1024;
constexpr size_t WS_X1P   = WS_UV + 32 * 2 * 20 * 4096;
constexpr size_t WS_ST1   = WS_X1P + 32 * 20 * 4096;
constexpr size_t WS_X1R   = WS_ST1 + 2048 * 8 * 8;
constexpr size_t WS_Z2N   = WS_X1R + 256 * 4 * 5 * 512;
constexpr size_t WS_ST2   = WS_Z2N + 64 * 55 * 1024;
constexpr size_t WS_Z1    = WS_ST2 + 2048 * 8 * 8;
constexpr size_t WS_P     = WS_Z1 + 256 * 4 * 7 * 512;
constexpr size_t WS_END   = WS_P + 32 * 28 * 4096;

struct Params {
  const bf16_t *x, *wsr, *wsi, *wdr, *wdi, *win, *wsp, *bsp, *wout, *c, *wmod;
  bf16_t* out;
  char* ws;
};

template <int N> DEV void wait_vm() { asm volatile("s_waitcnt vmcnt(%0)" ::"n"(N) : "memory"); }
DEV void wait_lgkm0() { asm volatile("s_waitcnt lgkmcnt(0)" ::: "memory"); }
DEV void bar() { asm volatile("" ::: "memory"); __builtin_amdgcn_s_barrier(); asm volatile("" ::: "memory"); }
DEV void sched_fence() { __builtin_amdgcn_sched_barrier(0); }
template <int I, int N, class Fn> DEV void static_for(Fn&& f) { if constexpr (I < N) { f(std::integral_constant<int, I>{}); static_for<I + 1, N>(f); } }

DEV float bf2f(unsigned v) { return __uint_as_float(v << 16); }
DEV float bflo(unsigned v) { return __uint_as_float(v << 16); }
DEV float bfhi(unsigned v) { return __uint_as_float(v & 0xffff0000u); }
DEV unsigned pack2(float lo, float hi) { bf2_t v; v[0] = (__bf16)lo; v[1] = (__bf16)hi; return __builtin_bit_cast(unsigned, v); }
DEV float rbf(float f) { return bflo(pack2(f, f) & 0xffffu); }
DEV float dot2(unsigned a, unsigned b, float c) { return __builtin_amdgcn_fdot2_f32_bf16(__builtin_bit_cast(bf2_t, a), __builtin_bit_cast(bf2_t, b), c, false); }
DEV f32x4 mfma(bf16x8 a, bf16x8 b, f32x4 c) { return __builtin_amdgcn_mfma_f32_16x16x32_bf16(a, b, c, 0, 0, 0); }
DEV __amdgpu_buffer_rsrc_t mkrsrc(const void* p, unsigned bytes) { return __builtin_amdgcn_make_buffer_rsrc((void*)p, 0, bytes, 0x00020000); }
template <int AUX = 0> DEV void dma(__amdgpu_buffer_rsrc_t r, LAS char* dst, unsigned voff, unsigned soff) {
  __builtin_amdgcn_raw_ptr_buffer_load_lds(r, (LAS void*)dst, 16, voff, soff, 0, AUX);
}
DEV float xsum_q(float v) { v += __shfl_xor(v, 16); v += __shfl_xor(v, 32); return v; }
DEV float xsum_16(float v) { v += __shfl_xor(v, 1); v += __shfl_xor(v, 2); v += __shfl_xor(v, 4); v += __shfl_xor(v, 8); return v; }
DEV float gelu_tanh(float x) {
  const float k1 = -2.0f * 0.7978845608028654f * 1.4426950408889634f;
  float t = x * __builtin_fmaf(x * x, 0.044715f, 1.0f);
  float e = __builtin_amdgcn_exp2f(t * k1);
  return x * __builtin_amdgcn_rcpf(1.0f + e);
}
DEV u32x4 swap_pair(u32x2 a, u32x2 b) {
  auto r0 = __builtin_amdgcn_permlane16_swap(a.x, b.x, false, false);
  auto r1 = __builtin_amdgcn_permlane16_swap(a.y, b.y, false, false);
  return (u32x4){r0[0], r1[0], r0[1], r1[1]};
}
DEV int ft0_of(int j) { return 27 * (j >> 2) + 7 * (j & 3); }
DEV int nft_of(int j) { return (j & 3) == 3 ? 6 : 7; }

struct Blk { int b, xcd, p, j; };
DEV Blk blk_of(int b) { Blk k; k.b = b; k.xcd = b & 7; int idx = b >> 3; k.p = k.xcd * 4 + (idx >> 3); k.j = idx & 7; return k; }

template <int NS, int S_, int CNT, class Issue>
DEV void loader_loop(Issue&& issue) {
  static_for<0, NS>([&](auto ic) { issue(decltype(ic)::value, decltype(ic)::value); });
  wait_vm<(NS - 1) * CNT>(); bar();
  int slot = 0;
  for (int n = 0; n < S_ - NS; ++n) {
    wait_vm<(NS - 2) * CNT>(); bar();
    issue(n + NS, slot);
    slot = slot + 1 == NS ? 0 : slot + 1;
  }
  static_for<0, NS - 1>([&](auto ic) { constexpr int r = NS - 2 - decltype(ic)::value; wait_vm<r * CNT>(); bar(); });
}

__global__ __launch_bounds__(NT, 2) void k0_prep(Params P) {
  extern __shared__ __attribute__((aligned(16))) char smem_[];
  LAS char* smem = (LAS char*)smem_;
  const int tid = threadIdx.x, lane = tid & 63, wid = __builtin_amdgcn_readfirstlane(tid >> 6), b = blockIdx.x;
  if (tid < 64) ((unsigned*)(P.ws + WS_FLAGS))[b * 64 + tid] = 0u;
  {
    const unsigned* xr = (const unsigned*)(P.x + (size_t)(8 * b + wid) * D);
    unsigned v[5];
#pragma unroll
    for (int i = 0; i < 5; ++i) v[i] = xr[lane + 64 * i];
    float s = 0.f;
#pragma unroll
    for (int i = 0; i < 5; ++i) s += bflo(v[i]) + bfhi(v[i]);
    s = xsum_q(xsum_16(s));
    const float mean = s * (1.0f / D);
    float q2 = 0.f;
#pragma unroll
    for (int i = 0; i < 5; ++i) { float a = bflo(v[i]) - mean, c = bfhi(v[i]) - mean; q2 += a * a + c * c; }
    q2 = xsum_q(xsum_16(q2));
    const float rs = 1.0f / sqrtf(q2 * (1.0f / D) + 1e-5f);
    LAS unsigned short* t = (LAS unsigned short*)smem;
#pragma unroll
    for (int i = 0; i < 5; ++i) {
      const unsigned pk = pack2((bflo(v[i]) - mean) * rs, (bfhi(v[i]) - mean) * rs);
      const int d = 2 * (lane + 64 * i);
      t[d * 8 + wid] = (unsigned short)(pk & 0xffffu); t[(d + 1) * 8 + wid] = (unsigned short)(pk >> 16);
    }
    __syncthreads();
    const int kb = b >> 2, q = b & 3;
    for (int d = tid; d < D; d += NT) {
      const u32x4 ch = *(LAS u32x4*)(smem + d * 16);
      *(u32x4*)(P.ws + WS_XN + ((size_t)(kb * 40 + (d >> 4)) * 64 + q * 16 + (d & 15)) * 16) = ch;
    }
  }
  if (wid < 4) {
    const int u = wid * 256 + b;
    if (u < 810) {
      const bool isin = u < 540;
      const int uu = isin ? u : u - 540;
      const int ncb = isin ? 27 : 10;
      const int kb = uu / ncb, cb = uu % ncb;
      const int ld = isin ? F : D;
      const bf16_t* src = (isin ? P.win : P.wout) + (size_t)(kb * 32) * ld + cb * 64;
      LAS char* t = smem + 16384 + wid * 4096;
#pragma unroll
      for (int i = 0; i < 4; ++i) {
        const int r = i * 8 + (lane >> 3);
        const u32x4 v = *(const u32x4*)(src + (size_t)r * ld + (lane & 7) * 8);
        *(LAS u32x4*)(t + r * 128 + (lane & 7) * 16) = v;
      }
      wait_lgkm0();
      char* dst = P.ws + (isin ? WS_WIN + (size_t)(kb * 109 + cb * 4) * 1024 : WS_WOUT + (size_t)(kb * 40 + cb * 4) * 1024);
      const int g = lane >> 4, rq = (lane & 15) >> 2, pp = lane & 3;
#pragma unroll
      for (int c = 0; c < 4; ++c) {
        const s16x4 lo = __builtin_amdgcn_ds_read_tr16_b64_v4i16((LAS s16x4*)(t + (8 * g + rq) * 128 + c * 32 + pp * 8));
        const s16x4 hi = __builtin_amdgcn_ds_read_tr16_b64_v4i16((LAS s16x4*)(t + (8 * g + 4 + rq) * 128 + c * 32 + pp * 8));
        const u32x2 l2 = __builtin_bit_cast(u32x2, lo), h2 = __builtin_bit_cast(u32x2, hi);
        *(u32x4*)(dst + c * 1024 + lane * 16) = (u32x4){l2.x, l2.y, h2.x, h2.y};
      }
    }
  }
  if (b < 60) {
    LAS float* sc = (LAS float*)(smem + 40960);
    LAS float* red = (LAS float*)(smem + 45056);
    for (int k = tid; k < D; k += NT) { const float x = bf2f(P.c[k]); sc[k] = rbf(x / (1.0f + __expf(-x))); }
    __syncthreads();
    const int rg = tid >> 3, cc = tid & 7;
    float a[8] = {0, 0, 0, 0, 0, 0, 0, 0};
    u32x4 w[10];
#pragma unroll
    for (int i = 0; i < 10; ++i) w[i] = *(const u32x4*)(P.wmod + (size_t)(rg + 64 * i) * (6 * D) + b * 64 + cc * 8);
#pragma unroll
    for (int i = 0; i < 10; ++i) {
      const float s = sc[rg + 64 * i];
      a[0] += s * bflo(w[i].x); a[1] += s * bfhi(w[i].x); a[2] += s * bflo(w[i].y); a[3] += s * bfhi(w[i].y);
      a[4] += s * bflo(w[i].z); a[5] += s * bfhi(w[i].z); a[6] += s * bflo(w[i].w); a[7] += s * bfhi(w[i].w);
    }
#pragma unroll
    for (int e = 0; e < 8; ++e) { a[e] += __shfl_xor(a[e], 8); a[e] += __shfl_xor(a[e], 16); a[e] += __shfl_xor(a[e], 32); }
    if (lane < 8) {
#pragma unroll
      for (int e = 0; e < 8; ++e) red[wid * 64 + lane * 8 + e] = a[e];
    }
    __syncthreads();
    if (tid < 64) {
      float s = 0.f;
#pragma unroll
      for (int w8 = 0; w8 < 8; ++w8) s += red[w8 * 64 + tid];
      s = rbf(s);
      const int n = b * 64 + tid, seg = n / D;
      if (seg == 1 || seg == 4) s = rbf(1.0f + s);
      ((float*)(P.ws + WS_MOD))[n] = s;
    }
  }
}

constexpr int K1_NS = 5, K1_SLOT = 26 * 1024;
__global__ __launch_bounds__(NT, 2) void k1_seqmix(Params P) {
  extern __shared__ __attribute__((aligned(16))) char smem_[];
  LAS char* smem = (LAS char*)smem_;
  const int tid = threadIdx.x, lane = tid & 63, wid = __builtin_amdgcn_readfirstlane(tid >> 6);
  const Blk B = blk_of(blockIdx.x);
  asm volatile("" ::"s"(P.ws), "s"(P.wsr), "s"(P.wsi));
  const int krot = B.xcd * 4;
  if (wid >= 4) {
    const int lw = wid - 4;
    __builtin_amdgcn_s_setprio(3);
    const auto rre = mkrsrc(P.wsr, S * S * 2), rim = mkrsrc(P.wsi, S * S * 2), rx = mkrsrc(P.ws + WS_XN, S * D * 2);
    const unsigned v0 = (unsigned)(lane >> 3) * 4096u + (unsigned)(((lane & 7) ^ (lane >> 4)) * 16);
    const unsigned v1 = (unsigned)(8 + (lane >> 3)) * 4096u + (unsigned)(((lane & 7) ^ (4 + (lane >> 4))) * 16);
    const unsigned vx = lane * 16;
    const unsigned wrow = (unsigned)(64 * B.p + 16 * lw) * 4096u;
    loader_loop<K1_NS, 32, 7>([&](int n, int slot) {
      const int kn = (n + krot) & 31;
      LAS char* sb = smem + slot * K1_SLOT;
      const unsigned sw = wrow + kn * 128;
      dma(rre, sb + lw * 4096, v0, sw); dma(rre, sb + lw * 4096 + 1024, v1, sw);
      dma(rim, sb + lw * 4096 + 2048, v0, sw); dma(rim, sb + lw * 4096 + 3072, v1, sw);
#pragma unroll
      for (int u = 0; u < 2; ++u) { const int i = lw * 2 + u; const int hi = i >= 5; dma(rx, sb + 16384 + i * 1024, vx, (unsigned)(((2 * kn + hi) * 40 + 5 * B.j + i - 5 * hi) * 1024)); }
      { const int i = 8 + (lw >> 1); if (lane < 32) dma(rx, sb + 16384 + i * 1024 + (lw & 1) * 512, vx, (unsigned)(((2 * kn + 1) * 40 + 5 * B.j + i - 5) * 1024 + (lw & 1) * 512)); }
    });
  } else {
    const int w = wid, q = lane >> 4, tk = lane & 15;
    f32x4 acc[2][5];
#pragma unroll
    for (int t = 0; t < 2; ++t)
#pragma unroll
      for (int i = 0; i < 5; ++i) acc[t][i] = (f32x4){0, 0, 0, 0};
    float rsum[2] = {0.f, 0.f};
    const unsigned wa0 = (unsigned)(w * 4096 + tk * 128 + ((q ^ ((lane >> 1) & 7)) * 16));
    const unsigned xa = 16384 + lane * 16;
    bf16x8 Wa[2], Xa[5], Wb[2], Xb[5];
    auto rd = [&](bf16x8(&Wf)[2], bf16x8(&Xf)[5], int slot, int kk) {
      LAS char* sb = smem + slot * K1_SLOT;
      const unsigned wa = wa0 ^ (kk * 64);
      Wf[0] = *(LAS bf16x8*)(sb + wa);
      Xf[0] = *(LAS bf16x8*)(sb + xa + (kk * 5) * 1024);
      Wf[1] = *(LAS bf16x8*)(sb + wa + 2048);
#pragma unroll
      for (int i = 1; i < 5; ++i) Xf[i] = *(LAS bf16x8*)(sb + xa + (kk * 5 + i) * 1024);
    };
    auto mm = [&](bf16x8(&Wf)[2], bf16x8(&Xf)[5]) {
#pragma unroll
      for (int i = 0; i < 5; ++i)
#pragma unroll
        for (int t = 0; t < 2; ++t) acc[t][i] = mfma(Xf[i], Wf[t], acc[t][i]);
#pragma unroll
      for (int t = 0; t < 2; ++t) { const u32x4 u = __builtin_bit_cast(u32x4, Wf[t]);
        rsum[t] = dot2(u.x, 0x3f803f80u, rsum[t]); rsum[t] = dot2(u.y, 0x3f803f80u, rsum[t]);
        rsum[t] = dot2(u.z, 0x3f803f80u, rsum[t]); rsum[t] = dot2(u.w, 0x3f803f80u, rsum[t]); }
    };
#define K1_ILV() do { _Pragma("unroll") for (int z = 0; z < 7; ++z) { __builtin_amdgcn_sched_group_barrier(0x8, 1, 0); __builtin_amdgcn_sched_group_barrier(0x100, 1, 0); __builtin_amdgcn_sched_group_barrier(0x2, 1, 0); } __builtin_amdgcn_sched_group_barrier(0x8, 3, 0); } while (0)
    f32x4 c1[5], sa[5];
    const float* mod = (const float*)(P.ws + WS_MOD);
#pragma unroll
    for (int i = 0; i < 5; ++i) { const int d = 80 * B.j + 16 * i + 4 * q; sa[i] = *(const f32x4*)(mod + d); c1[i] = *(const f32x4*)(mod + D + d); }
    bar();
    rd(Wa, Xa, 0, 0);
    int slot = 0;
    for (int n = 0; n < 31; ++n) {
      rd(Wb, Xb, slot, 1); mm(Wa, Xa); K1_ILV(); sched_fence();
      const int ns = slot + 1 == K1_NS ? 0 : slot + 1;
      wait_lgkm0(); bar(); sched_fence();
      rd(Wa, Xa, ns, 0); mm(Wb, Xb); K1_ILV(); sched_fence();
      slot = ns;
    }
    rd(Wb, Xb, slot, 1); mm(Wa, Xa); mm(Wb, Xb);
    const float rs0 = xsum_q(rsum[0]), rs1 = xsum_q(rsum[1]);
    char* uv = P.ws + WS_UV;
#pragma unroll
    for (int i = 0; i < 5; ++i) {
      u32x2 a, c;
      { const f32x4 u = acc[0][i] * c1[i] + rs0 * sa[i]; a.x = pack2(u[0], u[1]); a.y = pack2(u[2], u[3]); }
      { const f32x4 v = -(acc[1][i] * c1[i] + rs1 * sa[i]); c.x = pack2(v[0], v[1]); c.y = pack2(v[2], v[3]); }
      const u32x4 ch = swap_pair(a, c);
      const int dt = 5 * B.j + i, kb = dt >> 1, qq = 2 * (dt & 1) + (q >> 1);
      *(u32x4*)(uv + ((size_t)((B.p * 2 + (q & 1)) * 20 + kb) * 4 + w) * 1024 + (qq * 16 + tk) * 16) = ch;
    }
  }
}

template <int NS, int S_, int NA, int NB, class Rd, class Mm>
DEV void ksplit_loop(Rd&& rd, Mm&& mm) {
  bf16x8 A0[NA], B0[NB], A1[NA], B1[NB];
  bar();
  rd(A0, B0, 0);
  int slot = 0;
  constexpr int NR = NA + NB, NM = NA * NB;
#define KS_ILV() do { _Pragma("unroll") for (int z = 0; z < NR; ++z) { __builtin_amdgcn_sched_group_barrier(0x8, 1, 0); __builtin_amdgcn_sched_group_barrier(0x100, 1, 0); } __builtin_amdgcn_sched_group_barrier(0x8, NM - NR, 0); } while (0)
  for (int n = 0; n + 2 < S_; n += 2) {
    const int s1 = slot + 1 == NS ? 0 : slot + 1;
    wait_lgkm0(); bar(); sched_fence();
    rd(A1, B1, s1); mm(A0, B0); KS_ILV(); sched_fence();
    const int s2 = s1 + 1 == NS ? 0 : s1 + 1;
    wait_lgkm0(); bar(); sched_fence();
    rd(A0, B0, s2); mm(A1, B1); KS_ILV(); sched_fence();
    slot = s2;
  }
  if constexpr ((S_ & 1) == 0) {
    const int s1 = slot + 1 == NS ? 0 : slot + 1;
    wait_lgkm0(); bar(); sched_fence();
    rd(A1, B1, s1); mm(A0, B0); mm(A1, B1);
  } else {
    mm(A0, B0);
  }
}

template <int N>
DEV void kreduce(f32x4 (&acc)[N][2], f32x4 (&res)[N], LAS char* scratch, int w, int lane) {
  const int kh = w & 1;
  LAS char* mine = scratch + w * (N * 1024) + lane * 16;
  LAS char* other = scratch + (w ^ 1) * (N * 1024) + lane * 16;
  if (kh) {
#pragma unroll
    for (int i = 0; i < N; ++i) { *(LAS f32x4*)(mine + i * 1024) = acc[i][0]; res[i] = acc[i][1]; }
  } else {
#pragma unroll
    for (int i = 0; i < N; ++i) { *(LAS f32x4*)(mine + i * 1024) = acc[i][1]; res[i] = acc[i][0]; }
  }
  wait_lgkm0(); bar();
#pragma unroll
  for (int i = 0; i < N; ++i) res[i] += *(LAS f32x4*)(other + i * 1024);
}

constexpr int K2_NS = 6, K2_SLOT = 18 * 1024, K2_SCR = K2_NS * K2_SLOT;
__global__ __launch_bounds__(NT, 2) void k2_hidmix(Params P) {
  extern __shared__ __attribute__((aligned(16))) char smem_[];
  LAS char* smem = (LAS char*)smem_;
  const int tid = threadIdx.x, lane = tid & 63, wid = __builtin_amdgcn_readfirstlane(tid >> 6);
  const Blk B = blk_of(blockIdx.x);
  asm volatile("" ::"s"(P.ws), "s"(P.wdr), "s"(P.wdi), "s"(P.x));
  if (wid >= 4) {
    const int lw = wid - 4;
    __builtin_amdgcn_s_setprio(3);
    const auto rre = mkrsrc(P.wdr, D * D * 2), rim = mkrsrc(P.wdi, D * D * 2), rx = mkrsrc(P.ws + WS_UV, 32 * 2 * 20 * 4096);
    const unsigned v0 = (unsigned)(lane >> 3) * 1280u + (unsigned)(((lane & 7) ^ (lane >> 4)) * 16);
    const unsigned v1 = (unsigned)(lane >> 3) * 1280u + (unsigned)(((lane & 7) ^ (4 + (lane >> 4))) * 16);
    const unsigned vh = (unsigned)(lane >> 3) * 1280u + (unsigned)(((lane & 7) ^ ((4 * (lw >> 1) + 2 * (lw & 1) + (lane >> 4)) & 7)) * 16);
    const unsigned vx = lane * 16;
    loader_loop<K2_NS, 20, 5>([&](int n, int slot) {
      LAS char* sb = smem + slot * K2_SLOT;
      const int half = n >= 10, kn = n - 10 * half;
      const auto rw = half ? rim : rre;
      const unsigned sw = (unsigned)(80 * B.j) * 1280u + kn * 128;
      dma(rw, sb + (2 * lw) * 1024, v0, sw + (unsigned)(16 * lw) * 1280u);
      dma(rw, sb + (2 * lw + 1) * 1024, v1, sw + (unsigned)(16 * lw + 8) * 1280u);
      const unsigned sx = (unsigned)(((B.p * 2 + half) * 20 + 2 * kn) * 4096);
      dma(rx, sb + 10240 + (2 * lw) * 1024, vx, sx + (2 * lw) * 1024);
      dma(rx, sb + 10240 + (2 * lw + 1) * 1024, vx, sx + (2 * lw + 1) * 1024);
      if (lane < 32) dma(rw, sb + (8 + (lw >> 1)) * 1024 + (lw & 1) * 512, vh, sw + (unsigned)(64 + 8 * (lw >> 1) + 4 * (lw & 1)) * 1280u);
    });
    bar();
  } else {
    const int w = wid, q = lane >> 4, tk = lane & 15, mh = w >> 1, kh = w & 1;
    f32x4 acc[5][2];
#pragma unroll
    for (int i = 0; i < 5; ++i) { acc[i][0] = (f32x4){0, 0, 0, 0}; acc[i][1] = (f32x4){0, 0, 0, 0}; }
    const int token = 64 * B.p + 16 * w + tk;
    u32x2 xv[5]; f32x4 ga[5], c1b[5];
    const float* mod = (const float*)(P.ws + WS_MOD);
#pragma unroll
    for (int i = 0; i < 5; ++i) { const int e = 80 * B.j + 16 * i + 4 * q; xv[i] = *(const u32x2*)(P.x + (size_t)token * D + e); ga[i] = *(const f32x4*)(mod + 2 * D + e); c1b[i] = *(const f32x4*)(mod + 4 * D + e); }
    const unsigned wa = (unsigned)(tk * 128 + (((q + 4 * kh) ^ ((lane >> 1) & 7)) * 16));
    const unsigned xa = 10240 + (kh * 4 + 2 * mh) * 1024 + lane * 16;
    ksplit_loop<K2_NS, 20, 5, 2>(
        [&](bf16x8(&A)[5], bf16x8(&Bf)[2], int slot) { LAS char* sb = smem + slot * K2_SLOT;
          A[0] = *(LAS bf16x8*)(sb + wa); Bf[0] = *(LAS bf16x8*)(sb + xa); Bf[1] = *(LAS bf16x8*)(sb + xa + 1024);
#pragma unroll
          for (int i = 1; i < 5; ++i) A[i] = *(LAS bf16x8*)(sb + wa + i * 2048); },
        [&](bf16x8(&A)[5], bf16x8(&Bf)[2]) {
#pragma unroll
          for (int i = 0; i < 5; ++i) { acc[i][0] = mfma(A[i], Bf[0], acc[i][0]); acc[i][1] = mfma(A[i], Bf[1], acc[i][1]); } });
    f32x4 y[5];
    kreduce<5>(acc, y, smem + K2_SCR, w, lane);
    float xs[20]; u32x2 x1p[5], xp[5];
    float s = 0.f;
#pragma unroll
    for (int i = 0; i < 5; ++i) {
      const float x0 = bflo(xv[i].x) + ga[i][0] * y[i][0], x1 = bfhi(xv[i].x) + ga[i][1] * y[i][1], x2 = bflo(xv[i].y) + ga[i][2] * y[i][2], x3 = bfhi(xv[i].y) + ga[i][3] * y[i][3];
      x1p[i].x = pack2(x0, x1); x1p[i].y = pack2(x2, x3);
      xs[4 * i] = bflo(x1p[i].x); xs[4 * i + 1] = bfhi(x1p[i].x); xs[4 * i + 2] = bflo(x1p[i].y); xs[4 * i + 3] = bfhi(x1p[i].y);
      s += (xs[4 * i] + xs[4 * i + 1]) + (xs[4 * i + 2] + xs[4 * i + 3]);
      xp[i].x = pack2(xs[4 * i] * c1b[i][0], xs[4 * i + 1] * c1b[i][1]); xp[i].y = pack2(xs[4 * i + 2] * c1b[i][2], xs[4 * i + 3] * c1b[i][3]);
    }
    s = xsum_q(s); const float mean = s * (1.0f / 80.0f);
    float m2 = 0.f;
#pragma unroll
    for (int e = 0; e < 20; ++e) { const float d = xs[e] - mean; m2 += d * d; }
    m2 = xsum_q(m2);
    if (q == 0) ((f32x2*)(P.ws + WS_ST1))[token * 8 + B.j] = (f32x2){mean, m2};
    char* img = P.ws + WS_X1P + (size_t)B.p * 20 * 4096 + w * 1024;
#pragma unroll
    for (int i = 0; i < 6; i += 2) {
      const int i2 = i + 1 < 5 ? i + 1 : i;
      const u32x4 ch = swap_pair(xp[i], xp[i2]);
      const int dt = 5 * B.j + ((q & 1) ? i2 : i), kb = dt >> 1, qq = 2 * (dt & 1) + (q >> 1);
      *(u32x4*)(img + (size_t)kb * 4096 + (qq * 16 + tk) * 16) = ch;
    }
    char* xr = P.ws + WS_X1R + (size_t)(B.b * 4 + w) * 5 * 512 + lane * 8;
#pragma unroll
    for (int i = 0; i < 5; ++i) *(u32x2*)(xr + i * 512) = x1p[i];
  }
}

DEV bool spin_fail(unsigned& spins, gu32* tmo) {
  __builtin_amdgcn_s_sleep(1);
  if (++spins > (1u << 18)) { __hip_atomic_store(tmo, 1u, RLX_AGENT); return true; }
  return false;
}

constexpr int K3_NS = 5, K3_X = 0, K3_W = 80 * 1024, K3_WSLOT = 14 * 1024, K3_MISC = K3_W + K3_NS * K3_WSLOT;
constexpr int K3_MODB = K3_MISC;
constexpr int K3_ROWST = K3_MISC + 2592;
constexpr int K3_CV = K3_ROWST + 512;
constexpr int K3_ST2 = K3_CV + 1792;
constexpr int K3_LDS = K3_ST2 + 512;
__global__ __launch_bounds__(NT, 2) void k3_inproj(Params P) {
  extern __shared__ __attribute__((aligned(16))) char smem_[];
  LAS char* smem = (LAS char*)smem_;
  const int tid = threadIdx.x, lane = tid & 63, wid = __builtin_amdgcn_readfirstlane(tid >> 6);
  const Blk B = blk_of(blockIdx.x);
  asm volatile("" ::"s"(P.ws));
  const int ft0 = ft0_of(B.j), nft = nft_of(B.j);
  gu32* gflag = (gu32*)(P.ws + WS_FLAGS) + B.p * 64;
  gu32* tmo = (gu32*)(P.ws + WS_FLAGS) + 8192;
  if (wid >= 4) {
    const int lw = wid - 4;
    __builtin_amdgcn_s_setprio(3);
    const auto rw = mkrsrc(P.ws + WS_WIN, 20 * 109 * 1024), rx = mkrsrc(P.ws + WS_X1P, 32 * 20 * 4096);
    const unsigned vx = lane * 16;
    auto issueW = [&](int n, int slot) {
      LAS char* sb = smem + K3_W + slot * K3_WSLOT;
      const int z1 = n >= 10, kn = n - 10 * z1, ftb = (z1 ? 0 : 54) + ft0;
#pragma unroll
      for (int u = 0; u < 3; ++u) { const int i = lw * 3 + u, kk = i >= 7; dma(rw, sb + i * 1024, vx, (unsigned)(((2 * kn + kk) * 109 + ftb + i - 7 * kk) * 1024)); }
      { const int i = 12 + (lw >> 1); if (lane < 32) dma(rw, sb + i * 1024 + (lw & 1) * 512, vx, (unsigned)(((2 * kn + 1) * 109 + ftb + i - 7) * 1024 + (lw & 1) * 512)); }
    };
    auto issueX = [&](int n) {
      const unsigned sx = (unsigned)((B.p * 20 + 2 * n) * 4096);
      dma(rx, smem + K3_X + (2 * n) * 4096 + (2 * lw) * 1024, vx, sx + (2 * lw) * 1024);
      dma(rx, smem + K3_X + (2 * n) * 4096 + (2 * lw + 1) * 1024, vx, sx + (2 * lw + 1) * 1024);
    };
    static_for<0, K3_NS>([&](auto ic) { constexpr int n = decltype(ic)::value; issueW(n, n); issueX(n); });
    wait_vm<(K3_NS - 1) * 6>(); bar();
    static_for<0, 19>([&](auto ic) {
      constexpr int n = decltype(ic)::value;
      constexpr int last = n + K3_NS - 1 < 19 ? n + K3_NS - 1 : 19;
      constexpr int lo = n + 2;
      constexpr int n6 = (lo <= 9) ? ((last < 9 ? last : 9) - lo + 1) : 0;
      constexpr int n4 = (last >= 10) ? (last - (lo > 10 ? lo : 10) + 1) : 0;
      wait_vm<(n6 > 0 ? n6 : 0) * 6 + (n4 > 0 ? n4 : 0) * 4>();
      bar();
      if constexpr (n == 9) { bar(); bar(); bar(); bar(); bar(); }
      if constexpr (n + K3_NS < 20) { issueW(n + K3_NS, n % K3_NS); if constexpr (n + K3_NS < 10) issueX(n + K3_NS); }
    });
    bar();
  } else {
    const int w = wid, q = lane >> 4, tk = lane & 15;
    const float* mod = (const float*)(P.ws + WS_MOD);
    for (int d = tid; d < 2 * D; d += 256) { const float v = d < D ? mod[4 * D + d] : mod[3 * D + d - D]; ((LAS unsigned short*)(smem + K3_MODB))[d] = (unsigned short)(__float_as_uint(v) >> 16); }
    if (tid < 4) ((LAS unsigned*)(smem + K3_MODB + 2560))[tid] = 0u;
    if (lane < 16) {
      const int token = 64 * B.p + 16 * w + lane;
      const f32x4* st = (const f32x4*)(P.ws + WS_ST1 + (size_t)token * 64);
      float mu[8], m2[8];
#pragma unroll
      for (int k = 0; k < 4; ++k) { const f32x4 v = st[k]; mu[2 * k] = v[0]; m2[2 * k] = v[1]; mu[2 * k + 1] = v[2]; m2[2 * k + 1] = v[3]; }
      float mean = 0.f;
#pragma unroll
      for (int k = 0; k < 8; ++k) mean += mu[k];
      mean *= 0.125f; float M2 = 0.f;
#pragma unroll
      for (int k = 0; k < 8; ++k) { const float dm = mu[k] - mean; M2 += m2[k] + 80.0f * dm * dm; }
      ((LAS f32x2*)(smem + K3_ROWST))[16 * w + lane] = (f32x2){mean, 1.0f / sqrtf(M2 * (1.0f / D) + 1e-5f)};
    }
    wait_lgkm0();
    f32x4 acc[7], aux[2];
#pragma unroll
    for (int i = 0; i < 7; ++i) acc[i] = (f32x4){0, 0, 0, 0};
    aux[0] = aux[1] = (f32x4){0, 0, 0, 0};
    const unsigned xa = K3_X + w * 1024 + lane * 16;
    const unsigned wa = K3_W + lane * 16;
    const int r4 = tk & 3;
    const unsigned ma0 = K3_MODB + (r4 == 0 ? q * 16 : r4 == 1 ? 1280 + q * 16 : 2560);
    const unsigned mstride = r4 < 2 ? 64 : 0;
    struct Fr { bf16x8 X, W[7], M, A0, A1; };
    Fr Fa, Fb;
    const unsigned auxo0 = w * 1024, auxo1 = (w < 3 ? w + 4 : 6) * 1024;
    auto rd = [&](Fr& f, int slot, int kb) {
      f.X = *(LAS bf16x8*)(smem + xa + kb * 4096);
      LAS char* sb = smem + wa + slot * K3_WSLOT + (kb & 1) * 7168;
#pragma unroll
      for (int i = 0; i < 7; ++i) f.W[i] = *(LAS bf16x8*)(sb + i * 1024);
      f.M = *(LAS bf16x8*)(smem + ma0 + kb * mstride);
      f.A0 = *(LAS bf16x8*)(sb + auxo0); f.A1 = *(LAS bf16x8*)(sb + auxo1);
    };
    auto mm = [&](auto z1c, Fr& f) {
      constexpr bool z1 = decltype(z1c)::value;
#pragma unroll
      for (int i = 0; i < 7; ++i) acc[i] = z1 ? mfma(f.W[i], f.X, acc[i]) : mfma(f.X, f.W[i], acc[i]);
      aux[0] = mfma(f.M, f.A0, aux[0]); aux[1] = mfma(f.M, f.A1, aux[1]);
    };
    auto pass = [&](auto z1c, int slot0) {
      int slot = slot0;
      rd(Fa, slot, 0);
      for (int n = 0; n < 9; ++n) {
        rd(Fb, slot, 2 * n + 1); mm(z1c, Fa); sched_fence();
        const int ns = slot + 1 == K3_NS ? 0 : slot + 1;
        wait_lgkm0(); bar(); sched_fence();
        rd(Fa, ns, 2 * n + 2); mm(z1c, Fb); sched_fence();
        slot = ns;
      }
      rd(Fb, slot, 19); mm(z1c, Fa); mm(z1c, Fb);
    };
    auto publish_cv = [&](int pass_) {
      LAS float* cv = (LAS float*)(smem + K3_CV) + pass_ * 224;
      if (q == 0) { cv[16 * w + tk] = aux[0][0]; cv[112 + 16 * w + tk] = aux[0][1];
        if (w < 3) { cv[16 * (w + 4) + tk] = aux[1][0]; cv[112 + 16 * (w + 4) + tk] = aux[1][1]; } }
    };
    bar();
    pass(std::false_type{}, 0);
    publish_cv(0);
    wait_lgkm0(); bar();
    float z[7][4];
    {
      const LAS float* cv = (const LAS float*)(smem + K3_CV);
      f32x2 rs[4];
#pragma unroll
      for (int r = 0; r < 4; ++r) rs[r] = ((LAS f32x2*)(smem + K3_ROWST))[16 * w + 4 * q + r];
#pragma unroll
      for (int i = 0; i < 7; ++i) { const float c1 = cv[16 * i + tk], c2 = cv[112 + 16 * i + tk];
#pragma unroll
        for (int r = 0; r < 4; ++r) z[i][r] = rbf(gelu_tanh(rs[r][1] * (acc[i][r] - rs[r][0] * c1) + c2)); }
    }
    float mu[4], m2[4];
    const float inv = 1.0f / (16.0f * nft);
#pragma unroll
    for (int r = 0; r < 4; ++r) { float s = 0.f;
#pragma unroll
      for (int i = 0; i < 7; ++i) s += (i < 6 || nft == 7) ? z[i][r] : 0.f;
      mu[r] = xsum_16(s) * inv; float t = 0.f;
#pragma unroll
      for (int i = 0; i < 7; ++i) { const float d = z[i][r] - mu[r]; t += (i < 6 || nft == 7) ? d * d : 0.f; }
      m2[r] = xsum_16(t); }
    if (tk < 4) { const float a = tk == 0 ? mu[0] : tk == 1 ? mu[1] : tk == 2 ? mu[2] : mu[3], c = tk == 0 ? m2[0] : tk == 1 ? m2[1] : tk == 2 ? m2[2] : m2[3];
      const int token = 64 * B.p + 16 * w + 4 * q + tk;
      __hip_atomic_store((gu64*)(P.ws + WS_ST2) + token * 8 + B.j, ((unsigned long long)__float_as_uint(c) << 32) | __float_as_uint(a), RLX_AGENT); }
    wait_vm<0>(); bar();
    if (w == 0) {
      if (lane == 0) __hip_atomic_store(gflag + B.j, 1u, RLX_AGENT);
      unsigned spins = 0;
      for (;;) { const unsigned v = lane < 8 ? __hip_atomic_load(gflag + lane, RLX_AGENT) : 1u; if (__all(v == 1u)) break; if (spin_fail(spins, tmo)) break; }
    }
    bar();
    if (lane < 16) {
      const int token = 64 * B.p + 16 * w + lane;
      float pm[8], pq[8]; float mean = 0.f;
#pragma unroll
      for (int k = 0; k < 8; ++k) { const unsigned long long v = __hip_atomic_load((gu64*)(P.ws + WS_ST2) + token * 8 + k, RLX_AGENT); pm[k] = __uint_as_float((unsigned)v); pq[k] = __uint_as_float((unsigned)(v >> 32)); mean += pm[k] * ((k & 3) == 3 ? 96.0f : 112.0f); }
      mean *= (1.0f / FH); float M2 = 0.f;
#pragma unroll
      for (int k = 0; k < 8; ++k) { const float dm = pm[k] - mean; M2 += pq[k] + ((k & 3) == 3 ? 96.0f : 112.0f) * dm * dm; }
      ((LAS f32x2*)(smem + K3_ST2))[16 * w + lane] = (f32x2){mean, 1.0f / sqrtf(M2 * (1.0f / FH) + 1e-5f)};
    }
    wait_lgkm0(); bar();
    {
      f32x2 rs[4];
#pragma unroll
      for (int r = 0; r < 4; ++r) rs[r] = ((LAS f32x2*)(smem + K3_ST2))[16 * w + 4 * q + r];
      u32x2 pk[7];
#pragma unroll
      for (int i = 0; i < 7; ++i) { pk[i].x = pack2((z[i][0] - rs[0][0]) * rs[0][1], (z[i][1] - rs[1][0]) * rs[1][1]); pk[i].y = pack2((z[i][2] - rs[2][0]) * rs[2][1], (z[i][3] - rs[3][0]) * rs[3][1]); }
      char* img = P.ws + WS_Z2N + (size_t)(2 * B.p + (w >> 1)) * 55 * 1024 + ((2 * (w & 1) + (q >> 1)) * 16 + tk) * 16;
#pragma unroll
      for (int i = 0; i < 8; i += 2) {
        const int i2 = i + 1 < 7 ? i + 1 : i;
        const u32x4 ch = swap_pair(pk[i], pk[i2]);
        const int ti = (q & 1) ? i2 : i;
        if (ti < nft) *(u32x4*)(img + (size_t)(ft0 + ti) * 1024) = ch;
      }
    }
    bar(); bar();
#pragma unroll
    for (int i = 0; i < 7; ++i) acc[i] = (f32x4){0, 0, 0, 0};
    aux[0] = aux[1] = (f32x4){0, 0, 0, 0};
    pass(std::true_type{}, 10 % K3_NS);
    publish_cv(1);
    wait_lgkm0(); bar();
    {
      const LAS float* cv = (const LAS float*)(smem + K3_CV) + 224;
      const f32x2 rs = ((LAS f32x2*)(smem + K3_ROWST))[16 * w + tk];
      char* z1 = P.ws + WS_Z1 + (size_t)(B.b * 4 + w) * 7 * 512 + lane * 8;
#pragma unroll
      for (int i = 0; i < 7; ++i) {
        const f32x4 c1 = *(const LAS f32x4*)(cv + 16 * i + 4 * q), c2 = *(const LAS f32x4*)(cv + 112 + 16 * i + 4 * q);
        float g[4];
#pragma unroll
        for (int r = 0; r < 4; ++r) g[r] = gelu_tanh(rs[1] * (acc[i][r] - rs[0] * c1[r]) + c2[r]);
        *(u32x2*)(z1 + i * 512) = (u32x2){pack2(g[0], g[1]), pack2(g[2], g[3])};
      }
    }
  }
}

constexpr int K4_NS = 6, K4_SLOT = 22 * 1024, K4_SCR = K4_NS * K4_SLOT;
__global__ __launch_bounds__(NT, 2) void k4_spatial(Params P) {
  extern __shared__ __attribute__((aligned(16))) char smem_[];
  LAS char* smem = (LAS char*)smem_;
  const int tid = threadIdx.x, lane = tid & 63, wid = __builtin_amdgcn_readfirstlane(tid >> 6);
  const Blk B = blk_of(blockIdx.x);
  asm volatile("" ::"s"(P.ws), "s"(P.wsp), "s"(P.bsp));
  const int ft0 = ft0_of(B.j), nft = nft_of(B.j);
  const int krot = B.xcd * 4;
  if (wid >= 4) {
    const int lw = wid - 4;
    __builtin_amdgcn_s_setprio(3);
    const auto rw = mkrsrc(P.wsp, S * S * 2), rx = mkrsrc(P.ws + WS_Z2N, 64 * 55 * 1024);
    const unsigned v0 = (unsigned)(lane >> 3) * 4096u + (unsigned)(((lane & 7) ^ (lane >> 4)) * 16);
    const unsigned v1 = (unsigned)(8 + (lane >> 3)) * 4096u + (unsigned)(((lane & 7) ^ (4 + (lane >> 4))) * 16);
    const unsigned vx = lane * 16;
    const unsigned wrow = (unsigned)(64 * B.p + 16 * lw) * 4096u;
    loader_loop<K4_NS, 32, 6>([&](int n, int slot) {
      const int kn = (n + krot) & 31;
      LAS char* sb = smem + slot * K4_SLOT;
      dma(rw, sb + lw * 2048, v0, wrow + kn * 128); dma(rw, sb + lw * 2048 + 1024, v1, wrow + kn * 128);
#pragma unroll
      for (int u = 0; u < 3; ++u) { const int i = lw * 3 + u, kk = i >= 7; dma(rx, sb + 8192 + i * 1024, vx, (unsigned)(((2 * kn + kk) * 55 + ft0 + i - 7 * kk) * 1024)); }
      { const int i = 12 + (lw >> 1); if (lane < 32) dma(rx, sb + 8192 + i * 1024 + (lw & 1) * 512, vx, (unsigned)(((2 * kn + 1) * 55 + ft0 + i - 7) * 1024 + (lw & 1) * 512)); }
    });
    bar();
  } else {
    const int w = wid, q = lane >> 4, tk = lane & 15, mh = w >> 1, kh = w & 1;
    f32x4 acc[7][2];
#pragma unroll
    for (int i = 0; i < 7; ++i) { acc[i][0] = (f32x4){0, 0, 0, 0}; acc[i][1] = (f32x4){0, 0, 0, 0}; }
    const int token = 64 * B.p + 16 * w + tk;
    const float bias = bf2f(P.bsp[token]);
    u32x2 z1[7];
    const char* z1p = P.ws + WS_Z1 + (size_t)(B.b * 4 + w) * 7 * 512 + lane * 8;
#pragma unroll
    for (int i = 0; i < 7; ++i) z1[i] = *(const u32x2*)(z1p + i * 512);
    const unsigned wa = (unsigned)((2 * mh) * 2048 + tk * 128 + (((q + 4 * kh) ^ ((lane >> 1) & 7)) * 16));
    const unsigned xa = 8192 + kh * 7168 + lane * 16;
    ksplit_loop<K4_NS, 32, 7, 2>(
        [&](bf16x8(&A)[7], bf16x8(&Bf)[2], int slot) { LAS char* sb = smem + slot * K4_SLOT;
          Bf[0] = *(LAS bf16x8*)(sb + wa); A[0] = *(LAS bf16x8*)(sb + xa); Bf[1] = *(LAS bf16x8*)(sb + wa + 2048);
#pragma unroll
          for (int i = 1; i < 7; ++i) A[i] = *(LAS bf16x8*)(sb + xa + i * 1024); },
        [&](bf16x8(&A)[7], bf16x8(&Bf)[2]) {
#pragma unroll
          for (int i = 0; i < 7; ++i) { acc[i][0] = mfma(A[i], Bf[0], acc[i][0]); acc[i][1] = mfma(A[i], Bf[1], acc[i][1]); } });
    f32x4 g[7];
    kreduce<7>(acc, g, smem + K4_SCR, w, lane);
    u32x2 pk[7];
#pragma unroll
    for (int i = 0; i < 7; ++i) {
      pk[i].x = pack2(bflo(z1[i].x) * (g[i][0] + bias), bfhi(z1[i].x) * (g[i][1] + bias));
      pk[i].y = pack2(bflo(z1[i].y) * (g[i][2] + bias), bfhi(z1[i].y) * (g[i][3] + bias));
    }
    char* img = P.ws + WS_P + (size_t)B.p * 28 * 4096 + w * 1024;
#pragma unroll
    for (int i = 0; i < 8; i += 2) {
      const int i2 = i + 1 < 7 ? i + 1 : i;
      const u32x4 ch = swap_pair(pk[i], pk[i2]);
      const int ti = (q & 1) ? i2 : i, ft = ft0 + ti, kb = ft >> 1, qq = 2 * (ft & 1) + (q >> 1);
      if (ti < nft) *(u32x4*)(img + (size_t)kb * 4096 + (qq * 16 + tk) * 16) = ch;
    }
  }
}

constexpr int K5_NS = 6, K5_SLOT = 18 * 1024, K5_SCR = K5_NS * K5_SLOT;
__global__ __launch_bounds__(NT, 2) void k5_outproj(Params P) {
  extern __shared__ __attribute__((aligned(16))) char smem_[];
  LAS char* smem = (LAS char*)smem_;
  const int tid = threadIdx.x, lane = tid & 63, wid = __builtin_amdgcn_readfirstlane(tid >> 6);
  const Blk B = blk_of(blockIdx.x);
  asm volatile("" ::"s"(P.ws), "s"(P.out));
  if (wid >= 4) {
    const int lw = wid - 4;
    __builtin_amdgcn_s_setprio(3);
    const auto rw = mkrsrc(P.ws + WS_WOUT, 27 * 40 * 1024), rx = mkrsrc(P.ws + WS_P, 32 * 28 * 4096);
    const unsigned vx = lane * 16;
    loader_loop<K5_NS, 14, 5>([&](int n, int slot) {
      LAS char* sb = smem + slot * K5_SLOT;
#pragma unroll
      for (int u = 0; u < 2; ++u) { const int i = lw * 2 + u, kk = i >= 5; dma(rw, sb + i * 1024, vx, (unsigned)(((2 * n + kk) * 40 + 5 * B.j + i - 5 * kk) * 1024)); }
      { const int i = 8 + (lw >> 1); if (lane < 32) dma(rw, sb + i * 1024 + (lw & 1) * 512, vx, (unsigned)(((2 * n + 1) * 40 + 5 * B.j + i - 5) * 1024 + (lw & 1) * 512)); }
      const unsigned sx = (unsigned)((B.p * 28 + 2 * n) * 4096);
      dma(rx, sb + 10240 + (2 * lw) * 1024, vx, sx + (2 * lw) * 1024);
      dma(rx, sb + 10240 + (2 * lw + 1) * 1024, vx, sx + (2 * lw + 1) * 1024);
    });
    bar();
  } else {
    const int w = wid, q = lane >> 4, tk = lane & 15, mh = w >> 1, kh = w & 1;
    f32x4 acc[5][2];
#pragma unroll
    for (int i = 0; i < 5; ++i) { acc[i][0] = (f32x4){0, 0, 0, 0}; acc[i][1] = (f32x4){0, 0, 0, 0}; }
    const int token = 64 * B.p + 16 * w + tk;
    u32x2 x1[5]; f32x4 gb[5];
    const float* mod = (const float*)(P.ws + WS_MOD);
    const char* xr = P.ws + WS_X1R + (size_t)(B.b * 4 + w) * 5 * 512 + lane * 8;
#pragma unroll
    for (int i = 0; i < 5; ++i) { x1[i] = *(const u32x2*)(xr + i * 512); gb[i] = *(const f32x4*)(mod + 5 * D + 80 * B.j + 16 * i + 4 * q); }
    const unsigned wa = kh * 5120 + lane * 16;
    const unsigned xa = 10240 + (kh * 4 + 2 * mh) * 1024 + lane * 16;
    int step = 0;
    ksplit_loop<K5_NS, 14, 5, 2>(
        [&](bf16x8(&A)[5], bf16x8(&Bf)[2], int slot) { LAS char* sb = smem + slot * K5_SLOT;
          A[0] = *(LAS bf16x8*)(sb + wa); Bf[0] = *(LAS bf16x8*)(sb + xa); Bf[1] = *(LAS bf16x8*)(sb + xa + 1024);
#pragma unroll
          for (int i = 1; i < 5; ++i) A[i] = *(LAS bf16x8*)(sb + wa + i * 1024); },
        [&](bf16x8(&A)[5], bf16x8(&Bf)[2]) {
          const bool skip = (step == 13) && kh;
          if (!skip) {
#pragma unroll
            for (int i = 0; i < 5; ++i) { acc[i][0] = mfma(A[i], Bf[0], acc[i][0]); acc[i][1] = mfma(A[i], Bf[1], acc[i][1]); } }
          ++step; });
    f32x4 o[5];
    kreduce<5>(acc, o, smem + K5_SCR, w, lane);
    u32x2 pk[5];
#pragma unroll
    for (int i = 0; i < 5; ++i) {
      pk[i].x = pack2(bflo(x1[i].x) + gb[i][0] * o[i][0], bfhi(x1[i].x) + gb[i][1] * o[i][1]);
      pk[i].y = pack2(bflo(x1[i].y) + gb[i][2] * o[i][2], bfhi(x1[i].y) + gb[i][3] * o[i][3]);
    }
    bf16_t* orow = P.out + (size_t)token * D + 80 * B.j;
#pragma unroll
    for (int i = 0; i < 6; i += 2) {
      const int i2 = i + 1 < 5 ? i + 1 : i;
      const u32x4 ch = swap_pair(pk[i], pk[i2]);
      const int ti = (q & 1) ? i2 : i;
      *(u32x4*)(orow + 16 * ti + 8 * (q >> 1)) = ch;
    }
  }
}

extern "C" void kernel_launch(void* const* d_in, const int* in_sizes, int n_in, void* d_out, int out_size, void* d_ws, size_t ws_size, hipStream_t stream) {
  Params P;
  P.x = (const bf16_t*)d_in[0]; P.wsr = (const bf16_t*)d_in[1]; P.wsi = (const bf16_t*)d_in[2];
  P.wdr = (const bf16_t*)d_in[3]; P.wdi = (const bf16_t*)d_in[4]; P.win = (const bf16_t*)d_in[6];
  P.wsp = (const bf16_t*)d_in[7]; P.bsp = (const bf16_t*)d_in[8]; P.wout = (const bf16_t*)d_in[9];
  P.c = (const bf16_t*)d_in[10]; P.wmod = (const bf16_t*)d_in[11];
  P.out = (bf16_t*)d_out; P.ws = (char*)d_ws;
  constexpr int L0 = 49152, L1 = K1_NS * K1_SLOT, L2 = K2_SCR + 20480, L3 = K3_LDS, L4 = K4_SCR + 28672, L5 = K5_SCR + 20480;
  (void)hipFuncSetAttribute((const void*)k1_seqmix, hipFuncAttributeMaxDynamicSharedMemorySize, L1);
  (void)hipFuncSetAttribute((const void*)k2_hidmix, hipFuncAttributeMaxDynamicSharedMemorySize, L2);
  (void)hipFuncSetAttribute((const void*)k3_inproj, hipFuncAttributeMaxDynamicSharedMemorySize, L3);
  (void)hipFuncSetAttribute((const void*)k4_spatial, hipFuncAttributeMaxDynamicSharedMemorySize, L4);
  (void)hipFuncSetAttribute((const void*)k5_outproj, hipFuncAttributeMaxDynamicSharedMemorySize, L5);
  k0_prep<<<256, NT, L0, stream>>>(P);
  k1_seqmix<<<256, NT, L1, stream>>>(P);
  k2_hidmix<<<256, NT, L2, stream>>>(P);
  k3_inproj<<<256, NT, L3, stream>>>(P);
  k4_spatial<<<256, NT, L4, stream>>>(P);
  k5_outproj<<<256, NT, L5, stream>>>(P);
}
```

```cpp
#include <hip/hip_runtime.h>
#include <stdint.h>
#include <type_traits>

typedef unsigned short bf16_t;
typedef short bf16x8 __attribute__((ext_vector_type(8)));
typedef short s16x4 __attribute__((ext_vector_type(4)));
typedef float f32x4 __attribute__((ext_vector_type(4)));
typedef float f32x2 __attribute__((ext_vector_type(2)));
typedef unsigned u32x4 __attribute__((ext_vector_type(4)));
typedef unsigned u32x2 __attribute__((ext_vector_type(2)));
typedef __bf16 bf2_t __attribute__((ext_vector_type(2)));
#define LAS __attribute__((address_space(3)))
#define GAS __attribute__((address_space(1)))
typedef GAS unsigned gu32;
typedef GAS unsigned long long gu64;
#define RLX_AGENT __ATOMIC_RELAXED, __HIP_MEMORY_SCOPE_AGENT
#define DEV __device__ __forceinline__

constexpr int S = 2048, D = 640, F = 1728, FH = 864;
constexpr int NT = 512;

constexpr unsigned WS_EPOCH = 0;
constexpr unsigned WS_TMO   = 256;
constexpr unsigned WS_FN = 4096, WS_F2 = 8192, WS_FU = 12288, WS_FX = 16384, WS_FP = 20480, WS_FZ = 24576;
constexpr unsigned WS_MOD  = 65536;
constexpr unsigned WS_MODB = WS_MOD + 16384;

constexpr unsigned WS_XN   = 131072;
constexpr unsigned WS_WIN  = WS_XN + 64 * 40 * 1024;
constexpr unsigned WS_WOUT = WS_WIN + 20 * 109 * 1024;
constexpr unsigned WS_UV   = WS_WOUT + 27 * 40 * 1024;
constexpr unsigned WS_X1P  = WS_UV + 32 * 2 * 20 * 4096;
constexpr unsigned WS_ST1  = WS_X1P + 32 * 20 * 4096;
constexpr unsigned WS_Z2N  = WS_ST1 + 2048 * 64;
constexpr unsigned WS_ST2  = WS_Z2N + 64 * 55 * 1024;
constexpr unsigned WS_P    = WS_ST2 + 2048 * 128;
constexpr unsigned WS_END  = WS_P + 32 * 28 * 4096;

struct Params {
  const bf16_t *x, *wsr, *wsi, *wdr, *wdi, *win, *wsp, *bsp, *wout, *c, *wmod;
  bf16_t* out;
  char* ws;
};

#define STAMP_DECL
#define STAMP(i) do {} while (0)
#define STAMP_FLUSH() do {} while (0)
#define CSTAMP(i) do {} while (0)

template <int N> DEV void wait_vm() { asm volatile("s_waitcnt vmcnt(%0)" ::"n"(N) : "memory"); }
DEV void wait_lgkm0() { asm volatile("s_waitcnt lgkmcnt(0)" ::: "memory"); }
DEV void bar() { asm volatile("" ::: "memory"); __builtin_amdgcn_s_barrier(); asm volatile("" ::: "memory"); }
DEV void sched_fence() { __builtin_amdgcn_sched_barrier(0); }
template <int I, int N, class Fn> DEV void static_for(Fn&& f) { if constexpr (I < N) { f(std::integral_constant<int, I>{}); static_for<I + 1, N>(f); } }

DEV float bflo(unsigned v) { return __uint_as_float(v << 16); }
DEV float bfhi(unsigned v) { return __uint_as_float(v & 0xffff0000u); }
DEV unsigned pack2(float lo, float hi) { bf2_t v; v[0] = (__bf16)lo; v[1] = (__bf16)hi; return __builtin_bit_cast(unsigned, v); }
DEV float rbf(float f) { return bflo(pack2(f, f)); }
DEV float dot2(unsigned a, unsigned b, float c) { return __builtin_amdgcn_fdot2_f32_bf16(__builtin_bit_cast(bf2_t, a), __builtin_bit_cast(bf2_t, b), c, false); }
DEV f32x4 mfma(bf16x8 a, bf16x8 b, f32x4 c) { return __builtin_amdgcn_mfma_f32_16x16x32_bf16(a, b, c, 0, 0, 0); }

DEV u32x4 mkrs(const void* p, unsigned bytes) {
  const unsigned long long a = (unsigned long long)p;
  u32x4 r; r.x = (unsigned)a; r.y = (unsigned)(a >> 32) & 0xffffu; r.z = bytes; r.w = 0x00020000u; return r;
}
DEV __amdgpu_buffer_rsrc_t mkrsrc(const void* p, unsigned bytes) { return __builtin_amdgcn_make_buffer_rsrc((void*)p, 0, bytes, 0x00020000); }
template <bool SC1> DEV void dma(u32x4 r, unsigned lds, unsigned voff, unsigned soff) {
  if constexpr (SC1) asm volatile("s_mov_b32 m0, %0\n\ts_nop 0\n\tbuffer_load_dwordx4 %1, %2, %3 offen sc1 lds" ::"s"(lds), "v"(voff), "s"(r), "s"(soff) : "memory");
  else asm volatile("s_mov_b32 m0, %0\n\ts_nop 0\n\tbuffer_load_dwordx4 %1, %2, %3 offen lds" ::"s"(lds), "v"(voff), "s"(r), "s"(soff) : "memory");
}
template <bool SC1> DEV void dma_half(u32x4 r, unsigned lds, unsigned voff, unsigned soff) {
  if constexpr (SC1) asm volatile("s_mov_b32 m0, %0\n\ts_mov_b32 exec_hi, 0\n\tbuffer_load_dwordx4 %1, %2, %3 offen sc1 lds\n\ts_mov_b32 exec_hi, -1" ::"s"(lds), "v"(voff), "s"(r), "s"(soff) : "memory");
  else asm volatile("s_mov_b32 m0, %0\n\ts_mov_b32 exec_hi, 0\n\tbuffer_load_dwordx4 %1, %2, %3 offen lds\n\ts_mov_b32 exec_hi, -1" ::"s"(lds), "v"(voff), "s"(r), "s"(soff) : "memory");
}
constexpr int SC1 = 16;
DEV void st16(__amdgpu_buffer_rsrc_t r, unsigned off, u32x4 v) { __builtin_amdgcn_raw_buffer_store_b128(v, r, off, 0, SC1); }
DEV u32x4 ld16(__amdgpu_buffer_rsrc_t r, unsigned off) { return __builtin_amdgcn_raw_buffer_load_b128(r, off, 0, SC1); }
DEV f32x4 ld16f(__amdgpu_buffer_rsrc_t r, unsigned off) { return __builtin_bit_cast(f32x4, ld16(r, off)); }

template <int CTRL> DEV float dpp_add(float v) { return v + __builtin_bit_cast(float, __builtin_amdgcn_update_dpp(0, __builtin_bit_cast(int, v), CTRL, 0xf, 0xf, true)); }
DEV float xsum_16(float v) { v = dpp_add<0xB1>(v); v = dpp_add<0x4E>(v); v = dpp_add<0x141>(v); return dpp_add<0x140>(v); }
DEV float xsum_r16(float v) { auto r = __builtin_amdgcn_permlane16_swap(__float_as_uint(v), __float_as_uint(v), false, false); return __uint_as_float(r[0]) + __uint_as_float(r[1]); }
DEV float xsum_r32(float v) { auto r = __builtin_amdgcn_permlane32_swap(__float_as_uint(v), __float_as_uint(v), false, false); return __uint_as_float(r[0]) + __uint_as_float(r[1]); }
DEV float xsum_q(float v) { return xsum_r32(xsum_r16(v)); }
DEV float gelu_tanh(float x) {
  const float k1 = -2.0f * 0.7978845608028654f * 1.4426950408889634f;
  const float t = x * __builtin_fmaf(x * x, 0.044715f * k1, k1);
  return x * __builtin_amdgcn_rcpf(1.0f + __builtin_amdgcn_exp2f(t));
}
DEV f32x2 gelu2(f32x2 x) {
  const float k1 = -2.0f * 0.7978845608028654f * 1.4426950408889634f;
  const f32x2 t = x * ((x * x) * (0.044715f * k1) + k1);
  const f32x2 d = (f32x2){__builtin_amdgcn_exp2f(t[0]), __builtin_amdgcn_exp2f(t[1])} + 1.0f;
  return x * (f32x2){__builtin_amdgcn_rcpf(d[0]), __builtin_amdgcn_rcpf(d[1])};
}
DEV u32x4 swap_pair(u32x2 a, u32x2 b) {
  auto r0 = __builtin_amdgcn_permlane16_swap(a.x, b.x, false, false);
  auto r1 = __builtin_amdgcn_permlane16_swap(a.y, b.y, false, false);
  return (u32x4){r0[0], r1[0], r0[1], r1[1]};
}

DEV bool spin_fail(unsigned& spins, char* ws) {
  __builtin_amdgcn_s_sleep(1);
  if (++spins > (1u << 17)) { __hip_atomic_store((gu32*)(ws + WS_TMO), 1u, RLX_AGENT); return true; }
  return false;
}
DEV void set_flag(char* ws, unsigned off, unsigned tag) { __hip_atomic_store((gu32*)(ws + off), tag, RLX_AGENT); }
DEV void wait_flags8(char* ws, unsigned off, unsigned tag, int lane) {
  for (unsigned spins = 0;;) {
    const unsigned v = lane < 8 ? __hip_atomic_load((gu32*)(ws + off) + lane, RLX_AGENT) : tag;
    if (__all(v == tag)) break;
    if (spin_fail(spins, ws)) break;
  }
}
DEV bool flags_ok(u32x4 v, unsigned tag) { return __all(v.x == tag && v.y == tag && v.z == tag && v.w == tag); }
DEV u32x4 peek256(__amdgpu_buffer_rsrc_t rf, unsigned off, int lane) { return ld16(rf, off + (lane >> 1) * 128 + (lane & 1) * 16); }
DEV void wait256(__amdgpu_buffer_rsrc_t rf, char* ws, unsigned off, unsigned tag, int lane) {
  for (unsigned spins = 0;;) { if (flags_ok(peek256(rf, off, lane), tag)) break; if (spin_fail(spins, ws)) break; }
}

DEV void wait_panels(__amdgpu_buffer_rsrc_t rf, char* ws, unsigned off, int first, int count, unsigned tag, int lane) {
  const unsigned o = lane < 2 * count ? off + (((lane >> 1) + first) & 31) * 128 + (lane & 1) * 16 : off + (first & 31) * 128;
  for (unsigned spins = 0;;) { if (flags_ok(ld16(rf, o), tag)) break; if (spin_fail(spins, ws)) break; }
}

template <int NS, int S_, int CW, int CX, class IW, class IX>
DEV void loader_run(IW&& issueW, IX&& issueX) {
  static_for<0, NS>([&](auto ic) { issueX(decltype(ic)::value, decltype(ic)::value); });
  wait_vm<(NS - 1) * CX>(); bar();
  static_for<0, NS - 2>([&](auto ic) { constexpr int n = decltype(ic)::value;
    wait_vm<(NS - 2) * CX + n * CW>(); bar(); issueW(n + NS, n); issueX(n + NS, n); });
  int slot = NS - 2;
#pragma unroll 1
  for (int n = NS - 2; n < S_ - NS; ++n) {
    wait_vm<(NS - 2) * (CW + CX)>(); bar();
    issueW(n + NS, slot); issueX(n + NS, slot);
    slot = slot + 1 == NS ? 0 : slot + 1;
  }
  static_for<0, NS - 1>([&](auto ic) { constexpr int r = NS - 2 - decltype(ic)::value; wait_vm<r*(CW + CX)>(); bar(); });
}

template <int NS, int S_, int NA, int NB, int NM, class Rd, class Mm, class Hook, class Post>
DEV void ksplit_steps(bf16x8 (&A0)[NA], bf16x8 (&B0)[NB], bf16x8 (&A1)[NA], bf16x8 (&B1)[NB], int& slot, int n0, int n1, Rd&& rd, Mm&& mm, Hook&& hook, Post&& post) {
  constexpr int NR = NA + NB;
#define KS_ILV() do { _Pragma("unroll") for (int z = 0; z < NR; ++z) { __builtin_amdgcn_sched_group_barrier(0x8, 1, 0); __builtin_amdgcn_sched_group_barrier(0x100, 1, 0); } __builtin_amdgcn_sched_group_barrier(0x8, NM - NR, 0); } while (0)
#pragma unroll 1
  for (int n = n0; n < n1; n += 2) {
    const int s1 = slot + 1 == NS ? 0 : slot + 1;
    hook(n); wait_lgkm0(); bar(); post(n); sched_fence();
    rd(A1, B1, s1, n + 1); mm(A0, B0, false); KS_ILV(); sched_fence();
    const int s2 = s1 + 1 == NS ? 0 : s1 + 1;
    hook(n + 1); wait_lgkm0(); bar(); post(n + 1); sched_fence();
    rd(A0, B0, s2, n + 2); mm(A1, B1, false); KS_ILV(); sched_fence();
    slot = s2;
  }
}
template <int NS, int S_, int NA, int NB, class Rd, class Mm>
DEV void ksplit_last(bf16x8 (&A0)[NA], bf16x8 (&B0)[NB], bf16x8 (&A1)[NA], bf16x8 (&B1)[NB], int& slot, Rd&& rd, Mm&& mm) {
  const int s1 = slot + 1 == NS ? 0 : slot + 1;
  wait_lgkm0(); bar(); sched_fence();
  rd(A1, B1, s1, S_ - 1); mm(A0, B0, false); mm(A1, B1, true);
  slot = s1;
}
template <int NS, int S_, int NA, int NB, class Rd, class Mm>
DEV void ksplit_loop(Rd&& rd, Mm&& mm) {
  static_assert((S_ & 1) == 0, "even step count");
  bf16x8 A0[NA], B0[NB], A1[NA], B1[NB];
  bar();
  rd(A0, B0, 0, 0);
  int slot = 0;
  ksplit_steps<NS, S_, NA, NB, NA * NB>(A0, B0, A1, B1, slot, 0, S_ - 2, rd, mm, [](int) {}, [](int) {});
  ksplit_last<NS, S_, NA, NB>(A0, B0, A1, B1, slot, rd, mm);
}
template <int N>
DEV void kreduce(f32x4 (&acc)[N][2], f32x4 (&res)[N], LAS char* scratch, int w, int lane) {
  const int kh = w & 1;
  LAS char* mine = scratch + w * (N * 1024) + lane * 16;
  LAS char* other = scratch + (w ^ 1) * (N * 1024) + lane * 16;
  if (kh) {
#pragma unroll
    for (int i = 0; i < N; ++i) { *(LAS f32x4*)(mine + i * 1024) = acc[i][0]; res[i] = acc[i][1]; }
  } else {
#pragma unroll
    for (int i = 0; i < N; ++i) { *(LAS f32x4*)(mine + i * 1024) = acc[i][1]; res[i] = acc[i][0]; }
  }
  wait_lgkm0(); bar();
#pragma unroll
  for (int i = 0; i < N; ++i) res[i] += *(LAS f32x4*)(other + i * 1024);
}

constexpr int A_NS = 5, A_SLOT = 26 * 1024;
constexpr int P0_T = 130 * 1024, P0_PT = 140 * 1024, P0_RED = 156 * 1024;
constexpr int B_NS = 6, B_SLOT = 18 * 1024, B_SCR = 36 * 1024;
constexpr int C_NS = 5, C_X = 0, C_W = 82 * 1024, C_WSLOT = 14 * 1024;
constexpr int C_SCR = C_W + 2 * C_WSLOT;
constexpr int D_NS = 5, D_SLOT = 22 * 1024, D_SCR = 110 * 1024;
constexpr int E_NS = 6, E_SLOT = 18 * 1024, E_SCR = 110 * 1024;
constexpr int T_MODB = 152 * 1024;
constexpr int T_CV = T_MODB + 3072 + 64;
constexpr int T_MOD = 158 * 1024;
constexpr int LDS_BYTES = 160 * 1024;
static_assert(T_CV + 896 <= T_MOD && C_W + C_NS * C_WSLOT <= T_MODB && D_SCR + 28 * 1024 <= T_MODB && E_SCR + 20 * 1024 <= T_MODB, "LDS map");

DEV void loader_main(const Params& P, const unsigned lds0, LAS char* smem, const int lw, const int lane_, const int b, const int p, const int j, const int krot) {
  STAMP_DECL STAMP(0);
  char* const ws = P.ws;
  const int ft0 = 27 * (j >> 2) + 7 * (j & 3);
#define LLANE int lane = lane_; asm volatile("" : "+v"(lane)); const unsigned vx = lane * 16; (void)vx;
#define LV4K const unsigned v4k0 = (unsigned)(lane >> 3) * 4096u + (unsigned)(((lane & 7) ^ (lane >> 4)) * 16), v4k1 = (unsigned)(8 + (lane >> 3)) * 4096u + (unsigned)(((lane & 7) ^ (4 + (lane >> 4))) * 16);
  {
    LLANE LV4K
    const u32x4 rre = mkrs(P.wsr, S * S * 2), rim = mkrs(P.wsi, S * S * 2), rx = mkrs(ws + WS_XN, S * D * 2);
    const unsigned wrow = (unsigned)(64 * p + 16 * lw) * 4096u;
    auto issueW = [&](int n, int slot) {
      const unsigned sb = lds0 + slot * A_SLOT + lw * 4096, sw = wrow + ((n + krot) & 31) * 128;
      dma<false>(rre, sb, v4k0, sw); dma<false>(rre, sb + 1024, v4k1, sw); dma<false>(rim, sb + 2048, v4k0, sw); dma<false>(rim, sb + 3072, v4k1, sw);
    };
    const int xi0 = lw * 2, xi1 = lw * 2 + 1, xi2 = 8 + (lw >> 1);
    const unsigned xo0 = (unsigned)(((xi0 >= 5) * 40 + 5 * j + xi0 - 5 * (xi0 >= 5)) * 1024), xo1 = (unsigned)(((xi1 >= 5) * 40 + 5 * j + xi1 - 5 * (xi1 >= 5)) * 1024);
    const unsigned xo2 = (unsigned)((40 + 5 * j + xi2 - 5) * 1024 + (lw & 1) * 512);
    auto issueX = [&](int n, int slot) {
      const unsigned sb = lds0 + slot * A_SLOT + 16384, so = ((n + krot) & 31) * 80 * 1024;
      dma<true>(rx, sb + xi0 * 1024, vx, so + xo0); dma<true>(rx, sb + xi1 * 1024, vx, so + xo1); dma_half<true>(rx, sb + xi2 * 1024 + (lw & 1) * 512, vx, so + xo2);
    };
    issueW(0, 0); issueW(1, 1);
    bar();
    bar();
    STAMP(30);
    bar();
    STAMP(33);
    __builtin_amdgcn_s_setprio(3);
    issueX(0, 0); issueX(1, 1);
    static_for<2, A_NS>([&](auto ic) { issueW(decltype(ic)::value, decltype(ic)::value); issueX(decltype(ic)::value, decltype(ic)::value); });
    const int idx = b >> 3, unit0 = (idx & 3) + 4 * ((b & 7) + 8 * (idx >> 2));
    const bool domod = unit0 < 240; const int unit = domod ? unit0 : 0;
    u32x4 wm[5]; unsigned cs[5];
#pragma unroll
    for (int i = 0; i < 5; ++i) { const int row = lw * 160 + i * 32 + (lane >> 1);
      const bf16_t* pw = P.wmod + (size_t)row * (6 * D) + unit * 16 + (lane & 1) * 8; const bf16_t* pc = P.c + row;
      asm volatile("global_load_dwordx4 %0, %1, off" : "=v"(wm[i]) : "v"(pw) : "memory");
      asm volatile("global_load_ushort %0, %1, off" : "=v"(cs[i]) : "v"(pc) : "memory"); }
    STAMP(34);
    wait_vm<3 + 3 * 7 + 10>();
    bar();
    wait_vm<3 * 7 + 10>(); bar(); issueW(A_NS, 0); issueX(A_NS, 0);
    wait_vm<2 * 7 + 10 + 7>(); bar(); issueW(A_NS + 1, 1); issueX(A_NS + 1, 1);
    wait_vm<14>();
    asm volatile("" : "+v"(wm[0]), "+v"(wm[1]), "+v"(wm[2]), "+v"(wm[3]), "+v"(wm[4]), "+v"(cs[0]), "+v"(cs[1]), "+v"(cs[2]), "+v"(cs[3]), "+v"(cs[4]));
    bar(); issueW(A_NS + 2, 2); issueX(A_NS + 2, 2);
    {
      float a[8] = {0, 0, 0, 0, 0, 0, 0, 0};
#pragma unroll
      for (int i = 0; i < 5; ++i) { const float x = bflo(cs[i]); const float s = rbf(x * __builtin_amdgcn_rcpf(1.0f + __expf(-x)));
        a[0] += s * bflo(wm[i].x); a[1] += s * bfhi(wm[i].x); a[2] += s * bflo(wm[i].y); a[3] += s * bfhi(wm[i].y);
        a[4] += s * bflo(wm[i].z); a[5] += s * bfhi(wm[i].z); a[6] += s * bflo(wm[i].w); a[7] += s * bfhi(wm[i].w); }
#pragma unroll
      for (int e = 0; e < 8; ++e) { float v = dpp_add<0x4E>(a[e]);
        v = dpp_add<0x124>(v); v = dpp_add<0x128>(v);
        a[e] = xsum_q(v); }
      if (lane < 2) {
#pragma unroll
        for (int e = 0; e < 8; ++e) ((LAS float*)(smem + P0_RED))[lw * 16 + lane * 8 + e] = a[e];
      }
      wait_lgkm0();
    }
    wait_vm<(A_NS - 2) * 7>(); bar(); issueW(A_NS + 3, 3); issueX(A_NS + 3, 3);
    if (lw == 0 && domod && lane < 16) {
      const LAS float* red = (const LAS float*)(smem + P0_RED);
      float s = rbf((red[lane] + red[16 + lane]) + (red[32 + lane] + red[48 + lane]));
      const int n = unit * 16 + lane, seg = n / D;
      if (seg == 1 || seg == 4) s = rbf(1.0f + s);
      __hip_atomic_store((gu32*)(ws + WS_MOD) + n, __float_as_uint(s), RLX_AGENT);
      const unsigned hi = __float_as_uint(s) >> 16, nb = (unsigned)__builtin_amdgcn_update_dpp(0, (int)hi, 0xB1, 0xf, 0xf, true);
      if ((seg == 3 || seg == 4) && !(lane & 1)) __hip_atomic_store((gu32*)(ws + WS_MODB) + (((seg == 4 ? 0 : D) + n - seg * D) >> 1), hi | (nb << 16), RLX_AGENT);
    }
    static_assert(A_NS == 5, "the start-up schedule above is written for five slots");
    const u32x4 rmod = mkrs(ws + WS_MOD, 3840 * 4);
    const int tl = lane / 20, tc = lane - 20 * tl;
    const unsigned tv0 = lane < 60 ? (unsigned)((tl * D + 80 * j) * 4 + tc * 16) : 0x7ffffff0u;
    const unsigned tv1 = lane < 40 ? (unsigned)(((4 + tl) * D + 80 * j) * 4 + tc * 16) : 0x7ffffff0u;
    int slot = 4;
#pragma unroll 1
    for (int n = 4; n < 32 - A_NS; ++n) {
      wait_vm<(A_NS - 2) * 7>(); bar();
      issueW(n + A_NS, slot); issueX(n + A_NS, slot);
      if (n == 20 && lw == 0) { dma<true>(rmod, lds0 + T_MOD, tv0, 0); dma<true>(rmod, lds0 + T_MOD + 960, tv1, 0); }
      slot = slot + 1 == A_NS ? 0 : slot + 1;
    }
    static_for<0, A_NS - 1>([&](auto ic) { constexpr int r = A_NS - 2 - decltype(ic)::value; wait_vm<r * 7>(); bar(); });
    bar();
  }
  {
    LLANE
    const u32x4 rre = mkrs(P.wdr, D * D * 2), rim = mkrs(P.wdi, D * D * 2), rx = mkrs(ws + WS_UV, 32 * 2 * 20 * 4096);
    const unsigned v0 = (unsigned)(lane >> 3) * 1280u + (unsigned)(((lane & 7) ^ (lane >> 4)) * 16);
    const unsigned v1 = (unsigned)(lane >> 3) * 1280u + (unsigned)(((lane & 7) ^ (4 + (lane >> 4))) * 16);
    const unsigned vh = (unsigned)(lane >> 3) * 1280u + (unsigned)(((lane & 7) ^ ((4 * (lw >> 1) + 2 * (lw & 1) + (lane >> 4)) & 7)) * 16);
    const unsigned wb = (unsigned)(80 * j) * 1280u;
    auto issueW = [&](int n, int slot) {
      const unsigned sb = lds0 + slot * B_SLOT; const int half = n >= 10; const unsigned sw = wb + (n - 10 * half) * 128;
      const u32x4 rw = half ? rim : rre;
      dma<false>(rw, sb + (2 * lw) * 1024, v0, sw + (unsigned)(16 * lw) * 1280u);
      dma<false>(rw, sb + (2 * lw + 1) * 1024, v1, sw + (unsigned)(16 * lw + 8) * 1280u);
      dma_half<false>(rw, sb + (8 + (lw >> 1)) * 1024 + (lw & 1) * 512, vh, sw + (unsigned)(64 + 8 * (lw >> 1) + 4 * (lw & 1)) * 1280u);
    };
    auto issueX = [&](int n, int slot) {
      const unsigned sb = lds0 + slot * B_SLOT + 10240 + lw * 2048, sx = (unsigned)(p * 40 + 2 * n) * 4096u + lw * 2048;
      dma<true>(rx, sb, vx, sx); dma<true>(rx, sb + 1024, vx, sx + 1024);
    };
    __builtin_amdgcn_s_setprio(0);
    bar();
    static_for<0, B_NS>([&](auto ic) { issueW(decltype(ic)::value, decltype(ic)::value); });
    bar();
    __builtin_amdgcn_s_setprio(3);
    loader_run<B_NS, 20, 3, 2>(issueW, issueX);
    bar();
  }
  {
    LLANE
    const u32x4 rw = mkrs(ws + WS_WIN, 20 * 109 * 1024), rx = mkrs(ws + WS_X1P, 32 * 20 * 4096), rmb = mkrs(ws + WS_MODB, 2560);
    unsigned wo[4], wl[4];
#pragma unroll
    for (int u = 0; u < 3; ++u) { const int i = lw * 3 + u, kk = i >= 7; wo[u] = (unsigned)((kk * 109 + i - 7 * kk) * 1024); wl[u] = i * 1024; }
    { const int i = 12 + (lw >> 1); wo[3] = (unsigned)((109 + i - 7) * 1024 + (lw & 1) * 512); wl[3] = i * 1024 + (lw & 1) * 512; }
    auto issueW = [&](int n, int slot) {
      const unsigned sb = lds0 + C_W + slot * C_WSLOT; const int z1 = n >= 10;
      const unsigned so = (unsigned)((2 * (n - 10 * z1) * 109 + (z1 ? 0 : 54) + ft0) * 1024);
      dma<true>(rw, sb + wl[0], vx, so + wo[0]); dma<true>(rw, sb + wl[1], vx, so + wo[1]); dma<true>(rw, sb + wl[2], vx, so + wo[2]); dma_half<true>(rw, sb + wl[3], vx, so + wo[3]);
    };
    auto issueX = [&](int n) {
      const unsigned o = (unsigned)(2 * n) * 4096u + lw * 2048, sx = (unsigned)p * 20 * 4096u + o;
      dma<true>(rx, lds0 + C_X + o, vx, sx); dma<true>(rx, lds0 + C_X + o + 1024, vx, sx + 1024);
    };
    __builtin_amdgcn_s_setprio(0);
    bar();
    if (lw < 3) dma<true>(rmb, lds0 + T_MODB + lw * 1024, vx + lw * 1024, 0);
    static_for<0, C_NS>([&](auto ic) { issueW(decltype(ic)::value, decltype(ic)::value); });
    bar();
    __builtin_amdgcn_s_setprio(3);
    static_for<0, C_NS>([&](auto ic) { issueX(decltype(ic)::value); });
    wait_vm<(C_NS - 1) * 2>(); bar();
    static_for<0, 9>([&](auto ic) {
      constexpr int n = decltype(ic)::value, need = n + 1;
      constexpr int last = n + C_NS - 1 < 11 ? n + C_NS - 1 : 11;
      constexpr int cnt = [] { int c = 0;
        if (need < C_NS) { c += (C_NS - 1 - need) * 2; for (int s = C_NS; s <= last; ++s) c += (s < 10 ? 6 : 4); }
        else { for (int s = need + 1; s <= last; ++s) c += (s < 10 ? 6 : 4); }
        return c; }();
      wait_vm<cnt>(); bar();
      if constexpr (n + C_NS < 12) { issueW(n + C_NS, n % C_NS); if constexpr (n + C_NS < 10) issueX(n + C_NS); }
    });
    bar();
    bar();
    issueW(12, 2); issueW(13, 3); issueW(14, 4);
    wait_vm<16>(); bar();
    static_for<10, 19>([&](auto ic) {
      constexpr int n = decltype(ic)::value;
      constexpr int last = n + 4 < 19 ? n + 4 : 19;
      wait_vm<(last - (n + 1)) * 4>(); bar();
      if constexpr (n + C_NS < 20) issueW(n + C_NS, n % C_NS);
    });
    bar();
  }
  {
    LLANE LV4K
    const u32x4 rw = mkrs(P.wsp, S * S * 2), rx = mkrs(ws + WS_Z2N, 64 * 55 * 1024);
    const unsigned wrow = (unsigned)(64 * p + 16 * lw) * 4096u;
    unsigned xo[4], xl[4];
#pragma unroll
    for (int u = 0; u < 3; ++u) { const int i = lw * 3 + u, kk = i >= 7; xo[u] = (unsigned)((kk * 55 + ft0 + i - 7 * kk) * 1024); xl[u] = 8192 + i * 1024; }
    { const int i = 12 + (lw >> 1); xo[3] = (unsigned)((55 + ft0 + i - 7) * 1024 + (lw & 1) * 512); xl[3] = 8192 + i * 1024 + (lw & 1) * 512; }
    auto issueW = [&](int n, int slot) {
      const unsigned sb = lds0 + slot * D_SLOT + lw * 2048, sw = wrow + ((n + krot) & 31) * 128;
      dma<false>(rw, sb, v4k0, sw); dma<false>(rw, sb + 1024, v4k1, sw);
    };
    auto issueX = [&](int n, int slot) {
      const unsigned sb = lds0 + slot * D_SLOT, so = ((n + krot) & 31) * 110 * 1024;
      dma<true>(rx, sb + xl[0], vx, so + xo[0]); dma<true>(rx, sb + xl[1], vx, so + xo[1]); dma<true>(rx, sb + xl[2], vx, so + xo[2]); dma_half<true>(rx, sb + xl[3], vx, so + xo[3]);
    };
    static_for<0, D_NS>([&](auto ic) { issueW(decltype(ic)::value, decltype(ic)::value); });
    loader_run<D_NS, 32, 2, 4>(issueW, issueX);
    bar();
  }
  {
    LLANE
    const u32x4 rw = mkrs(ws + WS_WOUT, 27 * 40 * 1024), rx = mkrs(ws + WS_P, 32 * 28 * 4096);
    const int i0 = lw * 2, i1 = lw * 2 + 1, i2 = 8 + (lw >> 1);
    const unsigned o0 = (unsigned)(((i0 >= 5) * 40 + 5 * j + i0 - 5 * (i0 >= 5)) * 1024), o1 = (unsigned)(((i1 >= 5) * 40 + 5 * j + i1 - 5 * (i1 >= 5)) * 1024);
    const unsigned o2 = (unsigned)((40 + 5 * j + i2 - 5) * 1024 + (lw & 1) * 512);
    auto issueW = [&](int n, int slot) {
      const unsigned sb = lds0 + slot * E_SLOT, so = (unsigned)n * 80 * 1024;
      dma<true>(rw, sb + i0 * 1024, vx, so + o0); dma<true>(rw, sb + i1 * 1024, vx, so + o1); dma_half<true>(rw, sb + i2 * 1024 + (lw & 1) * 512, vx, so + o2);
    };
    auto issueX = [&](int n, int slot) {
      const unsigned sb = lds0 + slot * E_SLOT + 10240 + lw * 2048, sx = (unsigned)(p * 28 + 2 * n) * 4096u + lw * 2048;
      dma<true>(rx, sb, vx, sx); dma<true>(rx, sb + 1024, vx, sx + 1024);
    };
    __builtin_amdgcn_s_setprio(0);
    bar();
    static_for<0, E_NS>([&](auto ic) { issueW(decltype(ic)::value, decltype(ic)::value); });
    bar();
    __builtin_amdgcn_s_setprio(3);
    loader_run<E_NS, 14, 3, 2>(issueW, issueX);
    bar();
  }
  STAMP_FLUSH();
}

DEV void consumer_main(const Params& P, LAS char* smem, const int w, const int lane_, const int b, const int p, const int j) {
  STAMP_DECL STAMP(0);
  char* const ws = P.ws;
  unsigned tagv = __hip_atomic_load((gu32*)(ws + WS_EPOCH), RLX_AGENT) + 1u, tag;
  const int mh = w >> 1, kh = w & 1;
  const auto rws = mkrsrc(ws, WS_END);
  const int ft0 = 27 * (j >> 2) + 7 * (j & 3), nft = (j & 3) == 3 ? 6 : 7;
  auto rot = [&](int i) { return i < 3 ? i + 4 * mh : i - 3 * mh; };
#define LANE_IDS int lane = lane_; asm volatile("" : "+v"(lane)); const int q = lane >> 4, tk = lane & 15, tid = w * 64 + lane, token = 64 * p + 16 * w + tk; (void)q; (void)tk; (void)tid; (void)token;
  {
    LANE_IDS
    unsigned v[2][5];
#pragma unroll
    for (int t = 0; t < 2; ++t) { const unsigned* xr = (const unsigned*)(P.x + (size_t)(64 * p + 8 * j + 2 * w + t) * D);
#pragma unroll
      for (int i = 0; i < 5; ++i) v[t][i] = xr[lane + 64 * i]; }
    const int u0 = w * 256 + b; const bool dopt = u0 < 810; const int u = dopt ? u0 : 0; const bool isin = u < 540;
    const int uu = isin ? u : u - 540, ncb = isin ? 27 : 10, pkb = uu / ncb, pcb = uu - pkb * ncb, ld = isin ? F : D;
    u32x4 pv[4];
    { const bf16_t* src = (isin ? P.win : P.wout) + (size_t)(pkb * 32) * ld + pcb * 64;
#pragma unroll
      for (int i = 0; i < 4; ++i) pv[i] = *(const u32x4*)(src + (size_t)(i * 8 + (lane >> 3)) * ld + (lane & 7) * 8); }
    LAS unsigned* t32 = (LAS unsigned*)(smem + P0_T);
    unsigned pk[2][5];
#pragma unroll
    for (int t = 0; t < 2; ++t) {
      float s = 0.f;
#pragma unroll
      for (int i = 0; i < 5; ++i) s += bflo(v[t][i]) + bfhi(v[t][i]);
      const float mean = xsum_q(xsum_16(s)) * (1.0f / D);
      float q2 = 0.f;
#pragma unroll
      for (int i = 0; i < 5; ++i) { const float a = bflo(v[t][i]) - mean, c = bfhi(v[t][i]) - mean; q2 += a * a + c * c; }
      const float rs = 1.0f / sqrtf(xsum_q(xsum_16(q2)) * (1.0f / D) + 1e-5f);
#pragma unroll
      for (int i = 0; i < 5; ++i) pk[t][i] = pack2((bflo(v[t][i]) - mean) * rs, (bfhi(v[t][i]) - mean) * rs);
    }
#pragma unroll
    for (int i = 0; i < 5; ++i) { const int d = 2 * (lane + 64 * i);
      t32[d * 4 + w] = (pk[0][i] & 0xffffu) | (pk[1][i] << 16); t32[(d + 1) * 4 + w] = (pk[0][i] >> 16) | (pk[1][i] & 0xffff0000u); }
    asm volatile("" : "+v"(tagv)); tag = __builtin_amdgcn_readfirstlane(tagv);
    wait_lgkm0(); bar();
    const unsigned xb = WS_XN + (unsigned)((2 * p + (j >> 2)) * 40 * 1024 + (j & 3) * 256);
#pragma unroll
    for (int i = 0; i < 3; ++i) { const int d = tid + 256 * i;
      if (d < D) st16(rws, xb + (d >> 4) * 1024 + (d & 15) * 16, *(LAS u32x4*)(smem + P0_T + d * 16)); }
    wait_vm<0>();
    bar();
    STAMP(1);
    if (w == 0 && lane == 0) set_flag(ws, WS_FN + p * 128 + j * 4, tag);
    {
      LAS char* t = smem + P0_PT + w * 4096;
#pragma unroll
      for (int i = 0; i < 4; ++i) *(LAS u32x4*)(t + (i * 8 + (lane >> 3)) * 128 + (lane & 7) * 16) = pv[i];
      const unsigned dst = dopt ? (isin ? WS_WIN + (unsigned)(pkb * 109 + pcb * 4) * 1024 : WS_WOUT + (unsigned)(pkb * 40 + pcb * 4) * 1024) + lane * 16 : 0xfffff000u;
      const int rq = tk >> 2, pp = lane & 3;
#pragma unroll
      for (int c = 0; c < 4; ++c) {
        const u32x2 l2 = __builtin_bit_cast(u32x2, __builtin_amdgcn_ds_read_tr16_b64_v4i16((LAS s16x4*)(t + (8 * q + rq) * 128 + c * 32 + pp * 8)));
        const u32x2 h2 = __builtin_bit_cast(u32x2, __builtin_amdgcn_ds_read_tr16_b64_v4i16((LAS s16x4*)(t + (8 * q + 4 + rq) * 128 + c * 32 + pp * 8)));
        st16(rws, dst + (dopt ? c * 1024 : 0), (u32x4){l2.x, l2.y, h2.x, h2.y});
      }
    }
    if (w == 0) { wait256(rws, ws, WS_FN, tag, lane); STAMP(23); }
    bar();
    STAMP(2);
  }
  u32x2 xv[5];
  {
    LANE_IDS
    f32x4 acc[2][5];
#pragma unroll
    for (int t = 0; t < 2; ++t)
#pragma unroll
      for (int i = 0; i < 5; ++i) acc[t][i] = (f32x4){0, 0, 0, 0};
    f32x4 rsum[2] = {(f32x4){0, 0, 0, 0}, (f32x4){0, 0, 0, 0}};
    const bf16x8 ones = __builtin_bit_cast(bf16x8, (u32x4){0x3f803f80u, 0x3f803f80u, 0x3f803f80u, 0x3f803f80u});
    const unsigned wa0 = (unsigned)(w * 4096 + tk * 128 + ((q ^ ((lane >> 1) & 7)) * 16));
    const unsigned xa = 16384 + lane * 16;
    bf16x8 Wa[2], Xa[5], Wb[2], Xb[5];
    auto rdm = [&](bf16x8(&Wn)[2], bf16x8(&Xn)[5], int slot, int kk, bf16x8(&Wf)[2], bf16x8(&Xf)[5]) {
      LAS char* sb = smem + slot * A_SLOT; const unsigned wa = wa0 ^ (kk * 64);
      static_for<0, 12>([&](auto ic) { constexpr int z = decltype(ic)::value, i = z >> 1, t = z & 1;
        if constexpr (z < 10) acc[t][i] = mfma(Xf[i], Wf[t], acc[t][i]); else rsum[t] = mfma(ones, Wf[t], rsum[t]);
        if constexpr (z == 0) Wn[0] = *(LAS bf16x8*)(sb + wa);
        if constexpr (z == 1) Xn[0] = *(LAS bf16x8*)(sb + xa + (kk * 5) * 1024);
        if constexpr (z == 2) Wn[1] = *(LAS bf16x8*)(sb + wa + 2048);
        if constexpr (z >= 3 && z < 7) Xn[z - 2] = *(LAS bf16x8*)(sb + xa + (kk * 5 + z - 2) * 1024);
        sched_fence(); });
    };
    auto mmlast = [&](bf16x8(&Wf)[2], bf16x8(&Xf)[5]) {
#pragma unroll
      for (int i = 0; i < 5; ++i) { acc[0][i] = mfma(Xf[i], Wf[0], acc[0][i]); acc[1][i] = mfma(Xf[i], Wf[1], acc[1][i]); }
      rsum[0] = mfma(ones, Wf[0], rsum[0]); rsum[1] = mfma(ones, Wf[1], rsum[1]);
    };
    int slot = 0;
    auto steps = [&](int n0, int n1, bool hooks) {
#pragma unroll 1
      for (int n = n0; n < n1; ++n) {
        rdm(Wb, Xb, slot, 1, Wa, Xa);
        const int ns = slot + 1 == A_NS ? 0 : slot + 1;
        if (hooks && n == 8) wait_vm<0>();
        wait_lgkm0(); bar(); sched_fence();
        if (hooks && n == 8 && w == 0 && lane == 0) set_flag(ws, WS_F2 + p * 128 + j * 4, tag);
        rdm(Wa, Xa, ns, 0, Wb, Xb);
        slot = ns;
      }
    };
    bar();
    STAMP(3); CSTAMP(40);
    { LAS char* sb = smem; Wa[0] = *(LAS bf16x8*)(sb + wa0); Wa[1] = *(LAS bf16x8*)(sb + wa0 + 2048);
#pragma unroll
      for (int i = 0; i < 5; ++i) Xa[i] = *(LAS bf16x8*)(sb + xa + i * 1024); }
    steps(0, 12, true);
    const u32x4 f2 = peek256(rws, WS_F2, lane);
    steps(12, 18, false);
    if (!flags_ok(f2, tag)) wait256(rws, ws, WS_F2, tag, lane);
    if (b == 0 && w == 0 && lane == 0) set_flag(ws, WS_EPOCH, tag + 15);
#pragma unroll
    for (int i = 0; i < 5; ++i) xv[i] = *(const u32x2*)(P.x + (size_t)token * D + 80 * j + 16 * i + 4 * q);
    steps(18, 31, false);
    { LAS char* sb = smem + slot * A_SLOT; const unsigned wa = wa0 ^ 64;
      Wb[0] = *(LAS bf16x8*)(sb + wa); Wb[1] = *(LAS bf16x8*)(sb + wa + 2048);
#pragma unroll
      for (int i = 0; i < 5; ++i) Xb[i] = *(LAS bf16x8*)(sb + xa + (5 + i) * 1024); }
    mmlast(Wa, Xa);
    wait_lgkm0(); bar();
    mmlast(Wb, Xb);
    STAMP(4); CSTAMP(41);
    const float rs0 = rsum[0][0], rs1 = rsum[1][0];
#pragma unroll
    for (int i = 0; i < 5; ++i) {
      u32x2 a, c;
      const f32x4 sa = *(const LAS f32x4*)(smem + T_MOD + (16 * i + 4 * q) * 4), c1 = *(const LAS f32x4*)(smem + T_MOD + (80 + 16 * i + 4 * q) * 4);
      { const f32x4 u = acc[0][i] * c1 + rs0 * sa; a.x = pack2(u[0], u[1]); a.y = pack2(u[2], u[3]); }
      { const f32x4 v = -(acc[1][i] * c1 + rs1 * sa); c.x = pack2(v[0], v[1]); c.y = pack2(v[2], v[3]); }
      const int dt = 5 * j + i, kb = dt >> 1, qq = 2 * (dt & 1) + (q >> 1);
      st16(rws, WS_UV + (unsigned)(((p * 2 + (q & 1)) * 20 + kb) * 4 + w) * 1024 + (qq * 16 + tk) * 16, swap_pair(a, c));
    }
    wait_vm<0>(); bar();
    STAMP(5);
    if (w == 0) { if (lane == 0) set_flag(ws, WS_FU + p * 128 + j * 4, tag); wait_flags8(ws, WS_FU + p * 128, tag, lane); }
    bar();
    STAMP(6); CSTAMP(44);
  }
  u32x2 x1p[5];
  {
    LANE_IDS
    f32x4 acc[5][2];
#pragma unroll
    for (int i = 0; i < 5; ++i) { acc[i][0] = (f32x4){0, 0, 0, 0}; acc[i][1] = (f32x4){0, 0, 0, 0}; }
    const unsigned wa = (unsigned)(tk * 128 + (((q + 4 * kh) ^ ((lane >> 1) & 7)) * 16));
    const unsigned xa = 10240 + (kh * 4 + 2 * mh) * 1024 + lane * 16;
    ksplit_loop<B_NS, 20, 5, 2>(
        [&](bf16x8(&A)[5], bf16x8(&Bf)[2], int slot, int) { LAS char* sb = smem + slot * B_SLOT;
          A[0] = *(LAS bf16x8*)(sb + wa); Bf[0] = *(LAS bf16x8*)(sb + xa); Bf[1] = *(LAS bf16x8*)(sb + xa + 1024);
#pragma unroll
          for (int i = 1; i < 5; ++i) A[i] = *(LAS bf16x8*)(sb + wa + i * 2048); },
        [&](bf16x8(&A)[5], bf16x8(&Bf)[2], bool) {
#pragma unroll
          for (int i = 0; i < 5; ++i) { acc[i][0] = mfma(A[i], Bf[0], acc[i][0]); acc[i][1] = mfma(A[i], Bf[1], acc[i][1]); } });
    STAMP(7); CSTAMP(45);
    f32x4 y[5];
    kreduce<5>(acc, y, smem + B_SCR, w, lane);
    float xs[20]; u32x2 xp[5];
    float s = 0.f;
#pragma unroll
    for (int i = 0; i < 5; ++i) {
      const f32x4 ga = *(const LAS f32x4*)(smem + T_MOD + (160 + 16 * i + 4 * q) * 4), c1b = *(const LAS f32x4*)(smem + T_MOD + 960 + (16 * i + 4 * q) * 4);
      x1p[i].x = pack2(bflo(xv[i].x) + ga[0] * y[i][0], bfhi(xv[i].x) + ga[1] * y[i][1]);
      x1p[i].y = pack2(bflo(xv[i].y) + ga[2] * y[i][2], bfhi(xv[i].y) + ga[3] * y[i][3]);
      asm volatile("" : "+v"(x1p[i].x), "+v"(x1p[i].y));
      xs[4 * i] = bflo(x1p[i].x); xs[4 * i + 1] = bfhi(x1p[i].x); xs[4 * i + 2] = bflo(x1p[i].y); xs[4 * i + 3] = bfhi(x1p[i].y);
      s += (xs[4 * i] + xs[4 * i + 1]) + (xs[4 * i + 2] + xs[4 * i + 3]);
      xp[i].x = pack2(xs[4 * i] * c1b[0], xs[4 * i + 1] * c1b[1]); xp[i].y = pack2(xs[4 * i + 2] * c1b[2], xs[4 * i + 3] * c1b[3]);
    }
    const unsigned img = WS_X1P + (unsigned)p * 20 * 4096 + w * 1024;
#pragma unroll
    for (int i = 0; i < 6; i += 2) {
      const int i2 = i + 1 < 5 ? i + 1 : i;
      const int dt = 5 * j + ((q & 1) ? i2 : i), kb = dt >> 1, qq = 2 * (dt & 1) + (q >> 1);
      st16(rws, img + kb * 4096 + (qq * 16 + tk) * 16, swap_pair(xp[i], xp[i2]));
    }
    const float mean = xsum_q(s) * (1.0f / 80.0f);
    float m2 = 0.f;
#pragma unroll
    for (int e = 0; e < 20; ++e) { const float d = xs[e] - mean; m2 += d * d; }
    m2 = xsum_q(m2);
    if (q == 0) __hip_atomic_store((gu64*)(ws + WS_ST1) + token * 8 + j, ((unsigned long long)__float_as_uint(m2) << 32) | __float_as_uint(mean), RLX_AGENT);
    wait_vm<0>(); bar();
    STAMP(8);
    if (w == 0) { if (lane == 0) set_flag(ws, WS_FX + p * 128 + j * 4, tag); wait_flags8(ws, WS_FX + p * 128, tag, lane); }
    bar();
    STAMP(9);
  }
  u32x2 z1p[7]; float bias;
  {
    LANE_IDS
    u32x4 st[4];
#pragma unroll
    for (int k = 0; k < 4; ++k) st[k] = ld16(rws, WS_ST1 + (unsigned)token * 64 + k * 16);
    bias = bflo(P.bsp[token]);
    for (int i = tid; i < 448; i += 256) ((LAS float*)(smem + T_CV))[i] = 0.f;
    f32x4 acc[7][2], aux[4];
    auto clear = [&]() {
#pragma unroll
      for (int i = 0; i < 7; ++i) { acc[i][0] = (f32x4){0, 0, 0, 0}; acc[i][1] = (f32x4){0, 0, 0, 0}; }
#pragma unroll
      for (int i = 0; i < 4; ++i) aux[i] = (f32x4){0, 0, 0, 0};
    };
    const unsigned xa = C_X + (kh * 4 + 2 * mh) * 1024 + lane * 16, wlo = C_W + kh * 7168 + lane * 16 + mh * 4096, whi = C_W + kh * 7168 + lane * 16 - mh * 3072;
    const int r4 = tk & 3;
    const unsigned ma = T_MODB + (r4 == 0 ? q * 16 + kh * 64 : r4 == 1 ? 1280 + q * 16 + kh * 64 : 2560), mstride = r4 < 2 ? 128 : 0;
    int nbase = 0;
    auto rd = [&](bf16x8(&A)[7], bf16x8(&Bf)[3], int slot, int n) {
      LAS char* sl = smem + wlo + slot * C_WSLOT; LAS char* sh = smem + whi + slot * C_WSLOT; LAS char* xb = smem + xa + n * 8192;
      Bf[0] = *(LAS bf16x8*)(xb); A[0] = *(LAS bf16x8*)(sl); Bf[1] = *(LAS bf16x8*)(xb + 1024);
      A[1] = *(LAS bf16x8*)(sl + 1024); A[2] = *(LAS bf16x8*)(sl + 2048);
#pragma unroll
      for (int i = 3; i < 7; ++i) A[i] = *(LAS bf16x8*)(sh + i * 1024);
      Bf[2] = *(LAS bf16x8*)(smem + ma + n * mstride);
    };
    auto auxmm = [&](bf16x8(&A)[7], bf16x8(&Bf)[3]) {
#pragma unroll
      for (int i = 0; i < 4; ++i) aux[i] = mfma(Bf[2], A[i], aux[i]);
    };
    auto mm2 = [&](bf16x8(&A)[7], bf16x8(&Bf)[3], bool) {
#pragma unroll
      for (int i = 0; i < 7; ++i) { acc[i][0] = mfma(Bf[0], A[i], acc[i][0]); acc[i][1] = mfma(Bf[1], A[i], acc[i][1]); }
      auxmm(A, Bf); };
    auto mm1 = [&](bf16x8(&A)[7], bf16x8(&Bf)[3], bool) {
#pragma unroll
      for (int i = 0; i < 7; ++i) { acc[i][0] = mfma(A[i], Bf[0], acc[i][0]); acc[i][1] = mfma(A[i], Bf[1], acc[i][1]); }
      auxmm(A, Bf); };
    auto put_cv = [&](int pass_) {
      LAS float* cv = (LAS float*)(smem + T_CV) + pass_ * 224;
      if (q == 0) {
#pragma unroll
        for (int i = 0; i < 4; ++i) if (mh == 0 || i < 3) { const int t = rot(i);
          __hip_atomic_fetch_add(cv + 16 * t + tk, aux[i][0], __ATOMIC_RELAXED, __HIP_MEMORY_SCOPE_WORKGROUP);
          __hip_atomic_fetch_add(cv + 112 + 16 * t + tk, aux[i][1], __ATOMIC_RELAXED, __HIP_MEMORY_SCOPE_WORKGROUP); }
      }
    };
    float rmean, rrstd;
    { float mu[8], mean = 0.f;
#pragma unroll
      for (int k = 0; k < 4; ++k) { mu[2 * k] = __uint_as_float(st[k].x); mu[2 * k + 1] = __uint_as_float(st[k].z); mean += mu[2 * k] + mu[2 * k + 1]; }
      mean *= 0.125f; float M2 = 0.f;
#pragma unroll
      for (int k = 0; k < 4; ++k) { const float d0 = mu[2 * k] - mean, d1 = mu[2 * k + 1] - mean; M2 += __uint_as_float(st[k].y) + __uint_as_float(st[k].w) + 80.0f * (d0 * d0 + d1 * d1); }
      rmean = mean; rrstd = 1.0f / sqrtf(M2 * (1.0f / D) + 1e-5f); }
    (void)nbase;
    bf16x8 A0[7], B0[3], A1[7], B1[3];
    int slot = 0;
    auto nohook = [](int) {};
    clear();
    wait_lgkm0(); bar();
    STAMP(10); CSTAMP(46);
    rd(A0, B0, 0, 0);
    ksplit_steps<C_NS, 10, 7, 3, 18>(A0, B0, A1, B1, slot, 0, 8, rd, mm2, nohook, nohook);
    ksplit_last<C_NS, 10, 7, 3>(A0, B0, A1, B1, slot, rd, mm2);
    STAMP(11); CSTAMP(47);
    put_cv(0);
    f32x4 zs[7];
    kreduce<7>(acc, zs, smem + C_SCR, w, lane);
    wait_lgkm0(); bar();
    f32x2 mr01, mr23, rr01, rr23;
    mr01 = (f32x2){__shfl(rmean, 4 * q), __shfl(rmean, 4 * q + 1)}; mr23 = (f32x2){__shfl(rmean, 4 * q + 2), __shfl(rmean, 4 * q + 3)};
    rr01 = (f32x2){__shfl(rrstd, 4 * q), __shfl(rrstd, 4 * q + 1)}; rr23 = (f32x2){__shfl(rrstd, 4 * q + 2), __shfl(rrstd, 4 * q + 3)};
    f32x2 z01[7], z23[7];
#pragma unroll
    for (int i = 0; i < 7; ++i) {
      const float c1 = ((const LAS float*)(smem + T_CV))[16 * rot(i) + tk], c2 = ((const LAS float*)(smem + T_CV))[112 + 16 * rot(i) + tk];
      z01[i] = gelu2(rr01 * ((f32x2){zs[i][0], zs[i][1]} - mr01 * c1) + c2);
      z23[i] = gelu2(rr23 * ((f32x2){zs[i][2], zs[i][3]} - mr23 * c1) + c2);
    }
    {
      const float inv = 1.0f / (16.0f * nft);
      const f32x2 zero2 = (f32x2){0.f, 0.f};
      f32x2 s01 = zero2, s23 = zero2;
#pragma unroll
      for (int i = 0; i < 7; ++i) { const bool ok = (i != 2 && i != 6) || rot(i) < nft; s01 += ok ? z01[i] : zero2; s23 += ok ? z23[i] : zero2; }
      const f32x2 m01 = (f32x2){xsum_16(s01[0]), xsum_16(s01[1])} * inv, m23 = (f32x2){xsum_16(s23[0]), xsum_16(s23[1])} * inv;
      f32x2 t01 = zero2, t23 = zero2;
#pragma unroll
      for (int i = 0; i < 7; ++i) { const bool ok = (i != 2 && i != 6) || rot(i) < nft; const f32x2 a = ok ? z01[i] - m01 : zero2, c = ok ? z23[i] - m23 : zero2; t01 += a * a; t23 += c * c; }
      const float q0 = xsum_16(t01[0]), q1 = xsum_16(t01[1]), q2 = xsum_16(t23[0]), q3 = xsum_16(t23[1]);
      if (tk < 8) { const int r = tk & 3; const float a = r == 0 ? m01[0] : r == 1 ? m01[1] : r == 2 ? m23[0] : m23[1], c = r == 0 ? q0 : r == 1 ? q1 : r == 2 ? q2 : q3;
        const int tok = 64 * p + 16 * w + 4 * q + r;
        __hip_atomic_store((gu64*)(ws + WS_ST2) + (tok * 8 + j) * 2 + (tk >> 2), ((unsigned long long)tag << 32) | __float_as_uint(tk < 4 ? a : c), RLX_AGENT); }
    }
    STAMP(12);
    {
      u32x4 g0, g1;
      const unsigned go = WS_ST2 + (unsigned)(token * 8 + 2 * q) * 16;
      for (unsigned spins = 0;;) { g0 = ld16(rws, go); g1 = ld16(rws, go + 16);
        if (__all(g0.y == tag && g0.w == tag && g1.y == tag && g1.w == tag)) break; if (spin_fail(spins, ws)) break; }
      const float n0 = 112.0f, n1 = (q & 1) ? 96.0f : 112.0f;
      const float m0 = __uint_as_float(g0.x), m1 = __uint_as_float(g1.x);
      const float mean = xsum_q(n0 * m0 + n1 * m1) * (1.0f / FH);
      const float d0 = m0 - mean, d1 = m1 - mean;
      const float M2 = xsum_q(__uint_as_float(g0.z) + __uint_as_float(g1.z) + n0 * d0 * d0 + n1 * d1 * d1);
      const float rstd = 1.0f / sqrtf(M2 * (1.0f / FH) + 1e-5f);
      mr01 = (f32x2){__shfl(mean, 4 * q), __shfl(mean, 4 * q + 1)}; mr23 = (f32x2){__shfl(mean, 4 * q + 2), __shfl(mean, 4 * q + 3)};
      rr01 = (f32x2){__shfl(rstd, 4 * q), __shfl(rstd, 4 * q + 1)}; rr23 = (f32x2){__shfl(rstd, 4 * q + 2), __shfl(rstd, 4 * q + 3)};
    }
    STAMP(13);
    {
      u32x2 pk[7];
#pragma unroll
      for (int i = 0; i < 7; ++i) { const f32x2 a = (z01[i] - mr01) * rr01, c = (z23[i] - mr23) * rr23; pk[i].x = pack2(a[0], a[1]); pk[i].y = pack2(c[0], c[1]); }
      const unsigned img = WS_Z2N + (unsigned)(2 * p + (w >> 1)) * 55 * 1024 + ((2 * (w & 1) + (q >> 1)) * 16 + tk) * 16;
#pragma unroll
      for (int i = 0; i < 8; i += 2) {
        const int i2 = i + 1 < 7 ? i + 1 : i;
        const u32x4 ch = swap_pair(pk[i], pk[i2]);
        const int ti = (q & 1) ? rot(i2) : rot(i);
        st16(rws, ti < nft ? img + (ft0 + ti) * 1024 : 0xfffff000u, ch);
      }
    }
    STAMP(14); CSTAMP(48);
    clear();
    slot = 0;
    bar();
    rd(A0, B0, 0, 0);
    ksplit_steps<C_NS, 10, 7, 3, 18>(A0, B0, A1, B1, slot, 0, 6, rd, mm1,
        [&](int n) { if (n == 2) wait_vm<0>(); },
        [&](int n) { if (n == 2 && w == 0 && lane == 0) set_flag(ws, WS_FZ + (j * 4 + (p >> 3)) * 128 + (p & 7) * 4, tag); });
    const unsigned fzo = WS_FZ + (j * 4 + ((lane & 7) >> 1)) * 128 + (lane & 1) * 16;
    const u32x4 fz = ld16(rws, fzo);
    ksplit_steps<C_NS, 10, 7, 3, 18>(A0, B0, A1, B1, slot, 6, 8, rd, mm1, nohook, nohook);
    ksplit_last<C_NS, 10, 7, 3>(A0, B0, A1, B1, slot, rd, mm1);
    if (!flags_ok(fz, tag)) { for (unsigned spins = 0;;) { if (flags_ok(ld16(rws, fzo), tag)) break; if (spin_fail(spins, ws)) break; } }
    STAMP(15); CSTAMP(49);
    put_cv(1);
    kreduce<7>(acc, zs, smem + C_SCR, w, lane);
#pragma unroll
    for (int i = 0; i < 7; ++i) {
      const LAS float* cv = (const LAS float*)(smem + T_CV) + 224;
      const f32x4 c1 = *(const LAS f32x4*)(cv + 16 * rot(i) + 4 * q), c2 = *(const LAS f32x4*)(cv + 112 + 16 * rot(i) + 4 * q);
      const f32x2 a = gelu2(rrstd * ((f32x2){zs[i][0], zs[i][1]} - rmean * (f32x2){c1[0], c1[1]}) + (f32x2){c2[0], c2[1]});
      const f32x2 c = gelu2(rrstd * ((f32x2){zs[i][2], zs[i][3]} - rmean * (f32x2){c1[2], c1[3]}) + (f32x2){c2[2], c2[3]});
      z1p[i] = (u32x2){pack2(a[0], a[1]), pack2(c[0], c[1])};
      asm volatile("" : "+v"(z1p[i].x), "+v"(z1p[i].y));
    }
    STAMP(16); CSTAMP(42);
  }
  {
    LANE_IDS
    f32x4 acc[7][2];
#pragma unroll
    for (int i = 0; i < 7; ++i) { acc[i][0] = (f32x4){0, 0, 0, 0}; acc[i][1] = (f32x4){0, 0, 0, 0}; }
    const unsigned wa = (unsigned)((2 * mh) * 2048 + tk * 128 + (((q + 4 * kh) ^ ((lane >> 1) & 7)) * 16));
    const unsigned xlo = 8192 + kh * 7168 + lane * 16 + mh * 4096, xhi = 8192 + kh * 7168 + lane * 16 - mh * 3072;
    ksplit_loop<D_NS, 32, 7, 2>(
        [&](bf16x8(&A)[7], bf16x8(&Bf)[2], int slot, int) { LAS char* sb = smem + slot * D_SLOT;
          Bf[0] = *(LAS bf16x8*)(sb + wa); A[0] = *(LAS bf16x8*)(sb + xlo); Bf[1] = *(LAS bf16x8*)(sb + wa + 2048);
          A[1] = *(LAS bf16x8*)(sb + xlo + 1024); A[2] = *(LAS bf16x8*)(sb + xlo + 2048);
#pragma unroll
          for (int i = 3; i < 7; ++i) A[i] = *(LAS bf16x8*)(sb + xhi + i * 1024); },
        [&](bf16x8(&A)[7], bf16x8(&Bf)[2], bool) {
#pragma unroll
          for (int i = 0; i < 7; ++i) { acc[i][0] = mfma(A[i], Bf[0], acc[i][0]); acc[i][1] = mfma(A[i], Bf[1], acc[i][1]); } });
    STAMP(17); CSTAMP(43);
    f32x4 g[7];
    kreduce<7>(acc, g, smem + D_SCR, w, lane);
    u32x2 pk[7];
#pragma unroll
    for (int i = 0; i < 7; ++i) {
      pk[i].x = pack2(bflo(z1p[i].x) * (g[i][0] + bias), bfhi(z1p[i].x) * (g[i][1] + bias));
      pk[i].y = pack2(bflo(z1p[i].y) * (g[i][2] + bias), bfhi(z1p[i].y) * (g[i][3] + bias));
    }
    const unsigned img = WS_P + (unsigned)p * 28 * 4096 + w * 1024;
#pragma unroll
    for (int i = 0; i < 8; i += 2) {
      const int i2 = i + 1 < 7 ? i + 1 : i;
      const u32x4 ch = swap_pair(pk[i], pk[i2]);
      const int ti = (q & 1) ? rot(i2) : rot(i), ft = ft0 + ti, kb = ft >> 1, qq = 2 * (ft & 1) + (q >> 1);
      st16(rws, ti < nft ? img + kb * 4096 + (qq * 16 + tk) * 16 : 0xfffff000u, ch);
    }
    wait_vm<0>(); bar();
    STAMP(18);
    if (w == 0) { if (lane == 0) set_flag(ws, WS_FP + p * 128 + j * 4, tag); wait_flags8(ws, WS_FP + p * 128, tag, lane); }
    bar();
    STAMP(19);
  }
  {
    LANE_IDS
    f32x4 acc[5][2];
#pragma unroll
    for (int i = 0; i < 5; ++i) { acc[i][0] = (f32x4){0, 0, 0, 0}; acc[i][1] = (f32x4){0, 0, 0, 0}; }
    const unsigned wa = kh * 5120 + lane * 16, xa = 10240 + (kh * 4 + 2 * mh) * 1024 + lane * 16;
    ksplit_loop<E_NS, 14, 5, 2>(
        [&](bf16x8(&A)[5], bf16x8(&Bf)[2], int slot, int) { LAS char* sb = smem + slot * E_SLOT;
          A[0] = *(LAS bf16x8*)(sb + wa); Bf[0] = *(LAS bf16x8*)(sb + xa); Bf[1] = *(LAS bf16x8*)(sb + xa + 1024);
#pragma unroll
          for (int i = 1; i < 5; ++i) A[i] = *(LAS bf16x8*)(sb + wa + i * 1024); },
        [&](bf16x8(&A)[5], bf16x8(&Bf)[2], bool last) {
          if (!(last && kh)) {
#pragma unroll
            for (int i = 0; i < 5; ++i) { acc[i][0] = mfma(A[i], Bf[0], acc[i][0]); acc[i][1] = mfma(A[i], Bf[1], acc[i][1]); } } });
    STAMP(20);
    f32x4 o[5];
    kreduce<5>(acc, o, smem + E_SCR, w, lane);
    u32x2 pk[5];
#pragma unroll
    for (int i = 0; i < 5; ++i) {
      const f32x4 gb = *(const LAS f32x4*)(smem + T_MOD + 960 + (80 + 16 * i + 4 * q) * 4);
      pk[i].x = pack2(bflo(x1p[i].x) + gb[0] * o[i][0], bfhi(x1p[i].x) + gb[1] * o[i][1]);
      pk[i].y = pack2(bflo(x1p[i].y) + gb[2] * o[i][2], bfhi(x1p[i].y) + gb[3] * o[i][3]);
    }
    bf16_t* orow = P.out + (size_t)token * D + 80 * j;
#pragma unroll
    for (int i = 0; i < 6; i += 2) {
      const int i2 = i + 1 < 5 ? i + 1 : i;
      const u32x4 ch = swap_pair(pk[i], pk[i2]);
      *(u32x4*)(orow + 16 * ((q & 1) ? i2 : i) + 8 * (q >> 1)) = ch;
    }
    STAMP(21);
  }
  STAMP_FLUSH();
}

__global__ __launch_bounds__(NT, 2) void fused_block(Params P) {
  extern __shared__ __attribute__((aligned(16))) char smem_[];
  LAS char* smem = (LAS char*)smem_;
  const int tid = threadIdx.x, lane = tid & 63, wid = __builtin_amdgcn_readfirstlane(tid >> 6), b = blockIdx.x;
  const int xcd = b & 7, idx = b >> 3, p = xcd * 4 + (idx >> 3), j = idx & 7;
  if (wid >= 4) loader_main(P, (unsigned)(size_t)smem, smem, wid - 4, lane, b, p, j, xcd * 4);
  else consumer_main(P, smem, wid, lane, b, p, j);
}

extern "C" void kernel_launch(void* const* d_in, const int* in_sizes, int n_in, void* d_out, int out_size, void* d_ws, size_t ws_size, hipStream_t stream) {
  Params P;
  P.x = (const bf16_t*)d_in[0]; P.wsr = (const bf16_t*)d_in[1]; P.wsi = (const bf16_t*)d_in[2];
  P.wdr = (const bf16_t*)d_in[3]; P.wdi = (const bf16_t*)d_in[4]; P.win = (const bf16_t*)d_in[6];
  P.wsp = (const bf16_t*)d_in[7]; P.bsp = (const bf16_t*)d_in[8]; P.wout = (const bf16_t*)d_in[9];
  P.c = (const bf16_t*)d_in[10]; P.wmod = (const bf16_t*)d_in[11];
  P.out = (bf16_t*)d_out; P.ws = (char*)d_ws;
  (void)hipFuncSetAttribute((const void*)fused_block, hipFuncAttributeMaxDynamicSharedMemorySize, LDS_BYTES);
  fused_block<<<256, NT, LDS_BYTES, stream>>>(P);
}
```

```cpp
#include <hip/hip_runtime.h>
#include <stdint.h>
#include <type_traits>

typedef unsigned short bf16_t;
typedef short bf16x8 __attribute__((ext_vector_type(8)));
typedef short s16x4 __attribute__((ext_vector_type(4)));
typedef float f32x4 __attribute__((ext_vector_type(4)));
typedef float f32x2 __attribute__((ext_vector_type(2)));
typedef unsigned u32x4 __attribute__((ext_vector_type(4)));
typedef unsigned u32x2 __attribute__((ext_vector_type(2)));
typedef __bf16 bf2_t __attribute__((ext_vector_type(2)));
#define LAS __attribute__((address_space(3)))
#define GAS __attribute__((address_space(1)))
typedef GAS unsigned gu32;
typedef GAS unsigned long long gu64;
#define RLX_AGENT __ATOMIC_RELAXED, __HIP_MEMORY_SCOPE_AGENT
#define DEV __device__ __forceinline__

constexpr int S = 2048, D = 640, F = 1728, FH = 864;
constexpr int NT = 512;
constexpr int CODE_BYTES = 53000;

constexpr unsigned WS_EPOCH = 0;
constexpr unsigned WS_TMO   = 256;
constexpr unsigned WS_FN = 4096, WS_F2 = 8192, WS_FU = 12288, WS_FX = 16384, WS_FP = 20480, WS_FZ = 24576;
constexpr unsigned WS_MOD  = 65536;
constexpr unsigned WS_MODB = WS_MOD + 16384;

constexpr unsigned WS_XN   = 131072;
constexpr unsigned WS_WIN  = WS_XN + 64 * 40 * 1024;
constexpr unsigned WS_WOUT = WS_WIN + 20 * 109 * 1024;
constexpr unsigned WS_UV   = WS_WOUT + 27 * 40 * 1024;
constexpr unsigned WS_X1P  = WS_UV + 32 * 2 * 20 * 4096;
constexpr unsigned WS_ST1  = WS_X1P + 32 * 20 * 4096;
constexpr unsigned WS_Z2N  = WS_ST1 + 2048 * 64;
constexpr unsigned WS_ST2  = WS_Z2N + 64 * 55 * 1024;
constexpr unsigned WS_P    = WS_ST2 + 2048 * 128;
constexpr unsigned WS_END  = WS_P + 32 * 28 * 4096;

struct Params {
  const bf16_t *x, *wsr, *wsi, *wdr, *wdi, *win, *wsp, *bsp, *wout, *c, *wmod;
  bf16_t* out;
  char* ws;
};

#define STAMP_DECL
#define STAMP(i) do {} while (0)
#define STAMP_FLUSH() do {} while (0)
#define CSTAMP(i) do {} while (0)

template <int N> DEV void wait_vm() { asm volatile("s_waitcnt vmcnt(%0)" ::"n"(N) : "memory"); }
DEV void wait_lgkm0() { asm volatile("s_waitcnt lgkmcnt(0)" ::: "memory"); }
DEV void bar() { asm volatile("" ::: "memory"); __builtin_amdgcn_s_barrier(); asm volatile("" ::: "memory"); }
DEV void sched_fence() { __builtin_amdgcn_sched_barrier(0); }
template <int I, int N, class Fn> DEV void static_for(Fn&& f) { if constexpr (I < N) { f(std::integral_constant<int, I>{}); static_for<I + 1, N>(f); } }

DEV float bflo(unsigned v) { return __uint_as_float(v << 16); }
DEV float bfhi(unsigned v) { return __uint_as_float(v & 0xffff0000u); }
DEV unsigned pack2(float lo, float hi) { bf2_t v; v[0] = (__bf16)lo; v[1] = (__bf16)hi; return __builtin_bit_cast(unsigned, v); }
DEV float rbf(float f) { return bflo(pack2(f, f)); }
DEV float dot2(unsigned a, unsigned b, float c) { return __builtin_amdgcn_fdot2_f32_bf16(__builtin_bit_cast(bf2_t, a), __builtin_bit_cast(bf2_t, b), c, false); }
DEV f32x4 mfma(bf16x8 a, bf16x8 b, f32x4 c) { return __builtin_amdgcn_mfma_f32_16x16x32_bf16(a, b, c, 0, 0, 0); }

DEV u32x4 mkrs(const void* p, unsigned bytes) {
  const unsigned long long a = (unsigned long long)p;
  u32x4 r; r.x = (unsigned)a; r.y = (unsigned)(a >> 32) & 0xffffu; r.z = bytes; r.w = 0x00020000u; return r;
}
DEV __amdgpu_buffer_rsrc_t mkrsrc(const void* p, unsigned bytes) { return __builtin_amdgcn_make_buffer_rsrc((void*)p, 0, bytes, 0x00020000); }
template <bool SC1> DEV void dma(u32x4 r, unsigned lds, unsigned voff, unsigned soff) {
  if constexpr (SC1) asm volatile("s_mov_b32 m0, %0\n\ts_nop 0\n\tbuffer_load_dwordx4 %1, %2, %3 offen sc1 lds" ::"s"(lds), "v"(voff), "s"(r), "s"(soff) : "memory");
  else asm volatile("s_mov_b32 m0, %0\n\ts_nop 0\n\tbuffer_load_dwordx4 %1, %2, %3 offen lds" ::"s"(lds), "v"(voff), "s"(r), "s"(soff) : "memory");
}
template <bool SC1> DEV void dma_half(u32x4 r, unsigned lds, unsigned voff, unsigned soff) {
  if constexpr (SC1) asm volatile("s_mov_b32 m0, %0\n\ts_mov_b32 exec_hi, 0\n\tbuffer_load_dwordx4 %1, %2, %3 offen sc1 lds\n\ts_mov_b32 exec_hi, -1" ::"s"(lds), "v"(voff), "s"(r), "s"(soff) : "memory");
  else asm volatile("s_mov_b32 m0, %0\n\ts_mov_b32 exec_hi, 0\n\tbuffer_load_dwordx4 %1, %2, %3 offen lds\n\ts_mov_b32 exec_hi, -1" ::"s"(lds), "v"(voff), "s"(r), "s"(soff) : "memory");
}
constexpr int SC1 = 16;
DEV void st16(__amdgpu_buffer_rsrc_t r, unsigned off, u32x4 v) { __builtin_amdgcn_raw_buffer_store_b128(v, r, off, 0, SC1); }
DEV u32x4 ld16(__amdgpu_buffer_rsrc_t r, unsigned off) { return __builtin_amdgcn_raw_buffer_load_b128(r, off, 0, SC1); }
DEV f32x4 ld16f(__amdgpu_buffer_rsrc_t r, unsigned off) { return __builtin_bit_cast(f32x4, ld16(r, off)); }

template <int CTRL> DEV float dpp_add(float v) { return v + __builtin_bit_cast(float, __builtin_amdgcn_update_dpp(0, __builtin_bit_cast(int, v), CTRL, 0xf, 0xf, true)); }
DEV float xsum_16(float v) { v = dpp_add<0xB1>(v); v = dpp_add<0x4E>(v); v = dpp_add<0x141>(v); return dpp_add<0x140>(v); }
DEV float xsum_r16(float v) { auto r = __builtin_amdgcn_permlane16_swap(__float_as_uint(v), __float_as_uint(v), false, false); return __uint_as_float(r[0]) + __uint_as_float(r[1]); }
DEV float xsum_r32(float v) { auto r = __builtin_amdgcn_permlane32_swap(__float_as_uint(v), __float_as_uint(v), false, false); return __uint_as_float(r[0]) + __uint_as_float(r[1]); }
DEV float xsum_q(float v) { return xsum_r32(xsum_r16(v)); }
DEV float gelu_tanh(float x) {
  const float k1 = -2.0f * 0.7978845608028654f * 1.4426950408889634f;
  const float t = x * __builtin_fmaf(x * x, 0.044715f * k1, k1);
  return x * __builtin_amdgcn_rcpf(1.0f + __builtin_amdgcn_exp2f(t));
}
DEV f32x2 gelu2(f32x2 x) {
  const float k1 = -2.0f * 0.7978845608028654f * 1.4426950408889634f;
  const f32x2 t = x * ((x * x) * (0.044715f * k1) + k1);
  const f32x2 d = (f32x2){__builtin_amdgcn_exp2f(t[0]), __builtin_amdgcn_exp2f(t[1])} + 1.0f;
  return x * (f32x2){__builtin_amdgcn_rcpf(d[0]), __builtin_amdgcn_rcpf(d[1])};
}
DEV u32x4 swap_pair(u32x2 a, u32x2 b) {
  auto r0 = __builtin_amdgcn_permlane16_swap(a.x, b.x, false, false);
  auto r1 = __builtin_amdgcn_permlane16_swap(a.y, b.y, false, false);
  return (u32x4){r0[0], r1[0], r0[1], r1[1]};
}

DEV bool spin_fail(unsigned& spins, char* ws) {
  __builtin_amdgcn_s_sleep(1);
  if (++spins > (1u << 17)) { __hip_atomic_store((gu32*)(ws + WS_TMO), 1u, RLX_AGENT); return true; }
  return false;
}
DEV void set_flag(char* ws, unsigned off, unsigned tag) { __hip_atomic_store((gu32*)(ws + off), tag, RLX_AGENT); }
DEV void wait_flags8(char* ws, unsigned off, unsigned tag, int lane) {
  for (unsigned spins = 0;;) {
    const unsigned v = lane < 8 ? __hip_atomic_load((gu32*)(ws + off) + lane, RLX_AGENT) : tag;
    if (__all(v == tag)) break;
    if (spin_fail(spins, ws)) break;
  }
}
DEV bool flags_ok(u32x4 v, unsigned tag) { return __all(v.x == tag && v.y == tag && v.z == tag && v.w == tag); }
DEV u32x4 peek256(__amdgpu_buffer_rsrc_t rf, unsigned off, int lane) { return ld16(rf, off + (lane >> 1) * 128 + (lane & 1) * 16); }
DEV void wait256(__amdgpu_buffer_rsrc_t rf, char* ws, unsigned off, unsigned tag, int lane) {
  for (unsigned spins = 0;;) { if (flags_ok(peek256(rf, off, lane), tag)) break; if (spin_fail(spins, ws)) break; }
}

DEV void wait_panels(__amdgpu_buffer_rsrc_t rf, char* ws, unsigned off, int first, int count, unsigned tag, int lane) {
  const unsigned o = lane < 2 * count ? off + (((lane >> 1) + first) & 31) * 128 + (lane & 1) * 16 : off + (first & 31) * 128;
  for (unsigned spins = 0;;) { if (flags_ok(ld16(rf, o), tag)) break; if (spin_fail(spins, ws)) break; }
}

template <int NS, int S_, int CW, int CX, class IW, class IX>
DEV void loader_run(IW&& issueW, IX&& issueX) {
  static_for<0, NS>([&](auto ic) { issueX(decltype(ic)::value, decltype(ic)::value); });
  wait_vm<(NS - 1) * CX>(); bar();
  static_for<0, NS - 2>([&](auto ic) { constexpr int n = decltype(ic)::value;
    wait_vm<(NS - 2) * CX + n * CW>(); bar(); issueW(n + NS, n); issueX(n + NS, n); });
  int slot = NS - 2;
#pragma unroll 1
  for (int n = NS - 2; n < S_ - NS; ++n) {
    wait_vm<(NS - 2) * (CW + CX)>(); bar();
    issueW(n + NS, slot); issueX(n + NS, slot);
    slot = slot + 1 == NS ? 0 : slot + 1;
  }
  static_for<0, NS - 1>([&](auto ic) { constexpr int r = NS - 2 - decltype(ic)::value; wait_vm<r*(CW + CX)>(); bar(); });
}

template <int NS, int S_, int NA, int NB, int NM, class Rd, class Mm, class Hook, class Post>
DEV void ksplit_steps(bf16x8 (&A0)[NA], bf16x8 (&B0)[NB], bf16x8 (&A1)[NA], bf16x8 (&B1)[NB], int& slot, int n0, int n1, Rd&& rd, Mm&& mm, Hook&& hook, Post&& post) {
  constexpr int NR = NA + NB;
#define KS_ILV() do { _Pragma("unroll") for (int z = 0; z < NR; ++z) { __builtin_amdgcn_sched_group_barrier(0x8, 1, 0); __builtin_amdgcn_sched_group_barrier(0x100, 1, 0); } __builtin_amdgcn_sched_group_barrier(0x8, NM - NR, 0); } while (0)
#pragma unroll 1
  for (int n = n0; n < n1; n += 2) {
    const int s1 = slot + 1 == NS ? 0 : slot + 1;
    hook(n); wait_lgkm0(); bar(); post(n); sched_fence();
    rd(A1, B1, s1, n + 1); mm(A0, B0, false); KS_ILV(); sched_fence();
    const int s2 = s1 + 1 == NS ? 0 : s1 + 1;
    hook(n + 1); wait_lgkm0(); bar(); post(n + 1); sched_fence();
    rd(A0, B0, s2, n + 2); mm(A1, B1, false); KS_ILV(); sched_fence();
    slot = s2;
  }
}
template <int NS, int S_, int NA, int NB, class Rd, class Mm>
DEV void ksplit_last(bf16x8 (&A0)[NA], bf16x8 (&B0)[NB], bf16x8 (&A1)[NA], bf16x8 (&B1)[NB], int& slot, Rd&& rd, Mm&& mm) {
  const int s1 = slot + 1 == NS ? 0 : slot + 1;
  wait_lgkm0(); bar(); sched_fence();
  rd(A1, B1, s1, S_ - 1); mm(A0, B0, false); mm(A1, B1, true);
  slot = s1;
}
template <int NS, int S_, int NA, int NB, class Rd, class Mm>
DEV void ksplit_loop(Rd&& rd, Mm&& mm) {
  static_assert((S_ & 1) == 0, "even step count");
  bf16x8 A0[NA], B0[NB], A1[NA], B1[NB];
  bar();
  rd(A0, B0, 0, 0);
  int slot = 0;
  ksplit_steps<NS, S_, NA, NB, NA * NB>(A0, B0, A1, B1, slot, 0, S_ - 2, rd, mm, [](int) {}, [](int) {});
  ksplit_last<NS, S_, NA, NB>(A0, B0, A1, B1, slot, rd, mm);
}
template <int N>
DEV void kreduce(f32x4 (&acc)[N][2], f32x4 (&res)[N], LAS char* scratch, int w, int lane) {
  const int kh = w & 1;
  LAS char* mine = scratch + w * (N * 1024) + lane * 16;
  LAS char* other = scratch + (w ^ 1) * (N * 1024) + lane * 16;
  if (kh) {
#pragma unroll
    for (int i = 0; i < N; ++i) { *(LAS f32x4*)(mine + i * 1024) = acc[i][0]; res[i] = acc[i][1]; }
  } else {
#pragma unroll
    for (int i = 0; i < N; ++i) { *(LAS f32x4*)(mine + i * 1024) = acc[i][1]; res[i] = acc[i][0]; }
  }
  wait_lgkm0(); bar();
#pragma unroll
  for (int i = 0; i < N; ++i) res[i] += *(LAS f32x4*)(other + i * 1024);
}

constexpr int A_NS = 5, A_SLOT = 26 * 1024;
constexpr int P0_T = 130 * 1024, P0_PT = 140 * 1024, P0_RED = 156 * 1024;
constexpr int B_NS = 6, B_SLOT = 18 * 1024, B_SCR = 36 * 1024;
constexpr int C_NS = 5, C_X = 0, C_W = 82 * 1024, C_WSLOT = 14 * 1024;
constexpr int C_SCR = C_W + 2 * C_WSLOT;
constexpr int D_NS = 5, D_SLOT = 22 * 1024, D_SCR = 110 * 1024;
constexpr int E_NS = 6, E_SLOT = 18 * 1024, E_SCR = 110 * 1024;
constexpr int T_MODB = 152 * 1024;
constexpr int T_CV = T_MODB + 3072 + 64;
constexpr int T_MOD = 158 * 1024;
constexpr int LDS_BYTES = 160 * 1024;
static_assert(T_CV + 896 <= T_MOD && C_W + C_NS * C_WSLOT <= T_MODB && D_SCR + 28 * 1024 <= T_MODB && E_SCR + 20 * 1024 <= T_MODB, "LDS map");

DEV void loader_main(const Params& P, const unsigned lds0, LAS char* smem, const int lw, const int lane_, const int b, const int p, const int j, const int krot) {
  STAMP_DECL STAMP(0);
  char* const ws = P.ws;
  {
    const int idx_ = b >> 3;
    if (lw == 0 && idx_ < (CODE_BYTES + 8191) / 8192) {
      unsigned long long pc; unsigned off;
      asm volatile("s_getpc_b64 %0\n.Lpc%=:\n\ts_mov_b32 %1, .Lpc%=-fused_block" : "=s"(pc), "=s"(off));
      const u32x4 rc = mkrs((const void*)(pc - off), CODE_BYTES);
      asm volatile("s_mov_b32 m0, %0\n\ts_nop 0\n\tbuffer_load_dword %1, %2, 0 offen lds" ::"s"(lds0 + T_MOD + 1792), "v"(lane_ * 128 + idx_ * 8192), "s"(rc) : "memory");
    }
  }
  const int ft0 = 27 * (j >> 2) + 7 * (j & 3);
#define LLANE int lane = lane_; asm volatile("" : "+v"(lane)); const unsigned vx = lane * 16; (void)vx;
#define LV4K const unsigned v4k0 = (unsigned)(lane >> 3) * 4096u + (unsigned)(((lane & 7) ^ (lane >> 4)) * 16), v4k1 = (unsigned)(8 + (lane >> 3)) * 4096u + (unsigned)(((lane & 7) ^ (4 + (lane >> 4))) * 16);
  {
    LLANE LV4K
    const u32x4 rre = mkrs(P.wsr, S * S * 2), rim = mkrs(P.wsi, S * S * 2), rx = mkrs(ws + WS_XN, S * D * 2);
    const unsigned wrow = (unsigned)(64 * p + 16 * lw) * 4096u;
    auto issueW = [&](int n, int slot) {
      const unsigned sb = lds0 + slot * A_SLOT + lw * 4096, sw = wrow + ((n + krot) & 31) * 128;
      dma<false>(rre, sb, v4k0, sw); dma<false>(rre, sb + 1024, v4k1, sw); dma<false>(rim, sb + 2048, v4k0, sw); dma<false>(rim, sb + 3072, v4k1, sw);
    };
    const int xi0 = lw * 2, xi1 = lw * 2 + 1, xi2 = 8 + (lw >> 1);
    const unsigned xo0 = (unsigned)(((xi0 >= 5) * 40 + 5 * j + xi0 - 5 * (xi0 >= 5)) * 1024), xo1 = (unsigned)(((xi1 >= 5) * 40 + 5 * j + xi1 - 5 * (xi1 >= 5)) * 1024);
    const unsigned xo2 = (unsigned)((40 + 5 * j + xi2 - 5) * 1024 + (lw & 1) * 512);
    auto issueX = [&](int n, int slot) {
      const unsigned sb = lds0 + slot * A_SLOT + 16384, so = ((n + krot) & 31) * 80 * 1024;
      dma<true>(rx, sb + xi0 * 1024, vx, so + xo0); dma<true>(rx, sb + xi1 * 1024, vx, so + xo1); dma_half<true>(rx, sb + xi2 * 1024 + (lw & 1) * 512, vx, so + xo2);
    };
    issueW(0, 0); issueW(1, 1);
    bar();
    bar();
    STAMP(30);
    bar();
    STAMP(33);
    __builtin_amdgcn_s_setprio(3);
    issueX(0, 0); issueX(1, 1);
    static_for<2, A_NS>([&](auto ic) { issueW(decltype(ic)::value, decltype(ic)::value); issueX(decltype(ic)::value, decltype(ic)::value); });
    const int idx = b >> 3, unit0 = (idx & 3) + 4 * ((b & 7) + 8 * (idx >> 2));
    const bool domod = unit0 < 240; const int unit = domod ? unit0 : 0;
    u32x4 wm[5]; unsigned cs[5];
#pragma unroll
    for (int i = 0; i < 5; ++i) { const int row = lw * 160 + i * 32 + (lane >> 1);
      const bf16_t* pw = P.wmod + (size_t)row * (6 * D) + unit * 16 + (lane & 1) * 8; const bf16_t* pc = P.c + row;
      asm volatile("global_load_dwordx4 %0, %1, off" : "=v"(wm[i]) : "v"(pw) : "memory");
      asm volatile("global_load_ushort %0, %1, off" : "=v"(cs[i]) : "v"(pc) : "memory"); }
    STAMP(34);
    wait_vm<3 + 3 * 7 + 10>();
    bar();
    wait_vm<3 * 7 + 10>(); bar(); issueW(A_NS, 0); issueX(A_NS, 0);
    wait_vm<2 * 7 + 10 + 7>(); bar(); issueW(A_NS + 1, 1); issueX(A_NS + 1, 1);
    wait_vm<14>();
    asm volatile("" : "+v"(wm[0]), "+v"(wm[1]), "+v"(wm[2]), "+v"(wm[3]), "+v"(wm[4]), "+v"(cs[0]), "+v"(cs[1]), "+v"(cs[2]), "+v"(cs[3]), "+v"(cs[4]));
    bar(); issueW(A_NS + 2, 2); issueX(A_NS + 2, 2);
    {
      float a[8] = {0, 0, 0, 0, 0, 0, 0, 0};
#pragma unroll
      for (int i = 0; i < 5; ++i) { const float x = bflo(cs[i]); const float s = rbf(x * __builtin_amdgcn_rcpf(1.0f + __expf(-x)));
        a[0] += s * bflo(wm[i].x); a[1] += s * bfhi(wm[i].x); a[2] += s * bflo(wm[i].y); a[3] += s * bfhi(wm[i].y);
        a[4] += s * bflo(wm[i].z); a[5] += s * bfhi(wm[i].z); a[6] += s * bflo(wm[i].w); a[7] += s * bfhi(wm[i].w); }
#pragma unroll
      for (int e = 0; e < 8; ++e) { float v = dpp_add<0x4E>(a[e]);
        v = dpp_add<0x124>(v); v = dpp_add<0x128>(v);
        a[e] = xsum_q(v); }
      if (lane < 2) {
#pragma unroll
        for (int e = 0; e < 8; ++e) ((LAS float*)(smem + P0_RED))[lw * 16 + lane * 8 + e] = a[e];
      }
      wait_lgkm0();
    }
    wait_vm<(A_NS - 2) * 7>(); bar(); issueW(A_NS + 3, 3); issueX(A_NS + 3, 3);
    if (lw == 0 && domod && lane < 16) {
      const LAS float* red = (const LAS float*)(smem + P0_RED);
      float s = rbf((red[lane] + red[16 + lane]) + (red[32 + lane] + red[48 + lane]));
      const int n = unit * 16 + lane, seg = n / D;
      if (seg == 1 || seg == 4) s = rbf(1.0f + s);
      __hip_atomic_store((gu32*)(ws + WS_MOD) + n, __float_as_uint(s), RLX_AGENT);
      const unsigned hi = __float_as_uint(s) >> 16, nb = (unsigned)__builtin_amdgcn_update_dpp(0, (int)hi, 0xB1, 0xf, 0xf, true);
      if ((seg == 3 || seg == 4) && !(lane & 1)) __hip_atomic_store((gu32*)(ws + WS_MODB) + (((seg == 4 ? 0 : D) + n - seg * D) >> 1), hi | (nb << 16), RLX_AGENT);
    }
    static_assert(A_NS == 5, "the start-up schedule above is written for five slots");
    const u32x4 rmod = mkrs(ws + WS_MOD, 3840 * 4);
    const int tl = lane / 20, tc = lane - 20 * tl;
    const unsigned tv0 = lane < 60 ? (unsigned)((tl * D + 80 * j) * 4 + tc * 16) : 0x7ffffff0u;
    const unsigned tv1 = lane < 40 ? (unsigned)(((4 + tl) * D + 80 * j) * 4 + tc * 16) : 0x7ffffff0u;
    int slot = 4;
#pragma unroll 1
    for (int n = 4; n < 32 - A_NS; ++n) {
      wait_vm<(A_NS - 2) * 7>(); bar();
      issueW(n + A_NS, slot); issueX(n + A_NS, slot);
      if (n == 20 && lw == 0) { dma<true>(rmod, lds0 + T_MOD, tv0, 0); dma<true>(rmod, lds0 + T_MOD + 960, tv1, 0); }
      slot = slot + 1 == A_NS ? 0 : slot + 1;
    }
    static_for<0, A_NS - 1>([&](auto ic) { constexpr int r = A_NS - 2 - decltype(ic)::value; wait_vm<r * 7>(); bar(); });
    bar();
  }
  {
    LLANE
    const u32x4 rre = mkrs(P.wdr, D * D * 2), rim = mkrs(P.wdi, D * D * 2), rx = mkrs(ws + WS_UV, 32 * 2 * 20 * 4096);
    const unsigned v0 = (unsigned)(lane >> 3) * 1280u + (unsigned)(((lane & 7) ^ (lane >> 4)) * 16);
    const unsigned v1 = (unsigned)(lane >> 3) * 1280u + (unsigned)(((lane & 7) ^ (4 + (lane >> 4))) * 16);
    const unsigned vh = (unsigned)(lane >> 3) * 1280u + (unsigned)(((lane & 7) ^ ((4 * (lw >> 1) + 2 * (lw & 1) + (lane >> 4)) & 7)) * 16);
    const unsigned wb = (unsigned)(80 * j) * 1280u;
    auto issueW = [&](int n, int slot) {
      const unsigned sb = lds0 + slot * B_SLOT; const int half = n >= 10; const unsigned sw = wb + (n - 10 * half) * 128;
      const u32x4 rw = half ? rim : rre;
      dma<false>(rw, sb + (2 * lw) * 1024, v0, sw + (unsigned)(16 * lw) * 1280u);
      dma<false>(rw, sb + (2 * lw + 1) * 1024, v1, sw + (unsigned)(16 * lw + 8) * 1280u);
      dma_half<false>(rw, sb + (8 + (lw >> 1)) * 1024 + (lw & 1) * 512, vh, sw + (unsigned)(64 + 8 * (lw >> 1) + 4 * (lw & 1)) * 1280u);
    };
    auto issueX = [&](int n, int slot) {
      const unsigned sb = lds0 + slot * B_SLOT + 10240 + lw * 2048, sx = (unsigned)(p * 40 + 2 * n) * 4096u + lw * 2048;
      dma<true>(rx, sb, vx, sx); dma<true>(rx, sb + 1024, vx, sx + 1024);
    };
    __builtin_amdgcn_s_setprio(0);
    bar();
    static_for<0, B_NS>([&](auto ic) { issueW(decltype(ic)::value, decltype(ic)::value); });
    bar();
    __builtin_amdgcn_s_setprio(3);
    loader_run<B_NS, 20, 3, 2>(issueW, issueX);
    bar();
  }
  {
    LLANE
    const u32x4 rw = mkrs(ws + WS_WIN, 20 * 109 * 1024), rx = mkrs(ws + WS_X1P, 32 * 20 * 4096), rmb = mkrs(ws + WS_MODB, 2560);
    unsigned wo[4], wl[4];
#pragma unroll
    for (int u = 0; u < 3; ++u) { const int i = lw * 3 + u, kk = i >= 7; wo[u] = (unsigned)((kk * 109 + i - 7 * kk) * 1024); wl[u] = i * 1024; }
    { const int i = 12 + (lw >> 1); wo[3] = (unsigned)((109 + i - 7) * 1024 + (lw & 1) * 512); wl[3] = i * 1024 + (lw & 1) * 512; }
    auto issueW = [&](int n, int slot) {
      const unsigned sb = lds0 + C_W + slot * C_WSLOT; const int z1 = n >= 10;
      const unsigned so = (unsigned)((2 * (n - 10 * z1) * 109 + (z1 ? 0 : 54) + ft0) * 1024);
      dma<true>(rw, sb + wl[0], vx, so + wo[0]); dma<true>(rw, sb + wl[1], vx, so + wo[1]); dma<true>(rw, sb + wl[2], vx, so + wo[2]); dma_half<true>(rw, sb + wl[3], vx, so + wo[3]);
    };
    auto issueX = [&](int n) {
      const unsigned o = (unsigned)(2 * n) * 4096u + lw * 2048, sx = (unsigned)p * 20 * 4096u + o;
      dma<true>(rx, lds0 + C_X + o, vx, sx); dma<true>(rx, lds0 + C_X + o + 1024, vx, sx + 1024);
    };
    __builtin_amdgcn_s_setprio(0);
    bar();
    if (lw < 3) dma<true>(rmb, lds0 + T_MODB + lw * 1024, vx + lw * 1024, 0);
    static_for<0, C_NS>([&](auto ic) { issueW(decltype(ic)::value, decltype(ic)::value); });
    bar();
    __builtin_amdgcn_s_setprio(3);
    static_for<0, C_NS>([&](auto ic) { issueX(decltype(ic)::value); });
    wait_vm<(C_NS - 1) * 2>(); bar();
    static_for<0, 9>([&](auto ic) {
      constexpr int n = decltype(ic)::value, need = n + 1;
      constexpr int last = n + C_NS - 1 < 11 ? n + C_NS - 1 : 11;
      constexpr int cnt = [] { int c = 0;
        if (need < C_NS) { c += (C_NS - 1 - need) * 2; for (int s = C_NS; s <= last; ++s) c += (s < 10 ? 6 : 4); }
        else { for (int s = need + 1; s <= last; ++s) c += (s < 10 ? 6 : 4); }
        return c; }();
      wait_vm<cnt>(); bar();
      if constexpr (n + C_NS < 12) { issueW(n + C_NS, n % C_NS); if constexpr (n + C_NS < 10) issueX(n + C_NS); }
    });
    bar();
    bar();
    issueW(12, 2); issueW(13, 3); issueW(14, 4);
    wait_vm<16>(); bar();
    static_for<10, 19>([&](auto ic) {
      constexpr int n = decltype(ic)::value;
      constexpr int last = n + 4 < 19 ? n + 4 : 19;
      wait_vm<(last - (n + 1)) * 4>(); bar();
      if constexpr (n + C_NS < 20) issueW(n + C_NS, n % C_NS);
    });
    bar();
  }
  {
    LLANE LV4K
    const u32x4 rw = mkrs(P.wsp, S * S * 2), rx = mkrs(ws + WS_Z2N, 64 * 55 * 1024);
    const unsigned wrow = (unsigned)(64 * p + 16 * lw) * 4096u;
    unsigned xo[4], xl[4];
#pragma unroll
    for (int u = 0; u < 3; ++u) { const int i = lw * 3 + u, kk = i >= 7; xo[u] = (unsigned)((kk * 55 + ft0 + i - 7 * kk) * 1024); xl[u] = 8192 + i * 1024; }
    { const int i = 12 + (lw >> 1); xo[3] = (unsigned)((55 + ft0 + i - 7) * 1024 + (lw & 1) * 512); xl[3] = 8192 + i * 1024 + (lw & 1) * 512; }
    auto issueW = [&](int n, int slot) {
      const unsigned sb = lds0 + slot * D_SLOT + lw * 2048, sw = wrow + ((n + krot) & 31) * 128;
      dma<false>(rw, sb, v4k0, sw); dma<false>(rw, sb + 1024, v4k1, sw);
    };
    auto issueX = [&](int n, int slot) {
      const unsigned sb = lds0 + slot * D_SLOT, so = ((n + krot) & 31) * 110 * 1024;
      dma<true>(rx, sb + xl[0], vx, so + xo[0]); dma<true>(rx, sb + xl[1], vx, so + xo[1]); dma<true>(rx, sb + xl[2], vx, so + xo[2]); dma_half<true>(rx, sb + xl[3], vx, so + xo[3]);
    };
    static_for<0, D_NS>([&](auto ic) { issueW(decltype(ic)::value, decltype(ic)::value); });
    loader_run<D_NS, 32, 2, 4>(issueW, issueX);
    bar();
  }
  {
    LLANE
    const u32x4 rw = mkrs(ws + WS_WOUT, 27 * 40 * 1024), rx = mkrs(ws + WS_P, 32 * 28 * 4096);
    const int i0 = lw * 2, i1 = lw * 2 + 1, i2 = 8 + (lw >> 1);
    const unsigned o0 = (unsigned)(((i0 >= 5) * 40 + 5 * j + i0 - 5 * (i0 >= 5)) * 1024), o1 = (unsigned)(((i1 >= 5) * 40 + 5 * j + i1 - 5 * (i1 >= 5)) * 1024);
    const unsigned o2 = (unsigned)((40 + 5 * j + i2 - 5) * 1024 + (lw & 1) * 512);
    auto issueW = [&](int n, int slot) {
      const unsigned sb = lds0 + slot * E_SLOT, so = (unsigned)n * 80 * 1024;
      dma<true>(rw, sb + i0 * 1024, vx, so + o0); dma<true>(rw, sb + i1 * 1024, vx, so + o1); dma_half<true>(rw, sb + i2 * 1024 + (lw & 1) * 512, vx, so + o2);
    };
    auto issueX = [&](int n, int slot) {
      const unsigned sb = lds0 + slot * E_SLOT + 10240 + lw * 2048, sx = (unsigned)(p * 28 + 2 * n) * 4096u + lw * 2048;
      dma<true>(rx, sb, vx, sx); dma<true>(rx, sb + 1024, vx, sx + 1024);
    };
    __builtin_amdgcn_s_setprio(0);
    bar();
    static_for<0, E_NS>([&](auto ic) { issueW(decltype(ic)::value, decltype(ic)::value); });
    bar();
    __builtin_amdgcn_s_setprio(3);
    loader_run<E_NS, 14, 3, 2>(issueW, issueX);
    bar();
  }
  STAMP_FLUSH();
}

DEV void consumer_main(const Params& P, LAS char* smem, const int w, const int lane_, const int b, const int p, const int j) {
  STAMP_DECL STAMP(0);
  char* const ws = P.ws;
  unsigned tagv = __hip_atomic_load((gu32*)(ws + WS_EPOCH), RLX_AGENT) + 1u, tag;
  const int mh = w >> 1, kh = w & 1;
  const auto rws = mkrsrc(ws, WS_END);
  const int ft0 = 27 * (j >> 2) + 7 * (j & 3), nft = (j & 3) == 3 ? 6 : 7;
  auto rot = [&](int i) { return i < 3 ? i + 4 * mh : i - 3 * mh; };
#define LANE_IDS int lane = lane_; asm volatile("" : "+v"(lane)); const int q = lane >> 4, tk = lane & 15, tid = w * 64 + lane, token = 64 * p + 16 * w + tk; (void)q; (void)tk; (void)tid; (void)token;
  {
    LANE_IDS
    unsigned v[2][5];
#pragma unroll
    for (int t = 0; t < 2; ++t) { const unsigned* xr = (const unsigned*)(P.x + (size_t)(64 * p + 8 * j + 2 * w + t) * D);
#pragma unroll
      for (int i = 0; i < 5; ++i) v[t][i] = xr[lane + 64 * i]; }
    const int u0 = w * 256 + b; const bool dopt = u0 < 810; const int u = dopt ? u0 : 0; const bool isin = u < 540;
    const int uu = isin ? u : u - 540, ncb = isin ? 27 : 10, pkb = uu / ncb, pcb = uu - pkb * ncb, ld = isin ? F : D;
    u32x4 pv[4];
    { const bf16_t* src = (isin ? P.win : P.wout) + (size_t)(pkb * 32) * ld + pcb * 64;
#pragma unroll
      for (int i = 0; i < 4; ++i) pv[i] = *(const u32x4*)(src + (size_t)(i * 8 + (lane >> 3)) * ld + (lane & 7) * 8); }
    LAS unsigned* t32 = (LAS unsigned*)(smem + P0_T);
    unsigned pk[2][5];
#pragma unroll
    for (int t = 0; t < 2; ++t) {
      float s = 0.f;
#pragma unroll
      for (int i = 0; i < 5; ++i) s += bflo(v[t][i]) + bfhi(v[t][i]);
      const float mean = xsum_q(xsum_16(s)) * (1.0f / D);
      float q2 = 0.f;
#pragma unroll
      for (int i = 0; i < 5; ++i) { const float a = bflo(v[t][i]) - mean, c = bfhi(v[t][i]) - mean; q2 += a * a + c * c; }
      const float rs = 1.0f / sqrtf(xsum_q(xsum_16(q2)) * (1.0f / D) + 1e-5f);
#pragma unroll
      for (int i = 0; i < 5; ++i) pk[t][i] = pack2((bflo(v[t][i]) - mean) * rs, (bfhi(v[t][i]) - mean) * rs);
    }
#pragma unroll
    for (int i = 0; i < 5; ++i) { const int d = 2 * (lane + 64 * i);
      t32[d * 4 + w] = (pk[0][i] & 0xffffu) | (pk[1][i] << 16); t32[(d + 1) * 4 + w] = (pk[0][i] >> 16) | (pk[1][i] & 0xffff0000u); }
    asm volatile("" : "+v"(tagv)); tag = __builtin_amdgcn_readfirstlane(tagv);
    wait_lgkm0(); bar();
    const unsigned xb = WS_XN + (unsigned)((2 * p + (j >> 2)) * 40 * 1024 + (j & 3) * 256);
#pragma unroll
    for (int i = 0; i < 3; ++i) { const int d = tid + 256 * i;
      if (d < D) st16(rws, xb + (d >> 4) * 1024 + (d & 15) * 16, *(LAS u32x4*)(smem + P0_T + d * 16)); }
    wait_vm<0>();
    bar();
    STAMP(1);
    if (w == 0 && lane == 0) set_flag(ws, WS_FN + p * 128 + j * 4, tag);
    {
      LAS char* t = smem + P0_PT + w * 4096;
#pragma unroll
      for (int i = 0; i < 4; ++i) *(LAS u32x4*)(t + (i * 8 + (lane >> 3)) * 128 + (lane & 7) * 16) = pv[i];
      const unsigned dst = dopt ? (isin ? WS_WIN + (unsigned)(pkb * 109 + pcb * 4) * 1024 : WS_WOUT + (unsigned)(pkb * 40 + pcb * 4) * 1024) + lane * 16 : 0xfffff000u;
      const int rq = tk >> 2, pp = lane & 3;
#pragma unroll
      for (int c = 0; c < 4; ++c) {
        const u32x2 l2 = __builtin_bit_cast(u32x2, __builtin_amdgcn_ds_read_tr16_b64_v4i16((LAS s16x4*)(t + (8 * q + rq) * 128 + c * 32 + pp * 8)));
        const u32x2 h2 = __builtin_bit_cast(u32x2, __builtin_amdgcn_ds_read_tr16_b64_v4i16((LAS s16x4*)(t + (8 * q + 4 + rq) * 128 + c * 32 + pp * 8)));
        st16(rws, dst + (dopt ? c * 1024 : 0), (u32x4){l2.x, l2.y, h2.x, h2.y});
      }
    }
    if (w == 0) { wait256(rws, ws, WS_FN, tag, lane); STAMP(23); }
    bar();
    STAMP(2);
  }
  u32x2 xv[5];
  {
    LANE_IDS
    f32x4 acc[2][5];
#pragma unroll
    for (int t = 0; t < 2; ++t)
#pragma unroll
      for (int i = 0; i < 5; ++i) acc[t][i] = (f32x4){0, 0, 0, 0};
    f32x4 rsum[2] = {(f32x4){0, 0, 0, 0}, (f32x4){0, 0, 0, 0}};
    const bf16x8 ones = __builtin_bit_cast(bf16x8, (u32x4){0x3f803f80u, 0x3f803f80u, 0x3f803f80u, 0x3f803f80u});
    const unsigned wa0 = (unsigned)(w * 4096 + tk * 128 + ((q ^ ((lane >> 1) & 7)) * 16));
    const unsigned xa = 16384 + lane * 16;
    bf16x8 Wa[2], Xa[5], Wb[2], Xb[5];
    auto rdm = [&](bf16x8(&Wn)[2], bf16x8(&Xn)[5], int slot, int kk, bf16x8(&Wf)[2], bf16x8(&Xf)[5]) {
      LAS char* sb = smem + slot * A_SLOT; const unsigned wa = wa0 ^ (kk * 64);
      static_for<0, 12>([&](auto ic) { constexpr int z = decltype(ic)::value, i = z >> 1, t = z & 1;
        if constexpr (z < 10) acc[t][i] = mfma(Xf[i], Wf[t], acc[t][i]); else rsum[t] = mfma(ones, Wf[t], rsum[t]);
        if constexpr (z == 0) Wn[0] = *(LAS bf16x8*)(sb + wa);
        if constexpr (z == 1) Xn[0] = *(LAS bf16x8*)(sb + xa + (kk * 5) * 1024);
        if constexpr (z == 2) Wn[1] = *(LAS bf16x8*)(sb + wa + 2048);
        if constexpr (z >= 3 && z < 7) Xn[z - 2] = *(LAS bf16x8*)(sb + xa + (kk * 5 + z - 2) * 1024);
        sched_fence(); });
    };
    auto mmlast = [&](bf16x8(&Wf)[2], bf16x8(&Xf)[5]) {
#pragma unroll
      for (int i = 0; i < 5; ++i) { acc[0][i] = mfma(Xf[i], Wf[0], acc[0][i]); acc[1][i] = mfma(Xf[i], Wf[1], acc[1][i]); }
      rsum[0] = mfma(ones, Wf[0], rsum[0]); rsum[1] = mfma(ones, Wf[1], rsum[1]);
    };
    int slot = 0;
    auto steps = [&](int n0, int n1, bool hooks) {
#pragma unroll 1
      for (int n = n0; n < n1; ++n) {
        rdm(Wb, Xb, slot, 1, Wa, Xa);
        const int ns = slot + 1 == A_NS ? 0 : slot + 1;
        if (hooks && n == 8) wait_vm<0>();
        wait_lgkm0(); bar(); sched_fence();
        if (hooks && n == 8 && w == 0 && lane == 0) set_flag(ws, WS_F2 + p * 128 + j * 4, tag);
        rdm(Wa, Xa, ns, 0, Wb, Xb);
        slot = ns;
      }
    };
    bar();
    STAMP(3); CSTAMP(40);
    { LAS char* sb = smem; Wa[0] = *(LAS bf16x8*)(sb + wa0); Wa[1] = *(LAS bf16x8*)(sb + wa0 + 2048);
#pragma unroll
      for (int i = 0; i < 5; ++i) Xa[i] = *(LAS bf16x8*)(sb + xa + i * 1024); }
    steps(0, 12, true);
    const u32x4 f2 = peek256(rws, WS_F2, lane);
    steps(12, 18, false);
    if (!flags_ok(f2, tag)) wait256(rws, ws, WS_F2, tag, lane);
    if (b == 0 && w == 0 && lane == 0) set_flag(ws, WS_EPOCH, tag + 15);
#pragma unroll
    for (int i = 0; i < 5; ++i) xv[i] = *(const u32x2*)(P.x + (size_t)token * D + 80 * j + 16 * i + 4 * q);
    steps(18, 31, false);
    { LAS char* sb = smem + slot * A_SLOT; const unsigned wa = wa0 ^ 64;
      Wb[0] = *(LAS bf16x8*)(sb + wa); Wb[1] = *(LAS bf16x8*)(sb + wa + 2048);
#pragma unroll
      for (int i = 0; i < 5; ++i) Xb[i] = *(LAS bf16x8*)(sb + xa + (5 + i) * 1024); }
    mmlast(Wa, Xa);
    wait_lgkm0(); bar();
    mmlast(Wb, Xb);
    STAMP(4); CSTAMP(41);
    const float rs0 = rsum[0][0], rs1 = rsum[1][0];
#pragma unroll
    for (int i = 0; i < 5; ++i) {
      u32x2 a, c;
      const f32x4 sa = *(const LAS f32x4*)(smem + T_MOD + (16 * i + 4 * q) * 4), c1 = *(const LAS f32x4*)(smem + T_MOD + (80 + 16 * i + 4 * q) * 4);
      { const f32x4 u = acc[0][i] * c1 + rs0 * sa; a.x = pack2(u[0], u[1]); a.y = pack2(u[2], u[3]); }
      { const f32x4 v = -(acc[1][i] * c1 + rs1 * sa); c.x = pack2(v[0], v[1]); c.y = pack2(v[2], v[3]); }
      const int dt = 5 * j + i, kb = dt >> 1, qq = 2 * (dt & 1) + (q >> 1);
      st16(rws, WS_UV + (unsigned)(((p * 2 + (q & 1)) * 20 + kb) * 4 + w) * 1024 + (qq * 16 + tk) * 16, swap_pair(a, c));
    }
    wait_vm<0>(); bar();
    STAMP(5);
    if (w == 0) { if (lane == 0) set_flag(ws, WS_FU + p * 128 + j * 4, tag); wait_flags8(ws, WS_FU + p * 128, tag, lane); }
    bar();
    STAMP(6); CSTAMP(44);
  }
  u32x2 x1p[5];
  {
    LANE_IDS
    f32x4 acc[5][2];
#pragma unroll
    for (int i = 0; i < 5; ++i) { acc[i][0] = (f32x4){0, 0, 0, 0}; acc[i][1] = (f32x4){0, 0, 0, 0}; }
    const unsigned wa = (unsigned)(tk * 128 + (((q + 4 * kh) ^ ((lane >> 1) & 7)) * 16));
    const unsigned xa = 10240 + (kh * 4 + 2 * mh) * 1024 + lane * 16;
    ksplit_loop<B_NS, 20, 5, 2>(
        [&](bf16x8(&A)[5], bf16x8(&Bf)[2], int slot, int) { LAS char* sb = smem + slot * B_SLOT;
          A[0] = *(LAS bf16x8*)(sb + wa); Bf[0] = *(LAS bf16x8*)(sb + xa); Bf[1] = *(LAS bf16x8*)(sb + xa + 1024);
#pragma unroll
          for (int i = 1; i < 5; ++i) A[i] = *(LAS bf16x8*)(sb + wa + i * 2048); },
        [&](bf16x8(&A)[5], bf16x8(&Bf)[2], bool) {
#pragma unroll
          for (int i = 0; i < 5; ++i) { acc[i][0] = mfma(A[i], Bf[0], acc[i][0]); acc[i][1] = mfma(A[i], Bf[1], acc[i][1]); } });
    STAMP(7); CSTAMP(45);
    f32x4 y[5];
    kreduce<5>(acc, y, smem + B_SCR, w, lane);
    float xs[20]; u32x2 xp[5];
    float s = 0.f;
#pragma unroll
    for (int i = 0; i < 5; ++i) {
      const f32x4 ga = *(const LAS f32x4*)(smem + T_MOD + (160 + 16 * i + 4 * q) * 4), c1b = *(const LAS f32x4*)(smem + T_MOD + 960 + (16 * i + 4 * q) * 4);
      x1p[i].x = pack2(bflo(xv[i].x) + ga[0] * y[i][0], bfhi(xv[i].x) + ga[1] * y[i][1]);
      x1p[i].y = pack2(bflo(xv[i].y) + ga[2] * y[i][2], bfhi(xv[i].y) + ga[3] * y[i][3]);
      asm volatile("" : "+v"(x1p[i].x), "+v"(x1p[i].y));
      xs[4 * i] = bflo(x1p[i].x); xs[4 * i + 1] = bfhi(x1p[i].x); xs[4 * i + 2] = bflo(x1p[i].y); xs[4 * i + 3] = bfhi(x1p[i].y);
      s += (xs[4 * i] + xs[4 * i + 1]) + (xs[4 * i + 2] + xs[4 * i + 3]);
      xp[i].x = pack2(xs[4 * i] * c1b[0], xs[4 * i + 1] * c1b[1]); xp[i].y = pack2(xs[4 * i + 2] * c1b[2], xs[4 * i + 3] * c1b[3]);
    }
    const unsigned img = WS_X1P + (unsigned)p * 20 * 4096 + w * 1024;
#pragma unroll
    for (int i = 0; i < 6; i += 2) {
      const int i2 = i + 1 < 5 ? i + 1 : i;
      const int dt = 5 * j + ((q & 1) ? i2 : i), kb = dt >> 1, qq = 2 * (dt & 1) + (q >> 1);
      st16(rws, img + kb * 4096 + (qq * 16 + tk) * 16, swap_pair(xp[i], xp[i2]));
    }
    const float mean = xsum_q(s) * (1.0f / 80.0f);
    float m2 = 0.f;
#pragma unroll
    for (int e = 0; e < 20; ++e) { const float d = xs[e] - mean; m2 += d * d; }
    m2 = xsum_q(m2);
    if (q == 0) __hip_atomic_store((gu64*)(ws + WS_ST1) + token * 8 + j, ((unsigned long long)__float_as_uint(m2) << 32) | __float_as_uint(mean), RLX_AGENT);
    wait_vm<0>(); bar();
    STAMP(8);
    if (w == 0) { if (lane == 0) set_flag(ws, WS_FX + p * 128 + j * 4, tag); wait_flags8(ws, WS_FX + p * 128, tag, lane); }
    bar();
    STAMP(9);
  }
  u32x2 z1p[7]; float bias;
  {
    LANE_IDS
    u32x4 st[4];
#pragma unroll
    for (int k = 0; k < 4; ++k) st[k] = ld16(rws, WS_ST1 + (unsigned)token * 64 + k * 16);
    bias = bflo(P.bsp[token]);
    for (int i = tid; i < 448; i += 256) ((LAS float*)(smem + T_CV))[i] = 0.f;
    f32x4 acc[7][2], aux[4];
    auto clear = [&]() {
#pragma unroll
      for (int i = 0; i < 7; ++i) { acc[i][0] = (f32x4){0, 0, 0, 0}; acc[i][1] = (f32x4){0, 0, 0, 0}; }
#pragma unroll
      for (int i = 0; i < 4; ++i) aux[i] = (f32x4){0, 0, 0, 0};
    };
    const unsigned xa = C_X + (kh * 4 + 2 * mh) * 1024 + lane * 16, wlo = C_W + kh * 7168 + lane * 16 + mh * 4096, whi = C_W + kh * 7168 + lane * 16 - mh * 3072;
    const int r4 = tk & 3;
    const unsigned ma = T_MODB + (r4 == 0 ? q * 16 + kh * 64 : r4 == 1 ? 1280 + q * 16 + kh * 64 : 2560), mstride = r4 < 2 ? 128 : 0;
    int nbase = 0;
    auto rd = [&](bf16x8(&A)[7], bf16x8(&Bf)[3], int slot, int n) {
      LAS char* sl = smem + wlo + slot * C_WSLOT; LAS char* sh = smem + whi + slot * C_WSLOT; LAS char* xb = smem + xa + n * 8192;
      Bf[0] = *(LAS bf16x8*)(xb); A[0] = *(LAS bf16x8*)(sl); Bf[1] = *(LAS bf16x8*)(xb + 1024);
      A[1] = *(LAS bf16x8*)(sl + 1024); A[2] = *(LAS bf16x8*)(sl + 2048);
#pragma unroll
      for (int i = 3; i < 7; ++i) A[i] = *(LAS bf16x8*)(sh + i * 1024);
      Bf[2] = *(LAS bf16x8*)(smem + ma + n * mstride);
    };
    auto auxmm = [&](bf16x8(&A)[7], bf16x8(&Bf)[3]) {
#pragma unroll
      for (int i = 0; i < 4; ++i) aux[i] = mfma(Bf[2], A[i], aux[i]);
    };
    auto mm2 = [&](bf16x8(&A)[7], bf16x8(&Bf)[3], bool) {
#pragma unroll
      for (int i = 0; i < 7; ++i) { acc[i][0] = mfma(Bf[0], A[i], acc[i][0]); acc[i][1] = mfma(Bf[1], A[i], acc[i][1]); }
      auxmm(A, Bf); };
    auto mm1 = [&](bf16x8(&A)[7], bf16x8(&Bf)[3], bool) {
#pragma unroll
      for (int i = 0; i < 7; ++i) { acc[i][0] = mfma(A[i], Bf[0], acc[i][0]); acc[i][1] = mfma(A[i], Bf[1], acc[i][1]); }
      auxmm(A, Bf); };
    auto put_cv = [&](int pass_) {
      LAS float* cv = (LAS float*)(smem + T_CV) + pass_ * 224;
      if (q == 0) {
#pragma unroll
        for (int i = 0; i < 4; ++i) if (mh == 0 || i < 3) { const int t = rot(i);
          __hip_atomic_fetch_add(cv + 16 * t + tk, aux[i][0], __ATOMIC_RELAXED, __HIP_MEMORY_SCOPE_WORKGROUP);
          __hip_atomic_fetch_add(cv + 112 + 16 * t + tk, aux[i][1], __ATOMIC_RELAXED, __HIP_MEMORY_SCOPE_WORKGROUP); }
      }
    };
    float rmean, rrstd;
    { float mu[8], mean = 0.f;
#pragma unroll
      for (int k = 0; k < 4; ++k) { mu[2 * k] = __uint_as_float(st[k].x); mu[2 * k + 1] = __uint_as_float(st[k].z); mean += mu[2 * k] + mu[2 * k + 1]; }
      mean *= 0.125f; float M2 = 0.f;
#pragma unroll
      for (int k = 0; k < 4; ++k) { const float d0 = mu[2 * k] - mean, d1 = mu[2 * k + 1] - mean; M2 += __uint_as_float(st[k].y) + __uint_as_float(st[k].w) + 80.0f * (d0 * d0 + d1 * d1); }
      rmean = mean; rrstd = 1.0f / sqrtf(M2 * (1.0f / D) + 1e-5f); }
    (void)nbase;
    bf16x8 A0[7], B0[3], A1[7], B1[3];
    int slot = 0;
    auto nohook = [](int) {};
    clear();
    wait_lgkm0(); bar();
    STAMP(10); CSTAMP(46);
    rd(A0, B0, 0, 0);
    ksplit_steps<C_NS, 10, 7, 3, 18>(A0, B0, A1, B1, slot, 0, 8, rd, mm2, nohook, nohook);
    ksplit_last<C_NS, 10, 7, 3>(A0, B0, A1, B1, slot, rd, mm2);
    STAMP(11); CSTAMP(47);
    put_cv(0);
    f32x4 zs[7];
    kreduce<7>(acc, zs, smem + C_SCR, w, lane);
    wait_lgkm0(); bar();
    f32x2 mr01, mr23, rr01, rr23;
    mr01 = (f32x2){__shfl(rmean, 4 * q), __shfl(rmean, 4 * q + 1)}; mr23 = (f32x2){__shfl(rmean, 4 * q + 2), __shfl(rmean, 4 * q + 3)};
    rr01 = (f32x2){__shfl(rrstd, 4 * q), __shfl(rrstd, 4 * q + 1)}; rr23 = (f32x2){__shfl(rrstd, 4 * q + 2), __shfl(rrstd, 4 * q + 3)};
    f32x2 z01[7], z23[7];
#pragma unroll
    for (int i = 0; i < 7; ++i) {
      const float c1 = ((const LAS float*)(smem + T_CV))[16 * rot(i) + tk], c2 = ((const LAS float*)(smem + T_CV))[112 + 16 * rot(i) + tk];
      z01[i] = gelu2(rr01 * ((f32x2){zs[i][0], zs[i][1]} - mr01 * c1) + c2);
      z23[i] = gelu2(rr23 * ((f32x2){zs[i][2], zs[i][3]} - mr23 * c1) + c2);
    }
    {
      const float inv = 1.0f / (16.0f * nft);
      const f32x2 zero2 = (f32x2){0.f, 0.f};
      f32x2 s01 = zero2, s23 = zero2;
#pragma unroll
      for (int i = 0; i < 7; ++i) { const bool ok = (i != 2 && i != 6) || rot(i) < nft; s01 += ok ? z01[i] : zero2; s23 += ok ? z23[i] : zero2; }
      const f32x2 m01 = (f32x2){xsum_16(s01[0]), xsum_16(s01[1])} * inv, m23 = (f32x2){xsum_16(s23[0]), xsum_16(s23[1])} * inv;
      f32x2 t01 = zero2, t23 = zero2;
#pragma unroll
      for (int i = 0; i < 7; ++i) { const bool ok = (i != 2 && i != 6) || rot(i) < nft; const f32x2 a = ok ? z01[i] - m01 : zero2, c = ok ? z23[i] - m23 : zero2; t01 += a * a; t23 += c * c; }
      const float q0 = xsum_16(t01[0]), q1 = xsum_16(t01[1]), q2 = xsum_16(t23[0]), q3 = xsum_16(t23[1]);
      if (tk < 8) { const int r = tk & 3; const float a = r == 0 ? m01[0] : r == 1 ? m01[1] : r == 2 ? m23[0] : m23[1], c = r == 0 ? q0 : r == 1 ? q1 : r == 2 ? q2 : q3;
        const int tok = 64 * p + 16 * w + 4 * q + r;
        __hip_atomic_store((gu64*)(ws + WS_ST2) + (tok * 8 + j) * 2 + (tk >> 2), ((unsigned long long)tag << 32) | __float_as_uint(tk < 4 ? a : c), RLX_AGENT); }
    }
    STAMP(12);
    {
      u32x4 g0, g1;
      const unsigned go = WS_ST2 + (unsigned)(token * 8 + 2 * q) * 16;
      for (unsigned spins = 0;;) { g0 = ld16(rws, go); g1 = ld16(rws, go + 16);
        if (__all(g0.y == tag && g0.w == tag && g1.y == tag && g1.w == tag)) break; if (spin_fail(spins, ws)) break; }
      const float n0 = 112.0f, n1 = (q & 1) ? 96.0f : 112.0f;
      const float m0 = __uint_as_float(g0.x), m1 = __uint_as_float(g1.x);
      const float mean = xsum_q(n0 * m0 + n1 * m1) * (1.0f / FH);
      const float d0 = m0 - mean, d1 = m1 - mean;
      const float M2 = xsum_q(__uint_as_float(g0.z) + __uint_as_float(g1.z) + n0 * d0 * d0 + n1 * d1 * d1);
      const float rstd = 1.0f / sqrtf(M2 * (1.0f / FH) + 1e-5f);
      mr01 = (f32x2){__shfl(mean, 4 * q), __shfl(mean, 4 * q + 1)}; mr23 = (f32x2){__shfl(mean, 4 * q + 2), __shfl(mean, 4 * q + 3)};
      rr01 = (f32x2){__shfl(rstd, 4 * q), __shfl(rstd, 4 * q + 1)}; rr23 = (f32x2){__shfl(rstd, 4 * q + 2), __shfl(rstd, 4 * q + 3)};
    }
    STAMP(13);
    {
      u32x2 pk[7];
#pragma unroll
      for (int i = 0; i < 7; ++i) { const f32x2 a = (z01[i] - mr01) * rr01, c = (z23[i] - mr23) * rr23; pk[i].x = pack2(a[0], a[1]); pk[i].y = pack2(c[0], c[1]); }
      const unsigned img = WS_Z2N + (unsigned)(2 * p + (w >> 1)) * 55 * 1024 + ((2 * (w & 1) + (q >> 1)) * 16 + tk) * 16;
#pragma unroll
      for (int i = 0; i < 8; i += 2) {
        const int i2 = i + 1 < 7 ? i + 1 : i;
        const u32x4 ch = swap_pair(pk[i], pk[i2]);
        const int ti = (q & 1) ? rot(i2) : rot(i);
        st16(rws, ti < nft ? img + (ft0 + ti) * 1024 : 0xfffff000u, ch);
      }
    }
    STAMP(14); CSTAMP(48);
    clear();
    slot = 0;
    bar();
    rd(A0, B0, 0, 0);
    ksplit_steps<C_NS, 10, 7, 3, 18>(A0, B0, A1, B1, slot, 0, 6, rd, mm1,
        [&](int n) { if (n == 2) wait_vm<0>(); },
        [&](int n) { if (n == 2 && w == 0 && lane == 0) set_flag(ws, WS_FZ + (j * 4 + (p >> 3)) * 128 + (p & 7) * 4, tag); });
    const unsigned fzo = WS_FZ + (j * 4 + ((lane & 7) >> 1)) * 128 + (lane & 1) * 16;
    const u32x4 fz = ld16(rws, fzo);
    ksplit_steps<C_NS, 10, 7, 3, 18>(A0, B0, A1, B1, slot, 6, 8, rd, mm1, nohook, nohook);
    ksplit_last<C_NS, 10, 7, 3>(A0, B0, A1, B1, slot, rd, mm1);
    if (!flags_ok(fz, tag)) { for (unsigned spins = 0;;) { if (flags_ok(ld16(rws, fzo), tag)) break; if (spin_fail(spins, ws)) break; } }
    STAMP(15); CSTAMP(49);
    put_cv(1);
    kreduce<7>(acc, zs, smem + C_SCR, w, lane);
#pragma unroll
    for (int i = 0; i < 7; ++i) {
      const LAS float* cv = (const LAS float*)(smem + T_CV) + 224;
      const f32x4 c1 = *(const LAS f32x4*)(cv + 16 * rot(i) + 4 * q), c2 = *(const LAS f32x4*)(cv + 112 + 16 * rot(i) + 4 * q);
      const f32x2 a = gelu2(rrstd * ((f32x2){zs[i][0], zs[i][1]} - rmean * (f32x2){c1[0], c1[1]}) + (f32x2){c2[0], c2[1]});
      const f32x2 c = gelu2(rrstd * ((f32x2){zs[i][2], zs[i][3]} - rmean * (f32x2){c1[2], c1[3]}) + (f32x2){c2[2], c2[3]});
      z1p[i] = (u32x2){pack2(a[0], a[1]), pack2(c[0], c[1])};
      asm volatile("" : "+v"(z1p[i].x), "+v"(z1p[i].y));
    }
    STAMP(16); CSTAMP(42);
  }
  {
    LANE_IDS
    f32x4 acc[7][2];
#pragma unroll
    for (int i = 0; i < 7; ++i) { acc[i][0] = (f32x4){0, 0, 0, 0}; acc[i][1] = (f32x4){0, 0, 0, 0}; }
    const unsigned wa = (unsigned)((2 * mh) * 2048 + tk * 128 + (((q + 4 * kh) ^ ((lane >> 1) & 7)) * 16));
    const unsigned xlo = 8192 + kh * 7168 + lane * 16 + mh * 4096, xhi = 8192 + kh * 7168 + lane * 16 - mh * 3072;
    ksplit_loop<D_NS, 32, 7, 2>(
        [&](bf16x8(&A)[7], bf16x8(&Bf)[2], int slot, int) { LAS char* sb = smem + slot * D_SLOT;
          Bf[0] = *(LAS bf16x8*)(sb + wa); A[0] = *(LAS bf16x8*)(sb + xlo); Bf[1] = *(LAS bf16x8*)(sb + wa + 2048);
          A[1] = *(LAS bf16x8*)(sb + xlo + 1024); A[2] = *(LAS bf16x8*)(sb + xlo + 2048);
#pragma unroll
          for (int i = 3; i < 7; ++i) A[i] = *(LAS bf16x8*)(sb + xhi + i * 1024); },
        [&](bf16x8(&A)[7], bf16x8(&Bf)[2], bool) {
#pragma unroll
          for (int i = 0; i < 7; ++i) { acc[i][0] = mfma(A[i], Bf[0], acc[i][0]); acc[i][1] = mfma(A[i], Bf[1], acc[i][1]); } });
    STAMP(17); CSTAMP(43);
    f32x4 g[7];
    kreduce<7>(acc, g, smem + D_SCR, w, lane);
    u32x2 pk[7];
#pragma unroll
    for (int i = 0; i < 7; ++i) {
      pk[i].x = pack2(bflo(z1p[i].x) * (g[i][0] + bias), bfhi(z1p[i].x) * (g[i][1] + bias));
      pk[i].y = pack2(bflo(z1p[i].y) * (g[i][2] + bias), bfhi(z1p[i].y) * (g[i][3] + bias));
    }
    const unsigned img = WS_P + (unsigned)p * 28 * 4096 + w * 1024;
#pragma unroll
    for (int i = 0; i < 8; i += 2) {
      const int i2 = i + 1 < 7 ? i + 1 : i;
      const u32x4 ch = swap_pair(pk[i], pk[i2]);
      const int ti = (q & 1) ? rot(i2) : rot(i), ft = ft0 + ti, kb = ft >> 1, qq = 2 * (ft & 1) + (q >> 1);
      st16(rws, ti < nft ? img + kb * 4096 + (qq * 16 + tk) * 16 : 0xfffff000u, ch);
    }
    wait_vm<0>(); bar();
    STAMP(18);
    if (w == 0) { if (lane == 0) set_flag(ws, WS_FP + p * 128 + j * 4, tag); wait_flags8(ws, WS_FP + p * 128, tag, lane); }
    bar();
    STAMP(19);
  }
  {
    LANE_IDS
    f32x4 acc[5][2];
#pragma unroll
    for (int i = 0; i < 5; ++i) { acc[i][0] = (f32x4){0, 0, 0, 0}; acc[i][1] = (f32x4){0, 0, 0, 0}; }
    const unsigned wa = kh * 5120 + lane * 16, xa = 10240 + (kh * 4 + 2 * mh) * 1024 + lane * 16;
    ksplit_loop<E_NS, 14, 5, 2>(
        [&](bf16x8(&A)[5], bf16x8(&Bf)[2], int slot, int) { LAS char* sb = smem + slot * E_SLOT;
          A[0] = *(LAS bf16x8*)(sb + wa); Bf[0] = *(LAS bf16x8*)(sb + xa); Bf[1] = *(LAS bf16x8*)(sb + xa + 1024);
#pragma unroll
          for (int i = 1; i < 5; ++i) A[i] = *(LAS bf16x8*)(sb + wa + i * 1024); },
        [&](bf16x8(&A)[5], bf16x8(&Bf)[2], bool last) {
          if (!(last && kh)) {
#pragma unroll
            for (int i = 0; i < 5; ++i) { acc[i][0] = mfma(A[i], Bf[0], acc[i][0]); acc[i][1] = mfma(A[i], Bf[1], acc[i][1]); } } });
    STAMP(20);
    f32x4 o[5];
    kreduce<5>(acc, o, smem + E_SCR, w, lane);
    u32x2 pk[5];
#pragma unroll
    for (int i = 0; i < 5; ++i) {
      const f32x4 gb = *(const LAS f32x4*)(smem + T_MOD + 960 + (80 + 16 * i + 4 * q) * 4);
      pk[i].x = pack2(bflo(x1p[i].x) + gb[0] * o[i][0], bfhi(x1p[i].x) + gb[1] * o[i][1]);
      pk[i].y = pack2(bflo(x1p[i].y) + gb[2] * o[i][2], bfhi(x1p[i].y) + gb[3] * o[i][3]);
    }
    bf16_t* orow = P.out + (size_t)token * D + 80 * j;
#pragma unroll
    for (int i = 0; i < 6; i += 2) {
      const int i2 = i + 1 < 5 ? i + 1 : i;
      const u32x4 ch = swap_pair(pk[i], pk[i2]);
      *(u32x4*)(orow + 16 * ((q & 1) ? i2 : i) + 8 * (q >> 1)) = ch;
    }
    STAMP(21);
  }
  STAMP_FLUSH();
}

extern "C" __global__ __launch_bounds__(NT, 2) void fused_block(Params P) {
  extern __shared__ __attribute__((aligned(16))) char smem_[];
  LAS char* smem = (LAS char*)smem_;
  const int tid = threadIdx.x, lane = tid & 63, wid = __builtin_amdgcn_readfirstlane(tid >> 6), b = blockIdx.x;
  const int xcd = b & 7, idx = b >> 3, p = xcd * 4 + (idx >> 3), j = idx & 7;
  if (wid >= 4) loader_main(P, (unsigned)(size_t)smem, smem, wid - 4, lane, b, p, j, xcd * 4);
  else consumer_main(P, smem, wid, lane, b, p, j);
}

extern "C" void kernel_launch(void* const* d_in, const int* in_sizes, int n_in, void* d_out, int out_size, void* d_ws, size_t ws_size, hipStream_t stream) {
  Params P;
  P.x = (const bf16_t*)d_in[0]; P.wsr = (const bf16_t*)d_in[1]; P.wsi = (const bf16_t*)d_in[2];
  P.wdr = (const bf16_t*)d_in[3]; P.wdi = (const bf16_t*)d_in[4]; P.win = (const bf16_t*)d_in[6];
  P.wsp = (const bf16_t*)d_in[7]; P.bsp = (const bf16_t*)d_in[8]; P.wout = (const bf16_t*)d_in[9];
  P.c = (const bf16_t*)d_in[10]; P.wmod = (const bf16_t*)d_in[11];
  P.out = (bf16_t*)d_out; P.ws = (char*)d_ws;
  (void)hipFuncSetAttribute((const void*)fused_block, hipFuncAttributeMaxDynamicSharedMemorySize, LDS_BYTES);
  fused_block<<<256, NT, LDS_BYTES, stream>>>(P);
}
```

```cpp
#include <hip/hip_runtime.h>
#include <stdint.h>
#include <type_traits>

typedef unsigned short bf16_t;
typedef short bf16x8 __attribute__((ext_vector_type(8)));
typedef short s16x4 __attribute__((ext_vector_type(4)));
typedef float f32x4 __attribute__((ext_vector_type(4)));
typedef float f32x2 __attribute__((ext_vector_type(2)));
typedef unsigned u32x4 __attribute__((ext_vector_type(4)));
typedef unsigned u32x2 __attribute__((ext_vector_type(2)));
typedef __bf16 bf2_t __attribute__((ext_vector_type(2)));
#define LAS __attribute__((address_space(3)))
#define GAS __attribute__((address_space(1)))
typedef GAS unsigned gu32;
typedef GAS unsigned long long gu64;
#define RLX_AGENT __ATOMIC_RELAXED, __HIP_MEMORY_SCOPE_AGENT
#define DEV __device__ __forceinline__

constexpr int S = 2048, D = 640, F = 1728, FH = 864;
constexpr int NT = 512;

constexpr unsigned WS_EPOCH = 0;
constexpr unsigned WS_TMO   = 256;
constexpr unsigned WS_FN = 4096, WS_F2 = 8192, WS_FU = 12288, WS_FX = 16384, WS_FP = 20480, WS_FZ = 24576;
constexpr unsigned WS_MOD  = 65536;
constexpr unsigned WS_MODB = WS_MOD + 16384;

constexpr unsigned WS_XN   = 131072;
constexpr unsigned WS_WIN  = WS_XN + 64 * 40 * 1024;
constexpr unsigned WS_WOUT = WS_WIN + 20 * 109 * 1024;
constexpr unsigned WS_UV   = WS_WOUT + 27 * 40 * 1024;
constexpr unsigned WS_X1P  = WS_UV + 32 * 2 * 20 * 4096;
constexpr unsigned WS_ST1  = WS_X1P + 32 * 20 * 4096;
constexpr unsigned WS_Z2N  = WS_ST1 + 2048 * 64;
constexpr unsigned WS_ST2  = WS_Z2N + 64 * 55 * 1024;
constexpr unsigned WS_P    = WS_ST2 + 2048 * 128;
constexpr unsigned WS_END  = WS_P + 32 * 28 * 4096;

struct Params {
  const bf16_t *x, *wsr, *wsi, *wdr, *wdi, *win, *wsp, *bsp, *wout, *c, *wmod;
  bf16_t* out;
  char* ws;
};

#define STAMP_DECL
#define STAMP(i) do {} while (0)
#define STAMP_FLUSH() do {} while (0)
#define CSTAMP(i) do {} while (0)

template <int N> DEV void wait_vm() { asm volatile("s_waitcnt vmcnt(%0)" ::"n"(N) : "memory"); }
DEV void wait_lgkm0() { asm volatile("s_waitcnt lgkmcnt(0)" ::: "memory"); }
DEV void bar() { asm volatile("" ::: "memory"); __builtin_amdgcn_s_barrier(); asm volatile("" ::: "memory"); }
DEV void sched_fence() { __builtin_amdgcn_sched_barrier(0); }
template <int I, int N, class Fn> DEV void static_for(Fn&& f) { if constexpr (I < N) { f(std::integral_constant<int, I>{}); static_for<I + 1, N>(f); } }

DEV float bflo(unsigned v) { return __uint_as_float(v << 16); }
DEV float bfhi(unsigned v) { return __uint_as_float(v & 0xffff0000u); }
DEV unsigned pack2(float lo, float hi) { bf2_t v; v[0] = (__bf16)lo; v[1] = (__bf16)hi; return __builtin_bit_cast(unsigned, v); }
DEV float rbf(float f) { return bflo(pack2(f, f)); }
DEV float dot2(unsigned a, unsigned b, float c) { return __builtin_amdgcn_fdot2_f32_bf16(__builtin_bit_cast(bf2_t, a), __builtin_bit_cast(bf2_t, b), c, false); }
DEV f32x4 mfma(bf16x8 a, bf16x8 b, f32x4 c) { return __builtin_amdgcn_mfma_f32_16x16x32_bf16(a, b, c, 0, 0, 0); }

DEV u32x4 mkrs(const void* p, unsigned bytes) {
  const unsigned long long a = (unsigned long long)p;
  u32x4 r; r.x = (unsigned)a; r.y = (unsigned)(a >> 32) & 0xffffu; r.z = bytes; r.w = 0x00020000u; return r;
}
DEV __amdgpu_buffer_rsrc_t mkrsrc(const void* p, unsigned bytes) { return __builtin_amdgcn_make_buffer_rsrc((void*)p, 0, bytes, 0x00020000); }
template <bool SC1> DEV void dma(u32x4 r, unsigned lds, unsigned voff, unsigned soff) {
  if constexpr (SC1) asm volatile("s_mov_b32 m0, %0\n\ts_nop 0\n\tbuffer_load_dwordx4 %1, %2, %3 offen sc1 lds" ::"s"(lds), "v"(voff), "s"(r), "s"(soff) : "memory");
  else asm volatile("s_mov_b32 m0, %0\n\ts_nop 0\n\tbuffer_load_dwordx4 %1, %2, %3 offen lds" ::"s"(lds), "v"(voff), "s"(r), "s"(soff) : "memory");
}
template <bool SC1> DEV void dma_half(u32x4 r, unsigned lds, unsigned voff, unsigned soff) {
  if constexpr (SC1) asm volatile("s_mov_b32 m0, %0\n\ts_mov_b32 exec_hi, 0\n\tbuffer_load_dwordx4 %1, %2, %3 offen sc1 lds\n\ts_mov_b32 exec_hi, -1" ::"s"(lds), "v"(voff), "s"(r), "s"(soff) : "memory");
  else asm volatile("s_mov_b32 m0, %0\n\ts_mov_b32 exec_hi, 0\n\tbuffer_load_dwordx4 %1, %2, %3 offen lds\n\ts_mov_b32 exec_hi, -1" ::"s"(lds), "v"(voff), "s"(r), "s"(soff) : "memory");
}
constexpr int SC1 = 16;
DEV void st16(__amdgpu_buffer_rsrc_t r, unsigned off, u32x4 v) { __builtin_amdgcn_raw_buffer_store_b128(v, r, off, 0, SC1); }
DEV u32x4 ld16(__amdgpu_buffer_rsrc_t r, unsigned off) { return __builtin_amdgcn_raw_buffer_load_b128(r, off, 0, SC1); }
DEV f32x4 ld16f(__amdgpu_buffer_rsrc_t r, unsigned off) { return __builtin_bit_cast(f32x4, ld16(r, off)); }

template <int CTRL> DEV float dpp_add(float v) { return v + __builtin_bit_cast(float, __builtin_amdgcn_update_dpp(0, __builtin_bit_cast(int, v), CTRL, 0xf, 0xf, true)); }
DEV float xsum_16(float v) { v = dpp_add<0xB1>(v); v = dpp_add<0x4E>(v); v = dpp_add<0x141>(v); return dpp_add<0x140>(v); }
DEV float xsum_r16(float v) { auto r = __builtin_amdgcn_permlane16_swap(__float_as_uint(v), __float_as_uint(v), false, false); return __uint_as_float(r[0]) + __uint_as_float(r[1]); }
DEV float xsum_r32(float v) { auto r = __builtin_amdgcn_permlane32_swap(__float_as_uint(v), __float_as_uint(v), false, false); return __uint_as_float(r[0]) + __uint_as_float(r[1]); }
DEV float xsum_q(float v) { return xsum_r32(xsum_r16(v)); }
DEV float gelu_tanh(float x) {
  const float k1 = -2.0f * 0.7978845608028654f * 1.4426950408889634f;
  const float t = x * __builtin_fmaf(x * x, 0.044715f * k1, k1);
  return x * __builtin_amdgcn_rcpf(1.0f + __builtin_amdgcn_exp2f(t));
}
DEV f32x2 gelu2(f32x2 x) {
  const float k1 = -2.0f * 0.7978845608028654f * 1.4426950408889634f;
  const f32x2 t = x * ((x * x) * (0.044715f * k1) + k1);
  const f32x2 d = (f32x2){__builtin_amdgcn_exp2f(t[0]), __builtin_amdgcn_exp2f(t[1])} + 1.0f;
  return x * (f32x2){__builtin_amdgcn_rcpf(d[0]), __builtin_amdgcn_rcpf(d[1])};
}
DEV u32x4 swap_pair(u32x2 a, u32x2 b) {
  auto r0 = __builtin_amdgcn_permlane16_swap(a.x, b.x, false, false);
  auto r1 = __builtin_amdgcn_permlane16_swap(a.y, b.y, false, false);
  return (u32x4){r0[0], r1[0], r0[1], r1[1]};
}

DEV bool spin_fail(unsigned& spins, char* ws) {
  __builtin_amdgcn_s_sleep(1);
  if (++spins > (1u << 17)) { __hip_atomic_store((gu32*)(ws + WS_TMO), 1u, RLX_AGENT); return true; }
  return false;
}
DEV void set_flag(char* ws, unsigned off, unsigned tag) { __hip_atomic_store((gu32*)(ws + off), tag, RLX_AGENT); }
DEV void wait_flags8(char* ws, unsigned off, unsigned tag, int lane) {
  for (unsigned spins = 0;;) {
    const unsigned v = lane < 8 ? __hip_atomic_load((gu32*)(ws + off) + lane, RLX_AGENT) : tag;
    if (__all(v == tag)) break;
    if (spin_fail(spins, ws)) break;
  }
}
DEV bool flags_ok(u32x4 v, unsigned tag) { return __all(v.x == tag && v.y == tag && v.z == tag && v.w == tag); }
DEV u32x4 peek256(__amdgpu_buffer_rsrc_t rf, unsigned off, int lane) { return ld16(rf, off + (lane >> 1) * 128 + (lane & 1) * 16); }
DEV void wait256(__amdgpu_buffer_rsrc_t rf, char* ws, unsigned off, unsigned tag, int lane) {
  for (unsigned spins = 0;;) { if (flags_ok(peek256(rf, off, lane), tag)) break; if (spin_fail(spins, ws)) break; }
}

DEV void wait_panels(__amdgpu_buffer_rsrc_t rf, char* ws, unsigned off, int first, int count, unsigned tag, int lane) {
  const unsigned o = lane < 2 * count ? off + (((lane >> 1) + first) & 31) * 128 + (lane & 1) * 16 : off + (first & 31) * 128;
  for (unsigned spins = 0;;) { if (flags_ok(ld16(rf, o), tag)) break; if (spin_fail(spins, ws)) break; }
}

template <int NS, int S_, int CW, int CX, class IW, class IX>
DEV void loader_run(IW&& issueW, IX&& issueX) {
  static_for<0, NS>([&](auto ic) { issueX(decltype(ic)::value, decltype(ic)::value); });
  wait_vm<(NS - 1) * CX>(); bar();
  static_for<0, NS - 2>([&](auto ic) { constexpr int n = decltype(ic)::value;
    wait_vm<(NS - 2) * CX + n * CW>(); bar(); issueW(n + NS, n); issueX(n + NS, n); });
  int slot = NS - 2;
#pragma unroll 1
  for (int n = NS - 2; n < S_ - NS; ++n) {
    wait_vm<(NS - 2) * (CW + CX)>(); bar();
    issueW(n + NS, slot); issueX(n + NS, slot);
    slot = slot + 1 == NS ? 0 : slot + 1;
  }
  static_for<0, NS - 1>([&](auto ic) { constexpr int r = NS - 2 - decltype(ic)::value; wait_vm<r*(CW + CX)>(); bar(); });
}

template <int NS, int S_, int NA, int NB, int NM, class Rd, class Mm, class Hook, class Post>
DEV void ksplit_steps(bf16x8 (&A0)[NA], bf16x8 (&B0)[NB], bf16x8 (&A1)[NA], bf16x8 (&B1)[NB], int& slot, int n0, int n1, Rd&& rd, Mm&& mm, Hook&& hook, Post&& post) {
  constexpr int NR = NA + NB;
#define KS_ILV() do { _Pragma("unroll") for (int z = 0; z < NR; ++z) { __builtin_amdgcn_sched_group_barrier(0x8, 1, 0); __builtin_amdgcn_sched_group_barrier(0x100, 1, 0); } __builtin_amdgcn_sched_group_barrier(0x8, NM - NR, 0); } while (0)
#pragma unroll 1
  for (int n = n0; n < n1; n += 2) {
    const int s1 = slot + 1 == NS ? 0 : slot + 1;
    hook(n); wait_lgkm0(); bar(); post(n); sched_fence();
    rd(A1, B1, s1, n + 1); mm(A0, B0, false); KS_ILV(); sched_fence();
    const int s2 = s1 + 1 == NS ? 0 : s1 + 1;
    hook(n + 1); wait_lgkm0(); bar(); post(n + 1); sched_fence();
    rd(A0, B0, s2, n + 2); mm(A1, B1, false); KS_ILV(); sched_fence();
    slot = s2;
  }
}
template <int NS, int S_, int NA, int NB, class Rd, class Mm>
DEV void ksplit_last(bf16x8 (&A0)[NA], bf16x8 (&B0)[NB], bf16x8 (&A1)[NA], bf16x8 (&B1)[NB], int& slot, Rd&& rd, Mm&& mm) {
  const int s1 = slot + 1 == NS ? 0 : slot + 1;
  wait_lgkm0(); bar(); sched_fence();
  rd(A1, B1, s1, S_ - 1); mm(A0, B0, false); mm(A1, B1, true);
  slot = s1;
}
template <int NS, int S_, int NA, int NB, class Rd, class Mm>
DEV void ksplit_loop(Rd&& rd, Mm&& mm) {
  static_assert((S_ & 1) == 0, "even step count");
  bf16x8 A0[NA], B0[NB], A1[NA], B1[NB];
  bar();
  rd(A0, B0, 0, 0);
  int slot = 0;
  ksplit_steps<NS, S_, NA, NB, NA * NB>(A0, B0, A1, B1, slot, 0, S_ - 2, rd, mm, [](int) {}, [](int) {});
  ksplit_last<NS, S_, NA, NB>(A0, B0, A1, B1, slot, rd, mm);
}
template <int N>
DEV void kreduce(f32x4 (&acc)[N][2], f32x4 (&res)[N], LAS char* scratch, int w, int lane) {
  const int kh = w & 1;
  LAS char* mine = scratch + w * (N * 1024) + lane * 16;
  LAS char* other = scratch + (w ^ 1) * (N * 1024) + lane * 16;
  if (kh) {
#pragma unroll
    for (int i = 0; i < N; ++i) { *(LAS f32x4*)(mine + i * 1024) = acc[i][0]; res[i] = acc[i][1]; }
  } else {
#pragma unroll
    for (int i = 0; i < N; ++i) { *(LAS f32x4*)(mine + i * 1024) = acc[i][1]; res[i] = acc[i][0]; }
  }
  wait_lgkm0(); bar();
#pragma unroll
  for (int i = 0; i < N; ++i) res[i] += *(LAS f32x4*)(other + i * 1024);
}

constexpr int A_NS = 5, A_SLOT = 26 * 1024;
constexpr int P0_T = 130 * 1024, P0_PT = 140 * 1024, P0_RED = 156 * 1024;
constexpr int B_NS = 6, B_SLOT = 18 * 1024, B_SCR = 36 * 1024;
constexpr int C_NS = 5, C_X = 0, C_W = 82 * 1024, C_WSLOT = 14 * 1024;
constexpr int C_SCR = C_W + 2 * C_WSLOT;
constexpr int D_NS = 5, D_SLOT = 22 * 1024, D_SCR = 110 * 1024;
constexpr int E_NS = 6, E_SLOT = 18 * 1024, E_SCR = 110 * 1024;
constexpr int T_MODB = 152 * 1024;
constexpr int T_CV = T_MODB + 3072 + 64;
constexpr int T_MOD = 158 * 1024;
constexpr int LDS_BYTES = 160 * 1024;
static_assert(T_CV + 896 <= T_MOD && C_W + C_NS * C_WSLOT <= T_MODB && D_SCR + 28 * 1024 <= T_MODB && E_SCR + 20 * 1024 <= T_MODB, "LDS map");

DEV void loader_main(const Params& P, const unsigned lds0, LAS char* smem, const int lw, const int lane_, const int b, const int p, const int j, const int krot) {
  STAMP_DECL STAMP(0);
  char* const ws = P.ws;
  const int ft0 = 27 * (j >> 2) + 7 * (j & 3);
#define LLANE int lane = lane_; asm volatile("" : "+v"(lane)); const unsigned vx = lane * 16; (void)vx;
#define LV4K const unsigned v4k0 = (unsigned)(lane >> 3) * 4096u + (unsigned)(((lane & 7) ^ (lane >> 4)) * 16), v4k1 = (unsigned)(8 + (lane >> 3)) * 4096u + (unsigned)(((lane & 7) ^ (4 + (lane >> 4))) * 16);
  {
    LLANE LV4K
    const u32x4 rre = mkrs(P.wsr, S * S * 2), rim = mkrs(P.wsi, S * S * 2), rx = mkrs(ws + WS_XN, S * D * 2);
    const unsigned wrow = (unsigned)(64 * p + 16 * lw) * 4096u;
    auto issueW = [&](int n, int slot) {
      const unsigned sb = lds0 + slot * A_SLOT + lw * 4096, sw = wrow + ((n + krot) & 31) * 128;
      dma<false>(rre, sb, v4k0, sw); dma<false>(rre, sb + 1024, v4k1, sw); dma<false>(rim, sb + 2048, v4k0, sw); dma<false>(rim, sb + 3072, v4k1, sw);
    };
    const int xi0 = lw * 2, xi1 = lw * 2 + 1, xi2 = 8 + (lw >> 1);
    const unsigned xo0 = (unsigned)(((xi0 >= 5) * 40 + 5 * j + xi0 - 5 * (xi0 >= 5)) * 1024), xo1 = (unsigned)(((xi1 >= 5) * 40 + 5 * j + xi1 - 5 * (xi1 >= 5)) * 1024);
    const unsigned xo2 = (unsigned)((40 + 5 * j + xi2 - 5) * 1024 + (lw & 1) * 512);
    auto issueX = [&](int n, int slot) {
      const unsigned sb = lds0 + slot * A_SLOT + 16384, so = ((n + krot) & 31) * 80 * 1024;
      dma<true>(rx, sb + xi0 * 1024, vx, so + xo0); dma<true>(rx, sb + xi1 * 1024, vx, so + xo1); dma_half<true>(rx, sb + xi2 * 1024 + (lw & 1) * 512, vx, so + xo2);
    };
    issueW(0, 0); issueW(1, 1);
    bar();
    bar();
    STAMP(30);
    bar();
    STAMP(33);
    __builtin_amdgcn_s_setprio(3);
    issueX(0, 0); issueX(1, 1);
    static_for<2, A_NS>([&](auto ic) { issueW(decltype(ic)::value, decltype(ic)::value); issueX(decltype(ic)::value, decltype(ic)::value); });
    const int idx = b >> 3, unit0 = (idx & 3) + 4 * ((b & 7) + 8 * (idx >> 2));
    const bool domod = unit0 < 240; const int unit = domod ? unit0 : 0;
    u32x4 wm[5]; unsigned cs[5];
#pragma unroll
    for (int i = 0; i < 5; ++i) { const int row = lw * 160 + i * 32 + (lane >> 1);
      const bf16_t* pw = P.wmod + (size_t)row * (6 * D) + unit * 16 + (lane & 1) * 8; const bf16_t* pc = P.c + row;
      asm volatile("global_load_dwordx4 %0, %1, off" : "=v"(wm[i]) : "v"(pw) : "memory");
      asm volatile("global_load_ushort %0, %1, off" : "=v"(cs[i]) : "v"(pc) : "memory"); }
    STAMP(34);
    wait_vm<3 + 3 * 7 + 10>();
    bar();
    wait_vm<3 * 7 + 10>(); bar(); issueW(A_NS, 0); issueX(A_NS, 0);
    wait_vm<2 * 7 + 10 + 7>(); bar(); issueW(A_NS + 1, 1); issueX(A_NS + 1, 1);
    wait_vm<14>();
    asm volatile("" : "+v"(wm[0]), "+v"(wm[1]), "+v"(wm[2]), "+v"(wm[3]), "+v"(wm[4]), "+v"(cs[0]), "+v"(cs[1]), "+v"(cs[2]), "+v"(cs[3]), "+v"(cs[4]));
    bar(); issueW(A_NS + 2, 2); issueX(A_NS + 2, 2);
    {
      float a[8] = {0, 0, 0, 0, 0, 0, 0, 0};
#pragma unroll
      for (int i = 0; i < 5; ++i) { const float x = bflo(cs[i]); const float s = rbf(x * __builtin_amdgcn_rcpf(1.0f + __expf(-x)));
        a[0] += s * bflo(wm[i].x); a[1] += s * bfhi(wm[i].x); a[2] += s * bflo(wm[i].y); a[3] += s * bfhi(wm[i].y);
        a[4] += s * bflo(wm[i].z); a[5] += s * bfhi(wm[i].z); a[6] += s * bflo(wm[i].w); a[7] += s * bfhi(wm[i].w); }
#pragma unroll
      for (int e = 0; e < 8; ++e) { float v = dpp_add<0x4E>(a[e]);
        v = dpp_add<0x124>(v); v = dpp_add<0x128>(v);
        a[e] = xsum_q(v); }
      if (lane < 2) {
#pragma unroll
        for (int e = 0; e < 8; ++e) ((LAS float*)(smem + P0_RED))[lw * 16 + lane * 8 + e] = a[e];
      }
      wait_lgkm0();
    }
    wait_vm<(A_NS - 2) * 7>(); bar(); issueW(A_NS + 3, 3); issueX(A_NS + 3, 3);
    if (lw == 0 && domod && lane < 16) {
      const LAS float* red = (const LAS float*)(smem + P0_RED);
      float s = rbf((red[lane] + red[16 + lane]) + (red[32 + lane] + red[48 + lane]));
      const int n = unit * 16 + lane, seg = n / D;
      if (seg == 1 || seg == 4) s = rbf(1.0f + s);
      __hip_atomic_store((gu32*)(ws + WS_MOD) + n, __float_as_uint(s), RLX_AGENT);
      const unsigned hi = __float_as_uint(s) >> 16, nb = (unsigned)__builtin_amdgcn_update_dpp(0, (int)hi, 0xB1, 0xf, 0xf, true);
      if ((seg == 3 || seg == 4) && !(lane & 1)) __hip_atomic_store((gu32*)(ws + WS_MODB) + (((seg == 4 ? 0 : D) + n - seg * D) >> 1), hi | (nb << 16), RLX_AGENT);
    }
    static_assert(A_NS == 5, "the start-up schedule above is written for five slots");
    const u32x4 rmod = mkrs(ws + WS_MOD, 3840 * 4);
    const int tl = lane / 20, tc = lane - 20 * tl;
    const unsigned tv0 = lane < 60 ? (unsigned)((tl * D + 80 * j) * 4 + tc * 16) : 0x7ffffff0u;
    const unsigned tv1 = lane < 40 ? (unsigned)(((4 + tl) * D + 80 * j) * 4 + tc * 16) : 0x7ffffff0u;
    int slot = 4;
#pragma unroll 1
    for (int n = 4; n < 32 - A_NS; ++n) {
      wait_vm<(A_NS - 2) * 7>(); bar();
      issueW(n + A_NS, slot); issueX(n + A_NS, slot);
      if (n == 20 && lw == 0) { dma<true>(rmod, lds0 + T_MOD, tv0, 0); dma<true>(rmod, lds0 + T_MOD + 960, tv1, 0); }
      slot = slot + 1 == A_NS ? 0 : slot + 1;
    }
    static_for<0, A_NS - 1>([&](auto ic) { constexpr int r = A_NS - 2 - decltype(ic)::value; wait_vm<r * 7>(); bar(); });
    bar();
  }
  {
    LLANE
    const u32x4 rre = mkrs(P.wdr, D * D * 2), rim = mkrs(P.wdi, D * D * 2), rx = mkrs(ws + WS_UV, 32 * 2 * 20 * 4096);
    const unsigned v0 = (unsigned)(lane >> 3) * 1280u + (unsigned)(((lane & 7) ^ (lane >> 4)) * 16);
    const unsigned v1 = (unsigned)(lane >> 3) * 1280u + (unsigned)(((lane & 7) ^ (4 + (lane >> 4))) * 16);
    const unsigned vh = (unsigned)(lane >> 3) * 1280u + (unsigned)(((lane & 7) ^ ((4 * (lw >> 1) + 2 * (lw & 1) + (lane >> 4)) & 7)) * 16);
    const unsigned wb = (unsigned)(80 * j) * 1280u;
    auto issueW = [&](int n, int slot) {
      const unsigned sb = lds0 + slot * B_SLOT; const int half = n >= 10; const unsigned sw = wb + (n - 10 * half) * 128;
      const u32x4 rw = half ? rim : rre;
      dma<false>(rw, sb + (2 * lw) * 1024, v0, sw + (unsigned)(16 * lw) * 1280u);
      dma<false>(rw, sb + (2 * lw + 1) * 1024, v1, sw + (unsigned)(16 * lw + 8) * 1280u);
      dma_half<false>(rw, sb + (8 + (lw >> 1)) * 1024 + (lw & 1) * 512, vh, sw + (unsigned)(64 + 8 * (lw >> 1) + 4 * (lw & 1)) * 1280u);
    };
    auto issueX = [&](int n, int slot) {
      const unsigned sb = lds0 + slot * B_SLOT + 10240 + lw * 2048, sx = (unsigned)(p * 40 + 2 * n) * 4096u + lw * 2048;
      dma<true>(rx, sb, vx, sx); dma<true>(rx, sb + 1024, vx, sx + 1024);
    };
    __builtin_amdgcn_s_setprio(0);
    bar();
    static_for<0, B_NS>([&](auto ic) { issueW(decltype(ic)::value, decltype(ic)::value); });
    bar();
    __builtin_amdgcn_s_setprio(3);
    loader_run<B_NS, 20, 3, 2>(issueW, issueX);
    bar();
  }
  {
    LLANE
    const u32x4 rw = mkrs(ws + WS_WIN, 20 * 109 * 1024), rx = mkrs(ws + WS_X1P, 32 * 20 * 4096), rmb = mkrs(ws + WS_MODB, 2560);
    unsigned wo[4], wl[4];
#pragma unroll
    for (int u = 0; u < 3; ++u) { const int i = lw * 3 + u, kk = i >= 7; wo[u] = (unsigned)((kk * 109 + i - 7 * kk) * 1024); wl[u] = i * 1024; }
    { const int i = 12 + (lw >> 1); wo[3] = (unsigned)((109 + i - 7) * 1024 + (lw & 1) * 512); wl[3] = i * 1024 + (lw & 1) * 512; }
    auto issueW = [&](int n, int slot) {
      const unsigned sb = lds0 + C_W + slot * C_WSLOT; const int z1 = n >= 10;
      const unsigned so = (unsigned)((2 * (n - 10 * z1) * 109 + (z1 ? 0 : 54) + ft0) * 1024);
      dma<true>(rw, sb + wl[0], vx, so + wo[0]); dma<true>(rw, sb + wl[1], vx, so + wo[1]); dma<true>(rw, sb + wl[2], vx, so + wo[2]); dma_half<true>(rw, sb + wl[3], vx, so + wo[3]);
    };
    auto issueX = [&](int n) {
      const unsigned o = (unsigned)(2 * n) * 4096u + lw * 2048, sx = (unsigned)p * 20 * 4096u + o;
      dma<true>(rx, lds0 + C_X + o, vx, sx); dma<true>(rx, lds0 + C_X + o + 1024, vx, sx + 1024);
    };
    __builtin_amdgcn_s_setprio(0);
    bar();
    if (lw < 3) dma<true>(rmb, lds0 + T_MODB + lw * 1024, vx + lw * 1024, 0);
    static_for<0, C_NS>([&](auto ic) { issueW(decltype(ic)::value, decltype(ic)::value); });
    bar();
    __builtin_amdgcn_s_setprio(3);
    static_for<0, C_NS>([&](auto ic) { issueX(decltype(ic)::value); });
    wait_vm<(C_NS - 1) * 2>(); bar();
    static_for<0, 9>([&](auto ic) {
      constexpr int n = decltype(ic)::value, need = n + 1;
      constexpr int last = n + C_NS - 1 < 11 ? n + C_NS - 1 : 11;
      constexpr int cnt = [] { int c = 0;
        if (need < C_NS) { c += (C_NS - 1 - need) * 2; for (int s = C_NS; s <= last; ++s) c += (s < 10 ? 6 : 4); }
        else { for (int s = need + 1; s <= last; ++s) c += (s < 10 ? 6 : 4); }
        return c; }();
      wait_vm<cnt>(); bar();
      if constexpr (n + C_NS < 12) { issueW(n + C_NS, n % C_NS); if constexpr (n + C_NS < 10) issueX(n + C_NS); }
    });
    bar();
    bar();
    issueW(12, 2); issueW(13, 3); issueW(14, 4);
    wait_vm<16>(); bar();
    static_for<10, 19>([&](auto ic) {
      constexpr int n = decltype(ic)::value;
      constexpr int last = n + 4 < 19 ? n + 4 : 19;
      wait_vm<(last - (n + 1)) * 4>(); bar();
      if constexpr (n + C_NS < 20) issueW(n + C_NS, n % C_NS);
    });
    bar();
  }
  {
    LLANE LV4K
    const u32x4 rw = mkrs(P.wsp, S * S * 2), rx = mkrs(ws + WS_Z2N, 64 * 55 * 1024);
    const unsigned wrow = (unsigned)(64 * p + 16 * lw) * 4096u;
    unsigned xo[4], xl[4];
#pragma unroll
    for (int u = 0; u < 3; ++u) { const int i = lw * 3 + u, kk = i >= 7; xo[u] = (unsigned)((kk * 55 + ft0 + i - 7 * kk) * 1024); xl[u] = 8192 + i * 1024; }
    { const int i = 12 + (lw >> 1); xo[3] = (unsigned)((55 + ft0 + i - 7) * 1024 + (lw & 1) * 512); xl[3] = 8192 + i * 1024 + (lw & 1) * 512; }
    auto issueW = [&](int n, int slot) {
      const unsigned sb = lds0 + slot * D_SLOT + lw * 2048, sw = wrow + ((n + krot) & 31) * 128;
      dma<false>(rw, sb, v4k0, sw); dma<false>(rw, sb + 1024, v4k1, sw);
    };
    auto issueX = [&](int n, int slot) {
      const unsigned sb = lds0 + slot * D_SLOT, so = ((n + krot) & 31) * 110 * 1024;
      dma<true>(rx, sb + xl[0], vx, so + xo[0]); dma<true>(rx, sb + xl[1], vx, so + xo[1]); dma<true>(rx, sb + xl[2], vx, so + xo[2]); dma_half<true>(rx, sb + xl[3], vx, so + xo[3]);
    };
    static_for<0, D_NS>([&](auto ic) { issueW(decltype(ic)::value, decltype(ic)::value); });
    loader_run<D_NS, 32, 2, 4>(issueW, issueX);
    bar();
  }
  {
    LLANE
    const u32x4 rw = mkrs(ws + WS_WOUT, 27 * 40 * 1024), rx = mkrs(ws + WS_P, 32 * 28 * 4096);
    const int i0 = lw * 2, i1 = lw * 2 + 1, i2 = 8 + (lw >> 1);
    const unsigned o0 = (unsigned)(((i0 >= 5) * 40 + 5 * j + i0 - 5 * (i0 >= 5)) * 1024), o1 = (unsigned)(((i1 >= 5) * 40 + 5 * j + i1 - 5 * (i1 >= 5)) * 1024);
    const unsigned o2 = (unsigned)((40 + 5 * j + i2 - 5) * 1024 + (lw & 1) * 512);
    auto issueW = [&](int n, int slot) {
      const unsigned sb = lds0 + slot * E_SLOT, so = (unsigned)n * 80 * 1024;
      dma<true>(rw, sb + i0 * 1024, vx, so + o0); dma<true>(rw, sb + i1 * 1024, vx, so + o1); dma_half<true>(rw, sb + i2 * 1024 + (lw & 1) * 512, vx, so + o2);
    };
    auto issueX = [&](int n, int slot) {
      const unsigned sb = lds0 + slot * E_SLOT + 10240 + lw * 2048, sx = (unsigned)(p * 28 + 2 * n) * 4096u + lw * 2048;
      dma<true>(rx, sb, vx, sx); dma<true>(rx, sb + 1024, vx, sx + 1024);
    };
    __builtin_amdgcn_s_setprio(0);
    bar();
    static_for<0, E_NS>([&](auto ic) { issueW(decltype(ic)::value, decltype(ic)::value); });
    bar();
    __builtin_amdgcn_s_setprio(3);
    loader_run<E_NS, 14, 3, 2>(issueW, issueX);
    bar();
  }
  STAMP_FLUSH();
}

DEV void consumer_main(const Params& P, LAS char* smem, const int w, const int lane_, const int b, const int p, const int j) {
  STAMP_DECL STAMP(0);
  char* const ws = P.ws;
  unsigned tagv = __hip_atomic_load((gu32*)(ws + WS_EPOCH), RLX_AGENT) + 1u, tag;
  const int mh = w >> 1, kh = w & 1;
  const auto rws = mkrsrc(ws, WS_END);
  const int ft0 = 27 * (j >> 2) + 7 * (j & 3), nft = (j & 3) == 3 ? 6 : 7;
  auto rot = [&](int i) { return i < 3 ? i + 4 * mh : i - 3 * mh; };
#define LANE_IDS int lane = lane_; asm volatile("" : "+v"(lane)); const int q = lane >> 4, tk = lane & 15, tid = w * 64 + lane, token = 64 * p + 16 * w + tk; (void)q; (void)tk; (void)tid; (void)token;
  {
    LANE_IDS
    unsigned v[2][5];
#pragma unroll
    for (int t = 0; t < 2; ++t) { const unsigned* xr = (const unsigned*)(P.x + (size_t)(64 * p + 8 * j + 2 * w + t) * D);
#pragma unroll
      for (int i = 0; i < 5; ++i) v[t][i] = xr[lane + 64 * i]; }
    const int u0 = w * 256 + b; const bool dopt = u0 < 810; const int u = dopt ? u0 : 0; const bool isin = u < 540;
    const int uu = isin ? u : u - 540, ncb = isin ? 27 : 10, pkb = uu / ncb, pcb = uu - pkb * ncb, ld = isin ? F : D;
    u32x4 pv[4];
    { const bf16_t* src = (isin ? P.win : P.wout) + (size_t)(pkb * 32) * ld + pcb * 64;
#pragma unroll
      for (int i = 0; i < 4; ++i) pv[i] = *(const u32x4*)(src + (size_t)(i * 8 + (lane >> 3)) * ld + (lane & 7) * 8); }
    LAS unsigned* t32 = (LAS unsigned*)(smem + P0_T);
    unsigned pk[2][5];
#pragma unroll
    for (int t = 0; t < 2; ++t) {
      float s = 0.f;
#pragma unroll
      for (int i = 0; i < 5; ++i) s += bflo(v[t][i]) + bfhi(v[t][i]);
      const float mean = xsum_q(xsum_16(s)) * (1.0f / D);
      float q2 = 0.f;
#pragma unroll
      for (int i = 0; i < 5; ++i) { const float a = bflo(v[t][i]) - mean, c = bfhi(v[t][i]) - mean; q2 += a * a + c * c; }
      const float rs = 1.0f / sqrtf(xsum_q(xsum_16(q2)) * (1.0f / D) + 1e-5f);
#pragma unroll
      for (int i = 0; i < 5; ++i) pk[t][i] = pack2((bflo(v[t][i]) - mean) * rs, (bfhi(v[t][i]) - mean) * rs);
    }
#pragma unroll
    for (int i = 0; i < 5; ++i) { const int d = 2 * (lane + 64 * i);
      t32[d * 4 + w] = (pk[0][i] & 0xffffu) | (pk[1][i] << 16); t32[(d + 1) * 4 + w] = (pk[0][i] >> 16) | (pk[1][i] & 0xffff0000u); }
    asm volatile("" : "+v"(tagv)); tag = __builtin_amdgcn_readfirstlane(tagv);
    wait_lgkm0(); bar();
    const unsigned xb = WS_XN + (unsigned)((2 * p + (j >> 2)) * 40 * 1024 + (j & 3) * 256);
#pragma unroll
    for (int i = 0; i < 3; ++i) { const int d = tid + 256 * i;
      if (d < D) st16(rws, xb + (d >> 4) * 1024 + (d & 15) * 16, *(LAS u32x4*)(smem + P0_T + d * 16)); }
    wait_vm<0>();
    bar();
    STAMP(1);
    if (w == 0 && lane == 0) set_flag(ws, WS_FN + p * 128 + j * 4, tag);
    {
      LAS char* t = smem + P0_PT + w * 4096;
#pragma unroll
      for (int i = 0; i < 4; ++i) *(LAS u32x4*)(t + (i * 8 + (lane >> 3)) * 128 + (lane & 7) * 16) = pv[i];
      const unsigned dst = dopt ? (isin ? WS_WIN + (unsigned)(pkb * 109 + pcb * 4) * 1024 : WS_WOUT + (unsigned)(pkb * 40 + pcb * 4) * 1024) + lane * 16 : 0xfffff000u;
      const int rq = tk >> 2, pp = lane & 3;
#pragma unroll
      for (int c = 0; c < 4; ++c) {
        const u32x2 l2 = __builtin_bit_cast(u32x2, __builtin_amdgcn_ds_read_tr16_b64_v4i16((LAS s16x4*)(t + (8 * q + rq) * 128 + c * 32 + pp * 8)));
        const u32x2 h2 = __builtin_bit_cast(u32x2, __builtin_amdgcn_ds_read_tr16_b64_v4i16((LAS s16x4*)(t + (8 * q + 4 + rq) * 128 + c * 32 + pp * 8)));
        st16(rws, dst + (dopt ? c * 1024 : 0), (u32x4){l2.x, l2.y, h2.x, h2.y});
      }
    }
    if (w == 0) { wait256(rws, ws, WS_FN, tag, lane); STAMP(23); }
    bar();
    STAMP(2);
  }
  u32x2 xv[5];
  {
    LANE_IDS
    f32x4 acc[2][5];
#pragma unroll
    for (int t = 0; t < 2; ++t)
#pragma unroll
      for (int i = 0; i < 5; ++i) acc[t][i] = (f32x4){0, 0, 0, 0};
    f32x4 rsum[2] = {(f32x4){0, 0, 0, 0}, (f32x4){0, 0, 0, 0}};
    const bf16x8 ones = __builtin_bit_cast(bf16x8, (u32x4){0x3f803f80u, 0x3f803f80u, 0x3f803f80u, 0x3f803f80u});
    const unsigned wa0 = (unsigned)(w * 4096 + tk * 128 + ((q ^ ((lane >> 1) & 7)) * 16));
    const unsigned xa = 16384 + lane * 16;
    bf16x8 Wa[2], Xa[5], Wb[2], Xb[5];
    auto rdm = [&](bf16x8(&Wn)[2], bf16x8(&Xn)[5], int slot, int kk, bf16x8(&Wf)[2], bf16x8(&Xf)[5]) {
      LAS char* sb = smem + slot * A_SLOT; const unsigned wa = wa0 ^ (kk * 64);
      static_for<0, 12>([&](auto ic) { constexpr int z = decltype(ic)::value, i = z >> 1, t = z & 1;
        if constexpr (z < 10) acc[t][i] = mfma(Xf[i], Wf[t], acc[t][i]); else rsum[t] = mfma(ones, Wf[t], rsum[t]);
        if constexpr (z == 0) Wn[0] = *(LAS bf16x8*)(sb + wa);
        if constexpr (z == 1) Xn[0] = *(LAS bf16x8*)(sb + xa + (kk * 5) * 1024);
        if constexpr (z == 2) Wn[1] = *(LAS bf16x8*)(sb + wa + 2048);
        if constexpr (z >= 3 && z < 7) Xn[z - 2] = *(LAS bf16x8*)(sb + xa + (kk * 5 + z - 2) * 1024);
        sched_fence(); });
    };
    auto mmlast = [&](bf16x8(&Wf)[2], bf16x8(&Xf)[5]) {
#pragma unroll
      for (int i = 0; i < 5; ++i) { acc[0][i] = mfma(Xf[i], Wf[0], acc[0][i]); acc[1][i] = mfma(Xf[i], Wf[1], acc[1][i]); }
      rsum[0] = mfma(ones, Wf[0], rsum[0]); rsum[1] = mfma(ones, Wf[1], rsum[1]);
    };
    int slot = 0;
    auto steps = [&](int n0, int n1, bool hooks) {
#pragma unroll 1
      for (int n = n0; n < n1; ++n) {
        rdm(Wb, Xb, slot, 1, Wa, Xa);
        const int ns = slot + 1 == A_NS ? 0 : slot + 1;
        if (hooks && n == 8) wait_vm<0>();
        wait_lgkm0(); bar(); sched_fence();
        if (hooks && n == 8 && w == 0 && lane == 0) set_flag(ws, WS_F2 + p * 128 + j * 4, tag);
        rdm(Wa, Xa, ns, 0, Wb, Xb);
        slot = ns;
      }
    };
    bar();
    STAMP(3); CSTAMP(40);
    { LAS char* sb = smem; Wa[0] = *(LAS bf16x8*)(sb + wa0); Wa[1] = *(LAS bf16x8*)(sb + wa0 + 2048);
#pragma unroll
      for (int i = 0; i < 5; ++i) Xa[i] = *(LAS bf16x8*)(sb + xa + i * 1024); }
    steps(0, 12, true);
    const u32x4 f2 = peek256(rws, WS_F2, lane);
    steps(12, 18, false);
    if (!flags_ok(f2, tag)) wait256(rws, ws, WS_F2, tag, lane);
    if (b == 0 && w == 0 && lane == 0) set_flag(ws, WS_EPOCH, tag + 15);
#pragma unroll
    for (int i = 0; i < 5; ++i) xv[i] = *(const u32x2*)(P.x + (size_t)token * D + 80 * j + 16 * i + 4 * q);
    steps(18, 31, false);
    { LAS char* sb = smem + slot * A_SLOT; const unsigned wa = wa0 ^ 64;
      Wb[0] = *(LAS bf16x8*)(sb + wa); Wb[1] = *(LAS bf16x8*)(sb + wa + 2048);
#pragma unroll
      for (int i = 0; i < 5; ++i) Xb[i] = *(LAS bf16x8*)(sb + xa + (5 + i) * 1024); }
    mmlast(Wa, Xa);
    wait_lgkm0(); bar();
    mmlast(Wb, Xb);
    STAMP(4); CSTAMP(41);
    const float rs0 = rsum[0][0], rs1 = rsum[1][0];
#pragma unroll
    for (int i = 0; i < 5; ++i) {
      u32x2 a, c;
      const f32x4 sa = *(const LAS f32x4*)(smem + T_MOD + (16 * i + 4 * q) * 4), c1 = *(const LAS f32x4*)(smem + T_MOD + (80 + 16 * i + 4 * q) * 4);
      { const f32x4 u = acc[0][i] * c1 + rs0 * sa; a.x = pack2(u[0], u[1]); a.y = pack2(u[2], u[3]); }
      { const f32x4 v = -(acc[1][i] * c1 + rs1 * sa); c.x = pack2(v[0], v[1]); c.y = pack2(v[2], v[3]); }
      const int dt = 5 * j + i, kb = dt >> 1, qq = 2 * (dt & 1) + (q >> 1);
      st16(rws, WS_UV + (unsigned)(((p * 2 + (q & 1)) * 20 + kb) * 4 + w) * 1024 + (qq * 16 + tk) * 16, swap_pair(a, c));
    }
    wait_vm<0>(); bar();
    STAMP(5);
    if (w == 0) { if (lane == 0) set_flag(ws, WS_FU + p * 128 + j * 4, tag); wait_flags8(ws, WS_FU + p * 128, tag, lane); }
    bar();
    STAMP(6); CSTAMP(44);
  }
  u32x2 x1p[5];
  {
    LANE_IDS
    f32x4 acc[5][2];
#pragma unroll
    for (int i = 0; i < 5; ++i) { acc[i][0] = (f32x4){0, 0, 0, 0}; acc[i][1] = (f32x4){0, 0, 0, 0}; }
    const unsigned wa = (unsigned)(tk * 128 + (((q + 4 * kh) ^ ((lane >> 1) & 7)) * 16));
    const unsigned xa = 10240 + (kh * 4 + 2 * mh) * 1024 + lane * 16;
    ksplit_loop<B_NS, 20, 5, 2>(
        [&](bf16x8(&A)[5], bf16x8(&Bf)[2], int slot, int) { LAS char* sb = smem + slot * B_SLOT;
          A[0] = *(LAS bf16x8*)(sb + wa); Bf[0] = *(LAS bf16x8*)(sb + xa); Bf[1] = *(LAS bf16x8*)(sb + xa + 1024);
#pragma unroll
          for (int i = 1; i < 5; ++i) A[i] = *(LAS bf16x8*)(sb + wa + i * 2048); },
        [&](bf16x8(&A)[5], bf16x8(&Bf)[2], bool) {
#pragma unroll
          for (int i = 0; i < 5; ++i) { acc[i][0] = mfma(A[i], Bf[0], acc[i][0]); acc[i][1] = mfma(A[i], Bf[1], acc[i][1]); } });
    STAMP(7); CSTAMP(45);
    f32x4 y[5];
    kreduce<5>(acc, y, smem + B_SCR, w, lane);
    float xs[20]; u32x2 xp[5];
    float s = 0.f;
#pragma unroll
    for (int i = 0; i < 5; ++i) {
      const f32x4 ga = *(const LAS f32x4*)(smem + T_MOD + (160 + 16 * i + 4 * q) * 4), c1b = *(const LAS f32x4*)(smem + T_MOD + 960 + (16 * i + 4 * q) * 4);
      x1p[i].x = pack2(bflo(xv[i].x) + ga[0] * y[i][0], bfhi(xv[i].x) + ga[1] * y[i][1]);
      x1p[i].y = pack2(bflo(xv[i].y) + ga[2] * y[i][2], bfhi(xv[i].y) + ga[3] * y[i][3]);
      asm volatile("" : "+v"(x1p[i].x), "+v"(x1p[i].y));
      xs[4 * i] = bflo(x1p[i].x); xs[4 * i + 1] = bfhi(x1p[i].x); xs[4 * i + 2] = bflo(x1p[i].y); xs[4 * i + 3] = bfhi(x1p[i].y);
      s += (xs[4 * i] + xs[4 * i + 1]) + (xs[4 * i + 2] + xs[4 * i + 3]);
      xp[i].x = pack2(xs[4 * i] * c1b[0], xs[4 * i + 1] * c1b[1]); xp[i].y = pack2(xs[4 * i + 2] * c1b[2], xs[4 * i + 3] * c1b[3]);
    }
    const unsigned img = WS_X1P + (unsigned)p * 20 * 4096 + w * 1024;
#pragma unroll
    for (int i = 0; i < 6; i += 2) {
      const int i2 = i + 1 < 5 ? i + 1 : i;
      const int dt = 5 * j + ((q & 1) ? i2 : i), kb = dt >> 1, qq = 2 * (dt & 1) + (q >> 1);
      st16(rws, img + kb * 4096 + (qq * 16 + tk) * 16, swap_pair(xp[i], xp[i2]));
    }
    const float mean = xsum_q(s) * (1.0f / 80.0f);
    float m2 = 0.f;
#pragma unroll
    for (int e = 0; e < 20; ++e) { const float d = xs[e] - mean; m2 += d * d; }
    m2 = xsum_q(m2);
    if (q == 0) __hip_atomic_store((gu64*)(ws + WS_ST1) + token * 8 + j, ((unsigned long long)__float_as_uint(m2) << 32) | __float_as_uint(mean), RLX_AGENT);
    wait_vm<0>(); bar();
    STAMP(8);
    if (w == 0) { if (lane == 0) set_flag(ws, WS_FX + p * 128 + j * 4, tag); wait_flags8(ws, WS_FX + p * 128, tag, lane); }
    bar();
    STAMP(9);
  }
  u32x2 z1p[7]; float bias;
  {
    LANE_IDS
    u32x4 st[4];
#pragma unroll
    for (int k = 0; k < 4; ++k) st[k] = ld16(rws, WS_ST1 + (unsigned)token * 64 + k * 16);
    bias = bflo(P.bsp[token]);
    for (int i = tid; i < 448; i += 256) ((LAS float*)(smem + T_CV))[i] = 0.f;
    f32x4 acc[7][2], aux[4];
    auto clear = [&]() {
#pragma unroll
      for (int i = 0; i < 7; ++i) { acc[i][0] = (f32x4){0, 0, 0, 0}; acc[i][1] = (f32x4){0, 0, 0, 0}; }
#pragma unroll
      for (int i = 0; i < 4; ++i) aux[i] = (f32x4){0, 0, 0, 0};
    };
    const unsigned xa = C_X + (kh * 4 + 2 * mh) * 1024 + lane * 16, wlo = C_W + kh * 7168 + lane * 16 + mh * 4096, whi = C_W + kh * 7168 + lane * 16 - mh * 3072;
    const int r4 = tk & 3;
    const unsigned ma = T_MODB + (r4 == 0 ? q * 16 + kh * 64 : r4 == 1 ? 1280 + q * 16 + kh * 64 : 2560), mstride = r4 < 2 ? 128 : 0;
    int nbase = 0;
    auto rd = [&](bf16x8(&A)[7], bf16x8(&Bf)[3], int slot, int n) {
      LAS char* sl = smem + wlo + slot * C_WSLOT; LAS char* sh = smem + whi + slot * C_WSLOT; LAS char* xb = smem + xa + n * 8192;
      Bf[0] = *(LAS bf16x8*)(xb); A[0] = *(LAS bf16x8*)(sl); Bf[1] = *(LAS bf16x8*)(xb + 1024);
      A[1] = *(LAS bf16x8*)(sl + 1024); A[2] = *(LAS bf16x8*)(sl + 2048);
#pragma unroll
      for (int i = 3; i < 7; ++i) A[i] = *(LAS bf16x8*)(sh + i * 1024);
      Bf[2] = *(LAS bf16x8*)(smem + ma + n * mstride);
    };
    auto auxmm = [&](bf16x8(&A)[7], bf16x8(&Bf)[3]) {
#pragma unroll
      for (int i = 0; i < 4; ++i) aux[i] = mfma(Bf[2], A[i], aux[i]);
    };
    auto mm2 = [&](bf16x8(&A)[7], bf16x8(&Bf)[3], bool) {
#pragma unroll
      for (int i = 0; i < 7; ++i) { acc[i][0] = mfma(Bf[0], A[i], acc[i][0]); acc[i][1] = mfma(Bf[1], A[i], acc[i][1]); }
      auxmm(A, Bf); };
    auto mm1 = [&](bf16x8(&A)[7], bf16x8(&Bf)[3], bool) {
#pragma unroll
      for (int i = 0; i < 7; ++i) { acc[i][0] = mfma(A[i], Bf[0], acc[i][0]); acc[i][1] = mfma(A[i], Bf[1], acc[i][1]); }
      auxmm(A, Bf); };
    auto put_cv = [&](int pass_) {
      LAS float* cv = (LAS float*)(smem + T_CV) + pass_ * 224;
      if (q == 0) {
#pragma unroll
        for (int i = 0; i < 4; ++i) if (mh == 0 || i < 3) { const int t = rot(i);
          __hip_atomic_fetch_add(cv + 16 * t + tk, aux[i][0], __ATOMIC_RELAXED, __HIP_MEMORY_SCOPE_WORKGROUP);
          __hip_atomic_fetch_add(cv + 112 + 16 * t + tk, aux[i][1], __ATOMIC_RELAXED, __HIP_MEMORY_SCOPE_WORKGROUP); }
      }
    };
    float rmean, rrstd;
    { float mu[8], mean = 0.f;
#pragma unroll
      for (int k = 0; k < 4; ++k) { mu[2 * k] = __uint_as_float(st[k].x); mu[2 * k + 1] = __uint_as_float(st[k].z); mean += mu[2 * k] + mu[2 * k + 1]; }
      mean *= 0.125f; float M2 = 0.f;
#pragma unroll
      for (int k = 0; k < 4; ++k) { const float d0 = mu[2 * k] - mean, d1 = mu[2 * k + 1] - mean; M2 += __uint_as_float(st[k].y) + __uint_as_float(st[k].w) + 80.0f * (d0 * d0 + d1 * d1); }
      rmean = mean; rrstd = 1.0f / sqrtf(M2 * (1.0f / D) + 1e-5f); }
    (void)nbase;
    bf16x8 A0[7], B0[3], A1[7], B1[3];
    int slot = 0;
    auto nohook = [](int) {};
    clear();
    wait_lgkm0(); bar();
    STAMP(10); CSTAMP(46);
    rd(A0, B0, 0, 0);
    ksplit_steps<C_NS, 10, 7, 3, 18>(A0, B0, A1, B1, slot, 0, 8, rd, mm2, nohook, nohook);
    ksplit_last<C_NS, 10, 7, 3>(A0, B0, A1, B1, slot, rd, mm2);
    STAMP(11); CSTAMP(47);
    put_cv(0);
    f32x4 zs[7];
    kreduce<7>(acc, zs, smem + C_SCR, w, lane);
    wait_lgkm0(); bar();
    f32x2 mr01, mr23, rr01, rr23;
    mr01 = (f32x2){__shfl(rmean, 4 * q), __shfl(rmean, 4 * q + 1)}; mr23 = (f32x2){__shfl(rmean, 4 * q + 2), __shfl(rmean, 4 * q + 3)};
    rr01 = (f32x2){__shfl(rrstd, 4 * q), __shfl(rrstd, 4 * q + 1)}; rr23 = (f32x2){__shfl(rrstd, 4 * q + 2), __shfl(rrstd, 4 * q + 3)};
    f32x2 z01[7], z23[7];
#pragma unroll
    for (int i = 0; i < 7; ++i) {
      const float c1 = ((const LAS float*)(smem + T_CV))[16 * rot(i) + tk], c2 = ((const LAS float*)(smem + T_CV))[112 + 16 * rot(i) + tk];
      z01[i] = gelu2(rr01 * ((f32x2){zs[i][0], zs[i][1]} - mr01 * c1) + c2);
      z23[i] = gelu2(rr23 * ((f32x2){zs[i][2], zs[i][3]} - mr23 * c1) + c2);
    }
    {
      const float inv = 1.0f / (16.0f * nft);
      const f32x2 zero2 = (f32x2){0.f, 0.f};
      f32x2 s01 = zero2, s23 = zero2;
#pragma unroll
      for (int i = 0; i < 7; ++i) { const bool ok = (i != 2 && i != 6) || rot(i) < nft; s01 += ok ? z01[i] : zero2; s23 += ok ? z23[i] : zero2; }
      const f32x2 m01 = (f32x2){xsum_16(s01[0]), xsum_16(s01[1])} * inv, m23 = (f32x2){xsum_16(s23[0]), xsum_16(s23[1])} * inv;
      f32x2 t01 = zero2, t23 = zero2;
#pragma unroll
      for (int i = 0; i < 7; ++i) { const bool ok = (i != 2 && i != 6) || rot(i) < nft; const f32x2 a = ok ? z01[i] - m01 : zero2, c = ok ? z23[i] - m23 : zero2; t01 += a * a; t23 += c * c; }
      const float q0 = xsum_16(t01[0]), q1 = xsum_16(t01[1]), q2 = xsum_16(t23[0]), q3 = xsum_16(t23[1]);
      if (tk < 8) { const int r = tk & 3; const float a = r == 0 ? m01[0] : r == 1 ? m01[1] : r == 2 ? m23[0] : m23[1], c = r == 0 ? q0 : r == 1 ? q1 : r == 2 ? q2 : q3;
        const int tok = 64 * p + 16 * w + 4 * q + r;
        __hip_atomic_store((gu64*)(ws + WS_ST2) + (tok * 8 + j) * 2 + (tk >> 2), ((unsigned long long)tag << 32) | __float_as_uint(tk < 4 ? a : c), RLX_AGENT); }
    }
    STAMP(12);
    {
      u32x4 g0, g1;
      const unsigned go = WS_ST2 + (unsigned)(token * 8 + 2 * q) * 16;
      for (unsigned spins = 0;;) { g0 = ld16(rws, go); g1 = ld16(rws, go + 16);
        if (__all(g0.y == tag && g0.w == tag && g1.y == tag && g1.w == tag)) break; if (spin_fail(spins, ws)) break; }
      const float n0 = 112.0f, n1 = (q & 1) ? 96.0f : 112.0f;
      const float m0 = __uint_as_float(g0.x), m1 = __uint_as_float(g1.x);
      const float mean = xsum_q(n0 * m0 + n1 * m1) * (1.0f / FH);
      const float d0 = m0 - mean, d1 = m1 - mean;
      const float M2 = xsum_q(__uint_as_float(g0.z) + __uint_as_float(g1.z) + n0 * d0 * d0 + n1 * d1 * d1);
      const float rstd = 1.0f / sqrtf(M2 * (1.0f / FH) + 1e-5f);
      mr01 = (f32x2){__shfl(mean, 4 * q), __shfl(mean, 4 * q + 1)}; mr23 = (f32x2){__shfl(mean, 4 * q + 2), __shfl(mean, 4 * q + 3)};
      rr01 = (f32x2){__shfl(rstd, 4 * q), __shfl(rstd, 4 * q + 1)}; rr23 = (f32x2){__shfl(rstd, 4 * q + 2), __shfl(rstd, 4 * q + 3)};
    }
    STAMP(13);
    {
      u32x2 pk[7];
#pragma unroll
      for (int i = 0; i < 7; ++i) { const f32x2 a = (z01[i] - mr01) * rr01, c = (z23[i] - mr23) * rr23; pk[i].x = pack2(a[0], a[1]); pk[i].y = pack2(c[0], c[1]); }
      const unsigned img = WS_Z2N + (unsigned)(2 * p + (w >> 1)) * 55 * 1024 + ((2 * (w & 1) + (q >> 1)) * 16 + tk) * 16;
#pragma unroll
      for (int i = 0; i < 8; i += 2) {
        const int i2 = i + 1 < 7 ? i + 1 : i;
        const u32x4 ch = swap_pair(pk[i], pk[i2]);
        const int ti = (q & 1) ? rot(i2) : rot(i);
        st16(rws, ti < nft ? img + (ft0 + ti) * 1024 : 0xfffff000u, ch);
      }
    }
    STAMP(14); CSTAMP(48);
    clear();
    slot = 0;
    bar();
    rd(A0, B0, 0, 0);
    ksplit_steps<C_NS, 10, 7, 3, 18>(A0, B0, A1, B1, slot, 0, 6, rd, mm1,
        [&](int n) { if (n == 2) wait_vm<0>(); },
        [&](int n) { if (n == 2 && w == 0 && lane == 0) set_flag(ws, WS_FZ + (j * 4 + (p >> 3)) * 128 + (p & 7) * 4, tag); });
    const unsigned fzo = WS_FZ + (j * 4 + ((lane & 7) >> 1)) * 128 + (lane & 1) * 16;
    const u32x4 fz = ld16(rws, fzo);
    ksplit_steps<C_NS, 10, 7, 3, 18>(A0, B0, A1, B1, slot, 6, 8, rd, mm1, nohook, nohook);
    ksplit_last<C_NS, 10, 7, 3>(A0, B0, A1, B1, slot, rd, mm1);
    if (!flags_ok(fz, tag)) { for (unsigned spins = 0;;) { if (flags_ok(ld16(rws, fzo), tag)) break; if (spin_fail(spins, ws)) break; } }
    STAMP(15); CSTAMP(49);
    put_cv(1);
    kreduce<7>(acc, zs, smem + C_SCR, w, lane);
#pragma unroll
    for (int i = 0; i < 7; ++i) {
      const LAS float* cv = (const LAS float*)(smem + T_CV) + 224;
      const f32x4 c1 = *(const LAS f32x4*)(cv + 16 * rot(i) + 4 * q), c2 = *(const LAS f32x4*)(cv + 112 + 16 * rot(i) + 4 * q);
      const f32x2 a = gelu2(rrstd * ((f32x2){zs[i][0], zs[i][1]} - rmean * (f32x2){c1[0], c1[1]}) + (f32x2){c2[0], c2[1]});
      const f32x2 c = gelu2(rrstd * ((f32x2){zs[i][2], zs[i][3]} - rmean * (f32x2){c1[2], c1[3]}) + (f32x2){c2[2], c2[3]});
      z1p[i] = (u32x2){pack2(a[0], a[1]), pack2(c[0], c[1])};
      asm volatile("" : "+v"(z1p[i].x), "+v"(z1p[i].y));
    }
    STAMP(16); CSTAMP(42);
  }
  {
    LANE_IDS
    f32x4 acc[7][2];
#pragma unroll
    for (int i = 0; i < 7; ++i) { acc[i][0] = (f32x4){0, 0, 0, 0}; acc[i][1] = (f32x4){0, 0, 0, 0}; }
    const unsigned wa = (unsigned)((2 * mh) * 2048 + tk * 128 + (((q + 4 * kh) ^ ((lane >> 1) & 7)) * 16));
    const unsigned xlo = 8192 + kh * 7168 + lane * 16 + mh * 4096, xhi = 8192 + kh * 7168 + lane * 16 - mh * 3072;
    ksplit_loop<D_NS, 32, 7, 2>(
        [&](bf16x8(&A)[7], bf16x8(&Bf)[2], int slot, int) { LAS char* sb = smem + slot * D_SLOT;
          Bf[0] = *(LAS bf16x8*)(sb + wa); A[0] = *(LAS bf16x8*)(sb + xlo); Bf[1] = *(LAS bf16x8*)(sb + wa + 2048);
          A[1] = *(LAS bf16x8*)(sb + xlo + 1024); A[2] = *(LAS bf16x8*)(sb + xlo + 2048);
#pragma unroll
          for (int i = 3; i < 7; ++i) A[i] = *(LAS bf16x8*)(sb + xhi + i * 1024); },
        [&](bf16x8(&A)[7], bf16x8(&Bf)[2], bool) {
#pragma unroll
          for (int i = 0; i < 7; ++i) { acc[i][0] = mfma(A[i], Bf[0], acc[i][0]); acc[i][1] = mfma(A[i], Bf[1], acc[i][1]); } });
    STAMP(17); CSTAMP(43);
    f32x4 g[7];
    kreduce<7>(acc, g, smem + D_SCR, w, lane);
    u32x2 pk[7];
#pragma unroll
    for (int i = 0; i < 7; ++i) {
      pk[i].x = pack2(bflo(z1p[i].x) * (g[i][0] + bias), bfhi(z1p[i].x) * (g[i][1] + bias));
      pk[i].y = pack2(bflo(z1p[i].y) * (g[i][2] + bias), bfhi(z1p[i].y) * (g[i][3] + bias));
    }
    const unsigned img = WS_P + (unsigned)p * 28 * 4096 + w * 1024;
#pragma unroll
    for (int i = 0; i < 8; i += 2) {
      const int i2 = i + 1 < 7 ? i + 1 : i;
      const u32x4 ch = swap_pair(pk[i], pk[i2]);
      const int ti = (q & 1) ? rot(i2) : rot(i), ft = ft0 + ti, kb = ft >> 1, qq = 2 * (ft & 1) + (q >> 1);
      st16(rws, ti < nft ? img + kb * 4096 + (qq * 16 + tk) * 16 : 0xfffff000u, ch);
    }
    wait_vm<0>(); bar();
    STAMP(18);
    if (w == 0) { if (lane == 0) set_flag(ws, WS_FP + p * 128 + j * 4, tag); wait_flags8(ws, WS_FP + p * 128, tag, lane); }
    bar();
    STAMP(19);
  }
  {
    LANE_IDS
    f32x4 acc[5][2];
#pragma unroll
    for (int i = 0; i < 5; ++i) { acc[i][0] = (f32x4){0, 0, 0, 0}; acc[i][1] = (f32x4){0, 0, 0, 0}; }
    const unsigned wa = kh * 5120 + lane * 16, xa = 10240 + (kh * 4 + 2 * mh) * 1024 + lane * 16;
    ksplit_loop<E_NS, 14, 5, 2>(
        [&](bf16x8(&A)[5], bf16x8(&Bf)[2], int slot, int) { LAS char* sb = smem + slot * E_SLOT;
          A[0] = *(LAS bf16x8*)(sb + wa); Bf[0] = *(LAS bf16x8*)(sb + xa); Bf[1] = *(LAS bf16x8*)(sb + xa + 1024);
#pragma unroll
          for (int i = 1; i < 5; ++i) A[i] = *(LAS bf16x8*)(sb + wa + i * 1024); },
        [&](bf16x8(&A)[5], bf16x8(&Bf)[2], bool last) {
          if (!(last && kh)) {
#pragma unroll
            for (int i = 0; i < 5; ++i) { acc[i][0] = mfma(A[i], Bf[0], acc[i][0]); acc[i][1] = mfma(A[i], Bf[1], acc[i][1]); } } });
    STAMP(20);
    f32x4 o[5];
    kreduce<5>(acc, o, smem + E_SCR, w, lane);
    u32x2 pk[5];
#pragma unroll
    for (int i = 0; i < 5; ++i) {
      const f32x4 gb = *(const LAS f32x4*)(smem + T_MOD + 960 + (80 + 16 * i + 4 * q) * 4);
      pk[i].x = pack2(bflo(x1p[i].x) + gb[0] * o[i][0], bfhi(x1p[i].x) + gb[1] * o[i][1]);
      pk[i].y = pack2(bflo(x1p[i].y) + gb[2] * o[i][2], bfhi(x1p[i].y) + gb[3] * o[i][3]);
    }
    bf16_t* orow = P.out + (size_t)token * D + 80 * j;
#pragma unroll
    for (int i = 0; i < 6; i += 2) {
      const int i2 = i + 1 < 5 ? i + 1 : i;
      const u32x4 ch = swap_pair(pk[i], pk[i2]);
      *(u32x4*)(orow + 16 * ((q & 1) ? i2 : i) + 8 * (q >> 1)) = ch;
    }
    STAMP(21);
  }
  STAMP_FLUSH();
}

__global__ __launch_bounds__(NT, 2) void fused_block(Params P) {
  extern __shared__ __attribute__((aligned(16))) char smem_[];
  LAS char* smem = (LAS char*)smem_;
  const int tid = threadIdx.x, lane = tid & 63, wid = __builtin_amdgcn_readfirstlane(tid >> 6), b = blockIdx.x;
  const int xcd = b & 7, idx = b >> 3, p = xcd * 4 + (idx >> 3), j = idx & 7;
  if (wid >= 4) loader_main(P, (unsigned)(size_t)smem, smem, wid - 4, lane, b, p, j, xcd * 4);
  else consumer_main(P, smem, wid, lane, b, p, j);
}

struct TouchArgs { const char* ptr[12]; unsigned len[12]; unsigned stride; unsigned pad; char* ws; };
__global__ __launch_bounds__(256) void k_touch(TouchArgs T) {
  unsigned acc = 0;
  const unsigned t = (blockIdx.x >> 3) * 256 + threadIdx.x, nt = (gridDim.x >> 3) * 256;
#pragma unroll
  for (int a = 0; a < 12; ++a) { const unsigned n = (T.len[a] + T.stride - 1) / T.stride;
    for (unsigned i = t; i < n; i += nt) acc ^= *(const unsigned*)(T.ptr[a] + (size_t)i * T.stride); }
  if (acc == 0x9e3779b9u) ((unsigned*)(T.ws + WS_TMO))[1] = acc;
}

extern "C" void kernel_launch(void* const* d_in, const int* in_sizes, int n_in, void* d_out, int out_size, void* d_ws, size_t ws_size, hipStream_t stream) {
  Params P;
  P.x = (const bf16_t*)d_in[0]; P.wsr = (const bf16_t*)d_in[1]; P.wsi = (const bf16_t*)d_in[2];
  P.wdr = (const bf16_t*)d_in[3]; P.wdi = (const bf16_t*)d_in[4]; P.win = (const bf16_t*)d_in[6];
  P.wsp = (const bf16_t*)d_in[7]; P.bsp = (const bf16_t*)d_in[8]; P.wout = (const bf16_t*)d_in[9];
  P.c = (const bf16_t*)d_in[10]; P.wmod = (const bf16_t*)d_in[11];
  P.out = (bf16_t*)d_out; P.ws = (char*)d_ws;
  (void)hipFuncSetAttribute((const void*)fused_block, hipFuncAttributeMaxDynamicSharedMemorySize, LDS_BYTES);
  TouchArgs T;
  { const char* pp[12] = {(const char*)P.x,(const char*)P.wsr,(const char*)P.wsi,(const char*)P.wdr,(const char*)P.wdi,(const char*)P.win,(const char*)P.wsp,(const char*)P.wout,(const char*)P.wmod,(const char*)P.c,(const char*)P.out,(const char*)P.ws};
    const unsigned ll[12] = {S*D*2,S*S*2,S*S*2,D*D*2,D*D*2,D*F*2,S*S*2,FH*D*2,D*6*D*2,D*2,S*D*2,WS_END};
    for (int a = 0; a < 12; ++a) { T.ptr[a] = pp[a]; T.len[a] = (1 ? (a >= 10 ? ll[a] : 0u) : 0 ? (a < 10 ? ll[a] : 0u) : ll[a]); } }
  T.stride = 65536; T.pad = 0; T.ws = P.ws;
  k_touch<<<32, 256, 0, stream>>>(T);
  fused_block<<<256, NT, LDS_BYTES, stream>>>(P);
}
```

```cpp
#include <hip/hip_runtime.h>
#include <stdint.h>
#include <type_traits>

typedef unsigned short bf16_t;
typedef short bf16x8 __attribute__((ext_vector_type(8)));
typedef short s16x4 __attribute__((ext_vector_type(4)));
typedef float f32x4 __attribute__((ext_vector_type(4)));
typedef float f32x2 __attribute__((ext_vector_type(2)));
typedef unsigned u32x4 __attribute__((ext_vector_type(4)));
typedef unsigned u32x2 __attribute__((ext_vector_type(2)));
typedef __bf16 bf2_t __attribute__((ext_vector_type(2)));
#define LAS __attribute__((address_space(3)))
#define GAS __attribute__((address_space(1)))
typedef GAS unsigned gu32;
typedef GAS unsigned long long gu64;
#define RLX_AGENT __ATOMIC_RELAXED, __HIP_MEMORY_SCOPE_AGENT
#define DEV __device__ __forceinline__

constexpr int S = 2048, D = 640, F = 1728, FH = 864;
constexpr int NT = 512;

constexpr unsigned WS_EPOCH = 0;
constexpr unsigned WS_TMO   = 256;
constexpr unsigned WS_FN = 4096, WS_F2 = 8192, WS_FU = 12288, WS_FX = 16384, WS_FP = 20480, WS_FZ = 24576;
constexpr unsigned WS_MOD  = 65536;
constexpr unsigned WS_MODB = WS_MOD + 16384;

constexpr unsigned WS_XN   = 131072;
constexpr unsigned WS_WIN  = WS_XN + 64 * 40 * 1024;
constexpr unsigned WS_WOUT = WS_WIN + 20 * 109 * 1024;
constexpr unsigned WS_UV   = WS_WOUT + 27 * 40 * 1024;
constexpr unsigned WS_X1P  = WS_UV + 32 * 2 * 20 * 4096;
constexpr unsigned WS_ST1  = WS_X1P + 32 * 20 * 4096;
constexpr unsigned WS_Z2N  = WS_ST1 + 2048 * 64;
constexpr unsigned WS_ST2  = WS_Z2N + 64 * 55 * 1024;
constexpr unsigned WS_P    = WS_ST2 + 2048 * 128;
constexpr unsigned WS_END  = WS_P + 32 * 28 * 4096;

struct Params {
  const bf16_t *x, *wsr, *wsi, *wdr, *wdi, *win, *wsp, *bsp, *wout, *c, *wmod;
  bf16_t* out;
  char* ws;
};

#define STAMP_DECL
#define STAMP(i) do {} while (0)
#define STAMP_FLUSH() do {} while (0)
#define CSTAMP(i) do {} while (0)

template <int N> DEV void wait_vm() { asm volatile("s_waitcnt vmcnt(%0)" ::"n"(N) : "memory"); }
DEV void wait_lgkm0() { asm volatile("s_waitcnt lgkmcnt(0)" ::: "memory"); }
DEV void bar() { asm volatile("" ::: "memory"); __builtin_amdgcn_s_barrier(); asm volatile("" ::: "memory"); }
DEV void sched_fence() { __builtin_amdgcn_sched_barrier(0); }
template <int I, int N, class Fn> DEV void static_for(Fn&& f) { if constexpr (I < N) { f(std::integral_constant<int, I>{}); static_for<I + 1, N>(f); } }

DEV float bflo(unsigned v) { return __uint_as_float(v << 16); }
DEV float bfhi(unsigned v) { return __uint_as_float(v & 0xffff0000u); }
DEV unsigned pack2(float lo, float hi) { bf2_t v; v[0] = (__bf16)lo; v[1] = (__bf16)hi; return __builtin_bit_cast(unsigned, v); }
DEV float rbf(float f) { return bflo(pack2(f, f)); }
DEV float dot2(unsigned a, unsigned b, float c) { return __builtin_amdgcn_fdot2_f32_bf16(__builtin_bit_cast(bf2_t, a), __builtin_bit_cast(bf2_t, b), c, false); }
DEV f32x4 mfma(bf16x8 a, bf16x8 b, f32x4 c) { return __builtin_amdgcn_mfma_f32_16x16x32_bf16(a, b, c, 0, 0, 0); }

DEV u32x4 mkrs(const void* p, unsigned bytes) {
  const unsigned long long a = (unsigned long long)p;
  u32x4 r; r.x = (unsigned)a; r.y = (unsigned)(a >> 32) & 0xffffu; r.z = bytes; r.w = 0x00020000u; return r;
}
DEV __amdgpu_buffer_rsrc_t mkrsrc(const void* p, unsigned bytes) { return __builtin_amdgcn_make_buffer_rsrc((void*)p, 0, bytes, 0x00020000); }
template <bool SC1> DEV void dma(u32x4 r, unsigned lds, unsigned voff, unsigned soff) {
  if constexpr (SC1) asm volatile("s_mov_b32 m0, %0\n\ts_nop 0\n\tbuffer_load_dwordx4 %1, %2, %3 offen sc1 lds" ::"s"(lds), "v"(voff), "s"(r), "s"(soff) : "memory");
  else asm volatile("s_mov_b32 m0, %0\n\ts_nop 0\n\tbuffer_load_dwordx4 %1, %2, %3 offen lds" ::"s"(lds), "v"(voff), "s"(r), "s"(soff) : "memory");
}
template <bool SC1> DEV void dma_half(u32x4 r, unsigned lds, unsigned voff, unsigned soff) {
  if constexpr (SC1) asm volatile("s_mov_b32 m0, %0\n\ts_mov_b32 exec_hi, 0\n\tbuffer_load_dwordx4 %1, %2, %3 offen sc1 lds\n\ts_mov_b32 exec_hi, -1" ::"s"(lds), "v"(voff), "s"(r), "s"(soff) : "memory");
  else asm volatile("s_mov_b32 m0, %0\n\ts_mov_b32 exec_hi, 0\n\tbuffer_load_dwordx4 %1, %2, %3 offen lds\n\ts_mov_b32 exec_hi, -1" ::"s"(lds), "v"(voff), "s"(r), "s"(soff) : "memory");
}
constexpr int SC1 = 16;
DEV void st16(__amdgpu_buffer_rsrc_t r, unsigned off, u32x4 v) { __builtin_amdgcn_raw_buffer_store_b128(v, r, off, 0, SC1); }
DEV u32x4 ld16(__amdgpu_buffer_rsrc_t r, unsigned off) { return __builtin_amdgcn_raw_buffer_load_b128(r, off, 0, SC1); }
DEV f32x4 ld16f(__amdgpu_buffer_rsrc_t r, unsigned off) { return __builtin_bit_cast(f32x4, ld16(r, off)); }

template <int CTRL> DEV float dpp_add(float v) { return v + __builtin_bit_cast(float, __builtin_amdgcn_update_dpp(0, __builtin_bit_cast(int, v), CTRL, 0xf, 0xf, true)); }
DEV float xsum_16(float v) { v = dpp_add<0xB1>(v); v = dpp_add<0x4E>(v); v = dpp_add<0x141>(v); return dpp_add<0x140>(v); }
DEV float xsum_r16(float v) { auto r = __builtin_amdgcn_permlane16_swap(__float_as_uint(v), __float_as_uint(v), false, false); return __uint_as_float(r[0]) + __uint_as_float(r[1]); }
DEV float xsum_r32(float v) { auto r = __builtin_amdgcn_permlane32_swap(__float_as_uint(v), __float_as_uint(v), false, false); return __uint_as_float(r[0]) + __uint_as_float(r[1]); }
DEV float xsum_q(float v) { return xsum_r32(xsum_r16(v)); }
DEV float gelu_tanh(float x) {
  const float k1 = -2.0f * 0.7978845608028654f * 1.4426950408889634f;
  const float t = x * __builtin_fmaf(x * x, 0.044715f * k1, k1);
  return x * __builtin_amdgcn_rcpf(1.0f + __builtin_amdgcn_exp2f(t));
}
DEV f32x2 gelu2(f32x2 x) {
  const float k1 = -2.0f * 0.7978845608028654f * 1.4426950408889634f;
  const f32x2 t = x * ((x * x) * (0.044715f * k1) + k1);
  const f32x2 d = (f32x2){__builtin_amdgcn_exp2f(t[0]), __builtin_amdgcn_exp2f(t[1])} + 1.0f;
  return x * (f32x2){__builtin_amdgcn_rcpf(d[0]), __builtin_amdgcn_rcpf(d[1])};
}
DEV u32x4 swap_pair(u32x2 a, u32x2 b) {
  auto r0 = __builtin_amdgcn_permlane16_swap(a.x, b.x, false, false);
  auto r1 = __builtin_amdgcn_permlane16_swap(a.y, b.y, false, false);
  return (u32x4){r0[0], r1[0], r0[1], r1[1]};
}

DEV bool spin_fail(unsigned& spins, char* ws) {
  __builtin_amdgcn_s_sleep(1);
  if (++spins > (1u << 17)) { __hip_atomic_store((gu32*)(ws + WS_TMO), 1u, RLX_AGENT); return true; }
  return false;
}
DEV void set_flag(char* ws, unsigned off, unsigned tag) { __hip_atomic_store((gu32*)(ws + off), tag, RLX_AGENT); }
DEV void wait_flags8(char* ws, unsigned off, unsigned tag, int lane) {
  for (unsigned spins = 0;;) {
    const unsigned v = lane < 8 ? __hip_atomic_load((gu32*)(ws + off) + lane, RLX_AGENT) : tag;
    if (__all(v == tag)) break;
    if (spin_fail(spins, ws)) break;
  }
}
DEV bool flags_ok(u32x4 v, unsigned tag) { return __all(v.x == tag && v.y == tag && v.z == tag && v.w == tag); }
DEV u32x4 peek256(__amdgpu_buffer_rsrc_t rf, unsigned off, int lane) { return ld16(rf, off + (lane >> 1) * 128 + (lane & 1) * 16); }
DEV void wait256(__amdgpu_buffer_rsrc_t rf, char* ws, unsigned off, unsigned tag, int lane) {
  for (unsigned spins = 0;;) { if (flags_ok(peek256(rf, off, lane), tag)) break; if (spin_fail(spins, ws)) break; }
}

DEV void wait_panels(__amdgpu_buffer_rsrc_t rf, char* ws, unsigned off, int first, int count, unsigned tag, int lane) {
  const unsigned o = lane < 2 * count ? off + (((lane >> 1) + first) & 31) * 128 + (lane & 1) * 16 : off + (first & 31) * 128;
  for (unsigned spins = 0;;) { if (flags_ok(ld16(rf, o), tag)) break; if (spin_fail(spins, ws)) break; }
}

template <int NS, int S_, int CW, int CX, class IW, class IX>
DEV void loader_run(IW&& issueW, IX&& issueX) {
  static_for<0, NS>([&](auto ic) { issueX(decltype(ic)::value, decltype(ic)::value); });
  wait_vm<(NS - 1) * CX>(); bar();
  static_for<0, NS - 2>([&](auto ic) { constexpr int n = decltype(ic)::value;
    wait_vm<(NS - 2) * CX + n * CW>(); bar(); issueW(n + NS, n); issueX(n + NS, n); });
  int slot = NS - 2;
#pragma unroll 1
  for (int n = NS - 2; n < S_ - NS; ++n) {
    wait_vm<(NS - 2) * (CW + CX)>(); bar();
    issueW(n + NS, slot); issueX(n + NS, slot);
    slot = slot + 1 == NS ? 0 : slot + 1;
  }
  static_for<0, NS - 1>([&](auto ic) { constexpr int r = NS - 2 - decltype(ic)::value; wait_vm<r*(CW + CX)>(); bar(); });
}

template <int NS, int S_, int NA, int NB, int NM, class Rd, class Mm, class Hook, class Post>
DEV void ksplit_steps(bf16x8 (&A0)[NA], bf16x8 (&B0)[NB], bf16x8 (&A1)[NA], bf16x8 (&B1)[NB], int& slot, int n0, int n1, Rd&& rd, Mm&& mm, Hook&& hook, Post&& post) {
  constexpr int NR = NA + NB;
#define KS_ILV() do { _Pragma("unroll") for (int z = 0; z < NR; ++z) { __builtin_amdgcn_sched_group_barrier(0x8, 1, 0); __builtin_amdgcn_sched_group_barrier(0x100, 1, 0); } __builtin_amdgcn_sched_group_barrier(0x8, NM - NR, 0); } while (0)
#pragma unroll 1
  for (int n = n0; n < n1; n += 2) {
    const int s1 = slot + 1 == NS ? 0 : slot + 1;
    hook(n); wait_lgkm0(); bar(); post(n); sched_fence();
    rd(A1, B1, s1, n + 1); mm(A0, B0, false); KS_ILV(); sched_fence();
    const int s2 = s1 + 1 == NS ? 0 : s1 + 1;
    hook(n + 1); wait_lgkm0(); bar(); post(n + 1); sched_fence();
    rd(A0, B0, s2, n + 2); mm(A1, B1, false); KS_ILV(); sched_fence();
    slot = s2;
  }
}
template <int NS, int S_, int NA, int NB, class Rd, class Mm>
DEV void ksplit_last(bf16x8 (&A0)[NA], bf16x8 (&B0)[NB], bf16x8 (&A1)[NA], bf16x8 (&B1)[NB], int& slot, Rd&& rd, Mm&& mm) {
  const int s1 = slot + 1 == NS ? 0 : slot + 1;
  wait_lgkm0(); bar(); sched_fence();
  rd(A1, B1, s1, S_ - 1); mm(A0, B0, false); mm(A1, B1, true);
  slot = s1;
}
template <int NS, int S_, int NA, int NB, class Rd, class Mm>
DEV void ksplit_loop(Rd&& rd, Mm&& mm) {
  static_assert((S_ & 1) == 0, "even step count");
  bf16x8 A0[NA], B0[NB], A1[NA], B1[NB];
  bar();
  rd(A0, B0, 0, 0);
  int slot = 0;
  ksplit_steps<NS, S_, NA, NB, NA * NB>(A0, B0, A1, B1, slot, 0, S_ - 2, rd, mm, [](int) {}, [](int) {});
  ksplit_last<NS, S_, NA, NB>(A0, B0, A1, B1, slot, rd, mm);
}
template <int N>
DEV void kreduce(f32x4 (&acc)[N][2], f32x4 (&res)[N], LAS char* scratch, int w, int lane) {
  const int kh = w & 1;
  LAS char* mine = scratch + w * (N * 1024) + lane * 16;
  LAS char* other = scratch + (w ^ 1) * (N * 1024) + lane * 16;
  if (kh) {
#pragma unroll
    for (int i = 0; i < N; ++i) { *(LAS f32x4*)(mine + i * 1024) = acc[i][0]; res[i] = acc[i][1]; }
  } else {
#pragma unroll
    for (int i = 0; i < N; ++i) { *(LAS f32x4*)(mine + i * 1024) = acc[i][1]; res[i] = acc[i][0]; }
  }
  wait_lgkm0(); bar();
#pragma unroll
  for (int i = 0; i < N; ++i) res[i] += *(LAS f32x4*)(other + i * 1024);
}

constexpr int A_NS = 5, A_SLOT = 26 * 1024;
constexpr int P0_T = 130 * 1024, P0_PT = 140 * 1024, P0_RED = 156 * 1024;
constexpr int B_NS = 6, B_SLOT = 18 * 1024, B_SCR = 36 * 1024;
constexpr int C_NS = 5, C_X = 0, C_W = 82 * 1024, C_WSLOT = 14 * 1024;
constexpr int C_SCR = C_W + 2 * C_WSLOT;
constexpr int D_NS = 5, D_SLOT = 22 * 1024, D_SCR = 110 * 1024;
constexpr int E_NS = 6, E_SLOT = 18 * 1024, E_SCR = 110 * 1024;
constexpr int T_MODB = 152 * 1024;
constexpr int T_CV = T_MODB + 3072 + 64;
constexpr int T_MOD = 158 * 1024;
constexpr int LDS_BYTES = 160 * 1024;
static_assert(T_CV + 896 <= T_MOD && C_W + C_NS * C_WSLOT <= T_MODB && D_SCR + 28 * 1024 <= T_MODB && E_SCR + 20 * 1024 <= T_MODB, "LDS map");

DEV void loader_main(const Params& P, const unsigned lds0, LAS char* smem, const int lw, const int lane_, const int b, const int p, const int j, const int krot) {
  STAMP_DECL STAMP(0);
  char* const ws = P.ws;
  const int ft0 = 27 * (j >> 2) + 7 * (j & 3);
#define LLANE int lane = lane_; asm volatile("" : "+v"(lane)); const unsigned vx = lane * 16; (void)vx;
#define LV4K const unsigned v4k0 = (unsigned)(lane >> 3) * 4096u + (unsigned)(((lane & 7) ^ (lane >> 4)) * 16), v4k1 = (unsigned)(8 + (lane >> 3)) * 4096u + (unsigned)(((lane & 7) ^ (4 + (lane >> 4))) * 16);
  {
    LLANE LV4K
    const u32x4 rre = mkrs(P.wsr, S * S * 2), rim = mkrs(P.wsi, S * S * 2), rx = mkrs(ws + WS_XN, S * D * 2);
    const unsigned wrow = (unsigned)(64 * p + 16 * lw) * 4096u;
    auto issueW = [&](int n, int slot) {
      const unsigned sb = lds0 + slot * A_SLOT + lw * 4096, sw = wrow + ((n + krot) & 31) * 128;
      dma<false>(rre, sb, v4k0, sw); dma<false>(rre, sb + 1024, v4k1, sw); dma<false>(rim, sb + 2048, v4k0, sw); dma<false>(rim, sb + 3072, v4k1, sw);
    };
    const int xi0 = lw * 2, xi1 = lw * 2 + 1, xi2 = 8 + (lw >> 1);
    const unsigned xo0 = (unsigned)(((xi0 >= 5) * 40 + 5 * j + xi0 - 5 * (xi0 >= 5)) * 1024), xo1 = (unsigned)(((xi1 >= 5) * 40 + 5 * j + xi1 - 5 * (xi1 >= 5)) * 1024);
    const unsigned xo2 = (unsigned)((40 + 5 * j + xi2 - 5) * 1024 + (lw & 1) * 512);
    auto issueX = [&](int n, int slot) {
      const unsigned sb = lds0 + slot * A_SLOT + 16384, so = ((n + krot) & 31) * 80 * 1024;
      dma<true>(rx, sb + xi0 * 1024, vx, so + xo0); dma<true>(rx, sb + xi1 * 1024, vx, so + xo1); dma_half<true>(rx, sb + xi2 * 1024 + (lw & 1) * 512, vx, so + xo2);
    };
    issueW(0, 0); issueW(1, 1);
    bar();
    bar();
    STAMP(30);
    bar();
    STAMP(33);
    __builtin_amdgcn_s_setprio(3);
    issueX(0, 0); issueX(1, 1);
    static_for<2, A_NS>([&](auto ic) { issueW(decltype(ic)::value, decltype(ic)::value); issueX(decltype(ic)::value, decltype(ic)::value); });
    const int idx = b >> 3, unit0 = (idx & 3) + 4 * ((b & 7) + 8 * (idx >> 2));
    const bool domod = unit0 < 240; const int unit = domod ? unit0 : 0;
    u32x4 wm[5]; unsigned cs[5];
#pragma unroll
    for (int i = 0; i < 5; ++i) { const int row = lw * 160 + i * 32 + (lane >> 1);
      const bf16_t* pw = P.wmod + (size_t)row * (6 * D) + unit * 16 + (lane & 1) * 8; const bf16_t* pc = P.c + row;
      asm volatile("global_load_dwordx4 %0, %1, off" : "=v"(wm[i]) : "v"(pw) : "memory");
      asm volatile("global_load_ushort %0, %1, off" : "=v"(cs[i]) : "v"(pc) : "memory"); }
    STAMP(34);
    wait_vm<3 + 3 * 7 + 10>();
    bar();
    wait_vm<3 * 7 + 10>(); bar(); issueW(A_NS, 0); issueX(A_NS, 0);
    wait_vm<2 * 7 + 10 + 7>(); bar(); issueW(A_NS + 1, 1); issueX(A_NS + 1, 1);
    wait_vm<14>();
    asm volatile("" : "+v"(wm[0]), "+v"(wm[1]), "+v"(wm[2]), "+v"(wm[3]), "+v"(wm[4]), "+v"(cs[0]), "+v"(cs[1]), "+v"(cs[2]), "+v"(cs[3]), "+v"(cs[4]));
    bar(); issueW(A_NS + 2, 2); issueX(A_NS + 2, 2);
    {
      float a[8] = {0, 0, 0, 0, 0, 0, 0, 0};
#pragma unroll
      for (int i = 0; i < 5; ++i) { const float x = bflo(cs[i]); const float s = rbf(x * __builtin_amdgcn_rcpf(1.0f + __expf(-x)));
        a[0] += s * bflo(wm[i].x); a[1] += s * bfhi(wm[i].x); a[2] += s * bflo(wm[i].y); a[3] += s * bfhi(wm[i].y);
        a[4] += s * bflo(wm[i].z); a[5] += s * bfhi(wm[i].z); a[6] += s * bflo(wm[i].w); a[7] += s * bfhi(wm[i].w); }
#pragma unroll
      for (int e = 0; e < 8; ++e) { float v = dpp_add<0x4E>(a[e]);
        v = dpp_add<0x124>(v); v = dpp_add<0x128>(v);
        a[e] = xsum_q(v); }
      if (lane < 2) {
#pragma unroll
        for (int e = 0; e < 8; ++e) ((LAS float*)(smem + P0_RED))[lw * 16 + lane * 8 + e] = a[e];
      }
      wait_lgkm0();
    }
    wait_vm<(A_NS - 2) * 7>(); bar(); issueW(A_NS + 3, 3); issueX(A_NS + 3, 3);
    if (lw == 0 && domod && lane < 16) {
      const LAS float* red = (const LAS float*)(smem + P0_RED);
      float s = rbf((red[lane] + red[16 + lane]) + (red[32 + lane] + red[48 + lane]));
      const int n = unit * 16 + lane, seg = n / D;
      if (seg == 1 || seg == 4) s = rbf(1.0f + s);
      __hip_atomic_store((gu32*)(ws + WS_MOD) + n, __float_as_uint(s), RLX_AGENT);
      const unsigned hi = __float_as_uint(s) >> 16, nb = (unsigned)__builtin_amdgcn_update_dpp(0, (int)hi, 0xB1, 0xf, 0xf, true);
      if ((seg == 3 || seg == 4) && !(lane & 1)) __hip_atomic_store((gu32*)(ws + WS_MODB) + (((seg == 4 ? 0 : D) + n - seg * D) >> 1), hi | (nb << 16), RLX_AGENT);
    }
    static_assert(A_NS == 5, "the start-up schedule above is written for five slots");
    const u32x4 rmod = mkrs(ws + WS_MOD, 3840 * 4);
    const int tl = lane / 20, tc = lane - 20 * tl;
    const unsigned tv0 = lane < 60 ? (unsigned)((tl * D + 80 * j) * 4 + tc * 16) : 0x7ffffff0u;
    const unsigned tv1 = lane < 40 ? (unsigned)(((4 + tl) * D + 80 * j) * 4 + tc * 16) : 0x7ffffff0u;
    int slot = 4;
#pragma unroll 1
    for (int n = 4; n < 32 - A_NS; ++n) {
      wait_vm<(A_NS - 2) * 7>(); bar();
      issueW(n + A_NS, slot); issueX(n + A_NS, slot);
      if (n == 20 && lw == 0) { dma<true>(rmod, lds0 + T_MOD, tv0, 0); dma<true>(rmod, lds0 + T_MOD + 960, tv1, 0); }
      slot = slot + 1 == A_NS ? 0 : slot + 1;
    }
    static_for<0, A_NS - 1>([&](auto ic) { constexpr int r = A_NS - 2 - decltype(ic)::value; wait_vm<r * 7>(); bar(); });
    bar();
  }
  {
    LLANE
    const u32x4 rre = mkrs(P.wdr, D * D * 2), rim = mkrs(P.wdi, D * D * 2), rx = mkrs(ws + WS_UV, 32 * 2 * 20 * 4096);
    const unsigned v0 = (unsigned)(lane >> 3) * 1280u + (unsigned)(((lane & 7) ^ (lane >> 4)) * 16);
    const unsigned v1 = (unsigned)(lane >> 3) * 1280u + (unsigned)(((lane & 7) ^ (4 + (lane >> 4))) * 16);
    const unsigned vh = (unsigned)(lane >> 3) * 1280u + (unsigned)(((lane & 7) ^ ((4 * (lw >> 1) + 2 * (lw & 1) + (lane >> 4)) & 7)) * 16);
    const unsigned wb = (unsigned)(80 * j) * 1280u;
    auto issueW = [&](int n, int slot) {
      const unsigned sb = lds0 + slot * B_SLOT; const int half = n >= 10; const unsigned sw = wb + (n - 10 * half) * 128;
      const u32x4 rw = half ? rim : rre;
      dma<false>(rw, sb + (2 * lw) * 1024, v0, sw + (unsigned)(16 * lw) * 1280u);
      dma<false>(rw, sb + (2 * lw + 1) * 1024, v1, sw + (unsigned)(16 * lw + 8) * 1280u);
      dma_half<false>(rw, sb + (8 + (lw >> 1)) * 1024 + (lw & 1) * 512, vh, sw + (unsigned)(64 + 8 * (lw >> 1) + 4 * (lw & 1)) * 1280u);
    };
    auto issueX = [&](int n, int slot) {
      const unsigned sb = lds0 + slot * B_SLOT + 10240 + lw * 2048, sx = (unsigned)(p * 40 + 2 * n) * 4096u + lw * 2048;
      dma<true>(rx, sb, vx, sx); dma<true>(rx, sb + 1024, vx, sx + 1024);
    };
    __builtin_amdgcn_s_setprio(0);
    bar();
    static_for<0, B_NS>([&](auto ic) { issueW(decltype(ic)::value, decltype(ic)::value); });
    bar();
    __builtin_amdgcn_s_setprio(3);
    loader_run<B_NS, 20, 3, 2>(issueW, issueX);
    bar();
  }
  {
    LLANE
    const u32x4 rw = mkrs(ws + WS_WIN, 20 * 109 * 1024), rx = mkrs(ws + WS_X1P, 32 * 20 * 4096), rmb = mkrs(ws + WS_MODB, 2560);
    unsigned wo[4], wl[4];
#pragma unroll
    for (int u = 0; u < 3; ++u) { const int i = lw * 3 + u, kk = i >= 7; wo[u] = (unsigned)((kk * 109 + i - 7 * kk) * 1024); wl[u] = i * 1024; }
    { const int i = 12 + (lw >> 1); wo[3] = (unsigned)((109 + i - 7) * 1024 + (lw & 1) * 512); wl[3] = i * 1024 + (lw & 1) * 512; }
    auto issueW = [&](int n, int slot) {
      const unsigned sb = lds0 + C_W + slot * C_WSLOT; const int z1 = n >= 10;
      const unsigned so = (unsigned)((2 * (n - 10 * z1) * 109 + (z1 ? 0 : 54) + ft0) * 1024);
      dma<true>(rw, sb + wl[0], vx, so + wo[0]); dma<true>(rw, sb + wl[1], vx, so + wo[1]); dma<true>(rw, sb + wl[2], vx, so + wo[2]); dma_half<true>(rw, sb + wl[3], vx, so + wo[3]);
    };
    auto issueX = [&](int n) {
      const unsigned o = (unsigned)(2 * n) * 4096u + lw * 2048, sx = (unsigned)p * 20 * 4096u + o;
      dma<true>(rx, lds0 + C_X + o, vx, sx); dma<true>(rx, lds0 + C_X + o + 1024, vx, sx + 1024);
    };
    __builtin_amdgcn_s_setprio(0);
    bar();
    if (lw < 3) dma<true>(rmb, lds0 + T_MODB + lw * 1024, vx + lw * 1024, 0);
    static_for<0, C_NS>([&](auto ic) { issueW(decltype(ic)::value, decltype(ic)::value); });
    bar();
    __builtin_amdgcn_s_setprio(3);
    static_for<0, C_NS>([&](auto ic) { issueX(decltype(ic)::value); });
    wait_vm<(C_NS - 1) * 2>(); bar();
    static_for<0, 9>([&](auto ic) {
      constexpr int n = decltype(ic)::value, need = n + 1;
      constexpr int last = n + C_NS - 1 < 11 ? n + C_NS - 1 : 11;
      constexpr int cnt = [] { int c = 0;
        if (need < C_NS) { c += (C_NS - 1 - need) * 2; for (int s = C_NS; s <= last; ++s) c += (s < 10 ? 6 : 4); }
        else { for (int s = need + 1; s <= last; ++s) c += (s < 10 ? 6 : 4); }
        return c; }();
      wait_vm<cnt>(); bar();
      if constexpr (n + C_NS < 12) { issueW(n + C_NS, n % C_NS); if constexpr (n + C_NS < 10) issueX(n + C_NS); }
    });
    bar();
    bar();
    issueW(12, 2); issueW(13, 3); issueW(14, 4);
    wait_vm<16>(); bar();
    static_for<10, 19>([&](auto ic) {
      constexpr int n = decltype(ic)::value;
      constexpr int last = n + 4 < 19 ? n + 4 : 19;
      wait_vm<(last - (n + 1)) * 4>(); bar();
      if constexpr (n + C_NS < 20) issueW(n + C_NS, n % C_NS);
    });
    bar();
  }
  {
    LLANE LV4K
    const u32x4 rw = mkrs(P.wsp, S * S * 2), rx = mkrs(ws + WS_Z2N, 64 * 55 * 1024);
    const unsigned wrow = (unsigned)(64 * p + 16 * lw) * 4096u;
    unsigned xo[4], xl[4];
#pragma unroll
    for (int u = 0; u < 3; ++u) { const int i = lw * 3 + u, kk = i >= 7; xo[u] = (unsigned)((kk * 55 + ft0 + i - 7 * kk) * 1024); xl[u] = 8192 + i * 1024; }
    { const int i = 12 + (lw >> 1); xo[3] = (unsigned)((55 + ft0 + i - 7) * 1024 + (lw & 1) * 512); xl[3] = 8192 + i * 1024 + (lw & 1) * 512; }
    auto issueW = [&](int n, int slot) {
      const unsigned sb = lds0 + slot * D_SLOT + lw * 2048, sw = wrow + ((n + krot) & 31) * 128;
      dma<false>(rw, sb, v4k0, sw); dma<false>(rw, sb + 1024, v4k1, sw);
    };
    auto issueX = [&](int n, int slot) {
      const unsigned sb = lds0 + slot * D_SLOT, so = ((n + krot) & 31) * 110 * 1024;
      dma<true>(rx, sb + xl[0], vx, so + xo[0]); dma<true>(rx, sb + xl[1], vx, so + xo[1]); dma<true>(rx, sb + xl[2], vx, so + xo[2]); dma_half<true>(rx, sb + xl[3], vx, so + xo[3]);
    };
    static_for<0, D_NS>([&](auto ic) { issueW(decltype(ic)::value, decltype(ic)::value); });
    loader_run<D_NS, 32, 2, 4>(issueW, issueX);
    bar();
  }
  {
    LLANE
    const u32x4 rw = mkrs(ws + WS_WOUT, 27 * 40 * 1024), rx = mkrs(ws + WS_P, 32 * 28 * 4096);
    const int i0 = lw * 2, i1 = lw * 2 + 1, i2 = 8 + (lw >> 1);
    const unsigned o0 = (unsigned)(((i0 >= 5) * 40 + 5 * j + i0 - 5 * (i0 >= 5)) * 1024), o1 = (unsigned)(((i1 >= 5) * 40 + 5 * j + i1 - 5 * (i1 >= 5)) * 1024);
    const unsigned o2 = (unsigned)((40 + 5 * j + i2 - 5) * 1024 + (lw & 1) * 512);
    auto issueW = [&](int n, int slot) {
      const unsigned sb = lds0 + slot * E_SLOT, so = (unsigned)n * 80 * 1024;
      dma<true>(rw, sb + i0 * 1024, vx, so + o0); dma<true>(rw, sb + i1 * 1024, vx, so + o1); dma_half<true>(rw, sb + i2 * 1024 + (lw & 1) * 512, vx, so + o2);
    };
    auto issueX = [&](int n, int slot) {
      const unsigned sb = lds0 + slot * E_SLOT + 10240 + lw * 2048, sx = (unsigned)(p * 28 + 2 * n) * 4096u + lw * 2048;
      dma<true>(rx, sb, vx, sx); dma<true>(rx, sb + 1024, vx, sx + 1024);
    };
    __builtin_amdgcn_s_setprio(0);
    bar();
    static_for<0, E_NS>([&](auto ic) { issueW(decltype(ic)::value, decltype(ic)::value); });
    bar();
    __builtin_amdgcn_s_setprio(3);
    loader_run<E_NS, 14, 3, 2>(issueW, issueX);
    bar();
  }
  STAMP_FLUSH();
}

DEV void consumer_main(const Params& P, LAS char* smem, const int w, const int lane_, const int b, const int p, const int j) {
  STAMP_DECL STAMP(0);
  char* const ws = P.ws;
  unsigned tagv = __hip_atomic_load((gu32*)(ws + WS_EPOCH), RLX_AGENT) + 1u, tag;
  const int mh = w >> 1, kh = w & 1;
  const auto rws = mkrsrc(ws, WS_END);
  const int ft0 = 27 * (j >> 2) + 7 * (j & 3), nft = (j & 3) == 3 ? 6 : 7;
  auto rot = [&](int i) { return i < 3 ? i + 4 * mh : i - 3 * mh; };
#define LANE_IDS int lane = lane_; asm volatile("" : "+v"(lane)); const int q = lane >> 4, tk = lane & 15, tid = w * 64 + lane, token = 64 * p + 16 * w + tk; (void)q; (void)tk; (void)tid; (void)token;
  {
    LANE_IDS
    unsigned v[2][5];
#pragma unroll
    for (int t = 0; t < 2; ++t) { const unsigned* xr = (const unsigned*)(P.x + (size_t)(64 * p + 8 * j + 2 * w + t) * D);
#pragma unroll
      for (int i = 0; i < 5; ++i) v[t][i] = xr[lane + 64 * i]; }
    const int u0 = w * 256 + b; const bool dopt = u0 < 810; const int u = dopt ? u0 : 0; const bool isin = u < 540;
    const int uu = isin ? u : u - 540, ncb = isin ? 27 : 10, pkb = uu / ncb, pcb = uu - pkb * ncb, ld = isin ? F : D;
    u32x4 pv[4];
    { const bf16_t* src = (isin ? P.win : P.wout) + (size_t)(pkb * 32) * ld + pcb * 64;
#pragma unroll
      for (int i = 0; i < 4; ++i) pv[i] = *(const u32x4*)(src + (size_t)(i * 8 + (lane >> 3)) * ld + (lane & 7) * 8); }
    LAS unsigned* t32 = (LAS unsigned*)(smem + P0_T);
    unsigned pk[2][5];
#pragma unroll
    for (int t = 0; t < 2; ++t) {
      float s = 0.f;
#pragma unroll
      for (int i = 0; i < 5; ++i) s += bflo(v[t][i]) + bfhi(v[t][i]);
      const float mean = xsum_q(xsum_16(s)) * (1.0f / D);
      float q2 = 0.f;
#pragma unroll
      for (int i = 0; i < 5; ++i) { const float a = bflo(v[t][i]) - mean, c = bfhi(v[t][i]) - mean; q2 += a * a + c * c; }
      const float rs = 1.0f / sqrtf(xsum_q(xsum_16(q2)) * (1.0f / D) + 1e-5f);
#pragma unroll
      for (int i = 0; i < 5; ++i) pk[t][i] = pack2((bflo(v[t][i]) - mean) * rs, (bfhi(v[t][i]) - mean) * rs);
    }
#pragma unroll
    for (int i = 0; i < 5; ++i) { const int d = 2 * (lane + 64 * i);
      t32[d * 4 + w] = (pk[0][i] & 0xffffu) | (pk[1][i] << 16); t32[(d + 1) * 4 + w] = (pk[0][i] >> 16) | (pk[1][i] & 0xffff0000u); }
    asm volatile("" : "+v"(tagv)); tag = __builtin_amdgcn_readfirstlane(tagv);
    wait_lgkm0(); bar();
    const unsigned xb = WS_XN + (unsigned)((2 * p + (j >> 2)) * 40 * 1024 + (j & 3) * 256);
#pragma unroll
    for (int i = 0; i < 3; ++i) { const int d = tid + 256 * i;
      if (d < D) st16(rws, xb + (d >> 4) * 1024 + (d & 15) * 16, *(LAS u32x4*)(smem + P0_T + d * 16)); }
    wait_vm<0>();
    bar();
    STAMP(1);
    if (w == 0 && lane == 0) set_flag(ws, WS_FN + p * 128 + j * 4, tag);
    {
      LAS char* t = smem + P0_PT + w * 4096;
#pragma unroll
      for (int i = 0; i < 4; ++i) *(LAS u32x4*)(t + (i * 8 + (lane >> 3)) * 128 + (lane & 7) * 16) = pv[i];
      const unsigned dst = dopt ? (isin ? WS_WIN + (unsigned)(pkb * 109 + pcb * 4) * 1024 : WS_WOUT + (unsigned)(pkb * 40 + pcb * 4) * 1024) + lane * 16 : 0xfffff000u;
      const int rq = tk >> 2, pp = lane & 3;
#pragma unroll
      for (int c = 0; c < 4; ++c) {
        const u32x2 l2 = __builtin_bit_cast(u32x2, __builtin_amdgcn_ds_read_tr16_b64_v4i16((LAS s16x4*)(t + (8 * q + rq) * 128 + c * 32 + pp * 8)));
        const u32x2 h2 = __builtin_bit_cast(u32x2, __builtin_amdgcn_ds_read_tr16_b64_v4i16((LAS s16x4*)(t + (8 * q + 4 + rq) * 128 + c * 32 + pp * 8)));
        st16(rws, dst + (dopt ? c * 1024 : 0), (u32x4){l2.x, l2.y, h2.x, h2.y});
      }
    }
    if (w == 0) { wait256(rws, ws, WS_FN, tag, lane); STAMP(23); }
    bar();
    STAMP(2);
  }
  u32x2 xv[5];
  {
    LANE_IDS
    f32x4 acc[2][5];
#pragma unroll
    for (int t = 0; t < 2; ++t)
#pragma unroll
      for (int i = 0; i < 5; ++i) acc[t][i] = (f32x4){0, 0, 0, 0};
    f32x4 rsum[2] = {(f32x4){0, 0, 0, 0}, (f32x4){0, 0, 0, 0}};
    const bf16x8 ones = __builtin_bit_cast(bf16x8, (u32x4){0x3f803f80u, 0x3f803f80u, 0x3f803f80u, 0x3f803f80u});
    const unsigned wa0 = (unsigned)(w * 4096 + tk * 128 + ((q ^ ((lane >> 1) & 7)) * 16));
    const unsigned xa = 16384 + lane * 16;
    bf16x8 Wa[2], Xa[5], Wb[2], Xb[5];
    auto rdm = [&](bf16x8(&Wn)[2], bf16x8(&Xn)[5], int slot, int kk, bf16x8(&Wf)[2], bf16x8(&Xf)[5]) {
      LAS char* sb = smem + slot * A_SLOT; const unsigned wa = wa0 ^ (kk * 64);
      static_for<0, 12>([&](auto ic) { constexpr int z = decltype(ic)::value, i = z >> 1, t = z & 1;
        if constexpr (z < 10) acc[t][i] = mfma(Xf[i], Wf[t], acc[t][i]); else rsum[t] = mfma(ones, Wf[t], rsum[t]);
        if constexpr (z == 0) Wn[0] = *(LAS bf16x8*)(sb + wa);
        if constexpr (z == 1) Xn[0] = *(LAS bf16x8*)(sb + xa + (kk * 5) * 1024);
        if constexpr (z == 2) Wn[1] = *(LAS bf16x8*)(sb + wa + 2048);
        if constexpr (z >= 3 && z < 7) Xn[z - 2] = *(LAS bf16x8*)(sb + xa + (kk * 5 + z - 2) * 1024);
        sched_fence(); });
    };
    auto mmlast = [&](bf16x8(&Wf)[2], bf16x8(&Xf)[5]) {
#pragma unroll
      for (int i = 0; i < 5; ++i) { acc[0][i] = mfma(Xf[i], Wf[0], acc[0][i]); acc[1][i] = mfma(Xf[i], Wf[1], acc[1][i]); }
      rsum[0] = mfma(ones, Wf[0], rsum[0]); rsum[1] = mfma(ones, Wf[1], rsum[1]);
    };
    int slot = 0;
    auto steps = [&](int n0, int n1, bool hooks) {
#pragma unroll 1
      for (int n = n0; n < n1; ++n) {
        rdm(Wb, Xb, slot, 1, Wa, Xa);
        const int ns = slot + 1 == A_NS ? 0 : slot + 1;
        if (hooks && n == 8) wait_vm<0>();
        wait_lgkm0(); bar(); sched_fence();
        if (hooks && n == 8 && w == 0 && lane == 0) set_flag(ws, WS_F2 + p * 128 + j * 4, tag);
        rdm(Wa, Xa, ns, 0, Wb, Xb);
        slot = ns;
      }
    };
    bar();
    STAMP(3); CSTAMP(40);
    { LAS char* sb = smem; Wa[0] = *(LAS bf16x8*)(sb + wa0); Wa[1] = *(LAS bf16x8*)(sb + wa0 + 2048);
#pragma unroll
      for (int i = 0; i < 5; ++i) Xa[i] = *(LAS bf16x8*)(sb + xa + i * 1024); }
    steps(0, 12, true);
    const u32x4 f2 = peek256(rws, WS_F2, lane);
    steps(12, 18, false);
    if (!flags_ok(f2, tag)) wait256(rws, ws, WS_F2, tag, lane);
    if (b == 0 && w == 0 && lane == 0) set_flag(ws, WS_EPOCH, tag + 15);
#pragma unroll
    for (int i = 0; i < 5; ++i) xv[i] = *(const u32x2*)(P.x + (size_t)token * D + 80 * j + 16 * i + 4 * q);
    steps(18, 31, false);
    { LAS char* sb = smem + slot * A_SLOT; const unsigned wa = wa0 ^ 64;
      Wb[0] = *(LAS bf16x8*)(sb + wa); Wb[1] = *(LAS bf16x8*)(sb + wa + 2048);
#pragma unroll
      for (int i = 0; i < 5; ++i) Xb[i] = *(LAS bf16x8*)(sb + xa + (5 + i) * 1024); }
    mmlast(Wa, Xa);
    wait_lgkm0(); bar();
    mmlast(Wb, Xb);
    STAMP(4); CSTAMP(41);
    const float rs0 = rsum[0][0], rs1 = rsum[1][0];
#pragma unroll
    for (int i = 0; i < 5; ++i) {
      u32x2 a, c;
      const f32x4 sa = *(const LAS f32x4*)(smem + T_MOD + (16 * i + 4 * q) * 4), c1 = *(const LAS f32x4*)(smem + T_MOD + (80 + 16 * i + 4 * q) * 4);
      { const f32x4 u = acc[0][i] * c1 + rs0 * sa; a.x = pack2(u[0], u[1]); a.y = pack2(u[2], u[3]); }
      { const f32x4 v = -(acc[1][i] * c1 + rs1 * sa); c.x = pack2(v[0], v[1]); c.y = pack2(v[2], v[3]); }
      const int dt = 5 * j + i, kb = dt >> 1, qq = 2 * (dt & 1) + (q >> 1);
      st16(rws, WS_UV + (unsigned)(((p * 2 + (q & 1)) * 20 + kb) * 4 + w) * 1024 + (qq * 16 + tk) * 16, swap_pair(a, c));
    }
    wait_vm<0>(); bar();
    STAMP(5);
    if (w == 0) { if (lane == 0) set_flag(ws, WS_FU + p * 128 + j * 4, tag); wait_flags8(ws, WS_FU + p * 128, tag, lane); }
    bar();
    STAMP(6); CSTAMP(44);
  }
  u32x2 x1p[5];
  {
    LANE_IDS
    f32x4 acc[5][2];
#pragma unroll
    for (int i = 0; i < 5; ++i) { acc[i][0] = (f32x4){0, 0, 0, 0}; acc[i][1] = (f32x4){0, 0, 0, 0}; }
    const unsigned wa = (unsigned)(tk * 128 + (((q + 4 * kh) ^ ((lane >> 1) & 7)) * 16));
    const unsigned xa = 10240 + (kh * 4 + 2 * mh) * 1024 + lane * 16;
    ksplit_loop<B_NS, 20, 5, 2>(
        [&](bf16x8(&A)[5], bf16x8(&Bf)[2], int slot, int) { LAS char* sb = smem + slot * B_SLOT;
          A[0] = *(LAS bf16x8*)(sb + wa); Bf[0] = *(LAS bf16x8*)(sb + xa); Bf[1] = *(LAS bf16x8*)(sb + xa + 1024);
#pragma unroll
          for (int i = 1; i < 5; ++i) A[i] = *(LAS bf16x8*)(sb + wa + i * 2048); },
        [&](bf16x8(&A)[5], bf16x8(&Bf)[2], bool) {
#pragma unroll
          for (int i = 0; i < 5; ++i) { acc[i][0] = mfma(A[i], Bf[0], acc[i][0]); acc[i][1] = mfma(A[i], Bf[1], acc[i][1]); } });
    STAMP(7); CSTAMP(45);
    f32x4 y[5];
    kreduce<5>(acc, y, smem + B_SCR, w, lane);
    float xs[20]; u32x2 xp[5];
    float s = 0.f;
#pragma unroll
    for (int i = 0; i < 5; ++i) {
      const f32x4 ga = *(const LAS f32x4*)(smem + T_MOD + (160 + 16 * i + 4 * q) * 4), c1b = *(const LAS f32x4*)(smem + T_MOD + 960 + (16 * i + 4 * q) * 4);
      x1p[i].x = pack2(bflo(xv[i].x) + ga[0] * y[i][0], bfhi(xv[i].x) + ga[1] * y[i][1]);
      x1p[i].y = pack2(bflo(xv[i].y) + ga[2] * y[i][2], bfhi(xv[i].y) + ga[3] * y[i][3]);
      asm volatile("" : "+v"(x1p[i].x), "+v"(x1p[i].y));
      xs[4 * i] = bflo(x1p[i].x); xs[4 * i + 1] = bfhi(x1p[i].x); xs[4 * i + 2] = bflo(x1p[i].y); xs[4 * i + 3] = bfhi(x1p[i].y);
      s += (xs[4 * i] + xs[4 * i + 1]) + (xs[4 * i + 2] + xs[4 * i + 3]);
      xp[i].x = pack2(xs[4 * i] * c1b[0], xs[4 * i + 1] * c1b[1]); xp[i].y = pack2(xs[4 * i + 2] * c1b[2], xs[4 * i + 3] * c1b[3]);
    }
    const unsigned img = WS_X1P + (unsigned)p * 20 * 4096 + w * 1024;
#pragma unroll
    for (int i = 0; i < 6; i += 2) {
      const int i2 = i + 1 < 5 ? i + 1 : i;
      const int dt = 5 * j + ((q & 1) ? i2 : i), kb = dt >> 1, qq = 2 * (dt & 1) + (q >> 1);
      st16(rws, img + kb * 4096 + (qq * 16 + tk) * 16, swap_pair(xp[i], xp[i2]));
    }
    const float mean = xsum_q(s) * (1.0f / 80.0f);
    float m2 = 0.f;
#pragma unroll
    for (int e = 0; e < 20; ++e) { const float d = xs[e] - mean; m2 += d * d; }
    m2 = xsum_q(m2);
    if (q == 0) __hip_atomic_store((gu64*)(ws + WS_ST1) + token * 8 + j, ((unsigned long long)__float_as_uint(m2) << 32) | __float_as_uint(mean), RLX_AGENT);
    wait_vm<0>(); bar();
    STAMP(8);
    if (w == 0) { if (lane == 0) set_flag(ws, WS_FX + p * 128 + j * 4, tag); wait_flags8(ws, WS_FX + p * 128, tag, lane); }
    bar();
    STAMP(9);
  }
  u32x2 z1p[7]; float bias;
  {
    LANE_IDS
    u32x4 st[4];
#pragma unroll
    for (int k = 0; k < 4; ++k) st[k] = ld16(rws, WS_ST1 + (unsigned)token * 64 + k * 16);
    bias = bflo(P.bsp[token]);
    for (int i = tid; i < 448; i += 256) ((LAS float*)(smem + T_CV))[i] = 0.f;
    f32x4 acc[7][2], aux[4];
    auto clear = [&]() {
#pragma unroll
      for (int i = 0; i < 7; ++i) { acc[i][0] = (f32x4){0, 0, 0, 0}; acc[i][1] = (f32x4){0, 0, 0, 0}; }
#pragma unroll
      for (int i = 0; i < 4; ++i) aux[i] = (f32x4){0, 0, 0, 0};
    };
    const unsigned xa = C_X + (kh * 4 + 2 * mh) * 1024 + lane * 16, wlo = C_W + kh * 7168 + lane * 16 + mh * 4096, whi = C_W + kh * 7168 + lane * 16 - mh * 3072;
    const int r4 = tk & 3;
    const unsigned ma = T_MODB + (r4 == 0 ? q * 16 + kh * 64 : r4 == 1 ? 1280 + q * 16 + kh * 64 : 2560), mstride = r4 < 2 ? 128 : 0;
    int nbase = 0;
    auto rd = [&](bf16x8(&A)[7], bf16x8(&Bf)[3], int slot, int n) {
      LAS char* sl = smem + wlo + slot * C_WSLOT; LAS char* sh = smem + whi + slot * C_WSLOT; LAS char* xb = smem + xa + n * 8192;
      Bf[0] = *(LAS bf16x8*)(xb); A[0] = *(LAS bf16x8*)(sl); Bf[1] = *(LAS bf16x8*)(xb + 1024);
      A[1] = *(LAS bf16x8*)(sl + 1024); A[2] = *(LAS bf16x8*)(sl + 2048);
#pragma unroll
      for (int i = 3; i < 7; ++i) A[i] = *(LAS bf16x8*)(sh + i * 1024);
      Bf[2] = *(LAS bf16x8*)(smem + ma + n * mstride);
    };
    auto auxmm = [&](bf16x8(&A)[7], bf16x8(&Bf)[3]) {
#pragma unroll
      for (int i = 0; i < 4; ++i) aux[i] = mfma(Bf[2], A[i], aux[i]);
    };
    auto mm2 = [&](bf16x8(&A)[7], bf16x8(&Bf)[3], bool) {
#pragma unroll
      for (int i = 0; i < 7; ++i) { acc[i][0] = mfma(Bf[0], A[i], acc[i][0]); acc[i][1] = mfma(Bf[1], A[i], acc[i][1]); }
      auxmm(A, Bf); };
    auto mm1 = [&](bf16x8(&A)[7], bf16x8(&Bf)[3], bool) {
#pragma unroll
      for (int i = 0; i < 7; ++i) { acc[i][0] = mfma(A[i], Bf[0], acc[i][0]); acc[i][1] = mfma(A[i], Bf[1], acc[i][1]); }
      auxmm(A, Bf); };
    auto put_cv = [&](int pass_) {
      LAS float* cv = (LAS float*)(smem + T_CV) + pass_ * 224;
      if (q == 0) {
#pragma unroll
        for (int i = 0; i < 4; ++i) if (mh == 0 || i < 3) { const int t = rot(i);
          __hip_atomic_fetch_add(cv + 16 * t + tk, aux[i][0], __ATOMIC_RELAXED, __HIP_MEMORY_SCOPE_WORKGROUP);
          __hip_atomic_fetch_add(cv + 112 + 16 * t + tk, aux[i][1], __ATOMIC_RELAXED, __HIP_MEMORY_SCOPE_WORKGROUP); }
      }
    };
    float rmean, rrstd;
    { float mu[8], mean = 0.f;
#pragma unroll
      for (int k = 0; k < 4; ++k) { mu[2 * k] = __uint_as_float(st[k].x); mu[2 * k + 1] = __uint_as_float(st[k].z); mean += mu[2 * k] + mu[2 * k + 1]; }
      mean *= 0.125f; float M2 = 0.f;
#pragma unroll
      for (int k = 0; k < 4; ++k) { const float d0 = mu[2 * k] - mean, d1 = mu[2 * k + 1] - mean; M2 += __uint_as_float(st[k].y) + __uint_as_float(st[k].w) + 80.0f * (d0 * d0 + d1 * d1); }
      rmean = mean; rrstd = 1.0f / sqrtf(M2 * (1.0f / D) + 1e-5f); }
    (void)nbase;
    bf16x8 A0[7], B0[3], A1[7], B1[3];
    int slot = 0;
    auto nohook = [](int) {};
    clear();
    wait_lgkm0(); bar();
    STAMP(10); CSTAMP(46);
    rd(A0, B0, 0, 0);
    ksplit_steps<C_NS, 10, 7, 3, 18>(A0, B0, A1, B1, slot, 0, 8, rd, mm2, nohook, nohook);
    ksplit_last<C_NS, 10, 7, 3>(A0, B0, A1, B1, slot, rd, mm2);
    STAMP(11); CSTAMP(47);
    put_cv(0);
    f32x4 zs[7];
    kreduce<7>(acc, zs, smem + C_SCR, w, lane);
    wait_lgkm0(); bar();
    f32x2 mr01, mr23, rr01, rr23;
    mr01 = (f32x2){__shfl(rmean, 4 * q), __shfl(rmean, 4 * q + 1)}; mr23 = (f32x2){__shfl(rmean, 4 * q + 2), __shfl(rmean, 4 * q + 3)};
    rr01 = (f32x2){__shfl(rrstd, 4 * q), __shfl(rrstd, 4 * q + 1)}; rr23 = (f32x2){__shfl(rrstd, 4 * q + 2), __shfl(rrstd, 4 * q + 3)};
    f32x2 z01[7], z23[7];
#pragma unroll
    for (int i = 0; i < 7; ++i) {
      const float c1 = ((const LAS float*)(smem + T_CV))[16 * rot(i) + tk], c2 = ((const LAS float*)(smem + T_CV))[112 + 16 * rot(i) + tk];
      z01[i] = gelu2(rr01 * ((f32x2){zs[i][0], zs[i][1]} - mr01 * c1) + c2);
      z23[i] = gelu2(rr23 * ((f32x2){zs[i][2], zs[i][3]} - mr23 * c1) + c2);
    }
    {
      const float inv = 1.0f / (16.0f * nft);
      const f32x2 zero2 = (f32x2){0.f, 0.f};
      f32x2 s01 = zero2, s23 = zero2;
#pragma unroll
      for (int i = 0; i < 7; ++i) { const bool ok = (i != 2 && i != 6) || rot(i) < nft; s01 += ok ? z01[i] : zero2; s23 += ok ? z23[i] : zero2; }
      const f32x2 m01 = (f32x2){xsum_16(s01[0]), xsum_16(s01[1])} * inv, m23 = (f32x2){xsum_16(s23[0]), xsum_16(s23[1])} * inv;
      f32x2 t01 = zero2, t23 = zero2;
#pragma unroll
      for (int i = 0; i < 7; ++i) { const bool ok = (i != 2 && i != 6) || rot(i) < nft; const f32x2 a = ok ? z01[i] - m01 : zero2, c = ok ? z23[i] - m23 : zero2; t01 += a * a; t23 += c * c; }
      const float q0 = xsum_16(t01[0]), q1 = xsum_16(t01[1]), q2 = xsum_16(t23[0]), q3 = xsum_16(t23[1]);
      if (tk < 8) { const int r = tk & 3; const float a = r == 0 ? m01[0] : r == 1 ? m01[1] : r == 2 ? m23[0] : m23[1], c = r == 0 ? q0 : r == 1 ? q1 : r == 2 ? q2 : q3;
        const int tok = 64 * p + 16 * w + 4 * q + r;
        __hip_atomic_store((gu64*)(ws + WS_ST2) + (tok * 8 + j) * 2 + (tk >> 2), ((unsigned long long)tag << 32) | __float_as_uint(tk < 4 ? a : c), RLX_AGENT); }
    }
    STAMP(12);
    {
      u32x4 g0, g1;
      const unsigned go = WS_ST2 + (unsigned)(token * 8 + 2 * q) * 16;
      for (unsigned spins = 0;;) { g0 = ld16(rws, go); g1 = ld16(rws, go + 16);
        if (__all(g0.y == tag && g0.w == tag && g1.y == tag && g1.w == tag)) break; if (spin_fail(spins, ws)) break; }
      const float n0 = 112.0f, n1 = (q & 1) ? 96.0f : 112.0f;
      const float m0 = __uint_as_float(g0.x), m1 = __uint_as_float(g1.x);
      const float mean = xsum_q(n0 * m0 + n1 * m1) * (1.0f / FH);
      const float d0 = m0 - mean, d1 = m1 - mean;
      const float M2 = xsum_q(__uint_as_float(g0.z) + __uint_as_float(g1.z) + n0 * d0 * d0 + n1 * d1 * d1);
      const float rstd = 1.0f / sqrtf(M2 * (1.0f / FH) + 1e-5f);
      mr01 = (f32x2){__shfl(mean, 4 * q), __shfl(mean, 4 * q + 1)}; mr23 = (f32x2){__shfl(mean, 4 * q + 2), __shfl(mean, 4 * q + 3)};
      rr01 = (f32x2){__shfl(rstd, 4 * q), __shfl(rstd, 4 * q + 1)}; rr23 = (f32x2){__shfl(rstd, 4 * q + 2), __shfl(rstd, 4 * q + 3)};
    }
    STAMP(13);
    {
      u32x2 pk[7];
#pragma unroll
      for (int i = 0; i < 7; ++i) { const f32x2 a = (z01[i] - mr01) * rr01, c = (z23[i] - mr23) * rr23; pk[i].x = pack2(a[0], a[1]); pk[i].y = pack2(c[0], c[1]); }
      const unsigned img = WS_Z2N + (unsigned)(2 * p + (w >> 1)) * 55 * 1024 + ((2 * (w & 1) + (q >> 1)) * 16 + tk) * 16;
#pragma unroll
      for (int i = 0; i < 8; i += 2) {
        const int i2 = i + 1 < 7 ? i + 1 : i;
        const u32x4 ch = swap_pair(pk[i], pk[i2]);
        const int ti = (q & 1) ? rot(i2) : rot(i);
        st16(rws, ti < nft ? img + (ft0 + ti) * 1024 : 0xfffff000u, ch);
      }
    }
    STAMP(14); CSTAMP(48);
    clear();
    slot = 0;
    bar();
    rd(A0, B0, 0, 0);
    ksplit_steps<C_NS, 10, 7, 3, 18>(A0, B0, A1, B1, slot, 0, 6, rd, mm1,
        [&](int n) { if (n == 2) wait_vm<0>(); },
        [&](int n) { if (n == 2 && w == 0 && lane == 0) set_flag(ws, WS_FZ + (j * 4 + (p >> 3)) * 128 + (p & 7) * 4, tag); });
    const unsigned fzo = WS_FZ + (j * 4 + ((lane & 7) >> 1)) * 128 + (lane & 1) * 16;
    const u32x4 fz = ld16(rws, fzo);
    ksplit_steps<C_NS, 10, 7, 3, 18>(A0, B0, A1, B1, slot, 6, 8, rd, mm1, nohook, nohook);
    ksplit_last<C_NS, 10, 7, 3>(A0, B0, A1, B1, slot, rd, mm1);
    if (!flags_ok(fz, tag)) { for (unsigned spins = 0;;) { if (flags_ok(ld16(rws, fzo), tag)) break; if (spin_fail(spins, ws)) break; } }
    STAMP(15); CSTAMP(49);
    put_cv(1);
    kreduce<7>(acc, zs, smem + C_SCR, w, lane);
#pragma unroll
    for (int i = 0; i < 7; ++i) {
      const LAS float* cv = (const LAS float*)(smem + T_CV) + 224;
      const f32x4 c1 = *(const LAS f32x4*)(cv + 16 * rot(i) + 4 * q), c2 = *(const LAS f32x4*)(cv + 112 + 16 * rot(i) + 4 * q);
      const f32x2 a = gelu2(rrstd * ((f32x2){zs[i][0], zs[i][1]} - rmean * (f32x2){c1[0], c1[1]}) + (f32x2){c2[0], c2[1]});
      const f32x2 c = gelu2(rrstd * ((f32x2){zs[i][2], zs[i][3]} - rmean * (f32x2){c1[2], c1[3]}) + (f32x2){c2[2], c2[3]});
      z1p[i] = (u32x2){pack2(a[0], a[1]), pack2(c[0], c[1])};
      asm volatile("" : "+v"(z1p[i].x), "+v"(z1p[i].y));
    }
    STAMP(16); CSTAMP(42);
  }
  {
    LANE_IDS
    f32x4 acc[7][2];
#pragma unroll
    for (int i = 0; i < 7; ++i) { acc[i][0] = (f32x4){0, 0, 0, 0}; acc[i][1] = (f32x4){0, 0, 0, 0}; }
    const unsigned wa = (unsigned)((2 * mh) * 2048 + tk * 128 + (((q + 4 * kh) ^ ((lane >> 1) & 7)) * 16));
    const unsigned xlo = 8192 + kh * 7168 + lane * 16 + mh * 4096, xhi = 8192 + kh * 7168 + lane * 16 - mh * 3072;
    ksplit_loop<D_NS, 32, 7, 2>(
        [&](bf16x8(&A)[7], bf16x8(&Bf)[2], int slot, int) { LAS char* sb = smem + slot * D_SLOT;
          Bf[0] = *(LAS bf16x8*)(sb + wa); A[0] = *(LAS bf16x8*)(sb + xlo); Bf[1] = *(LAS bf16x8*)(sb + wa + 2048);
          A[1] = *(LAS bf16x8*)(sb + xlo + 1024); A[2] = *(LAS bf16x8*)(sb + xlo + 2048);
#pragma unroll
          for (int i = 3; i < 7; ++i) A[i] = *(LAS bf16x8*)(sb + xhi + i * 1024); },
        [&](bf16x8(&A)[7], bf16x8(&Bf)[2], bool) {
#pragma unroll
          for (int i = 0; i < 7; ++i) { acc[i][0] = mfma(A[i], Bf[0], acc[i][0]); acc[i][1] = mfma(A[i], Bf[1], acc[i][1]); } });
    STAMP(17); CSTAMP(43);
    f32x4 g[7];
    kreduce<7>(acc, g, smem + D_SCR, w, lane);
    u32x2 pk[7];
#pragma unroll
    for (int i = 0; i < 7; ++i) {
      pk[i].x = pack2(bflo(z1p[i].x) * (g[i][0] + bias), bfhi(z1p[i].x) * (g[i][1] + bias));
      pk[i].y = pack2(bflo(z1p[i].y) * (g[i][2] + bias), bfhi(z1p[i].y) * (g[i][3] + bias));
    }
    const unsigned img = WS_P + (unsigned)p * 28 * 4096 + w * 1024;
#pragma unroll
    for (int i = 0; i < 8; i += 2) {
      const int i2 = i + 1 < 7 ? i + 1 : i;
      const u32x4 ch = swap_pair(pk[i], pk[i2]);
      const int ti = (q & 1) ? rot(i2) : rot(i), ft = ft0 + ti, kb = ft >> 1, qq = 2 * (ft & 1) + (q >> 1);
      st16(rws, ti < nft ? img + kb * 4096 + (qq * 16 + tk) * 16 : 0xfffff000u, ch);
    }
    wait_vm<0>(); bar();
    STAMP(18);
    if (w == 0) { if (lane == 0) set_flag(ws, WS_FP + p * 128 + j * 4, tag); wait_flags8(ws, WS_FP + p * 128, tag, lane); }
    bar();
    STAMP(19);
  }
  {
    LANE_IDS
    f32x4 acc[5][2];
#pragma unroll
    for (int i = 0; i < 5; ++i) { acc[i][0] = (f32x4){0, 0, 0, 0}; acc[i][1] = (f32x4){0, 0, 0, 0}; }
    const unsigned wa = kh * 5120 + lane * 16, xa = 10240 + (kh * 4 + 2 * mh) * 1024 + lane * 16;
    ksplit_loop<E_NS, 14, 5, 2>(
        [&](bf16x8(&A)[5], bf16x8(&Bf)[2], int slot, int) { LAS char* sb = smem + slot * E_SLOT;
          A[0] = *(LAS bf16x8*)(sb + wa); Bf[0] = *(LAS bf16x8*)(sb + xa); Bf[1] = *(LAS bf16x8*)(sb + xa + 1024);
#pragma unroll
          for (int i = 1; i < 5; ++i) A[i] = *(LAS bf16x8*)(sb + wa + i * 1024); },
        [&](bf16x8(&A)[5], bf16x8(&Bf)[2], bool last) {
          if (!(last && kh)) {
#pragma unroll
            for (int i = 0; i < 5; ++i) { acc[i][0] = mfma(A[i], Bf[0], acc[i][0]); acc[i][1] = mfma(A[i], Bf[1], acc[i][1]); } } });
    STAMP(20);
    f32x4 o[5];
    kreduce<5>(acc, o, smem + E_SCR, w, lane);
    u32x2 pk[5];
#pragma unroll
    for (int i = 0; i < 5; ++i) {
      const f32x4 gb = *(const LAS f32x4*)(smem + T_MOD + 960 + (80 + 16 * i + 4 * q) * 4);
      pk[i].x = pack2(bflo(x1p[i].x) + gb[0] * o[i][0], bfhi(x1p[i].x) + gb[1] * o[i][1]);
      pk[i].y = pack2(bflo(x1p[i].y) + gb[2] * o[i][2], bfhi(x1p[i].y) + gb[3] * o[i][3]);
    }
    bf16_t* orow = P.out + (size_t)token * D + 80 * j;
#pragma unroll
    for (int i = 0; i < 6; i += 2) {
      const int i2 = i + 1 < 5 ? i + 1 : i;
      const u32x4 ch = swap_pair(pk[i], pk[i2]);
      *(u32x4*)(orow + 16 * ((q & 1) ? i2 : i) + 8 * (q >> 1)) = ch;
    }
    STAMP(21);
  }
  STAMP_FLUSH();
}

__global__ __launch_bounds__(NT, 2) void fused_block(Params P) {
  extern __shared__ __attribute__((aligned(16))) char smem_[];
  LAS char* smem = (LAS char*)smem_;
  const int tid = threadIdx.x, lane = tid & 63, wid = __builtin_amdgcn_readfirstlane(tid >> 6), b = blockIdx.x;
  const int xcd = b & 7, idx = b >> 3, p = xcd * 4 + (idx >> 3), j = idx & 7;
  if (wid >= 4) loader_main(P, (unsigned)(size_t)smem, smem, wid - 4, lane, b, p, j, xcd * 4);
  else consumer_main(P, smem, wid, lane, b, p, j);
}

__global__ __launch_bounds__(256) void k_spin(char* ws, int ticks) {
  const unsigned long long t0 = __builtin_amdgcn_s_memrealtime();
  float a = threadIdx.x, b = 1.0001f;
  while (__builtin_amdgcn_s_memrealtime() - t0 < (unsigned long long)ticks) { for (int i = 0; i < 64; ++i) a = a * b + 0.5f; }
  if (a == 123.456f) ((float*)(ws + WS_TMO))[1] = a;
}
__global__ __launch_bounds__(512) void k_stream(char* ws, unsigned bytes_per_block) {
  const char* base = ws + (64u << 20) + (size_t)blockIdx.x * bytes_per_block; unsigned acc = 0;
#pragma unroll 8
  for (unsigned o = threadIdx.x * 16; o < bytes_per_block; o += 512 * 16) { const u32x4 v = *(const u32x4*)(base + o); acc ^= v.x ^ v.w; }
  if (acc == 0x9e3779b9u) ((unsigned*)(ws + WS_TMO))[1] = acc;
}

extern "C" void kernel_launch(void* const* d_in, const int* in_sizes, int n_in, void* d_out, int out_size, void* d_ws, size_t ws_size, hipStream_t stream) {
  Params P;
  P.x = (const bf16_t*)d_in[0]; P.wsr = (const bf16_t*)d_in[1]; P.wsi = (const bf16_t*)d_in[2];
  P.wdr = (const bf16_t*)d_in[3]; P.wdi = (const bf16_t*)d_in[4]; P.win = (const bf16_t*)d_in[6];
  P.wsp = (const bf16_t*)d_in[7]; P.bsp = (const bf16_t*)d_in[8]; P.wout = (const bf16_t*)d_in[9];
  P.c = (const bf16_t*)d_in[10]; P.wmod = (const bf16_t*)d_in[11];
  P.out = (bf16_t*)d_out; P.ws = (char*)d_ws;
  (void)hipFuncSetAttribute((const void*)fused_block, hipFuncAttributeMaxDynamicSharedMemorySize, LDS_BYTES);
  k_spin<<<256, 256, 0, stream>>>(P.ws, 10);
  fused_block<<<256, NT, LDS_BYTES, stream>>>(P);
}
```

```cpp
#include <hip/hip_runtime.h>
#include <stdint.h>
#include <type_traits>

typedef unsigned short bf16_t;
typedef short bf16x8 __attribute__((ext_vector_type(8)));
typedef short s16x4 __attribute__((ext_vector_type(4)));
typedef float f32x4 __attribute__((ext_vector_type(4)));
typedef float f32x2 __attribute__((ext_vector_type(2)));
typedef unsigned u32x4 __attribute__((ext_vector_type(4)));
typedef unsigned u32x2 __attribute__((ext_vector_type(2)));
typedef __bf16 bf2_t __attribute__((ext_vector_type(2)));
#define LAS __attribute__((address_space(3)))
#define GAS __attribute__((address_space(1)))
typedef GAS unsigned gu32;
typedef GAS unsigned long long gu64;
#define RLX_AGENT __ATOMIC_RELAXED, __HIP_MEMORY_SCOPE_AGENT
#define DEV __device__ __forceinline__

constexpr int S = 2048, D = 640, F = 1728, FH = 864;
constexpr int NT = 512;

constexpr unsigned WS_EPOCH = 0;
constexpr unsigned WS_TMO   = 256;
constexpr unsigned WS_FN = 4096, WS_F2 = 8192, WS_FU = 12288, WS_FX = 16384, WS_FP = 20480, WS_FZ = 24576;
constexpr unsigned WS_MOD  = 65536;
constexpr unsigned WS_MODB = WS_MOD + 16384;

constexpr unsigned WS_XN   = 131072;
constexpr unsigned WS_WIN  = WS_XN + 64 * 40 * 1024;
constexpr unsigned WS_WOUT = WS_WIN + 20 * 109 * 1024;
constexpr unsigned WS_UV   = WS_WOUT + 27 * 40 * 1024;
constexpr unsigned WS_X1P  = WS_UV + 32 * 2 * 20 * 4096;
constexpr unsigned WS_ST1  = WS_X1P + 32 * 20 * 4096;
constexpr unsigned WS_Z2N  = WS_ST1 + 2048 * 64;
constexpr unsigned WS_ST2  = WS_Z2N + 64 * 55 * 1024;
constexpr unsigned WS_P    = WS_ST2 + 2048 * 128;
constexpr unsigned WS_END  = WS_P + 32 * 28 * 4096;

struct Params {
  const bf16_t *x, *wsr, *wsi, *wdr, *wdi, *win, *wsp, *bsp, *wout, *c, *wmod;
  bf16_t* out;
  char* ws;
};

#define STAMP_DECL
#define STAMP(i) do {} while (0)
#define STAMP_FLUSH() do {} while (0)
#define CSTAMP(i) do {} while (0)

template <int N> DEV void wait_vm() { asm volatile("s_waitcnt vmcnt(%0)" ::"n"(N) : "memory"); }
DEV void wait_lgkm0() { asm volatile("s_waitcnt lgkmcnt(0)" ::: "memory"); }
DEV void bar() { asm volatile("" ::: "memory"); __builtin_amdgcn_s_barrier(); asm volatile("" ::: "memory"); }
DEV void sched_fence() { __builtin_amdgcn_sched_barrier(0); }
template <int I, int N, class Fn> DEV void static_for(Fn&& f) { if constexpr (I < N) { f(std::integral_constant<int, I>{}); static_for<I + 1, N>(f); } }

DEV float bflo(unsigned v) { return __uint_as_float(v << 16); }
DEV float bfhi(unsigned v) { return __uint_as_float(v & 0xffff0000u); }
DEV unsigned pack2(float lo, float hi) { bf2_t v; v[0] = (__bf16)lo; v[1] = (__bf16)hi; return __builtin_bit_cast(unsigned, v); }
DEV float rbf(float f) { return bflo(pack2(f, f)); }
DEV float dot2(unsigned a, unsigned b, float c) { return __builtin_amdgcn_fdot2_f32_bf16(__builtin_bit_cast(bf2_t, a), __builtin_bit_cast(bf2_t, b), c, false); }
DEV f32x4 mfma(bf16x8 a, bf16x8 b, f32x4 c) { return __builtin_amdgcn_mfma_f32_16x16x32_bf16(a, b, c, 0, 0, 0); }

DEV u32x4 mkrs(const void* p, unsigned bytes) {
  const unsigned long long a = (unsigned long long)p;
  u32x4 r; r.x = (unsigned)a; r.y = (unsigned)(a >> 32) & 0xffffu; r.z = bytes; r.w = 0x00020000u; return r;
}
DEV __amdgpu_buffer_rsrc_t mkrsrc(const void* p, unsigned bytes) { return __builtin_amdgcn_make_buffer_rsrc((void*)p, 0, bytes, 0x00020000); }
template <bool SC1> DEV void dma(u32x4 r, unsigned lds, unsigned voff, unsigned soff) {
  if constexpr (SC1) asm volatile("s_mov_b32 m0, %0\n\ts_nop 0\n\tbuffer_load_dwordx4 %1, %2, %3 offen sc1 lds" ::"s"(lds), "v"(voff), "s"(r), "s"(soff) : "memory");
  else asm volatile("s_mov_b32 m0, %0\n\ts_nop 0\n\tbuffer_load_dwordx4 %1, %2, %3 offen lds" ::"s"(lds), "v"(voff), "s"(r), "s"(soff) : "memory");
}
template <bool SC1> DEV void dma_half(u32x4 r, unsigned lds, unsigned voff, unsigned soff) {
  if constexpr (SC1) asm volatile("s_mov_b32 m0, %0\n\ts_mov_b32 exec_hi, 0\n\tbuffer_load_dwordx4 %1, %2, %3 offen sc1 lds\n\ts_mov_b32 exec_hi, -1" ::"s"(lds), "v"(voff), "s"(r), "s"(soff) : "memory");
  else asm volatile("s_mov_b32 m0, %0\n\ts_mov_b32 exec_hi, 0\n\tbuffer_load_dwordx4 %1, %2, %3 offen lds\n\ts_mov_b32 exec_hi, -1" ::"s"(lds), "v"(voff), "s"(r), "s"(soff) : "memory");
}
constexpr int SC1 = 16;
DEV void st16(__amdgpu_buffer_rsrc_t r, unsigned off, u32x4 v) { __builtin_amdgcn_raw_buffer_store_b128(v, r, off, 0, SC1); }
DEV u32x4 ld16(__amdgpu_buffer_rsrc_t r, unsigned off) { return __builtin_amdgcn_raw_buffer_load_b128(r, off, 0, SC1); }
DEV f32x4 ld16f(__amdgpu_buffer_rsrc_t r, unsigned off) { return __builtin_bit_cast(f32x4, ld16(r, off)); }

template <int CTRL> DEV float dpp_add(float v) { return v + __builtin_bit_cast(float, __builtin_amdgcn_update_dpp(0, __builtin_bit_cast(int, v), CTRL, 0xf, 0xf, true)); }
DEV float xsum_16(float v) { v = dpp_add<0xB1>(v); v = dpp_add<0x4E>(v); v = dpp_add<0x141>(v); return dpp_add<0x140>(v); }
DEV float xsum_r16(float v) { auto r = __builtin_amdgcn_permlane16_swap(__float_as_uint(v), __float_as_uint(v), false, false); return __uint_as_float(r[0]) + __uint_as_float(r[1]); }
DEV float xsum_r32(float v) { auto r = __builtin_amdgcn_permlane32_swap(__float_as_uint(v), __float_as_uint(v), false, false); return __uint_as_float(r[0]) + __uint_as_float(r[1]); }
DEV float xsum_q(float v) { return xsum_r32(xsum_r16(v)); }
DEV float gelu_tanh(float x) {
  const float k1 = -2.0f * 0.7978845608028654f * 1.4426950408889634f;
  const float t = x * __builtin_fmaf(x * x, 0.044715f * k1, k1);
  return x * __builtin_amdgcn_rcpf(1.0f + __builtin_amdgcn_exp2f(t));
}
DEV f32x2 gelu2(f32x2 x) {
  const float k1 = -2.0f * 0.7978845608028654f * 1.4426950408889634f;
  const f32x2 t = x * ((x * x) * (0.044715f * k1) + k1);
  const f32x2 d = (f32x2){__builtin_amdgcn_exp2f(t[0]), __builtin_amdgcn_exp2f(t[1])} + 1.0f;
  return x * (f32x2){__builtin_amdgcn_rcpf(d[0]), __builtin_amdgcn_rcpf(d[1])};
}
DEV u32x4 swap_pair(u32x2 a, u32x2 b) {
  auto r0 = __builtin_amdgcn_permlane16_swap(a.x, b.x, false, false);
  auto r1 = __builtin_amdgcn_permlane16_swap(a.y, b.y, false, false);
  return (u32x4){r0[0], r1[0], r0[1], r1[1]};
}

DEV bool spin_fail(unsigned& spins, char* ws) {
  __builtin_amdgcn_s_sleep(1);
  if (++spins > (1u << 17)) { __hip_atomic_store((gu32*)(ws + WS_TMO), 1u, RLX_AGENT); return true; }
  return false;
}
DEV void set_flag(char* ws, unsigned off, unsigned tag) { __hip_atomic_store((gu32*)(ws + off), tag, RLX_AGENT); }
DEV void wait_flags8(char* ws, unsigned off, unsigned tag, int lane) {
  for (unsigned spins = 0;;) {
    const unsigned v = lane < 8 ? __hip_atomic_load((gu32*)(ws + off) + lane, RLX_AGENT) : tag;
    if (__all(v == tag)) break;
    if (spin_fail(spins, ws)) break;
  }
}
DEV bool flags_ok(u32x4 v, unsigned tag) { return __all(v.x == tag && v.y == tag && v.z == tag && v.w == tag); }
DEV u32x4 peek256(__amdgpu_buffer_rsrc_t rf, unsigned off, int lane) { return ld16(rf, off + (lane >> 1) * 128 + (lane & 1) * 16); }
DEV void wait256(__amdgpu_buffer_rsrc_t rf, char* ws, unsigned off, unsigned tag, int lane) {
  for (unsigned spins = 0;;) { if (flags_ok(peek256(rf, off, lane), tag)) break; if (spin_fail(spins, ws)) break; }
}

DEV void wait_panels(__amdgpu_buffer_rsrc_t rf, char* ws, unsigned off, int first, int count, unsigned tag, int lane) {
  const unsigned o = lane < 2 * count ? off + (((lane >> 1) + first) & 31) * 128 + (lane & 1) * 16 : off + (first & 31) * 128;
  for (unsigned spins = 0;;) { if (flags_ok(ld16(rf, o), tag)) break; if (spin_fail(spins, ws)) break; }
}

template <int NS, int S_, int CW, int CX, class IW, class IX>
DEV void loader_run(IW&& issueW, IX&& issueX) {
  static_for<0, NS>([&](auto ic) { issueX(decltype(ic)::value, decltype(ic)::value); });
  wait_vm<(NS - 1) * CX>(); bar();
  static_for<0, NS - 2>([&](auto ic) { constexpr int n = decltype(ic)::value;
    wait_vm<(NS - 2) * CX + n * CW>(); bar(); issueW(n + NS, n); issueX(n + NS, n); });
  int slot = NS - 2;
#pragma unroll 1
  for (int n = NS - 2; n < S_ - NS; ++n) {
    wait_vm<(NS - 2) * (CW + CX)>(); bar();
    issueW(n + NS, slot); issueX(n + NS, slot);
    slot = slot + 1 == NS ? 0 : slot + 1;
  }
  static_for<0, NS - 1>([&](auto ic) { constexpr int r = NS - 2 - decltype(ic)::value; wait_vm<r*(CW + CX)>(); bar(); });
}

template <int NS, int S_, int NA, int NB, int NM, class Rd, class Mm, class Hook, class Post>
DEV void ksplit_steps(bf16x8 (&A0)[NA], bf16x8 (&B0)[NB], bf16x8 (&A1)[NA], bf16x8 (&B1)[NB], int& slot, int n0, int n1, Rd&& rd, Mm&& mm, Hook&& hook, Post&& post) {
  constexpr int NR = NA + NB;
#define KS_ILV() do { _Pragma("unroll") for (int z = 0; z < NR; ++z) { __builtin_amdgcn_sched_group_barrier(0x8, 1, 0); __builtin_amdgcn_sched_group_barrier(0x100, 1, 0); } __builtin_amdgcn_sched_group_barrier(0x8, NM - NR, 0); } while (0)
#pragma unroll 1
  for (int n = n0; n < n1; n += 2) {
    const int s1 = slot + 1 == NS ? 0 : slot + 1;
    hook(n); wait_lgkm0(); bar(); post(n); sched_fence();
    rd(A1, B1, s1, n + 1); mm(A0, B0, false); KS_ILV(); sched_fence();
    const int s2 = s1 + 1 == NS ? 0 : s1 + 1;
    hook(n + 1); wait_lgkm0(); bar(); post(n + 1); sched_fence();
    rd(A0, B0, s2, n + 2); mm(A1, B1, false); KS_ILV(); sched_fence();
    slot = s2;
  }
}
template <int NS, int S_, int NA, int NB, class Rd, class Mm>
DEV void ksplit_last(bf16x8 (&A0)[NA], bf16x8 (&B0)[NB], bf16x8 (&A1)[NA], bf16x8 (&B1)[NB], int& slot, Rd&& rd, Mm&& mm) {
  const int s1 = slot + 1 == NS ? 0 : slot + 1;
  wait_lgkm0(); bar(); sched_fence();
  rd(A1, B1, s1, S_ - 1); mm(A0, B0, false); mm(A1, B1, true);
  slot = s1;
}
template <int NS, int S_, int NA, int NB, class Rd, class Mm>
DEV void ksplit_loop(Rd&& rd, Mm&& mm) {
  static_assert((S_ & 1) == 0, "even step count");
  bf16x8 A0[NA], B0[NB], A1[NA], B1[NB];
  bar();
  rd(A0, B0, 0, 0);
  int slot = 0;
  ksplit_steps<NS, S_, NA, NB, NA * NB>(A0, B0, A1, B1, slot, 0, S_ - 2, rd, mm, [](int) {}, [](int) {});
  ksplit_last<NS, S_, NA, NB>(A0, B0, A1, B1, slot, rd, mm);
}
template <int N>
DEV void kreduce(f32x4 (&acc)[N][2], f32x4 (&res)[N], LAS char* scratch, int w, int lane) {
  const int kh = w & 1;
  LAS char* mine = scratch + w * (N * 1024) + lane * 16;
  LAS char* other = scratch + (w ^ 1) * (N * 1024) + lane * 16;
  if (kh) {
#pragma unroll
    for (int i = 0; i < N; ++i) { *(LAS f32x4*)(mine + i * 1024) = acc[i][0]; res[i] = acc[i][1]; }
  } else {
#pragma unroll
    for (int i = 0; i < N; ++i) { *(LAS f32x4*)(mine + i * 1024) = acc[i][1]; res[i] = acc[i][0]; }
  }
  wait_lgkm0(); bar();
#pragma unroll
  for (int i = 0; i < N; ++i) res[i] += *(LAS f32x4*)(other + i * 1024);
}

constexpr int A_NS = 5, A_SLOT = 26 * 1024;
constexpr int P0_T = 130 * 1024, P0_PT = 140 * 1024, P0_RED = 156 * 1024;
constexpr int B_NS = 6, B_SLOT = 18 * 1024, B_SCR = 36 * 1024;
constexpr int C_NS = 5, C_X = 0, C_W = 82 * 1024, C_WSLOT = 14 * 1024;
constexpr int C_SCR = C_W + 2 * C_WSLOT;
constexpr int D_NS = 5, D_SLOT = 22 * 1024, D_SCR = 110 * 1024;
constexpr int E_NS = 6, E_SLOT = 18 * 1024, E_SCR = 110 * 1024;
constexpr int T_MODB = 152 * 1024;
constexpr int T_CV = T_MODB + 3072 + 64;
constexpr int T_MOD = 158 * 1024;
constexpr int LDS_BYTES = 160 * 1024;
static_assert(T_CV + 896 <= T_MOD && C_W + C_NS * C_WSLOT <= T_MODB && D_SCR + 28 * 1024 <= T_MODB && E_SCR + 20 * 1024 <= T_MODB, "LDS map");

DEV void loader_main(const Params& P, const unsigned lds0, LAS char* smem, const int lw, const int lane_, const int b, const int p, const int j, const int krot) {
  STAMP_DECL STAMP(0);
  char* const ws = P.ws;
  const int ft0 = 27 * (j >> 2) + 7 * (j & 3);
#define LLANE int lane = lane_; asm volatile("" : "+v"(lane)); const unsigned vx = lane * 16; (void)vx;
#define LV4K const unsigned v4k0 = (unsigned)(lane >> 3) * 4096u + (unsigned)(((lane & 7) ^ (lane >> 4)) * 16), v4k1 = (unsigned)(8 + (lane >> 3)) * 4096u + (unsigned)(((lane & 7) ^ (4 + (lane >> 4))) * 16);
  {
    LLANE LV4K
    const u32x4 rre = mkrs(P.wsr, S * S * 2), rim = mkrs(P.wsi, S * S * 2), rx = mkrs(ws + WS_XN, S * D * 2);
    const unsigned wrow = (unsigned)(64 * p + 16 * lw) * 4096u;
    auto issueW = [&](int n, int slot) {
      const unsigned sb = lds0 + slot * A_SLOT + lw * 4096, sw = wrow + ((n + krot) & 31) * 128;
      dma<false>(rre, sb, v4k0, sw); dma<false>(rre, sb + 1024, v4k1, sw); dma<false>(rim, sb + 2048, v4k0, sw); dma<false>(rim, sb + 3072, v4k1, sw);
    };
    const int xi0 = lw * 2, xi1 = lw * 2 + 1, xi2 = 8 + (lw >> 1);
    const unsigned xo0 = (unsigned)(((xi0 >= 5) * 40 + 5 * j + xi0 - 5 * (xi0 >= 5)) * 1024), xo1 = (unsigned)(((xi1 >= 5) * 40 + 5 * j + xi1 - 5 * (xi1 >= 5)) * 1024);
    const unsigned xo2 = (unsigned)((40 + 5 * j + xi2 - 5) * 1024 + (lw & 1) * 512);
    auto issueX = [&](int n, int slot) {
      const unsigned sb = lds0 + slot * A_SLOT + 16384, so = ((n + krot) & 31) * 80 * 1024;
      dma<true>(rx, sb + xi0 * 1024, vx, so + xo0); dma<true>(rx, sb + xi1 * 1024, vx, so + xo1); dma_half<true>(rx, sb + xi2 * 1024 + (lw & 1) * 512, vx, so + xo2);
    };
    issueW(0, 0); issueW(1, 1);
    bar();
    bar();
    STAMP(30);
    bar();
    STAMP(33);
    __builtin_amdgcn_s_setprio(3);
    issueX(0, 0); issueX(1, 1);
    static_for<2, A_NS>([&](auto ic) { issueW(decltype(ic)::value, decltype(ic)::value); issueX(decltype(ic)::value, decltype(ic)::value); });
    const int idx = b >> 3, unit0 = (idx & 3) + 4 * ((b & 7) + 8 * (idx >> 2));
    const bool domod = unit0 < 240; const int unit = domod ? unit0 : 0;
    u32x4 wm[5]; unsigned cs[5];
#pragma unroll
    for (int i = 0; i < 5; ++i) { const int row = lw * 160 + i * 32 + (lane >> 1);
      const bf16_t* pw = P.wmod + (size_t)row * (6 * D) + unit * 16 + (lane & 1) * 8; const bf16_t* pc = P.c + row;
      asm volatile("global_load_dwordx4 %0, %1, off" : "=v"(wm[i]) : "v"(pw) : "memory");
      asm volatile("global_load_ushort %0, %1, off" : "=v"(cs[i]) : "v"(pc) : "memory"); }
    STAMP(34);
    wait_vm<3 + 3 * 7 + 10>();
    bar();
    wait_vm<3 * 7 + 10>(); bar(); issueW(A_NS, 0); issueX(A_NS, 0);
    wait_vm<2 * 7 + 10 + 7>(); bar(); issueW(A_NS + 1, 1); issueX(A_NS + 1, 1);
    wait_vm<14>();
    asm volatile("" : "+v"(wm[0]), "+v"(wm[1]), "+v"(wm[2]), "+v"(wm[3]), "+v"(wm[4]), "+v"(cs[0]), "+v"(cs[1]), "+v"(cs[2]), "+v"(cs[3]), "+v"(cs[4]));
    bar(); issueW(A_NS + 2, 2); issueX(A_NS + 2, 2);
    {
      float a[8] = {0, 0, 0, 0, 0, 0, 0, 0};
#pragma unroll
      for (int i = 0; i < 5; ++i) { const float x = bflo(cs[i]); const float s = rbf(x * __builtin_amdgcn_rcpf(1.0f + __expf(-x)));
        a[0] += s * bflo(wm[i].x); a[1] += s * bfhi(wm[i].x); a[2] += s * bflo(wm[i].y); a[3] += s * bfhi(wm[i].y);
        a[4] += s * bflo(wm[i].z); a[5] += s * bfhi(wm[i].z); a[6] += s * bflo(wm[i].w); a[7] += s * bfhi(wm[i].w); }
#pragma unroll
      for (int e = 0; e < 8; ++e) { float v = dpp_add<0x4E>(a[e]);
        v = dpp_add<0x124>(v); v = dpp_add<0x128>(v);
        a[e] = xsum_q(v); }
      if (lane < 2) {
#pragma unroll
        for (int e = 0; e < 8; ++e) ((LAS float*)(smem + P0_RED))[lw * 16 + lane * 8 + e] = a[e];
      }
      wait_lgkm0();
    }
    wait_vm<(A_NS - 2) * 7>(); bar(); issueW(A_NS + 3, 3); issueX(A_NS + 3, 3);
    if (lw == 0 && domod && lane < 16) {
      const LAS float* red = (const LAS float*)(smem + P0_RED);
      float s = rbf((red[lane] + red[16 + lane]) + (red[32 + lane] + red[48 + lane]));
      const int n = unit * 16 + lane, seg = n / D;
      if (seg == 1 || seg == 4) s = rbf(1.0f + s);
      __hip_atomic_store((gu32*)(ws + WS_MOD) + n, __float_as_uint(s), RLX_AGENT);
      const unsigned hi = __float_as_uint(s) >> 16, nb = (unsigned)__builtin_amdgcn_update_dpp(0, (int)hi, 0xB1, 0xf, 0xf, true);
      if ((seg == 3 || seg == 4) && !(lane & 1)) __hip_atomic_store((gu32*)(ws + WS_MODB) + (((seg == 4 ? 0 : D) + n - seg * D) >> 1), hi | (nb << 16), RLX_AGENT);
    }
    static_assert(A_NS == 5, "the start-up schedule above is written for five slots");
    const u32x4 rmod = mkrs(ws + WS_MOD, 3840 * 4);
    const int tl = lane / 20, tc = lane - 20 * tl;
    const unsigned tv0 = lane < 60 ? (unsigned)((tl * D + 80 * j) * 4 + tc * 16) : 0x7ffffff0u;
    const unsigned tv1 = lane < 40 ? (unsigned)(((4 + tl) * D + 80 * j) * 4 + tc * 16) : 0x7ffffff0u;
    int slot = 4;
#pragma unroll 1
    for (int n = 4; n < 32 - A_NS; ++n) {
      wait_vm<(A_NS - 2) * 7>(); bar();
      issueW(n + A_NS, slot); issueX(n + A_NS, slot);
      if (n == 20 && lw == 0) { dma<true>(rmod, lds0 + T_MOD, tv0, 0); dma<true>(rmod, lds0 + T_MOD + 960, tv1, 0); }
      slot = slot + 1 == A_NS ? 0 : slot + 1;
    }
    static_for<0, A_NS - 1>([&](auto ic) { constexpr int r = A_NS - 2 - decltype(ic)::value; wait_vm<r * 7>(); bar(); });
    bar();
  }
  {
    LLANE
    const u32x4 rre = mkrs(P.wdr, D * D * 2), rim = mkrs(P.wdi, D * D * 2), rx = mkrs(ws + WS_UV, 32 * 2 * 20 * 4096);
    const unsigned v0 = (unsigned)(lane >> 3) * 1280u + (unsigned)(((lane & 7) ^ (lane >> 4)) * 16);
    const unsigned v1 = (unsigned)(lane >> 3) * 1280u + (unsigned)(((lane & 7) ^ (4 + (lane >> 4))) * 16);
    const unsigned vh = (unsigned)(lane >> 3) * 1280u + (unsigned)(((lane & 7) ^ ((4 * (lw >> 1) + 2 * (lw & 1) + (lane >> 4)) & 7)) * 16);
    const unsigned wb = (unsigned)(80 * j) * 1280u;
    auto issueW = [&](int n, int slot) {
      const unsigned sb = lds0 + slot * B_SLOT; const int half = n >= 10; const unsigned sw = wb + (n - 10 * half) * 128;
      const u32x4 rw = half ? rim : rre;
      dma<false>(rw, sb + (2 * lw) * 1024, v0, sw + (unsigned)(16 * lw) * 1280u);
      dma<false>(rw, sb + (2 * lw + 1) * 1024, v1, sw + (unsigned)(16 * lw + 8) * 1280u);
      dma_half<false>(rw, sb + (8 + (lw >> 1)) * 1024 + (lw & 1) * 512, vh, sw + (unsigned)(64 + 8 * (lw >> 1) + 4 * (lw & 1)) * 1280u);
    };
    auto issueX = [&](int n, int slot) {
      const unsigned sb = lds0 + slot * B_SLOT + 10240 + lw * 2048, sx = (unsigned)(p * 40 + 2 * n) * 4096u + lw * 2048;
      dma<true>(rx, sb, vx, sx); dma<true>(rx, sb + 1024, vx, sx + 1024);
    };
    __builtin_amdgcn_s_setprio(0);
    bar();
    static_for<0, B_NS>([&](auto ic) { issueW(decltype(ic)::value, decltype(ic)::value); });
    bar();
    __builtin_amdgcn_s_setprio(3);
    loader_run<B_NS, 20, 3, 2>(issueW, issueX);
    bar();
  }
  {
    LLANE
    const u32x4 rw = mkrs(ws + WS_WIN, 20 * 109 * 1024), rx = mkrs(ws + WS_X1P, 32 * 20 * 4096), rmb = mkrs(ws + WS_MODB, 2560);
    unsigned wo[4], wl[4];
#pragma unroll
    for (int u = 0; u < 3; ++u) { const int i = lw * 3 + u, kk = i >= 7; wo[u] = (unsigned)((kk * 109 + i - 7 * kk) * 1024); wl[u] = i * 1024; }
    { const int i = 12 + (lw >> 1); wo[3] = (unsigned)((109 + i - 7) * 1024 + (lw & 1) * 512); wl[3] = i * 1024 + (lw & 1) * 512; }
    auto issueW = [&](int n, int slot) {
      const unsigned sb = lds0 + C_W + slot * C_WSLOT; const int z1 = n >= 10;
      const unsigned so = (unsigned)((2 * (n - 10 * z1) * 109 + (z1 ? 0 : 54) + ft0) * 1024);
      dma<true>(rw, sb + wl[0], vx, so + wo[0]); dma<true>(rw, sb + wl[1], vx, so + wo[1]); dma<true>(rw, sb + wl[2], vx, so + wo[2]); dma_half<true>(rw, sb + wl[3], vx, so + wo[3]);
    };
    auto issueX = [&](int n) {
      const unsigned o = (unsigned)(2 * n) * 4096u + lw * 2048, sx = (unsigned)p * 20 * 4096u + o;
      dma<true>(rx, lds0 + C_X + o, vx, sx); dma<true>(rx, lds0 + C_X + o + 1024, vx, sx + 1024);
    };
    __builtin_amdgcn_s_setprio(0);
    bar();
    if (lw < 3) dma<true>(rmb, lds0 + T_MODB + lw * 1024, vx + lw * 1024, 0);
    static_for<0, C_NS>([&](auto ic) { issueW(decltype(ic)::value, decltype(ic)::value); });
    bar();
    __builtin_amdgcn_s_setprio(3);
    static_for<0, C_NS>([&](auto ic) { issueX(decltype(ic)::value); });
    wait_vm<(C_NS - 1) * 2>(); bar();
    static_for<0, 9>([&](auto ic) {
      constexpr int n = decltype(ic)::value, need = n + 1;
      constexpr int last = n + C_NS - 1 < 11 ? n + C_NS - 1 : 11;
      constexpr int cnt = [] { int c = 0;
        if (need < C_NS) { c += (C_NS - 1 - need) * 2; for (int s = C_NS; s <= last; ++s) c += (s < 10 ? 6 : 4); }
        else { for (int s = need + 1; s <= last; ++s) c += (s < 10 ? 6 : 4); }
        return c; }();
      wait_vm<cnt>(); bar();
      if constexpr (n + C_NS < 12) { issueW(n + C_NS, n % C_NS); if constexpr (n + C_NS < 10) issueX(n + C_NS); }
    });
    bar();
    bar();
    issueW(12, 2); issueW(13, 3); issueW(14, 4);
    wait_vm<16>(); bar();
    static_for<10, 19>([&](auto ic) {
      constexpr int n = decltype(ic)::value;
      constexpr int last = n + 4 < 19 ? n + 4 : 19;
      wait_vm<(last - (n + 1)) * 4>(); bar();
      if constexpr (n + C_NS < 20) issueW(n + C_NS, n % C_NS);
    });
    bar();
  }
  {
    LLANE LV4K
    const u32x4 rw = mkrs(P.wsp, S * S * 2), rx = mkrs(ws + WS_Z2N, 64 * 55 * 1024);
    const unsigned wrow = (unsigned)(64 * p + 16 * lw) * 4096u;
    unsigned xo[4], xl[4];
#pragma unroll
    for (int u = 0; u < 3; ++u) { const int i = lw * 3 + u, kk = i >= 7; xo[u] = (unsigned)((kk * 55 + ft0 + i - 7 * kk) * 1024); xl[u] = 8192 + i * 1024; }
    { const int i = 12 + (lw >> 1); xo[3] = (unsigned)((55 + ft0 + i - 7) * 1024 + (lw & 1) * 512); xl[3] = 8192 + i * 1024 + (lw & 1) * 512; }
    auto issueW = [&](int n, int slot) {
      const unsigned sb = lds0 + slot * D_SLOT + lw * 2048, sw = wrow + ((n + krot) & 31) * 128;
      dma<false>(rw, sb, v4k0, sw); dma<false>(rw, sb + 1024, v4k1, sw);
    };
    auto issueX = [&](int n, int slot) {
      const unsigned sb = lds0 + slot * D_SLOT, so = ((n + krot) & 31) * 110 * 1024;
      dma<true>(rx, sb + xl[0], vx, so + xo[0]); dma<true>(rx, sb + xl[1], vx, so + xo[1]); dma<true>(rx, sb + xl[2], vx, so + xo[2]); dma_half<true>(rx, sb + xl[3], vx, so + xo[3]);
    };
    static_for<0, D_NS>([&](auto ic) { issueW(decltype(ic)::value, decltype(ic)::value); });
    loader_run<D_NS, 32, 2, 4>(issueW, issueX);
    bar();
  }
  {
    LLANE
    const u32x4 rw = mkrs(ws + WS_WOUT, 27 * 40 * 1024), rx = mkrs(ws + WS_P, 32 * 28 * 4096);
    const int i0 = lw * 2, i1 = lw * 2 + 1, i2 = 8 + (lw >> 1);
    const unsigned o0 = (unsigned)(((i0 >= 5) * 40 + 5 * j + i0 - 5 * (i0 >= 5)) * 1024), o1 = (unsigned)(((i1 >= 5) * 40 + 5 * j + i1 - 5 * (i1 >= 5)) * 1024);
    const unsigned o2 = (unsigned)((40 + 5 * j + i2 - 5) * 1024 + (lw & 1) * 512);
    auto issueW = [&](int n, int slot) {
      const unsigned sb = lds0 + slot * E_SLOT, so = (unsigned)n * 80 * 1024;
      dma<true>(rw, sb + i0 * 1024, vx, so + o0); dma<true>(rw, sb + i1 * 1024, vx, so + o1); dma_half<true>(rw, sb + i2 * 1024 + (lw & 1) * 512, vx, so + o2);
    };
    auto issueX = [&](int n, int slot) {
      const unsigned sb = lds0 + slot * E_SLOT + 10240 + lw * 2048, sx = (unsigned)(p * 28 + 2 * n) * 4096u + lw * 2048;
      dma<true>(rx, sb, vx, sx); dma<true>(rx, sb + 1024, vx, sx + 1024);
    };
    __builtin_amdgcn_s_setprio(0);
    bar();
    static_for<0, E_NS>([&](auto ic) { issueW(decltype(ic)::value, decltype(ic)::value); });
    bar();
    __builtin_amdgcn_s_setprio(3);
    loader_run<E_NS, 14, 3, 2>(issueW, issueX);
    bar();
  }
  STAMP_FLUSH();
}

DEV void consumer_main(const Params& P, LAS char* smem, const int w, const int lane_, const int b, const int p, const int j) {
  const unsigned long long tstart_ = __builtin_amdgcn_s_memrealtime();
  STAMP_DECL STAMP(0);
  char* const ws = P.ws;
  unsigned tagv = __hip_atomic_load((gu32*)(ws + WS_EPOCH), RLX_AGENT) + 1u, tag;
  const int mh = w >> 1, kh = w & 1;
  const auto rws = mkrsrc(ws, WS_END);
  const int ft0 = 27 * (j >> 2) + 7 * (j & 3), nft = (j & 3) == 3 ? 6 : 7;
  auto rot = [&](int i) { return i < 3 ? i + 4 * mh : i - 3 * mh; };
#define LANE_IDS int lane = lane_; asm volatile("" : "+v"(lane)); const int q = lane >> 4, tk = lane & 15, tid = w * 64 + lane, token = 64 * p + 16 * w + tk; (void)q; (void)tk; (void)tid; (void)token;
  {
    LANE_IDS
    unsigned v[2][5];
#pragma unroll
    for (int t = 0; t < 2; ++t) { const unsigned* xr = (const unsigned*)(P.x + (size_t)(64 * p + 8 * j + 2 * w + t) * D);
#pragma unroll
      for (int i = 0; i < 5; ++i) v[t][i] = xr[lane + 64 * i]; }
    const int u0 = w * 256 + b; const bool dopt = u0 < 810; const int u = dopt ? u0 : 0; const bool isin = u < 540;
    const int uu = isin ? u : u - 540, ncb = isin ? 27 : 10, pkb = uu / ncb, pcb = uu - pkb * ncb, ld = isin ? F : D;
    u32x4 pv[4];
    { const bf16_t* src = (isin ? P.win : P.wout) + (size_t)(pkb * 32) * ld + pcb * 64;
#pragma unroll
      for (int i = 0; i < 4; ++i) pv[i] = *(const u32x4*)(src + (size_t)(i * 8 + (lane >> 3)) * ld + (lane & 7) * 8); }
    LAS unsigned* t32 = (LAS unsigned*)(smem + P0_T);
    unsigned pk[2][5];
#pragma unroll
    for (int t = 0; t < 2; ++t) {
      float s = 0.f;
#pragma unroll
      for (int i = 0; i < 5; ++i) s += bflo(v[t][i]) + bfhi(v[t][i]);
      const float mean = xsum_q(xsum_16(s)) * (1.0f / D);
      float q2 = 0.f;
#pragma unroll
      for (int i = 0; i < 5; ++i) { const float a = bflo(v[t][i]) - mean, c = bfhi(v[t][i]) - mean; q2 += a * a + c * c; }
      const float rs = 1.0f / sqrtf(xsum_q(xsum_16(q2)) * (1.0f / D) + 1e-5f);
#pragma unroll
      for (int i = 0; i < 5; ++i) pk[t][i] = pack2((bflo(v[t][i]) - mean) * rs, (bfhi(v[t][i]) - mean) * rs);
    }
#pragma unroll
    for (int i = 0; i < 5; ++i) { const int d = 2 * (lane + 64 * i);
      t32[d * 4 + w] = (pk[0][i] & 0xffffu) | (pk[1][i] << 16); t32[(d + 1) * 4 + w] = (pk[0][i] >> 16) | (pk[1][i] & 0xffff0000u); }
    asm volatile("" : "+v"(tagv)); tag = __builtin_amdgcn_readfirstlane(tagv);
    wait_lgkm0(); bar();
    const unsigned xb = WS_XN + (unsigned)((2 * p + (j >> 2)) * 40 * 1024 + (j & 3) * 256);
#pragma unroll
    for (int i = 0; i < 3; ++i) { const int d = tid + 256 * i;
      if (d < D) st16(rws, xb + (d >> 4) * 1024 + (d & 15) * 16, *(LAS u32x4*)(smem + P0_T + d * 16)); }
    wait_vm<0>();
    bar();
    STAMP(1);
    if (w == 0 && lane == 0) set_flag(ws, WS_FN + p * 128 + j * 4, tag);
    {
      LAS char* t = smem + P0_PT + w * 4096;
#pragma unroll
      for (int i = 0; i < 4; ++i) *(LAS u32x4*)(t + (i * 8 + (lane >> 3)) * 128 + (lane & 7) * 16) = pv[i];
      const unsigned dst = dopt ? (isin ? WS_WIN + (unsigned)(pkb * 109 + pcb * 4) * 1024 : WS_WOUT + (unsigned)(pkb * 40 + pcb * 4) * 1024) + lane * 16 : 0xfffff000u;
      const int rq = tk >> 2, pp = lane & 3;
#pragma unroll
      for (int c = 0; c < 4; ++c) {
        const u32x2 l2 = __builtin_bit_cast(u32x2, __builtin_amdgcn_ds_read_tr16_b64_v4i16((LAS s16x4*)(t + (8 * q + rq) * 128 + c * 32 + pp * 8)));
        const u32x2 h2 = __builtin_bit_cast(u32x2, __builtin_amdgcn_ds_read_tr16_b64_v4i16((LAS s16x4*)(t + (8 * q + 4 + rq) * 128 + c * 32 + pp * 8)));
        st16(rws, dst + (dopt ? c * 1024 : 0), (u32x4){l2.x, l2.y, h2.x, h2.y});
      }
    }
    if (w == 0) { wait256(rws, ws, WS_FN, tag, lane); STAMP(23); }
    bar();
    STAMP(2);
  }
  u32x2 xv[5];
  {
    LANE_IDS
    f32x4 acc[2][5];
#pragma unroll
    for (int t = 0; t < 2; ++t)
#pragma unroll
      for (int i = 0; i < 5; ++i) acc[t][i] = (f32x4){0, 0, 0, 0};
    f32x4 rsum[2] = {(f32x4){0, 0, 0, 0}, (f32x4){0, 0, 0, 0}};
    const bf16x8 ones = __builtin_bit_cast(bf16x8, (u32x4){0x3f803f80u, 0x3f803f80u, 0x3f803f80u, 0x3f803f80u});
    const unsigned wa0 = (unsigned)(w * 4096 + tk * 128 + ((q ^ ((lane >> 1) & 7)) * 16));
    const unsigned xa = 16384 + lane * 16;
    bf16x8 Wa[2], Xa[5], Wb[2], Xb[5];
    auto rdm = [&](bf16x8(&Wn)[2], bf16x8(&Xn)[5], int slot, int kk, bf16x8(&Wf)[2], bf16x8(&Xf)[5]) {
      LAS char* sb = smem + slot * A_SLOT; const unsigned wa = wa0 ^ (kk * 64);
      static_for<0, 12>([&](auto ic) { constexpr int z = decltype(ic)::value, i = z >> 1, t = z & 1;
        if constexpr (z < 10) acc[t][i] = mfma(Xf[i], Wf[t], acc[t][i]); else rsum[t] = mfma(ones, Wf[t], rsum[t]);
        if constexpr (z == 0) Wn[0] = *(LAS bf16x8*)(sb + wa);
        if constexpr (z == 1) Xn[0] = *(LAS bf16x8*)(sb + xa + (kk * 5) * 1024);
        if constexpr (z == 2) Wn[1] = *(LAS bf16x8*)(sb + wa + 2048);
        if constexpr (z >= 3 && z < 7) Xn[z - 2] = *(LAS bf16x8*)(sb + xa + (kk * 5 + z - 2) * 1024);
        sched_fence(); });
    };
    auto mmlast = [&](bf16x8(&Wf)[2], bf16x8(&Xf)[5]) {
#pragma unroll
      for (int i = 0; i < 5; ++i) { acc[0][i] = mfma(Xf[i], Wf[0], acc[0][i]); acc[1][i] = mfma(Xf[i], Wf[1], acc[1][i]); }
      rsum[0] = mfma(ones, Wf[0], rsum[0]); rsum[1] = mfma(ones, Wf[1], rsum[1]);
    };
    int slot = 0;
    auto steps = [&](int n0, int n1, bool hooks) {
#pragma unroll 1
      for (int n = n0; n < n1; ++n) {
        rdm(Wb, Xb, slot, 1, Wa, Xa);
        const int ns = slot + 1 == A_NS ? 0 : slot + 1;
        if (hooks && n == 8) wait_vm<0>();
        wait_lgkm0(); bar(); sched_fence();
        if (hooks && n == 8 && w == 0 && lane == 0) set_flag(ws, WS_F2 + p * 128 + j * 4, tag);
        rdm(Wa, Xa, ns, 0, Wb, Xb);
        slot = ns;
      }
    };
    bar();
    STAMP(3); CSTAMP(40);
    { LAS char* sb = smem; Wa[0] = *(LAS bf16x8*)(sb + wa0); Wa[1] = *(LAS bf16x8*)(sb + wa0 + 2048);
#pragma unroll
      for (int i = 0; i < 5; ++i) Xa[i] = *(LAS bf16x8*)(sb + xa + i * 1024); }
    steps(0, 12, true);
    const u32x4 f2 = peek256(rws, WS_F2, lane);
    steps(12, 18, false);
    if (!flags_ok(f2, tag)) wait256(rws, ws, WS_F2, tag, lane);
    if (b == 0 && w == 0 && lane == 0) set_flag(ws, WS_EPOCH, tag + 15);
#pragma unroll
    for (int i = 0; i < 5; ++i) xv[i] = *(const u32x2*)(P.x + (size_t)token * D + 80 * j + 16 * i + 4 * q);
    steps(18, 31, false);
    { LAS char* sb = smem + slot * A_SLOT; const unsigned wa = wa0 ^ 64;
      Wb[0] = *(LAS bf16x8*)(sb + wa); Wb[1] = *(LAS bf16x8*)(sb + wa + 2048);
#pragma unroll
      for (int i = 0; i < 5; ++i) Xb[i] = *(LAS bf16x8*)(sb + xa + (5 + i) * 1024); }
    mmlast(Wa, Xa);
    wait_lgkm0(); bar();
    mmlast(Wb, Xb);
    STAMP(4); CSTAMP(41);
    const float rs0 = rsum[0][0], rs1 = rsum[1][0];
#pragma unroll
    for (int i = 0; i < 5; ++i) {
      u32x2 a, c;
      const f32x4 sa = *(const LAS f32x4*)(smem + T_MOD + (16 * i + 4 * q) * 4), c1 = *(const LAS f32x4*)(smem + T_MOD + (80 + 16 * i + 4 * q) * 4);
      { const f32x4 u = acc[0][i] * c1 + rs0 * sa; a.x = pack2(u[0], u[1]); a.y = pack2(u[2], u[3]); }
      { const f32x4 v = -(acc[1][i] * c1 + rs1 * sa); c.x = pack2(v[0], v[1]); c.y = pack2(v[2], v[3]); }
      const int dt = 5 * j + i, kb = dt >> 1, qq = 2 * (dt & 1) + (q >> 1);
      st16(rws, WS_UV + (unsigned)(((p * 2 + (q & 1)) * 20 + kb) * 4 + w) * 1024 + (qq * 16 + tk) * 16, swap_pair(a, c));
    }
    wait_vm<0>(); bar();
    STAMP(5);
    if (w == 0) { if (lane == 0) set_flag(ws, WS_FU + p * 128 + j * 4, tag); wait_flags8(ws, WS_FU + p * 128, tag, lane); }
    bar();
    STAMP(6); CSTAMP(44);
  }
  u32x2 x1p[5];
  {
    LANE_IDS
    f32x4 acc[5][2];
#pragma unroll
    for (int i = 0; i < 5; ++i) { acc[i][0] = (f32x4){0, 0, 0, 0}; acc[i][1] = (f32x4){0, 0, 0, 0}; }
    const unsigned wa = (unsigned)(tk * 128 + (((q + 4 * kh) ^ ((lane >> 1) & 7)) * 16));
    const unsigned xa = 10240 + (kh * 4 + 2 * mh) * 1024 + lane * 16;
    ksplit_loop<B_NS, 20, 5, 2>(
        [&](bf16x8(&A)[5], bf16x8(&Bf)[2], int slot, int) { LAS char* sb = smem + slot * B_SLOT;
          A[0] = *(LAS bf16x8*)(sb + wa); Bf[0] = *(LAS bf16x8*)(sb + xa); Bf[1] = *(LAS bf16x8*)(sb + xa + 1024);
#pragma unroll
          for (int i = 1; i < 5; ++i) A[i] = *(LAS bf16x8*)(sb + wa + i * 2048); },
        [&](bf16x8(&A)[5], bf16x8(&Bf)[2], bool) {
#pragma unroll
          for (int i = 0; i < 5; ++i) { acc[i][0] = mfma(A[i], Bf[0], acc[i][0]); acc[i][1] = mfma(A[i], Bf[1], acc[i][1]); } });
    STAMP(7); CSTAMP(45);
    f32x4 y[5];
    kreduce<5>(acc, y, smem + B_SCR, w, lane);
    float xs[20]; u32x2 xp[5];
    float s = 0.f;
#pragma unroll
    for (int i = 0; i < 5; ++i) {
      const f32x4 ga = *(const LAS f32x4*)(smem + T_MOD + (160 + 16 * i + 4 * q) * 4), c1b = *(const LAS f32x4*)(smem + T_MOD + 960 + (16 * i + 4 * q) * 4);
      x1p[i].x = pack2(bflo(xv[i].x) + ga[0] * y[i][0], bfhi(xv[i].x) + ga[1] * y[i][1]);
      x1p[i].y = pack2(bflo(xv[i].y) + ga[2] * y[i][2], bfhi(xv[i].y) + ga[3] * y[i][3]);
      asm volatile("" : "+v"(x1p[i].x), "+v"(x1p[i].y));
      xs[4 * i] = bflo(x1p[i].x); xs[4 * i + 1] = bfhi(x1p[i].x); xs[4 * i + 2] = bflo(x1p[i].y); xs[4 * i + 3] = bfhi(x1p[i].y);
      s += (xs[4 * i] + xs[4 * i + 1]) + (xs[4 * i + 2] + xs[4 * i + 3]);
      xp[i].x = pack2(xs[4 * i] * c1b[0], xs[4 * i + 1] * c1b[1]); xp[i].y = pack2(xs[4 * i + 2] * c1b[2], xs[4 * i + 3] * c1b[3]);
    }
    const unsigned img = WS_X1P + (unsigned)p * 20 * 4096 + w * 1024;
#pragma unroll
    for (int i = 0; i < 6; i += 2) {
      const int i2 = i + 1 < 5 ? i + 1 : i;
      const int dt = 5 * j + ((q & 1) ? i2 : i), kb = dt >> 1, qq = 2 * (dt & 1) + (q >> 1);
      st16(rws, img + kb * 4096 + (qq * 16 + tk) * 16, swap_pair(xp[i], xp[i2]));
    }
    const float mean = xsum_q(s) * (1.0f / 80.0f);
    float m2 = 0.f;
#pragma unroll
    for (int e = 0; e < 20; ++e) { const float d = xs[e] - mean; m2 += d * d; }
    m2 = xsum_q(m2);
    if (q == 0) __hip_atomic_store((gu64*)(ws + WS_ST1) + token * 8 + j, ((unsigned long long)__float_as_uint(m2) << 32) | __float_as_uint(mean), RLX_AGENT);
    wait_vm<0>(); bar();
    STAMP(8);
    if (w == 0) { if (lane == 0) set_flag(ws, WS_FX + p * 128 + j * 4, tag); wait_flags8(ws, WS_FX + p * 128, tag, lane); }
    bar();
    STAMP(9);
  }
  u32x2 z1p[7]; float bias;
  {
    LANE_IDS
    u32x4 st[4];
#pragma unroll
    for (int k = 0; k < 4; ++k) st[k] = ld16(rws, WS_ST1 + (unsigned)token * 64 + k * 16);
    bias = bflo(P.bsp[token]);
    for (int i = tid; i < 448; i += 256) ((LAS float*)(smem + T_CV))[i] = 0.f;
    f32x4 acc[7][2], aux[4];
    auto clear = [&]() {
#pragma unroll
      for (int i = 0; i < 7; ++i) { acc[i][0] = (f32x4){0, 0, 0, 0}; acc[i][1] = (f32x4){0, 0, 0, 0}; }
#pragma unroll
      for (int i = 0; i < 4; ++i) aux[i] = (f32x4){0, 0, 0, 0};
    };
    const unsigned xa = C_X + (kh * 4 + 2 * mh) * 1024 + lane * 16, wlo = C_W + kh * 7168 + lane * 16 + mh * 4096, whi = C_W + kh * 7168 + lane * 16 - mh * 3072;
    const int r4 = tk & 3;
    const unsigned ma = T_MODB + (r4 == 0 ? q * 16 + kh * 64 : r4 == 1 ? 1280 + q * 16 + kh * 64 : 2560), mstride = r4 < 2 ? 128 : 0;
    int nbase = 0;
    auto rd = [&](bf16x8(&A)[7], bf16x8(&Bf)[3], int slot, int n) {
      LAS char* sl = smem + wlo + slot * C_WSLOT; LAS char* sh = smem + whi + slot * C_WSLOT; LAS char* xb = smem + xa + n * 8192;
      Bf[0] = *(LAS bf16x8*)(xb); A[0] = *(LAS bf16x8*)(sl); Bf[1] = *(LAS bf16x8*)(xb + 1024);
      A[1] = *(LAS bf16x8*)(sl + 1024); A[2] = *(LAS bf16x8*)(sl + 2048);
#pragma unroll
      for (int i = 3; i < 7; ++i) A[i] = *(LAS bf16x8*)(sh + i * 1024);
      Bf[2] = *(LAS bf16x8*)(smem + ma + n * mstride);
    };
    auto auxmm = [&](bf16x8(&A)[7], bf16x8(&Bf)[3]) {
#pragma unroll
      for (int i = 0; i < 4; ++i) aux[i] = mfma(Bf[2], A[i], aux[i]);
    };
    auto mm2 = [&](bf16x8(&A)[7], bf16x8(&Bf)[3], bool) {
#pragma unroll
      for (int i = 0; i < 7; ++i) { acc[i][0] = mfma(Bf[0], A[i], acc[i][0]); acc[i][1] = mfma(Bf[1], A[i], acc[i][1]); }
      auxmm(A, Bf); };
    auto mm1 = [&](bf16x8(&A)[7], bf16x8(&Bf)[3], bool) {
#pragma unroll
      for (int i = 0; i < 7; ++i) { acc[i][0] = mfma(A[i], Bf[0], acc[i][0]); acc[i][1] = mfma(A[i], Bf[1], acc[i][1]); }
      auxmm(A, Bf); };
    auto put_cv = [&](int pass_) {
      LAS float* cv = (LAS float*)(smem + T_CV) + pass_ * 224;
      if (q == 0) {
#pragma unroll
        for (int i = 0; i < 4; ++i) if (mh == 0 || i < 3) { const int t = rot(i);
          __hip_atomic_fetch_add(cv + 16 * t + tk, aux[i][0], __ATOMIC_RELAXED, __HIP_MEMORY_SCOPE_WORKGROUP);
          __hip_atomic_fetch_add(cv + 112 + 16 * t + tk, aux[i][1], __ATOMIC_RELAXED, __HIP_MEMORY_SCOPE_WORKGROUP); }
      }
    };
    float rmean, rrstd;
    { float mu[8], mean = 0.f;
#pragma unroll
      for (int k = 0; k < 4; ++k) { mu[2 * k] = __uint_as_float(st[k].x); mu[2 * k + 1] = __uint_as_float(st[k].z); mean += mu[2 * k] + mu[2 * k + 1]; }
      mean *= 0.125f; float M2 = 0.f;
#pragma unroll
      for (int k = 0; k < 4; ++k) { const float d0 = mu[2 * k] - mean, d1 = mu[2 * k + 1] - mean; M2 += __uint_as_float(st[k].y) + __uint_as_float(st[k].w) + 80.0f * (d0 * d0 + d1 * d1); }
      rmean = mean; rrstd = 1.0f / sqrtf(M2 * (1.0f / D) + 1e-5f); }
    (void)nbase;
    bf16x8 A0[7], B0[3], A1[7], B1[3];
    int slot = 0;
    auto nohook = [](int) {};
    clear();
    wait_lgkm0(); bar();
    STAMP(10); CSTAMP(46);
    rd(A0, B0, 0, 0);
    ksplit_steps<C_NS, 10, 7, 3, 18>(A0, B0, A1, B1, slot, 0, 8, rd, mm2, nohook, nohook);
    ksplit_last<C_NS, 10, 7, 3>(A0, B0, A1, B1, slot, rd, mm2);
    STAMP(11); CSTAMP(47);
    put_cv(0);
    f32x4 zs[7];
    kreduce<7>(acc, zs, smem + C_SCR, w, lane);
    wait_lgkm0(); bar();
    f32x2 mr01, mr23, rr01, rr23;
    mr01 = (f32x2){__shfl(rmean, 4 * q), __shfl(rmean, 4 * q + 1)}; mr23 = (f32x2){__shfl(rmean, 4 * q + 2), __shfl(rmean, 4 * q + 3)};
    rr01 = (f32x2){__shfl(rrstd, 4 * q), __shfl(rrstd, 4 * q + 1)}; rr23 = (f32x2){__shfl(rrstd, 4 * q + 2), __shfl(rrstd, 4 * q + 3)};
    f32x2 z01[7], z23[7];
#pragma unroll
    for (int i = 0; i < 7; ++i) {
      const float c1 = ((const LAS float*)(smem + T_CV))[16 * rot(i) + tk], c2 = ((const LAS float*)(smem + T_CV))[112 + 16 * rot(i) + tk];
      z01[i] = gelu2(rr01 * ((f32x2){zs[i][0], zs[i][1]} - mr01 * c1) + c2);
      z23[i] = gelu2(rr23 * ((f32x2){zs[i][2], zs[i][3]} - mr23 * c1) + c2);
    }
    {
      const float inv = 1.0f / (16.0f * nft);
      const f32x2 zero2 = (f32x2){0.f, 0.f};
      f32x2 s01 = zero2, s23 = zero2;
#pragma unroll
      for (int i = 0; i < 7; ++i) { const bool ok = (i != 2 && i != 6) || rot(i) < nft; s01 += ok ? z01[i] : zero2; s23 += ok ? z23[i] : zero2; }
      const f32x2 m01 = (f32x2){xsum_16(s01[0]), xsum_16(s01[1])} * inv, m23 = (f32x2){xsum_16(s23[0]), xsum_16(s23[1])} * inv;
      f32x2 t01 = zero2, t23 = zero2;
#pragma unroll
      for (int i = 0; i < 7; ++i) { const bool ok = (i != 2 && i != 6) || rot(i) < nft; const f32x2 a = ok ? z01[i] - m01 : zero2, c = ok ? z23[i] - m23 : zero2; t01 += a * a; t23 += c * c; }
      const float q0 = xsum_16(t01[0]), q1 = xsum_16(t01[1]), q2 = xsum_16(t23[0]), q3 = xsum_16(t23[1]);
      if (tk < 8) { const int r = tk & 3; const float a = r == 0 ? m01[0] : r == 1 ? m01[1] : r == 2 ? m23[0] : m23[1], c = r == 0 ? q0 : r == 1 ? q1 : r == 2 ? q2 : q3;
        const int tok = 64 * p + 16 * w + 4 * q + r;
        __hip_atomic_store((gu64*)(ws + WS_ST2) + (tok * 8 + j) * 2 + (tk >> 2), ((unsigned long long)tag << 32) | __float_as_uint(tk < 4 ? a : c), RLX_AGENT); }
    }
    STAMP(12);
    {
      u32x4 g0, g1;
      const unsigned go = WS_ST2 + (unsigned)(token * 8 + 2 * q) * 16;
      for (unsigned spins = 0;;) { g0 = ld16(rws, go); g1 = ld16(rws, go + 16);
        if (__all(g0.y == tag && g0.w == tag && g1.y == tag && g1.w == tag)) break; if (spin_fail(spins, ws)) break; }
      const float n0 = 112.0f, n1 = (q & 1) ? 96.0f : 112.0f;
      const float m0 = __uint_as_float(g0.x), m1 = __uint_as_float(g1.x);
      const float mean = xsum_q(n0 * m0 + n1 * m1) * (1.0f / FH);
      const float d0 = m0 - mean, d1 = m1 - mean;
      const float M2 = xsum_q(__uint_as_float(g0.z) + __uint_as_float(g1.z) + n0 * d0 * d0 + n1 * d1 * d1);
      const float rstd = 1.0f / sqrtf(M2 * (1.0f / FH) + 1e-5f);
      mr01 = (f32x2){__shfl(mean, 4 * q), __shfl(mean, 4 * q + 1)}; mr23 = (f32x2){__shfl(mean, 4 * q + 2), __shfl(mean, 4 * q + 3)};
      rr01 = (f32x2){__shfl(rstd, 4 * q), __shfl(rstd, 4 * q + 1)}; rr23 = (f32x2){__shfl(rstd, 4 * q + 2), __shfl(rstd, 4 * q + 3)};
    }
    STAMP(13);
    {
      u32x2 pk[7];
#pragma unroll
      for (int i = 0; i < 7; ++i) { const f32x2 a = (z01[i] - mr01) * rr01, c = (z23[i] - mr23) * rr23; pk[i].x = pack2(a[0], a[1]); pk[i].y = pack2(c[0], c[1]); }
      const unsigned img = WS_Z2N + (unsigned)(2 * p + (w >> 1)) * 55 * 1024 + ((2 * (w & 1) + (q >> 1)) * 16 + tk) * 16;
#pragma unroll
      for (int i = 0; i < 8; i += 2) {
        const int i2 = i + 1 < 7 ? i + 1 : i;
        const u32x4 ch = swap_pair(pk[i], pk[i2]);
        const int ti = (q & 1) ? rot(i2) : rot(i);
        st16(rws, ti < nft ? img + (ft0 + ti) * 1024 : 0xfffff000u, ch);
      }
    }
    STAMP(14); CSTAMP(48);
    clear();
    slot = 0;
    bar();
    rd(A0, B0, 0, 0);
    ksplit_steps<C_NS, 10, 7, 3, 18>(A0, B0, A1, B1, slot, 0, 6, rd, mm1,
        [&](int n) { if (n == 2) wait_vm<0>(); },
        [&](int n) { if (n == 2 && w == 0 && lane == 0) set_flag(ws, WS_FZ + (j * 4 + (p >> 3)) * 128 + (p & 7) * 4, tag); });
    const unsigned fzo = WS_FZ + (j * 4 + ((lane & 7) >> 1)) * 128 + (lane & 1) * 16;
    const u32x4 fz = ld16(rws, fzo);
    ksplit_steps<C_NS, 10, 7, 3, 18>(A0, B0, A1, B1, slot, 6, 8, rd, mm1, nohook, nohook);
    ksplit_last<C_NS, 10, 7, 3>(A0, B0, A1, B1, slot, rd, mm1);
    if (!flags_ok(fz, tag)) { for (unsigned spins = 0;;) { if (flags_ok(ld16(rws, fzo), tag)) break; if (spin_fail(spins, ws)) break; } }
    STAMP(15); CSTAMP(49);
    put_cv(1);
    kreduce<7>(acc, zs, smem + C_SCR, w, lane);
#pragma unroll
    for (int i = 0; i < 7; ++i) {
      const LAS float* cv = (const LAS float*)(smem + T_CV) + 224;
      const f32x4 c1 = *(const LAS f32x4*)(cv + 16 * rot(i) + 4 * q), c2 = *(const LAS f32x4*)(cv + 112 + 16 * rot(i) + 4 * q);
      const f32x2 a = gelu2(rrstd * ((f32x2){zs[i][0], zs[i][1]} - rmean * (f32x2){c1[0], c1[1]}) + (f32x2){c2[0], c2[1]});
      const f32x2 c = gelu2(rrstd * ((f32x2){zs[i][2], zs[i][3]} - rmean * (f32x2){c1[2], c1[3]}) + (f32x2){c2[2], c2[3]});
      z1p[i] = (u32x2){pack2(a[0], a[1]), pack2(c[0], c[1])};
      asm volatile("" : "+v"(z1p[i].x), "+v"(z1p[i].y));
    }
    STAMP(16); CSTAMP(42);
  }
  {
    LANE_IDS
    f32x4 acc[7][2];
#pragma unroll
    for (int i = 0; i < 7; ++i) { acc[i][0] = (f32x4){0, 0, 0, 0}; acc[i][1] = (f32x4){0, 0, 0, 0}; }
    const unsigned wa = (unsigned)((2 * mh) * 2048 + tk * 128 + (((q + 4 * kh) ^ ((lane >> 1) & 7)) * 16));
    const unsigned xlo = 8192 + kh * 7168 + lane * 16 + mh * 4096, xhi = 8192 + kh * 7168 + lane * 16 - mh * 3072;
    ksplit_loop<D_NS, 32, 7, 2>(
        [&](bf16x8(&A)[7], bf16x8(&Bf)[2], int slot, int) { LAS char* sb = smem + slot * D_SLOT;
          Bf[0] = *(LAS bf16x8*)(sb + wa); A[0] = *(LAS bf16x8*)(sb + xlo); Bf[1] = *(LAS bf16x8*)(sb + wa + 2048);
          A[1] = *(LAS bf16x8*)(sb + xlo + 1024); A[2] = *(LAS bf16x8*)(sb + xlo + 2048);
#pragma unroll
          for (int i = 3; i < 7; ++i) A[i] = *(LAS bf16x8*)(sb + xhi + i * 1024); },
        [&](bf16x8(&A)[7], bf16x8(&Bf)[2], bool) {
#pragma unroll
          for (int i = 0; i < 7; ++i) { acc[i][0] = mfma(A[i], Bf[0], acc[i][0]); acc[i][1] = mfma(A[i], Bf[1], acc[i][1]); } });
    STAMP(17); CSTAMP(43);
    f32x4 g[7];
    kreduce<7>(acc, g, smem + D_SCR, w, lane);
    u32x2 pk[7];
#pragma unroll
    for (int i = 0; i < 7; ++i) {
      pk[i].x = pack2(bflo(z1p[i].x) * (g[i][0] + bias), bfhi(z1p[i].x) * (g[i][1] + bias));
      pk[i].y = pack2(bflo(z1p[i].y) * (g[i][2] + bias), bfhi(z1p[i].y) * (g[i][3] + bias));
    }
    const unsigned img = WS_P + (unsigned)p * 28 * 4096 + w * 1024;
#pragma unroll
    for (int i = 0; i < 8; i += 2) {
      const int i2 = i + 1 < 7 ? i + 1 : i;
      const u32x4 ch = swap_pair(pk[i], pk[i2]);
      const int ti = (q & 1) ? rot(i2) : rot(i), ft = ft0 + ti, kb = ft >> 1, qq = 2 * (ft & 1) + (q >> 1);
      st16(rws, ti < nft ? img + kb * 4096 + (qq * 16 + tk) * 16 : 0xfffff000u, ch);
    }
    wait_vm<0>(); bar();
    STAMP(18);
    if (w == 0) { if (lane == 0) set_flag(ws, WS_FP + p * 128 + j * 4, tag); wait_flags8(ws, WS_FP + p * 128, tag, lane); }
    bar();
    STAMP(19);
  }
  {
    LANE_IDS
    f32x4 acc[5][2];
#pragma unroll
    for (int i = 0; i < 5; ++i) { acc[i][0] = (f32x4){0, 0, 0, 0}; acc[i][1] = (f32x4){0, 0, 0, 0}; }
    const unsigned wa = kh * 5120 + lane * 16, xa = 10240 + (kh * 4 + 2 * mh) * 1024 + lane * 16;
    ksplit_loop<E_NS, 14, 5, 2>(
        [&](bf16x8(&A)[5], bf16x8(&Bf)[2], int slot, int) { LAS char* sb = smem + slot * E_SLOT;
          A[0] = *(LAS bf16x8*)(sb + wa); Bf[0] = *(LAS bf16x8*)(sb + xa); Bf[1] = *(LAS bf16x8*)(sb + xa + 1024);
#pragma unroll
          for (int i = 1; i < 5; ++i) A[i] = *(LAS bf16x8*)(sb + wa + i * 1024); },
        [&](bf16x8(&A)[5], bf16x8(&Bf)[2], bool last) {
          if (!(last && kh)) {
#pragma unroll
            for (int i = 0; i < 5; ++i) { acc[i][0] = mfma(A[i], Bf[0], acc[i][0]); acc[i][1] = mfma(A[i], Bf[1], acc[i][1]); } } });
    STAMP(20);
    f32x4 o[5];
    kreduce<5>(acc, o, smem + E_SCR, w, lane);
    u32x2 pk[5];
#pragma unroll
    for (int i = 0; i < 5; ++i) {
      const f32x4 gb = *(const LAS f32x4*)(smem + T_MOD + 960 + (80 + 16 * i + 4 * q) * 4);
      pk[i].x = pack2(bflo(x1p[i].x) + gb[0] * o[i][0], bfhi(x1p[i].x) + gb[1] * o[i][1]);
      pk[i].y = pack2(bflo(x1p[i].y) + gb[2] * o[i][2], bfhi(x1p[i].y) + gb[3] * o[i][3]);
    }
    bf16_t* orow = P.out + (size_t)token * D + 80 * j;
#pragma unroll
    for (int i = 0; i < 6; i += 2) {
      const int i2 = i + 1 < 5 ? i + 1 : i;
      const u32x4 ch = swap_pair(pk[i], pk[i2]);
      *(u32x4*)(orow + 16 * ((q & 1) ? i2 : i) + 8 * (q >> 1)) = ch;
    }
    while (__builtin_amdgcn_s_memrealtime() - tstart_ < 4800ull) __builtin_amdgcn_s_sleep(1);
    STAMP(21);
  }
  STAMP_FLUSH();
}

__global__ __launch_bounds__(NT, 2) void fused_block(Params P) {
  extern __shared__ __attribute__((aligned(16))) char smem_[];
  LAS char* smem = (LAS char*)smem_;
  const int tid = threadIdx.x, lane = tid & 63, wid = __builtin_amdgcn_readfirstlane(tid >> 6), b = blockIdx.x;
  const int xcd = b & 7, idx = b >> 3, p = xcd * 4 + (idx >> 3), j = idx & 7;
  if (wid >= 4) loader_main(P, (unsigned)(size_t)smem, smem, wid - 4, lane, b, p, j, xcd * 4);
  else consumer_main(P, smem, wid, lane, b, p, j);
}

extern "C" void kernel_launch(void* const* d_in, const int* in_sizes, int n_in, void* d_out, int out_size, void* d_ws, size_t ws_size, hipStream_t stream) {
  Params P;
  P.x = (const bf16_t*)d_in[0]; P.wsr = (const bf16_t*)d_in[1]; P.wsi = (const bf16_t*)d_in[2];
  P.wdr = (const bf16_t*)d_in[3]; P.wdi = (const bf16_t*)d_in[4]; P.win = (const bf16_t*)d_in[6];
  P.wsp = (const bf16_t*)d_in[7]; P.bsp = (const bf16_t*)d_in[8]; P.wout = (const bf16_t*)d_in[9];
  P.c = (const bf16_t*)d_in[10]; P.wmod = (const bf16_t*)d_in[11];
  P.out = (bf16_t*)d_out; P.ws = (char*)d_ws;
  (void)hipFuncSetAttribute((const void*)fused_block, hipFuncAttributeMaxDynamicSharedMemorySize, LDS_BYTES);
  fused_block<<<256, NT, LDS_BYTES, stream>>>(P);
}
```

```cpp
#include <hip/hip_runtime.h>
#include <stdint.h>
#include <type_traits>

typedef unsigned short bf16_t;
typedef short bf16x8 __attribute__((ext_vector_type(8)));
typedef short s16x4 __attribute__((ext_vector_type(4)));
typedef float f32x4 __attribute__((ext_vector_type(4)));
typedef float f32x2 __attribute__((ext_vector_type(2)));
typedef unsigned u32x4 __attribute__((ext_vector_type(4)));
typedef unsigned u32x2 __attribute__((ext_vector_type(2)));
typedef __bf16 bf2_t __attribute__((ext_vector_type(2)));
#define LAS __attribute__((address_space(3)))
#define GAS __attribute__((address_space(1)))
typedef GAS unsigned gu32;
typedef GAS unsigned long long gu64;
#define RLX_AGENT __ATOMIC_RELAXED, __HIP_MEMORY_SCOPE_AGENT
#define DEV __device__ __forceinline__

constexpr int S = 2048, D = 640, F = 1728, FH = 864;
constexpr int NT = 512;

constexpr unsigned WS_EPOCH = 0;
constexpr unsigned WS_TMO   = 256;
constexpr unsigned WS_FN = 4096, WS_F2 = 8192, WS_FU = 12288, WS_FX = 16384, WS_FP = 20480, WS_FZ = 24576;
constexpr unsigned WS_MOD  = 65536;
constexpr unsigned WS_MODB = WS_MOD + 16384;

constexpr unsigned WS_XN   = 131072;
constexpr unsigned WS_WIN  = WS_XN + 64 * 40 * 1024;
constexpr unsigned WS_WOUT = WS_WIN + 20 * 109 * 1024;
constexpr unsigned WS_UV   = WS_WOUT + 27 * 40 * 1024;
constexpr unsigned WS_X1P  = WS_UV + 32 * 2 * 20 * 4096;
constexpr unsigned WS_ST1  = WS_X1P + 32 * 20 * 4096;
constexpr unsigned WS_Z2N  = WS_ST1 + 2048 * 64;
constexpr unsigned WS_ST2  = WS_Z2N + 64 * 55 * 1024;
constexpr unsigned WS_P    = WS_ST2 + 2048 * 128;
constexpr unsigned WS_END  = WS_P + 32 * 28 * 4096;

struct Params {
  const bf16_t *x, *wsr, *wsi, *wdr, *wdi, *win, *wsp, *bsp, *wout, *c, *wmod;
  bf16_t* out;
  char* ws;
};

#define STAMP_DECL
#define STAMP(i) do {} while (0)
#define STAMP_FLUSH() do {} while (0)
#define CSTAMP(i) do {} while (0)

template <int N> DEV void wait_vm() { asm volatile("s_waitcnt vmcnt(%0)" ::"n"(N) : "memory"); }
DEV void wait_lgkm0() { asm volatile("s_waitcnt lgkmcnt(0)" ::: "memory"); }
DEV void bar() { asm volatile("" ::: "memory"); __builtin_amdgcn_s_barrier(); asm volatile("" ::: "memory"); }
DEV void sched_fence() { __builtin_amdgcn_sched_barrier(0); }
template <int I, int N, class Fn> DEV void static_for(Fn&& f) { if constexpr (I < N) { f(std::integral_constant<int, I>{}); static_for<I + 1, N>(f); } }

DEV float bflo(unsigned v) { return __uint_as_float(v << 16); }
DEV float bfhi(unsigned v) { return __uint_as_float(v & 0xffff0000u); }
DEV unsigned pack2(float lo, float hi) { bf2_t v; v[0] = (__bf16)lo; v[1] = (__bf16)hi; return __builtin_bit_cast(unsigned, v); }
DEV float rbf(float f) { return bflo(pack2(f, f)); }
DEV float dot2(unsigned a, unsigned b, float c) { return __builtin_amdgcn_fdot2_f32_bf16(__builtin_bit_cast(bf2_t, a), __builtin_bit_cast(bf2_t, b), c, false); }
DEV f32x4 mfma(bf16x8 a, bf16x8 b, f32x4 c) { return __builtin_amdgcn_mfma_f32_16x16x32_bf16(a, b, c, 0, 0, 0); }

DEV u32x4 mkrs(const void* p, unsigned bytes) {
  const unsigned long long a = (unsigned long long)p;
  u32x4 r; r.x = (unsigned)a; r.y = (unsigned)(a >> 32) & 0xffffu; r.z = bytes; r.w = 0x00020000u; return r;
}
DEV __amdgpu_buffer_rsrc_t mkrsrc(const void* p, unsigned bytes) { return __builtin_amdgcn_make_buffer_rsrc((void*)p, 0, bytes, 0x00020000); }
#define LD_P(v, r, off, sc) "buffer_load_dwordx4 " v ", " r ", %0 offen offset:" off sc " lds\n\t"
#define LD_H(v, r, off, sc) "s_mov_b32 exec_hi, 0\n\tbuffer_load_dwordx4 " v ", " r ", %0 offen offset:" off sc " lds\n\ts_mov_b32 exec_hi, -1"
#define HD_OR  "s_add_u32 m0, %1, %2\n\ts_or_b32 %0, %3, %4\n\t"
#define HD_ADD "s_add_u32 m0, %1, %2\n\ts_add_u32 %0, %3, %4\n\t"
#define HD_MAD(M) "s_mul_i32 %0, %3, " M "\n\ts_add_u32 m0, %1, %2\n\ts_add_u32 %0, %0, %4\n\t"
#define DMA_IN(IMM) "s"(lds), "n"(IMM), "s"(a), "s"(b)
template <int IMM> DEV void dma_w4_or(unsigned lds, unsigned a, unsigned b, u32x4 ra, u32x4 rb, unsigned v0, unsigned v1, unsigned v2, unsigned v3) { unsigned t;
  asm volatile(HD_OR LD_P("%7", "%5", "0", "") LD_P("%8", "%5", "1024", "") LD_P("%9", "%6", "2048", "") LD_P("%10", "%6", "3072", "")
               : "=&s"(t) : DMA_IN(IMM), "s"(ra), "s"(rb), "v"(v0), "v"(v1), "v"(v2), "v"(v3) : "memory", "scc"); }
template <int IMM> DEV void dma_w2_or(unsigned lds, unsigned a, unsigned b, u32x4 r, unsigned v0, unsigned v1) { unsigned t;
  asm volatile(HD_OR LD_P("%6", "%5", "0", "") LD_P("%7", "%5", "1024", "") : "=&s"(t) : DMA_IN(IMM), "s"(r), "v"(v0), "v"(v1) : "memory", "scc"); }
template <int IMM, int SOFF> DEV void dma_w2h(unsigned lds, unsigned a, u32x4 r, unsigned v0, unsigned v1, unsigned v2) { unsigned t;
  asm volatile("s_add_u32 m0, %1, %2\n\ts_add_u32 %0, %3, %4\n\t" LD_P("%6", "%5", "0", "") LD_P("%7", "%5", "1024", "") LD_H("%8", "%5", "2048", "")
               : "=&s"(t) : "s"(lds), "n"(IMM), "s"(a), "n"(SOFF), "s"(r), "v"(v0), "v"(v1), "v"(v2) : "memory", "scc"); }
template <int IMM, int SOFF> DEV void dma_x2(unsigned lds, unsigned a, u32x4 r, unsigned v) { unsigned t;
  asm volatile("s_add_u32 m0, %1, %2\n\ts_add_u32 %0, %3, %4\n\t" LD_P("%6", "%5", "0", " sc1") LD_P("%6", "%5", "1024", " sc1") : "=&s"(t) : "s"(lds), "n"(IMM), "s"(a), "n"(SOFF), "s"(r), "v"(v) : "memory", "scc"); }
template <int IMM, int SOFF> DEV void dma_x2h(unsigned lds, unsigned a, u32x4 r, unsigned v) { unsigned t;
  asm volatile("s_add_u32 m0, %1, %2\n\ts_add_u32 %0, %3, %4\n\t" LD_P("%6", "%5", "0", " sc1") LD_P("%6", "%5", "1024", " sc1") LD_H("%6", "%5", "2048", " sc1") : "=&s"(t) : "s"(lds), "n"(IMM), "s"(a), "n"(SOFF), "s"(r), "v"(v) : "memory", "scc"); }
template <int IMM, int SOFF> DEV void dma_x3h(unsigned lds, unsigned a, u32x4 r, unsigned v) { unsigned t;
  asm volatile("s_add_u32 m0, %1, %2\n\ts_add_u32 %0, %3, %4\n\t" LD_P("%6", "%5", "0", " sc1") LD_P("%6", "%5", "1024", " sc1") LD_P("%6", "%5", "2048", " sc1") LD_H("%6", "%5", "3072", " sc1") : "=&s"(t) : "s"(lds), "n"(IMM), "s"(a), "n"(SOFF), "s"(r), "v"(v) : "memory", "scc"); }
template <int IMM> DEV void dma_x2h_mad640(unsigned lds, unsigned a, unsigned b, u32x4 r, unsigned v) { unsigned t;
  asm volatile(HD_MAD("640") LD_P("%6", "%5", "0", " sc1") LD_P("%6", "%5", "1024", " sc1") LD_H("%6", "%5", "2048", " sc1") : "=&s"(t) : DMA_IN(IMM), "s"(r), "v"(v) : "memory", "scc"); }
template <int IMM> DEV void dma_x3h_mad880(unsigned lds, unsigned a, unsigned b, u32x4 r, unsigned v) { unsigned t;
  asm volatile(HD_MAD("880") LD_P("%6", "%5", "0", " sc1") LD_P("%6", "%5", "1024", " sc1") LD_P("%6", "%5", "2048", " sc1") LD_H("%6", "%5", "3072", " sc1") : "=&s"(t) : DMA_IN(IMM), "s"(r), "v"(v) : "memory", "scc"); }
DEV void dma_1(unsigned lds, u32x4 r, unsigned v) { asm volatile("s_mov_b32 m0, %0\n\ts_nop 0\n\tbuffer_load_dwordx4 %2, %1, 0 offen sc1 lds" ::"s"(lds), "s"(r), "v"(v) : "memory"); }
template <int N> DEV void wait_bar() { asm volatile("s_waitcnt vmcnt(%0)\n\ts_barrier" ::"n"(N) : "memory"); }
DEV void warm(unsigned lds, u32x4 r, unsigned v) { asm volatile("s_mov_b32 m0, %0\n\ts_nop 0\n\tbuffer_load_dword %2, %1, 0 offen lds" ::"s"(lds), "s"(r), "v"(v) : "memory"); }
constexpr unsigned WBIAS = 4096;
DEV u32x4 mkrs_b(const void* p, unsigned bytes) { return mkrs((const char*)p - WBIAS, bytes + WBIAS); }
constexpr int SC1 = 16;
DEV void st16(__amdgpu_buffer_rsrc_t r, unsigned off, u32x4 v) { __builtin_amdgcn_raw_buffer_store_b128(v, r, off, 0, SC1); }
DEV u32x4 ld16(__amdgpu_buffer_rsrc_t r, unsigned off) { return __builtin_amdgcn_raw_buffer_load_b128(r, off, 0, SC1); }
DEV f32x4 ld16f(__amdgpu_buffer_rsrc_t r, unsigned off) { return __builtin_bit_cast(f32x4, ld16(r, off)); }

template <int CTRL> DEV float dpp_add(float v) { return v + __builtin_bit_cast(float, __builtin_amdgcn_update_dpp(0, __builtin_bit_cast(int, v), CTRL, 0xf, 0xf, true)); }
DEV float xsum_16(float v) { v = dpp_add<0xB1>(v); v = dpp_add<0x4E>(v); v = dpp_add<0x141>(v); return dpp_add<0x140>(v); }
DEV float xsum_r16(float v) { auto r = __builtin_amdgcn_permlane16_swap(__float_as_uint(v), __float_as_uint(v), false, false); return __uint_as_float(r[0]) + __uint_as_float(r[1]); }
DEV float xsum_r32(float v) { auto r = __builtin_amdgcn_permlane32_swap(__float_as_uint(v), __float_as_uint(v), false, false); return __uint_as_float(r[0]) + __uint_as_float(r[1]); }
DEV float xsum_q(float v) { return xsum_r32(xsum_r16(v)); }
DEV float gelu_tanh(float x) {
  const float k1 = -2.0f * 0.7978845608028654f * 1.4426950408889634f;
  const float t = x * __builtin_fmaf(x * x, 0.044715f * k1, k1);
  return x * __builtin_amdgcn_rcpf(1.0f + __builtin_amdgcn_exp2f(t));
}
DEV f32x2 gelu2(f32x2 x) {
  const float k1 = -2.0f * 0.7978845608028654f * 1.4426950408889634f;
  const f32x2 t = x * ((x * x) * (0.044715f * k1) + k1);
  const f32x2 d = (f32x2){__builtin_amdgcn_exp2f(t[0]), __builtin_amdgcn_exp2f(t[1])} + 1.0f;
  return x * (f32x2){__builtin_amdgcn_rcpf(d[0]), __builtin_amdgcn_rcpf(d[1])};
}
DEV u32x4 swap_pair(u32x2 a, u32x2 b) {
  auto r0 = __builtin_amdgcn_permlane16_swap(a.x, b.x, false, false);
  auto r1 = __builtin_amdgcn_permlane16_swap(a.y, b.y, false, false);
  return (u32x4){r0[0], r1[0], r0[1], r1[1]};
}

DEV bool spin_fail(unsigned& spins, char* ws) {
  __builtin_amdgcn_s_sleep(1);
  if (++spins > (1u << 17)) { __hip_atomic_store((gu32*)(ws + WS_TMO), 1u, RLX_AGENT); return true; }
  return false;
}
DEV void set_flag(char* ws, unsigned off, unsigned tag) { __hip_atomic_store((gu32*)(ws + off), tag, RLX_AGENT); }
DEV void wait_flags8(char* ws, unsigned off, unsigned tag, int lane) {
  for (unsigned spins = 0;;) {
    const unsigned v = lane < 8 ? __hip_atomic_load((gu32*)(ws + off) + lane, RLX_AGENT) : tag;
    if (__all(v == tag)) break;
    if (spin_fail(spins, ws)) break;
  }
}
DEV bool flags_ok(u32x4 v, unsigned tag) { return __all(v.x == tag && v.y == tag && v.z == tag && v.w == tag); }
DEV u32x4 peek256(__amdgpu_buffer_rsrc_t rf, unsigned off, int lane) { return ld16(rf, off + (lane >> 1) * 128 + (lane & 1) * 16); }
DEV void wait256(__amdgpu_buffer_rsrc_t rf, char* ws, unsigned off, unsigned tag, int lane) {
  for (unsigned spins = 0;;) { if (flags_ok(peek256(rf, off, lane), tag)) break; if (spin_fail(spins, ws)) break; }
}

DEV void wait_panels(__amdgpu_buffer_rsrc_t rf, char* ws, unsigned off, int first, int count, unsigned tag, int lane) {
  const unsigned o = lane < 2 * count ? off + (((lane >> 1) + first) & 31) * 128 + (lane & 1) * 16 : off + (first & 31) * 128;
  for (unsigned spins = 0;;) { if (flags_ok(ld16(rf, o), tag)) break; if (spin_fail(spins, ws)) break; }
}

template <int NS, int S_, int CW, int CX, class IW, class IX>
DEV void loader_run(IW&& issueW, IX&& issueX) {
  static_for<0, NS>([&](auto ic) { issueX(ic); });
  wait_bar<(NS - 1) * CX>();
  static_for<0, S_ - 1>([&](auto ic) { constexpr int n = decltype(ic)::value;
    constexpr int left = S_ - 2 - n;
    constexpr int cnt = n < NS - 2 ? (NS - 2) * CX + n * CW : (left < NS - 2 ? left : NS - 2) * (CW + CX);
    wait_bar<cnt>();
    if constexpr (n + NS < S_) { issueW(std::integral_constant<int, n + NS>{}); issueX(std::integral_constant<int, n + NS>{}); } });
}

template <int NS, int S_, int NA, int NB, int NM, class Rd, class Mm, class Hook, class Post>
DEV void ksplit_steps(bf16x8 (&A0)[NA], bf16x8 (&B0)[NB], bf16x8 (&A1)[NA], bf16x8 (&B1)[NB], int& slot, int n0, int n1, Rd&& rd, Mm&& mm, Hook&& hook, Post&& post) {
  constexpr int NR = NA + NB;
#define KS_ILV() do { _Pragma("unroll") for (int z = 0; z < NR; ++z) { __builtin_amdgcn_sched_group_barrier(0x8, 1, 0); __builtin_amdgcn_sched_group_barrier(0x100, 1, 0); } __builtin_amdgcn_sched_group_barrier(0x8, NM - NR, 0); } while (0)
#pragma unroll 1
  for (int n = n0; n < n1; n += 2) {
    const int s1 = slot + 1 == NS ? 0 : slot + 1;
    hook(n); wait_lgkm0(); bar(); post(n); sched_fence();
    rd(A1, B1, s1, n + 1); mm(A0, B0, false); KS_ILV(); sched_fence();
    const int s2 = s1 + 1 == NS ? 0 : s1 + 1;
    hook(n + 1); wait_lgkm0(); bar(); post(n + 1); sched_fence();
    rd(A0, B0, s2, n + 2); mm(A1, B1, false); KS_ILV(); sched_fence();
    slot = s2;
  }
}
template <int NS, int S_, int NA, int NB, class Rd, class Mm>
DEV void ksplit_last(bf16x8 (&A0)[NA], bf16x8 (&B0)[NB], bf16x8 (&A1)[NA], bf16x8 (&B1)[NB], int& slot, Rd&& rd, Mm&& mm) {
  const int s1 = slot + 1 == NS ? 0 : slot + 1;
  wait_lgkm0(); bar(); sched_fence();
  rd(A1, B1, s1, S_ - 1); mm(A0, B0, false); mm(A1, B1, true);
  slot = s1;
}
template <int NS, int S_, int NA, int NB, class Rd, class Mm>
DEV void ksplit_loop(Rd&& rd, Mm&& mm) {
  static_assert((S_ & 1) == 0, "even step count");
  bf16x8 A0[NA], B0[NB], A1[NA], B1[NB];
  bar();
  rd(A0, B0, 0, 0);
  int slot = 0;
  ksplit_steps<NS, S_, NA, NB, NA * NB>(A0, B0, A1, B1, slot, 0, S_ - 2, rd, mm, [](int) {}, [](int) {});
  ksplit_last<NS, S_, NA, NB>(A0, B0, A1, B1, slot, rd, mm);
}
template <int N>
DEV void kreduce(f32x4 (&acc)[N][2], f32x4 (&res)[N], LAS char* scratch, int w, int lane) {
  const int kh = w & 1;
  LAS char* mine = scratch + w * (N * 1024) + lane * 16;
  LAS char* other = scratch + (w ^ 1) * (N * 1024) + lane * 16;
  if (kh) { asm volatile("");
#pragma unroll
    for (int i = 0; i < N; ++i) { *(LAS f32x4*)(mine + i * 1024) = acc[i][0]; res[i] = acc[i][1]; }
  } else { asm volatile("");
#pragma unroll
    for (int i = 0; i < N; ++i) { *(LAS f32x4*)(mine + i * 1024) = acc[i][1]; res[i] = acc[i][0]; }
  }
  wait_lgkm0(); bar();
#pragma unroll
  for (int i = 0; i < N; ++i) res[i] += *(LAS f32x4*)(other + i * 1024);
}

#define WARM 1
#define JROT 1
#define A_PRE 1
constexpr int A_NS = 5, A_SLOT = 26 * 1024;
constexpr int A_SCR = 52 * 1024;
constexpr int P0_T = 130 * 1024, P0_PT = 140 * 1024, P0_RED = 156 * 1024;
constexpr int B_NS = 6, B_SLOT = 18 * 1024, B_SCR = 36 * 1024;
constexpr int C_NS = 5, C_X = 0, C_W = 82 * 1024, C_WSLOT = 14 * 1024;
constexpr int C_SCR = C_W + 2 * C_WSLOT;
constexpr int D_NS = 5, D_SLOT = 22 * 1024, D_SCR = 110 * 1024;
constexpr int E_NS = 6, E_SLOT = 18 * 1024, E_SCR = 110 * 1024;
constexpr int T_MODB = 152 * 1024;
constexpr int T_CV = T_MODB + 3072 + 64;
constexpr int T_MOD = 158 * 1024;
constexpr int T_DUMMY = T_MOD + 1792;
constexpr int LDS_BYTES = 160 * 1024;
static_assert(T_CV + 896 <= T_MOD && C_W + C_NS * C_WSLOT <= T_MODB && D_SCR + 28 * 1024 <= T_MODB && E_SCR + 20 * 1024 <= T_MODB, "LDS map");

#define IC(n) std::integral_constant<int, (n)>{}
DEV void loader_main(const Params& P, const unsigned lds0, LAS char* smem, const int lw, const int lane_, const int b, const int p, const int j, const int krot) {
  STAMP_DECL STAMP(0);
  char* const ws = P.ws;
  const int ft0 = 27 * (j >> 2) + 7 * (j & 3);
#define LLANE int lane = lane_; asm volatile("" : "+v"(lane)); const unsigned vx = lane * 16; (void)vx;
#define LV4K const unsigned v4k0 = (unsigned)(lane >> 3) * 4096u + (unsigned)(((lane & 7) ^ (lane >> 4)) * 16), v4k1 = (unsigned)(8 + (lane >> 3)) * 4096u + (unsigned)(((lane & 7) ^ (4 + (lane >> 4))) * 16);
  {
    LLANE LV4K
    const u32x4 rre = mkrs_b(P.wsr, S * S * 2), rim = mkrs_b(P.wsi, S * S * 2), rx = mkrs(ws + WS_XN, S * D * 2);
    const unsigned wrow = (unsigned)(64 * p + 16 * lw) * 4096u;
    const unsigned w0 = v4k0 + WBIAS, w1 = v4k1 + WBIAS - 1024, w2 = v4k0 + WBIAS - 2048, w3 = v4k1 + WBIAS - 3072;
    const unsigned lw_w = lds0 + lw * 4096, lw_x = lds0 + 16384 + lw * 2560;
    const unsigned xo = (unsigned)(((lw >> 1) * 40 + 5 * j) * 1024 + (lw & 1) * 2560);
    unsigned kw = krot * 128, kx = kw;
    auto issueW = [&](auto ic) { dma_w4_or<(decltype(ic)::value % A_NS) * A_SLOT>(lw_w, kw, wrow, rre, rim, w0, w1, w2, w3); kw = (kw + 128) & 0xf80; };
    auto issueX = [&](auto ic) { dma_x2h_mad640<(decltype(ic)::value % A_NS) * A_SLOT>(lw_x, kx, xo, rx, vx); kx = (kx + 128) & 0xf80; };
    static_for<0, A_PRE>(issueW);
    bar();
    bar();
    STAMP(30);
    const int idx = b >> 3, unit0 = (idx & 3) + 4 * ((b & 7) + 8 * (idx >> 2));
    const bool domod = unit0 < 240; const int unit = domod ? unit0 : 0;
    {
      u32x4 wm[5]; unsigned cs[5];
#pragma unroll
      for (int i = 0; i < 5; ++i) { const int row = lw * 160 + i * 32 + (lane >> 1);
        wm[i] = *(const u32x4*)(P.wmod + (size_t)row * (6 * D) + unit * 16 + (lane & 1) * 8); cs[i] = P.c[row]; }
      float a[8] = {0, 0, 0, 0, 0, 0, 0, 0};
#pragma unroll
      for (int i = 0; i < 5; ++i) { const float x = bflo(cs[i]); const float s = rbf(x * __builtin_amdgcn_rcpf(1.0f + __expf(-x)));
        a[0] += s * bflo(wm[i].x); a[1] += s * bfhi(wm[i].x); a[2] += s * bflo(wm[i].y); a[3] += s * bfhi(wm[i].y);
        a[4] += s * bflo(wm[i].z); a[5] += s * bfhi(wm[i].z); a[6] += s * bflo(wm[i].w); a[7] += s * bfhi(wm[i].w); }
#pragma unroll
      for (int e = 0; e < 8; ++e) { float v = dpp_add<0x4E>(a[e]);
        v = dpp_add<0x124>(v); v = dpp_add<0x128>(v);
        a[e] = xsum_q(v); }
      if (lane < 2) {
#pragma unroll
        for (int e = 0; e < 8; ++e) ((LAS float*)(smem + P0_RED))[lw * 16 + lane * 8 + e] = a[e];
      }
      wait_lgkm0();
    }
    STAMP(32);
    bar();
    STAMP(33);
    __builtin_amdgcn_s_setprio(3);
    auto istep = [&](auto ic) { if constexpr (decltype(ic)::value >= A_PRE) issueW(ic); issueX(ic); };
#define A_CNT(s_) (((s_) >= A_PRE ? 4 : 0) + 3)
    static_assert(A_NS == 5 && A_PRE <= 5, "the start-up schedule is written for five slots");
    istep(IC(0)); istep(IC(1));
    wait_bar<A_CNT(1)>();
    STAMP(34);
    istep(IC(2)); istep(IC(3));
    if (lw == 0 && domod && lane < 16) {
      const LAS float* red = (const LAS float*)(smem + P0_RED);
      float s = rbf((red[lane] + red[16 + lane]) + (red[32 + lane] + red[48 + lane]));
      const int n = unit * 16 + lane, seg = n / D;
      if (seg == 1 || seg == 4) s = rbf(1.0f + s);
      __hip_atomic_store((gu32*)(ws + WS_MOD) + n, __float_as_uint(s), RLX_AGENT);
      const unsigned hi = __float_as_uint(s) >> 16, nb = (unsigned)__builtin_amdgcn_update_dpp(0, (int)hi, 0xB1, 0xf, 0xf, true);
      if ((seg == 3 || seg == 4) && !(lane & 1)) __hip_atomic_store((gu32*)(ws + WS_MODB) + (((seg == 4 ? 0 : D) + n - seg * D) >> 1), hi | (nb << 16), RLX_AGENT);
    }
    wait_bar<A_CNT(2) + A_CNT(3)>();
    istep(IC(4)); istep(IC(5));
    wait_bar<A_CNT(3) + A_CNT(4) + A_CNT(5)>();
    istep(IC(6));
    wait_bar<A_CNT(4) + A_CNT(5) + A_CNT(6)>();
    istep(IC(7));
    const u32x4 rmod = mkrs(ws + WS_MOD, 3840 * 4);
    const int tl = lane / 20, tc = lane - 20 * tl;
    const unsigned tv0 = lane < 60 ? (unsigned)((tl * D + 80 * j) * 4 + tc * 16) : 0x7ffffff0u;
    const unsigned tv1 = lane < 40 ? (unsigned)(((4 + tl) * D + 80 * j) * 4 + tc * 16) : 0x7ffffff0u;
    static_for<3, 27>([&](auto ic) { constexpr int n = decltype(ic)::value;
      wait_bar<21>(); istep(IC(n + A_NS));
      if constexpr (n == 20) { if (lw == 0) { dma_1(lds0 + T_MOD, rmod, tv0); dma_1(lds0 + T_MOD + 960, rmod, tv1); } }
    });
    if (WARM) {
      { const int row = (j * 4 + lw) * 2 + (lane >> 5); warm(lds0 + T_DUMMY, mkrs(P.wsp, S * S * 2), (unsigned)(64 * p + row) * 4096u + (lane & 31) * 128); }
      warm(lds0 + T_DUMMY, mkrs((lw & 1) ? P.wdi : P.wdr, D * D * 2), lw < 2 && lane < 25 ? (unsigned)(b * 25 + lane) * 128 : 0x7ffffff0u);
    }
    static_for<0, A_NS - 1>([&](auto ic) { constexpr int r = A_NS - 2 - decltype(ic)::value; wait_bar<r * 7 + (WARM ? 2 : 0)>(); });
    bar();
  }
  {
    LLANE
    const u32x4 rre = mkrs_b(P.wdr, D * D * 2), rim = mkrs_b(P.wdi, D * D * 2), rx = mkrs(ws + WS_UV, 32 * 2 * 20 * 4096);
    auto wv = [&](int k) { const int r = 20 * lw + 8 * k + (lane >> 3); return (unsigned)(r * 1280 + (((lane & 7) ^ ((r >> 1) & 7)) * 16)) + WBIAS - 1024 * k; };
    const unsigned v0 = wv(0), v1 = wv(1), v2 = wv(2);
    const unsigned wb = (unsigned)(80 * j) * 1280u, lw_w = lds0 + lw * 2560, lw_x = lds0 + 10240 + lw * 2048, xb = (unsigned)p * 40 * 4096u + lw * 2048;
    auto issueW = [&](auto ic) { constexpr int n = decltype(ic)::value; dma_w2h<(n % B_NS) * B_SLOT, (n % 10) * 128>(lw_w, wb, n >= 10 ? rim : rre, v0, v1, v2); };
    auto issueX = [&](auto ic) { constexpr int n = decltype(ic)::value; dma_x2<(n % B_NS) * B_SLOT, 2 * n * 4096>(lw_x, xb, rx, vx); };
    __builtin_amdgcn_s_setprio(0);
    bar();
    static_for<0, B_NS>(issueW);
    bar();
    __builtin_amdgcn_s_setprio(3);
    loader_run<B_NS, 20, 3, 2>(issueW, issueX);
    bar();
  }
  {
    LLANE
    const u32x4 rw = mkrs(ws + WS_WIN, 20 * 109 * 1024), rx = mkrs(ws + WS_X1P, 32 * 20 * 4096), rmb = mkrs(ws + WS_MODB, 2560);
    const unsigned wo = (unsigned)(((lw >> 1) * 109 + ft0) * 1024 + (lw & 1) * 3584);
    const unsigned lw_w = lds0 + C_W + lw * 3584, lw_x = lds0 + C_X + lw * 2048, xb = (unsigned)p * 20 * 4096u + lw * 2048;
    auto issueW = [&](auto ic) { constexpr int n = decltype(ic)::value, z1 = n >= 10; dma_x3h<(n % C_NS) * C_WSLOT, (2 * (n - 10 * z1) * 109 + (z1 ? 0 : 54)) * 1024>(lw_w, wo, rw, vx); };
    auto issueX = [&](auto ic) { constexpr int n = decltype(ic)::value; dma_x2<2 * n * 4096, 2 * n * 4096>(lw_x, xb, rx, vx); };
    __builtin_amdgcn_s_setprio(0);
    bar();
    if (lw < 3) dma_1(lds0 + T_MODB + lw * 1024, rmb, vx + lw * 1024);
    static_for<0, C_NS>(issueW);
    bar();
    __builtin_amdgcn_s_setprio(3);
    static_for<0, C_NS>(issueX);
    wait_bar<(C_NS - 1) * 2>();
    static_for<0, 9>([&](auto ic) {
      constexpr int n = decltype(ic)::value, need = n + 1;
      constexpr int last = n + C_NS - 1 < 11 ? n + C_NS - 1 : 11;
      constexpr int cnt = [] { int c = 0;
        if (need < C_NS) { c += (C_NS - 1 - need) * 2; for (int s = C_NS; s <= last; ++s) c += (s < 10 ? 6 : 4); }
        else { for (int s = need + 1; s <= last; ++s) c += (s < 10 ? 6 : 4); }
        return c; }();
      wait_bar<cnt>();
      if constexpr (n + C_NS < 12) { issueW(IC(n + C_NS)); if constexpr (n + C_NS < 10) issueX(IC(n + C_NS)); }
    });
    bar();
    bar();
    issueW(IC(12)); issueW(IC(13)); issueW(IC(14));
    wait_bar<16>();
    static_for<10, 19>([&](auto ic) {
      constexpr int n = decltype(ic)::value;
      constexpr int last = n + 4 < 19 ? n + 4 : 19;
      wait_bar<(last - (n + 1)) * 4>();
      if constexpr (n + C_NS < 20) issueW(IC(n + C_NS));
    });
    bar();
  }
  {
    LLANE LV4K
    const u32x4 rw = mkrs_b(P.wsp, S * S * 2), rx = mkrs(ws + WS_Z2N, 64 * 55 * 1024);
    const unsigned wrow = (unsigned)(64 * p + 16 * lw) * 4096u, w0 = v4k0 + WBIAS, w1 = v4k1 + WBIAS - 1024;
    const unsigned xo = (unsigned)(((lw >> 1) * 55 + ft0) * 1024 + (lw & 1) * 3584), lw_w = lds0 + lw * 2048, lw_x = lds0 + 8192 + lw * 3584;
    unsigned kw = (b & 7) * 512, kx = kw;
    auto issueW = [&](auto ic) { dma_w2_or<(decltype(ic)::value % D_NS) * D_SLOT>(lw_w, kw, wrow, rw, w0, w1); kw = (kw + 128) & 0xf80; };
    auto issueX = [&](auto ic) { dma_x3h_mad880<(decltype(ic)::value % D_NS) * D_SLOT>(lw_x, kx, xo, rx, vx); kx = (kx + 128) & 0xf80; };
    static_for<0, D_NS>(issueW);
    loader_run<D_NS, 32, 2, 4>(issueW, issueX);
    bar();
  }
  {
    LLANE
    const u32x4 rw = mkrs(ws + WS_WOUT, 28 * 40 * 1024), rx = mkrs(ws + WS_P, 32 * 28 * 4096);
    const unsigned wo = (unsigned)(((lw >> 1) * 40 + 5 * j) * 1024 + (lw & 1) * 2560), lw_w = lds0 + lw * 2560, lw_x = lds0 + 10240 + lw * 2048, xb = (unsigned)p * 28 * 4096u + lw * 2048;
    auto issueW = [&](auto ic) { constexpr int n = decltype(ic)::value; dma_x2h<(n % E_NS) * E_SLOT, n * 80 * 1024>(lw_w, wo, rw, vx); };
    auto issueX = [&](auto ic) { constexpr int n = decltype(ic)::value; dma_x2<(n % E_NS) * E_SLOT, 2 * n * 4096>(lw_x, xb, rx, vx); };
    __builtin_amdgcn_s_setprio(0);
    bar();
    static_for<0, E_NS>(issueW);
    bar();
    __builtin_amdgcn_s_setprio(3);
    loader_run<E_NS, 14, 3, 2>(issueW, issueX);
    bar();
  }
  STAMP_FLUSH();
}

DEV void consumer_main(const Params& P, LAS char* smem, const int w, const int lane_, const int b, const int p, const int j) {
  STAMP_DECL STAMP(0);
  char* const ws = P.ws;
  unsigned tagv = __hip_atomic_load((gu32*)(ws + WS_EPOCH), RLX_AGENT) + 1u, tag;
  const int mh = w >> 1, kh = w & 1;
  const auto rws = mkrsrc(ws, WS_END);
  const int ft0 = 27 * (j >> 2) + 7 * (j & 3), nft = (j & 3) == 3 ? 6 : 7;
  auto rot = [&](int i) { return i < 3 ? i + 4 * mh : i - 3 * mh; };
#define LANE_IDS int lane = lane_; asm volatile("" : "+v"(lane)); const int q = lane >> 4, tk = lane & 15, tid = w * 64 + lane, token = 64 * p + 16 * w + tk; (void)q; (void)tk; (void)tid; (void)token;
  {
    LANE_IDS
    unsigned v[2][5];
#pragma unroll
    for (int t = 0; t < 2; ++t) { const unsigned* xr = (const unsigned*)(P.x + (size_t)(64 * p + 8 * j + 2 * w + t) * D);
#pragma unroll
      for (int i = 0; i < 5; ++i) v[t][i] = xr[lane + 64 * i]; }
    const int u0 = w * 256 + b; const bool dopt = u0 < 810; const int u = dopt ? u0 : 0; const bool isin = u < 540;
    const int uu = isin ? u : u - 540, ncb = isin ? 27 : 10, pkb = uu / ncb, pcb = uu - pkb * ncb, ld = isin ? F : D;
    u32x4 pv[4];
    { const bf16_t* src = (isin ? P.win : P.wout) + (size_t)(pkb * 32) * ld + pcb * 64;
#pragma unroll
      for (int i = 0; i < 4; ++i) pv[i] = *(const u32x4*)(src + (size_t)(i * 8 + (lane >> 3)) * ld + (lane & 7) * 8); }
    LAS unsigned* t32 = (LAS unsigned*)(smem + P0_T);
    unsigned pk[2][5];
#pragma unroll
    for (int t = 0; t < 2; ++t) {
      float s = 0.f;
#pragma unroll
      for (int i = 0; i < 5; ++i) s += bflo(v[t][i]) + bfhi(v[t][i]);
      const float mean = xsum_q(xsum_16(s)) * (1.0f / D);
      float q2 = 0.f;
#pragma unroll
      for (int i = 0; i < 5; ++i) { const float a = bflo(v[t][i]) - mean, c = bfhi(v[t][i]) - mean; q2 += a * a + c * c; }
      const float rs = 1.0f / sqrtf(xsum_q(xsum_16(q2)) * (1.0f / D) + 1e-5f);
#pragma unroll
      for (int i = 0; i < 5; ++i) pk[t][i] = pack2((bflo(v[t][i]) - mean) * rs, (bfhi(v[t][i]) - mean) * rs);
    }
#pragma unroll
    for (int i = 0; i < 5; ++i) { const int d = 2 * (lane + 64 * i);
      t32[d * 4 + w] = (pk[0][i] & 0xffffu) | (pk[1][i] << 16); t32[(d + 1) * 4 + w] = (pk[0][i] >> 16) | (pk[1][i] & 0xffff0000u); }
    asm volatile("" : "+v"(tagv)); tag = __builtin_amdgcn_readfirstlane(tagv);
    wait_lgkm0(); bar();
    const unsigned xb = WS_XN + (unsigned)((2 * p + (j >> 2)) * 40 * 1024 + (j & 3) * 256);
#pragma unroll
    for (int i = 0; i < 3; ++i) { const int d = tid + 256 * i;
      if (d < D) st16(rws, xb + (d >> 4) * 1024 + (d & 15) * 16, *(LAS u32x4*)(smem + P0_T + d * 16)); }
    wait_vm<0>();
    bar();
    STAMP(1);
    if (w == 0 && lane == 0) set_flag(ws, WS_FN + p * 128 + j * 4, tag);
    {
      LAS char* t = smem + P0_PT + w * 4096;
#pragma unroll
      for (int i = 0; i < 4; ++i) *(LAS u32x4*)(t + (i * 8 + (lane >> 3)) * 128 + (lane & 7) * 16) = pv[i];
      const unsigned dst = dopt ? (isin ? WS_WIN + (unsigned)(pkb * 109 + pcb * 4) * 1024 : WS_WOUT + (unsigned)(pkb * 40 + pcb * 4) * 1024) + lane * 16 : 0xfffff000u;
      const int rq = tk >> 2, pp = lane & 3;
#pragma unroll
      for (int c = 0; c < 4; ++c) {
        const u32x2 l2 = __builtin_bit_cast(u32x2, __builtin_amdgcn_ds_read_tr16_b64_v4i16((LAS s16x4*)(t + (8 * q + rq) * 128 + c * 32 + pp * 8)));
        const u32x2 h2 = __builtin_bit_cast(u32x2, __builtin_amdgcn_ds_read_tr16_b64_v4i16((LAS s16x4*)(t + (8 * q + 4 + rq) * 128 + c * 32 + pp * 8)));
        st16(rws, dst + (dopt ? c * 1024 : 0), (u32x4){l2.x, l2.y, h2.x, h2.y});
      }
    }
    if (w == 0) { wait256(rws, ws, WS_FN, tag, lane); STAMP(23); }
    bar();
    STAMP(2);
  }
  u32x2 xv[5];
  {
    LANE_IDS
    f32x4 acc[5][4], rsum[4];
#pragma unroll
    for (int t = 0; t < 4; ++t) { rsum[t] = (f32x4){0, 0, 0, 0};
#pragma unroll
      for (int i = 0; i < 5; ++i) acc[i][t] = (f32x4){0, 0, 0, 0}; }
    const bf16x8 ones = __builtin_bit_cast(bf16x8, (u32x4){0x3f803f80u, 0x3f803f80u, 0x3f803f80u, 0x3f803f80u});
    const unsigned wa = (unsigned)(mh * 8192 + tk * 128 + (((q + 4 * kh) ^ ((lane >> 1) & 7)) * 16));
    const unsigned xa = 16384 + kh * 5120 + lane * 16;
    auto rd = [&](bf16x8(&X)[5], bf16x8(&W)[4], int slot, int) { LAS char* sb = smem + slot * A_SLOT;
      W[0] = *(LAS bf16x8*)(sb + wa); X[0] = *(LAS bf16x8*)(sb + xa); W[1] = *(LAS bf16x8*)(sb + wa + 2048);
      W[2] = *(LAS bf16x8*)(sb + wa + 4096); W[3] = *(LAS bf16x8*)(sb + wa + 6144);
#pragma unroll
      for (int i = 1; i < 5; ++i) X[i] = *(LAS bf16x8*)(sb + xa + i * 1024); };
    auto mm = [&](bf16x8(&X)[5], bf16x8(&W)[4], bool) {
#pragma unroll
      for (int i = 0; i < 5; ++i)
#pragma unroll
        for (int t = 0; t < 4; ++t) acc[i][t] = mfma(X[i], W[t], acc[i][t]);
#pragma unroll
      for (int t = 0; t < 4; ++t) rsum[t] = mfma(ones, W[t], rsum[t]); };
    bf16x8 X0[5], W0[4], X1[5], W1[4];
    int slot = 0;
    auto nohook = [](int) {};
    bar();
    STAMP(3); CSTAMP(40);
    rd(X0, W0, 0, 0);
    ksplit_steps<A_NS, 32, 5, 4, 24>(X0, W0, X1, W1, slot, 0, 12, rd, mm,
        [&](int n) { if (n == 8) wait_vm<0>(); },
        [&](int n) { if (n == 8 && w == 0 && lane == 0) set_flag(ws, WS_F2 + p * 128 + j * 4, tag); });
    const u32x4 f2 = peek256(rws, WS_F2, lane);
    ksplit_steps<A_NS, 32, 5, 4, 24>(X0, W0, X1, W1, slot, 12, 18, rd, mm, nohook, nohook);
    if (!flags_ok(f2, tag)) wait256(rws, ws, WS_F2, tag, lane);
    if (b == 0 && w == 0 && lane == 0) set_flag(ws, WS_EPOCH, tag + 15);
#pragma unroll
    for (int i = 0; i < 5; ++i) xv[i] = *(const u32x2*)(P.x + (size_t)token * D + 80 * j + 16 * i + 4 * q);
    ksplit_steps<A_NS, 32, 5, 4, 24>(X0, W0, X1, W1, slot, 18, 30, rd, mm, nohook, nohook);
    ksplit_last<A_NS, 32, 5, 4>(X0, W0, X1, W1, slot, rd, mm);
    STAMP(4); CSTAMP(41);
    f32x4 ur[5], ui[5]; float rs0, rs1;
    {
      LAS char* mine = smem + A_SCR + w * 12288 + lane * 16; LAS char* other = smem + A_SCR + (w ^ 1) * 12288 + lane * 16;
      if (kh) { asm volatile("");
#pragma unroll
        for (int i = 0; i < 5; ++i) { *(LAS f32x4*)(mine + i * 2048) = acc[i][0]; *(LAS f32x4*)(mine + i * 2048 + 1024) = acc[i][1]; ur[i] = acc[i][2]; ui[i] = acc[i][3]; }
        *(LAS f32x2*)(mine + 10240 - lane * 8) = (f32x2){rsum[0][0], rsum[1][0]}; rs0 = rsum[2][0]; rs1 = rsum[3][0];
      } else { asm volatile("");
#pragma unroll
        for (int i = 0; i < 5; ++i) { *(LAS f32x4*)(mine + i * 2048) = acc[i][2]; *(LAS f32x4*)(mine + i * 2048 + 1024) = acc[i][3]; ur[i] = acc[i][0]; ui[i] = acc[i][1]; }
        *(LAS f32x2*)(mine + 10240 - lane * 8) = (f32x2){rsum[2][0], rsum[3][0]}; rs0 = rsum[0][0]; rs1 = rsum[1][0];
      }
      wait_lgkm0(); bar();
#pragma unroll
      for (int i = 0; i < 5; ++i) { ur[i] += *(LAS f32x4*)(other + i * 2048); ui[i] += *(LAS f32x4*)(other + i * 2048 + 1024); }
      const f32x2 r = *(LAS f32x2*)(other + 10240 - lane * 8); rs0 += r[0]; rs1 += r[1];
    }
#pragma unroll
    for (int i = 0; i < 5; ++i) {
      u32x2 a, c;
      const f32x4 sa = *(const LAS f32x4*)(smem + T_MOD + (16 * i + 4 * q) * 4), c1 = *(const LAS f32x4*)(smem + T_MOD + (80 + 16 * i + 4 * q) * 4);
      { const f32x4 u = ur[i] * c1 + rs0 * sa; a.x = pack2(u[0], u[1]); a.y = pack2(u[2], u[3]); }
      { const f32x4 v = -(ui[i] * c1 + rs1 * sa); c.x = pack2(v[0], v[1]); c.y = pack2(v[2], v[3]); }
      const int dt = 5 * j + i, kb = dt >> 1, qq = 2 * (dt & 1) + (q >> 1);
      st16(rws, WS_UV + (unsigned)(((p * 2 + (q & 1)) * 20 + kb) * 4 + w) * 1024 + (qq * 16 + tk) * 16, swap_pair(a, c));
    }
    wait_vm<0>(); bar();
    STAMP(5);
    if (w == 0) { if (lane == 0) set_flag(ws, WS_FU + p * 128 + j * 4, tag); wait_flags8(ws, WS_FU + p * 128, tag, lane); }
    bar();
    STAMP(6); CSTAMP(44);
  }
  u32x2 x1p[5];
  {
    LANE_IDS
    f32x4 acc[5][2];
#pragma unroll
    for (int i = 0; i < 5; ++i) { acc[i][0] = (f32x4){0, 0, 0, 0}; acc[i][1] = (f32x4){0, 0, 0, 0}; }
    const unsigned wa = (unsigned)(tk * 128 + (((q + 4 * kh) ^ ((lane >> 1) & 7)) * 16));
    const unsigned xa = 10240 + (kh * 4 + 2 * mh) * 1024 + lane * 16;
    ksplit_loop<B_NS, 20, 5, 2>(
        [&](bf16x8(&A)[5], bf16x8(&Bf)[2], int slot, int) { LAS char* sb = smem + slot * B_SLOT;
          A[0] = *(LAS bf16x8*)(sb + wa); Bf[0] = *(LAS bf16x8*)(sb + xa); Bf[1] = *(LAS bf16x8*)(sb + xa + 1024);
#pragma unroll
          for (int i = 1; i < 5; ++i) A[i] = *(LAS bf16x8*)(sb + wa + i * 2048); },
        [&](bf16x8(&A)[5], bf16x8(&Bf)[2], bool) {
#pragma unroll
          for (int i = 0; i < 5; ++i) { acc[i][0] = mfma(A[i], Bf[0], acc[i][0]); acc[i][1] = mfma(A[i], Bf[1], acc[i][1]); } });
    STAMP(7); CSTAMP(45);
    f32x4 y[5];
    kreduce<5>(acc, y, smem + B_SCR, w, lane);
    float xs[20]; u32x2 xp[5];
    float s = 0.f;
#pragma unroll
    for (int i = 0; i < 5; ++i) {
      const f32x4 ga = *(const LAS f32x4*)(smem + T_MOD + (160 + 16 * i + 4 * q) * 4), c1b = *(const LAS f32x4*)(smem + T_MOD + 960 + (16 * i + 4 * q) * 4);
      x1p[i].x = pack2(bflo(xv[i].x) + ga[0] * y[i][0], bfhi(xv[i].x) + ga[1] * y[i][1]);
      x1p[i].y = pack2(bflo(xv[i].y) + ga[2] * y[i][2], bfhi(xv[i].y) + ga[3] * y[i][3]);
      asm volatile("" : "+v"(x1p[i].x), "+v"(x1p[i].y));
      xs[4 * i] = bflo(x1p[i].x); xs[4 * i + 1] = bfhi(x1p[i].x); xs[4 * i + 2] = bflo(x1p[i].y); xs[4 * i + 3] = bfhi(x1p[i].y);
      s += (xs[4 * i] + xs[4 * i + 1]) + (xs[4 * i + 2] + xs[4 * i + 3]);
      xp[i].x = pack2(xs[4 * i] * c1b[0], xs[4 * i + 1] * c1b[1]); xp[i].y = pack2(xs[4 * i + 2] * c1b[2], xs[4 * i + 3] * c1b[3]);
    }
    const unsigned img = WS_X1P + (unsigned)p * 20 * 4096 + w * 1024;
#pragma unroll
    for (int i = 0; i < 6; i += 2) {
      const int i2 = i + 1 < 5 ? i + 1 : i;
      const int dt = 5 * j + ((q & 1) ? i2 : i), kb = dt >> 1, qq = 2 * (dt & 1) + (q >> 1);
      st16(rws, img + kb * 4096 + (qq * 16 + tk) * 16, swap_pair(xp[i], xp[i2]));
    }
    const float mean = xsum_q(s) * (1.0f / 80.0f);
    float m2 = 0.f;
#pragma unroll
    for (int e = 0; e < 20; ++e) { const float d = xs[e] - mean; m2 += d * d; }
    m2 = xsum_q(m2);
    if (q == 0) __hip_atomic_store((gu64*)(ws + WS_ST1) + token * 8 + j, ((unsigned long long)__float_as_uint(m2) << 32) | __float_as_uint(mean), RLX_AGENT);
    wait_vm<0>(); bar();
    STAMP(8);
    if (w == 0) { if (lane == 0) set_flag(ws, WS_FX + p * 128 + j * 4, tag); wait_flags8(ws, WS_FX + p * 128, tag, lane); }
    bar();
    STAMP(9);
  }
  u32x2 z1p[7]; float bias;
  {
    LANE_IDS
    u32x4 st[4];
#pragma unroll
    for (int k = 0; k < 4; ++k) st[k] = ld16(rws, WS_ST1 + (unsigned)token * 64 + k * 16);
    bias = bflo(P.bsp[token]);
    for (int i = tid; i < 448; i += 256) ((LAS float*)(smem + T_CV))[i] = 0.f;
    f32x4 acc[7][2], aux[4];
    auto clear = [&]() {
#pragma unroll
      for (int i = 0; i < 7; ++i) { acc[i][0] = (f32x4){0, 0, 0, 0}; acc[i][1] = (f32x4){0, 0, 0, 0}; }
#pragma unroll
      for (int i = 0; i < 4; ++i) aux[i] = (f32x4){0, 0, 0, 0};
    };
    const unsigned xa = C_X + (kh * 4 + 2 * mh) * 1024 + lane * 16, wlo = C_W + kh * 7168 + lane * 16 + mh * 4096, whi = C_W + kh * 7168 + lane * 16 - mh * 3072;
    const int r4 = tk & 3;
    const unsigned ma = T_MODB + (r4 == 0 ? q * 16 + kh * 64 : r4 == 1 ? 1280 + q * 16 + kh * 64 : 2560), mstride = r4 < 2 ? 128 : 0;
    int nbase = 0;
    auto rd = [&](bf16x8(&A)[7], bf16x8(&Bf)[3], int slot, int n) {
      LAS char* sl = smem + wlo + slot * C_WSLOT; LAS char* sh = smem + whi + slot * C_WSLOT; LAS char* xb = smem + xa + n * 8192;
      Bf[0] = *(LAS bf16x8*)(xb); A[0] = *(LAS bf16x8*)(sl); Bf[1] = *(LAS bf16x8*)(xb + 1024);
      A[1] = *(LAS bf16x8*)(sl + 1024); A[2] = *(LAS bf16x8*)(sl + 2048);
#pragma unroll
      for (int i = 3; i < 7; ++i) A[i] = *(LAS bf16x8*)(sh + i * 1024);
      Bf[2] = *(LAS bf16x8*)(smem + ma + n * mstride);
    };
    auto auxmm = [&](bf16x8(&A)[7], bf16x8(&Bf)[3]) {
#pragma unroll
      for (int i = 0; i < 4; ++i) aux[i] = mfma(Bf[2], A[i], aux[i]);
    };
    auto mm2 = [&](bf16x8(&A)[7], bf16x8(&Bf)[3], bool) {
#pragma unroll
      for (int i = 0; i < 7; ++i) { acc[i][0] = mfma(Bf[0], A[i], acc[i][0]); acc[i][1] = mfma(Bf[1], A[i], acc[i][1]); }
      auxmm(A, Bf); };
    auto mm1 = [&](bf16x8(&A)[7], bf16x8(&Bf)[3], bool) {
#pragma unroll
      for (int i = 0; i < 7; ++i) { acc[i][0] = mfma(A[i], Bf[0], acc[i][0]); acc[i][1] = mfma(A[i], Bf[1], acc[i][1]); }
      auxmm(A, Bf); };
    auto put_cv = [&](int pass_) {
      LAS float* cv = (LAS float*)(smem + T_CV) + pass_ * 224;
      if (q == 0) {
#pragma unroll
        for (int i = 0; i < 4; ++i) if (mh == 0 || i < 3) { const int t = rot(i);
          __hip_atomic_fetch_add(cv + 16 * t + tk, aux[i][0], __ATOMIC_RELAXED, __HIP_MEMORY_SCOPE_WORKGROUP);
          __hip_atomic_fetch_add(cv + 112 + 16 * t + tk, aux[i][1], __ATOMIC_RELAXED, __HIP_MEMORY_SCOPE_WORKGROUP); }
      }
    };
    float rmean, rrstd;
    { float mu[8], mean = 0.f;
#pragma unroll
      for (int k = 0; k < 4; ++k) { mu[2 * k] = __uint_as_float(st[k].x); mu[2 * k + 1] = __uint_as_float(st[k].z); mean += mu[2 * k] + mu[2 * k + 1]; }
      mean *= 0.125f; float M2 = 0.f;
#pragma unroll
      for (int k = 0; k < 4; ++k) { const float d0 = mu[2 * k] - mean, d1 = mu[2 * k + 1] - mean; M2 += __uint_as_float(st[k].y) + __uint_as_float(st[k].w) + 80.0f * (d0 * d0 + d1 * d1); }
      rmean = mean; rrstd = 1.0f / sqrtf(M2 * (1.0f / D) + 1e-5f); }
    (void)nbase;
    bf16x8 A0[7], B0[3], A1[7], B1[3];
    int slot = 0;
    auto nohook = [](int) {};
    clear();
    wait_lgkm0(); bar();
    STAMP(10); CSTAMP(46);
    rd(A0, B0, 0, 0);
    ksplit_steps<C_NS, 10, 7, 3, 18>(A0, B0, A1, B1, slot, 0, 8, rd, mm2, nohook, nohook);
    ksplit_last<C_NS, 10, 7, 3>(A0, B0, A1, B1, slot, rd, mm2);
    STAMP(11); CSTAMP(47);
    put_cv(0);
    f32x4 zs[7];
    kreduce<7>(acc, zs, smem + C_SCR, w, lane);
    wait_lgkm0(); bar();
    f32x2 mr01, mr23, rr01, rr23;
    mr01 = (f32x2){__shfl(rmean, 4 * q), __shfl(rmean, 4 * q + 1)}; mr23 = (f32x2){__shfl(rmean, 4 * q + 2), __shfl(rmean, 4 * q + 3)};
    rr01 = (f32x2){__shfl(rrstd, 4 * q), __shfl(rrstd, 4 * q + 1)}; rr23 = (f32x2){__shfl(rrstd, 4 * q + 2), __shfl(rrstd, 4 * q + 3)};
    f32x2 z01[7], z23[7];
#pragma unroll
    for (int i = 0; i < 7; ++i) {
      const float c1 = ((const LAS float*)(smem + T_CV))[16 * rot(i) + tk], c2 = ((const LAS float*)(smem + T_CV))[112 + 16 * rot(i) + tk];
      z01[i] = gelu2(rr01 * ((f32x2){zs[i][0], zs[i][1]} - mr01 * c1) + c2);
      z23[i] = gelu2(rr23 * ((f32x2){zs[i][2], zs[i][3]} - mr23 * c1) + c2);
    }
    {
      const float inv = 1.0f / (16.0f * nft);
      const f32x2 zero2 = (f32x2){0.f, 0.f};
      f32x2 s01 = zero2, s23 = zero2;
#pragma unroll
      for (int i = 0; i < 7; ++i) { const bool ok = (i != 2 && i != 6) || rot(i) < nft; s01 += ok ? z01[i] : zero2; s23 += ok ? z23[i] : zero2; }
      const f32x2 m01 = (f32x2){xsum_16(s01[0]), xsum_16(s01[1])} * inv, m23 = (f32x2){xsum_16(s23[0]), xsum_16(s23[1])} * inv;
      f32x2 t01 = zero2, t23 = zero2;
#pragma unroll
      for (int i = 0; i < 7; ++i) { const bool ok = (i != 2 && i != 6) || rot(i) < nft; const f32x2 a = ok ? z01[i] - m01 : zero2, c = ok ? z23[i] - m23 : zero2; t01 += a * a; t23 += c * c; }
      const float q0 = xsum_16(t01[0]), q1 = xsum_16(t01[1]), q2 = xsum_16(t23[0]), q3 = xsum_16(t23[1]);
      if (tk < 8) { const int r = tk & 3; const float a = r == 0 ? m01[0] : r == 1 ? m01[1] : r == 2 ? m23[0] : m23[1], c = r == 0 ? q0 : r == 1 ? q1 : r == 2 ? q2 : q3;
        const int tok = 64 * p + 16 * w + 4 * q + r;
        __hip_atomic_store((gu64*)(ws + WS_ST2) + (tok * 8 + j) * 2 + (tk >> 2), ((unsigned long long)tag << 32) | __float_as_uint(tk < 4 ? a : c), RLX_AGENT); }
    }
    STAMP(12);
    {
      u32x4 g0, g1;
      const unsigned go = WS_ST2 + (unsigned)(token * 8 + 2 * q) * 16;
      for (unsigned spins = 0;;) { g0 = ld16(rws, go); g1 = ld16(rws, go + 16);
        if (__all(g0.y == tag && g0.w == tag && g1.y == tag && g1.w == tag)) break; if (spin_fail(spins, ws)) break; }
      const float n0 = 112.0f, n1 = (q & 1) ? 96.0f : 112.0f;
      const float m0 = __uint_as_float(g0.x), m1 = __uint_as_float(g1.x);
      const float mean = xsum_q(n0 * m0 + n1 * m1) * (1.0f / FH);
      const float d0 = m0 - mean, d1 = m1 - mean;
      const float M2 = xsum_q(__uint_as_float(g0.z) + __uint_as_float(g1.z) + n0 * d0 * d0 + n1 * d1 * d1);
      const float rstd = 1.0f / sqrtf(M2 * (1.0f / FH) + 1e-5f);
      mr01 = (f32x2){__shfl(mean, 4 * q), __shfl(mean, 4 * q + 1)}; mr23 = (f32x2){__shfl(mean, 4 * q + 2), __shfl(mean, 4 * q + 3)};
      rr01 = (f32x2){__shfl(rstd, 4 * q), __shfl(rstd, 4 * q + 1)}; rr23 = (f32x2){__shfl(rstd, 4 * q + 2), __shfl(rstd, 4 * q + 3)};
    }
    STAMP(13);
    {
      u32x2 pk[7];
#pragma unroll
      for (int i = 0; i < 7; ++i) { const f32x2 a = (z01[i] - mr01) * rr01, c = (z23[i] - mr23) * rr23; pk[i].x = pack2(a[0], a[1]); pk[i].y = pack2(c[0], c[1]); }
      const unsigned img = WS_Z2N + (unsigned)(2 * p + (w >> 1)) * 55 * 1024 + ((2 * (w & 1) + (q >> 1)) * 16 + tk) * 16;
#pragma unroll
      for (int i = 0; i < 8; i += 2) {
        const int i2 = i + 1 < 7 ? i + 1 : i;
        const u32x4 ch = swap_pair(pk[i], pk[i2]);
        const int ti = (q & 1) ? rot(i2) : rot(i);
        st16(rws, ti < nft ? img + (ft0 + ti) * 1024 : 0xfffff000u, ch);
      }
    }
    STAMP(14); CSTAMP(48);
    clear();
    slot = 0;
    bar();
    rd(A0, B0, 0, 0);
    ksplit_steps<C_NS, 10, 7, 3, 18>(A0, B0, A1, B1, slot, 0, 6, rd, mm1,
        [&](int n) { if (n == 2) wait_vm<0>(); },
        [&](int n) { if (n == 2 && w == 0 && lane == 0) set_flag(ws, WS_FZ + (j * 4 + (p >> 3)) * 128 + (p & 7) * 4, tag); });
    const unsigned fzo = WS_FZ + (j * 4 + ((lane & 7) >> 1)) * 128 + (lane & 1) * 16;
    const u32x4 fz = ld16(rws, fzo);
    ksplit_steps<C_NS, 10, 7, 3, 18>(A0, B0, A1, B1, slot, 6, 8, rd, mm1, nohook, nohook);
    ksplit_last<C_NS, 10, 7, 3>(A0, B0, A1, B1, slot, rd, mm1);
    if (!flags_ok(fz, tag)) { for (unsigned spins = 0;;) { if (flags_ok(ld16(rws, fzo), tag)) break; if (spin_fail(spins, ws)) break; } }
    STAMP(15); CSTAMP(49);
    put_cv(1);
    kreduce<7>(acc, zs, smem + C_SCR, w, lane);
#pragma unroll
    for (int i = 0; i < 7; ++i) {
      const LAS float* cv = (const LAS float*)(smem + T_CV) + 224;
      const f32x4 c1 = *(const LAS f32x4*)(cv + 16 * rot(i) + 4 * q), c2 = *(const LAS f32x4*)(cv + 112 + 16 * rot(i) + 4 * q);
      const f32x2 a = gelu2(rrstd * ((f32x2){zs[i][0], zs[i][1]} - rmean * (f32x2){c1[0], c1[1]}) + (f32x2){c2[0], c2[1]});
      const f32x2 c = gelu2(rrstd * ((f32x2){zs[i][2], zs[i][3]} - rmean * (f32x2){c1[2], c1[3]}) + (f32x2){c2[2], c2[3]});
      z1p[i] = (u32x2){pack2(a[0], a[1]), pack2(c[0], c[1])};
      asm volatile("" : "+v"(z1p[i].x), "+v"(z1p[i].y));
    }
    STAMP(16); CSTAMP(42);
  }
  {
    LANE_IDS
    f32x4 acc[7][2];
#pragma unroll
    for (int i = 0; i < 7; ++i) { acc[i][0] = (f32x4){0, 0, 0, 0}; acc[i][1] = (f32x4){0, 0, 0, 0}; }
    const unsigned wa = (unsigned)((2 * mh) * 2048 + tk * 128 + (((q + 4 * kh) ^ ((lane >> 1) & 7)) * 16));
    const unsigned xlo = 8192 + kh * 7168 + lane * 16 + mh * 4096, xhi = 8192 + kh * 7168 + lane * 16 - mh * 3072;
    ksplit_loop<D_NS, 32, 7, 2>(
        [&](bf16x8(&A)[7], bf16x8(&Bf)[2], int slot, int) { LAS char* sb = smem + slot * D_SLOT;
          Bf[0] = *(LAS bf16x8*)(sb + wa); A[0] = *(LAS bf16x8*)(sb + xlo); Bf[1] = *(LAS bf16x8*)(sb + wa + 2048);
          A[1] = *(LAS bf16x8*)(sb + xlo + 1024); A[2] = *(LAS bf16x8*)(sb + xlo + 2048);
#pragma unroll
          for (int i = 3; i < 7; ++i) A[i] = *(LAS bf16x8*)(sb + xhi + i * 1024); },
        [&](bf16x8(&A)[7], bf16x8(&Bf)[2], bool) {
#pragma unroll
          for (int i = 0; i < 7; ++i) { acc[i][0] = mfma(A[i], Bf[0], acc[i][0]); acc[i][1] = mfma(A[i], Bf[1], acc[i][1]); } });
    STAMP(17); CSTAMP(43);
    f32x4 g[7];
    kreduce<7>(acc, g, smem + D_SCR, w, lane);
    u32x2 pk[7];
#pragma unroll
    for (int i = 0; i < 7; ++i) {
      pk[i].x = pack2(bflo(z1p[i].x) * (g[i][0] + bias), bfhi(z1p[i].x) * (g[i][1] + bias));
      pk[i].y = pack2(bflo(z1p[i].y) * (g[i][2] + bias), bfhi(z1p[i].y) * (g[i][3] + bias));
    }
    const unsigned img = WS_P + (unsigned)p * 28 * 4096 + w * 1024;
#pragma unroll
    for (int i = 0; i < 8; i += 2) {
      const int i2 = i + 1 < 7 ? i + 1 : i;
      const u32x4 ch = swap_pair(pk[i], pk[i2]);
      const int ti = (q & 1) ? rot(i2) : rot(i), ft = ft0 + ti, kb = ft >> 1, qq = 2 * (ft & 1) + (q >> 1);
      st16(rws, ti < nft ? img + kb * 4096 + (qq * 16 + tk) * 16 : 0xfffff000u, ch);
    }
    wait_vm<0>(); bar();
    STAMP(18);
    if (w == 0) { if (lane == 0) set_flag(ws, WS_FP + p * 128 + j * 4, tag); wait_flags8(ws, WS_FP + p * 128, tag, lane); }
    bar();
    STAMP(19);
  }
  {
    LANE_IDS
    f32x4 acc[5][2];
#pragma unroll
    for (int i = 0; i < 5; ++i) { acc[i][0] = (f32x4){0, 0, 0, 0}; acc[i][1] = (f32x4){0, 0, 0, 0}; }
    const unsigned wa = kh * 5120 + lane * 16, xa = 10240 + (kh * 4 + 2 * mh) * 1024 + lane * 16;
    ksplit_loop<E_NS, 14, 5, 2>(
        [&](bf16x8(&A)[5], bf16x8(&Bf)[2], int slot, int) { LAS char* sb = smem + slot * E_SLOT;
          A[0] = *(LAS bf16x8*)(sb + wa); Bf[0] = *(LAS bf16x8*)(sb + xa); Bf[1] = *(LAS bf16x8*)(sb + xa + 1024);
#pragma unroll
          for (int i = 1; i < 5; ++i) A[i] = *(LAS bf16x8*)(sb + wa + i * 1024); },
        [&](bf16x8(&A)[5], bf16x8(&Bf)[2], bool last) {
          if (!(last && kh)) {
#pragma unroll
            for (int i = 0; i < 5; ++i) { acc[i][0] = mfma(A[i], Bf[0], acc[i][0]); acc[i][1] = mfma(A[i], Bf[1], acc[i][1]); } } });
    STAMP(20);
    f32x4 o[5];
    kreduce<5>(acc, o, smem + E_SCR, w, lane);
    u32x2 pk[5];
#pragma unroll
    for (int i = 0; i < 5; ++i) {
      const f32x4 gb = *(const LAS f32x4*)(smem + T_MOD + 960 + (80 + 16 * i + 4 * q) * 4);
      pk[i].x = pack2(bflo(x1p[i].x) + gb[0] * o[i][0], bfhi(x1p[i].x) + gb[1] * o[i][1]);
      pk[i].y = pack2(bflo(x1p[i].y) + gb[2] * o[i][2], bfhi(x1p[i].y) + gb[3] * o[i][3]);
    }
    bf16_t* orow = P.out + (size_t)token * D + 80 * j;
#pragma unroll
    for (int i = 0; i < 6; i += 2) {
      const int i2 = i + 1 < 5 ? i + 1 : i;
      const u32x4 ch = swap_pair(pk[i], pk[i2]);
      *(u32x4*)(orow + 16 * ((q & 1) ? i2 : i) + 8 * (q >> 1)) = ch;
    }
    STAMP(21);
  }
  STAMP_FLUSH();
}

__global__ __launch_bounds__(NT, 2) void fused_block(Params P) {
  extern __shared__ __attribute__((aligned(16))) char smem_[];
  LAS char* smem = (LAS char*)smem_;
  const int tid = threadIdx.x, lane = tid & 63, wid = __builtin_amdgcn_readfirstlane(tid >> 6), b = blockIdx.x;
  const int xcd = b & 7, idx = b >> 3, p = xcd * 4 + (idx >> 3), j = idx & 7;
  if (wid >= 4) loader_main(P, (unsigned)(size_t)smem, smem, wid - 4, lane, b, p, j, JROT ? ((j + xcd) & 7) * 4 : xcd * 4);
  else consumer_main(P, smem, wid, lane, b, p, j);
}

extern "C" void kernel_launch(void* const* d_in, const int* in_sizes, int n_in, void* d_out, int out_size, void* d_ws, size_t ws_size, hipStream_t stream) {
  Params P;
  P.x = (const bf16_t*)d_in[0]; P.wsr = (const bf16_t*)d_in[1]; P.wsi = (const bf16_t*)d_in[2];
  P.wdr = (const bf16_t*)d_in[3]; P.wdi = (const bf16_t*)d_in[4]; P.win = (const bf16_t*)d_in[6];
  P.wsp = (const bf16_t*)d_in[7]; P.bsp = (const bf16_t*)d_in[8]; P.wout = (const bf16_t*)d_in[9];
  P.c = (const bf16_t*)d_in[10]; P.wmod = (const bf16_t*)d_in[11];
  P.out = (bf16_t*)d_out; P.ws = (char*)d_ws;
  (void)hipFuncSetAttribute((const void*)fused_block, hipFuncAttributeMaxDynamicSharedMemorySize, LDS_BYTES);
  fused_block<<<256, NT, LDS_BYTES, stream>>>(P);
}
```

```cpp
#include <hip/hip_runtime.h>
#include <stdint.h>
#include <type_traits>

typedef unsigned short bf16_t;
typedef short bf16x8 __attribute__((ext_vector_type(8)));
typedef short s16x4 __attribute__((ext_vector_type(4)));
typedef float f32x4 __attribute__((ext_vector_type(4)));
typedef float f32x2 __attribute__((ext_vector_type(2)));
typedef unsigned u32x4 __attribute__((ext_vector_type(4)));
typedef unsigned u32x2 __attribute__((ext_vector_type(2)));
typedef __bf16 bf2_t __attribute__((ext_vector_type(2)));
#define LAS __attribute__((address_space(3)))
#define GAS __attribute__((address_space(1)))
typedef GAS unsigned gu32;
typedef GAS unsigned long long gu64;
#define RLX_AGENT __ATOMIC_RELAXED, __HIP_MEMORY_SCOPE_AGENT
#define DEV __device__ __forceinline__

constexpr int S = 2048, D = 640, F = 1728, FH = 864;
constexpr int NT = 512;

constexpr unsigned WS_EPOCH = 0;
constexpr unsigned WS_TMO   = 256;
constexpr unsigned WS_FN = 4096, WS_F2 = 8192, WS_FU = 12288, WS_FX = 16384, WS_FP = 20480, WS_FZ = 24576;
constexpr unsigned WS_MOD  = 65536;
constexpr unsigned WS_MODB = WS_MOD + 16384;

constexpr unsigned WS_XN   = 131072;
constexpr unsigned WS_WIN  = WS_XN + 64 * 40 * 1024;
constexpr unsigned WS_WOUT = WS_WIN + 20 * 109 * 1024;
constexpr unsigned WS_UV   = WS_WOUT + 27 * 40 * 1024;
constexpr unsigned WS_X1P  = WS_UV + 32 * 2 * 20 * 4096;
constexpr unsigned WS_ST1  = WS_X1P + 32 * 20 * 4096;
constexpr unsigned WS_Z2N  = WS_ST1 + 2048 * 64;
constexpr unsigned WS_ST2  = WS_Z2N + 64 * 55 * 1024;
constexpr unsigned WS_P    = WS_ST2 + 2048 * 128;
constexpr unsigned WS_END  = WS_P + 32 * 28 * 4096;

struct Params {
  const bf16_t *x, *wsr, *wsi, *wdr, *wdi, *win, *wsp, *bsp, *wout, *c, *wmod;
  bf16_t* out;
  char* ws;
};

#define STAMP_DECL
#define STAMP(i) do {} while (0)
#define STAMP_FLUSH() do {} while (0)
#define CSTAMP(i) do {} while (0)

template <int N> DEV void wait_vm() { asm volatile("s_waitcnt vmcnt(%0)" ::"n"(N) : "memory"); }
DEV void wait_lgkm0() { asm volatile("s_waitcnt lgkmcnt(0)" ::: "memory"); }
DEV void bar() { asm volatile("" ::: "memory"); __builtin_amdgcn_s_barrier(); asm volatile("" ::: "memory"); }
DEV void sched_fence() { __builtin_amdgcn_sched_barrier(0); }
template <int I, int N, class Fn> DEV void static_for(Fn&& f) { if constexpr (I < N) { f(std::integral_constant<int, I>{}); static_for<I + 1, N>(f); } }

DEV float bflo(unsigned v) { return __uint_as_float(v << 16); }
DEV float bfhi(unsigned v) { return __uint_as_float(v & 0xffff0000u); }
DEV unsigned pack2(float lo, float hi) { bf2_t v; v[0] = (__bf16)lo; v[1] = (__bf16)hi; return __builtin_bit_cast(unsigned, v); }
DEV float rbf(float f) { return bflo(pack2(f, f)); }
DEV float dot2(unsigned a, unsigned b, float c) { return __builtin_amdgcn_fdot2_f32_bf16(__builtin_bit_cast(bf2_t, a), __builtin_bit_cast(bf2_t, b), c, false); }
DEV f32x4 mfma(bf16x8 a, bf16x8 b, f32x4 c) { return __builtin_amdgcn_mfma_f32_16x16x32_bf16(a, b, c, 0, 0, 0); }

DEV u32x4 mkrs(const void* p, unsigned bytes) {
  const unsigned long long a = (unsigned long long)p;
  u32x4 r; r.x = (unsigned)a; r.y = (unsigned)(a >> 32) & 0xffffu; r.z = bytes; r.w = 0x00020000u; return r;
}
DEV __amdgpu_buffer_rsrc_t mkrsrc(const void* p, unsigned bytes) { return __builtin_amdgcn_make_buffer_rsrc((void*)p, 0, bytes, 0x00020000); }
#define LD_P(v, r, off, sc) "buffer_load_dwordx4 " v ", " r ", %0 offen offset:" off sc " lds\n\t"
#define LD_H(v, r, off, sc) "s_mov_b32 exec_hi, 0\n\tbuffer_load_dwordx4 " v ", " r ", %0 offen offset:" off sc " lds\n\ts_mov_b32 exec_hi, -1"
#define HD_OR  "s_add_u32 m0, %1, %2\n\ts_or_b32 %0, %3, %4\n\t"
#define HD_ADD "s_add_u32 m0, %1, %2\n\ts_add_u32 %0, %3, %4\n\t"
#define HD_MAD(M) "s_mul_i32 %0, %3, " M "\n\ts_add_u32 m0, %1, %2\n\ts_add_u32 %0, %0, %4\n\t"
#define DMA_IN(IMM) "s"(lds), "n"(IMM), "s"(a), "s"(b)
template <int IMM> DEV void dma_w4_or(unsigned lds, unsigned a, unsigned b, u32x4 ra, u32x4 rb, unsigned v0, unsigned v1, unsigned v2, unsigned v3) { unsigned t;
  asm volatile(HD_OR LD_P("%7", "%5", "0", "") LD_P("%8", "%5", "1024", "") LD_P("%9", "%6", "2048", "") LD_P("%10", "%6", "3072", "")
               : "=&s"(t) : DMA_IN(IMM), "s"(ra), "s"(rb), "v"(v0), "v"(v1), "v"(v2), "v"(v3) : "memory", "scc"); }
template <int IMM> DEV void dma_w2_or(unsigned lds, unsigned a, unsigned b, u32x4 r, unsigned v0, unsigned v1) { unsigned t;
  asm volatile(HD_OR LD_P("%6", "%5", "0", "") LD_P("%7", "%5", "1024", "") : "=&s"(t) : DMA_IN(IMM), "s"(r), "v"(v0), "v"(v1) : "memory", "scc"); }
template <int IMM, int SOFF> DEV void dma_w2h(unsigned lds, unsigned a, u32x4 r, unsigned v0, unsigned v1, unsigned v2) { unsigned t;
  asm volatile("s_add_u32 m0, %1, %2\n\ts_add_u32 %0, %3, %4\n\t" LD_P("%6", "%5", "0", "") LD_P("%7", "%5", "1024", "") LD_H("%8", "%5", "2048", "")
               : "=&s"(t) : "s"(lds), "n"(IMM), "s"(a), "n"(SOFF), "s"(r), "v"(v0), "v"(v1), "v"(v2) : "memory", "scc"); }
template <int IMM, int SOFF> DEV void dma_x2(unsigned lds, unsigned a, u32x4 r, unsigned v) { unsigned t;
  asm volatile("s_add_u32 m0, %1, %2\n\ts_add_u32 %0, %3, %4\n\t" LD_P("%6", "%5", "0", " sc1") LD_P("%6", "%5", "1024", " sc1") : "=&s"(t) : "s"(lds), "n"(IMM), "s"(a), "n"(SOFF), "s"(r), "v"(v) : "memory", "scc"); }
template <int IMM, int SOFF> DEV void dma_x2h(unsigned lds, unsigned a, u32x4 r, unsigned v) { unsigned t;
  asm volatile("s_add_u32 m0, %1, %2\n\ts_add_u32 %0, %3, %4\n\t" LD_P("%6", "%5", "0", " sc1") LD_P("%6", "%5", "1024", " sc1") LD_H("%6", "%5", "2048", " sc1") : "=&s"(t) : "s"(lds), "n"(IMM), "s"(a), "n"(SOFF), "s"(r), "v"(v) : "memory", "scc"); }
template <int IMM, int SOFF> DEV void dma_x3h(unsigned lds, unsigned a, u32x4 r, unsigned v) { unsigned t;
  asm volatile("s_add_u32 m0, %1, %2\n\ts_add_u32 %0, %3, %4\n\t" LD_P("%6", "%5", "0", " sc1") LD_P("%6", "%5", "1024", " sc1") LD_P("%6", "%5", "2048", " sc1") LD_H("%6", "%5", "3072", " sc1") : "=&s"(t) : "s"(lds), "n"(IMM), "s"(a), "n"(SOFF), "s"(r), "v"(v) : "memory", "scc"); }
template <int IMM> DEV void dma_x2h_mad640(unsigned lds, unsigned a, unsigned b, u32x4 r, unsigned v) { unsigned t;
  asm volatile(HD_MAD("640") LD_P("%6", "%5", "0", " sc1") LD_P("%6", "%5", "1024", " sc1") LD_H("%6", "%5", "2048", " sc1") : "=&s"(t) : DMA_IN(IMM), "s"(r), "v"(v) : "memory", "scc"); }
template <int IMM> DEV void dma_x3h_mad880(unsigned lds, unsigned a, unsigned b, u32x4 r, unsigned v) { unsigned t;
  asm volatile(HD_MAD("880") LD_P("%6", "%5", "0", " sc1") LD_P("%6", "%5", "1024", " sc1") LD_P("%6", "%5", "2048", " sc1") LD_H("%6", "%5", "3072", " sc1") : "=&s"(t) : DMA_IN(IMM), "s"(r), "v"(v) : "memory", "scc"); }
DEV void dma_1(unsigned lds, u32x4 r, unsigned v) { asm volatile("s_mov_b32 m0, %0\n\ts_nop 0\n\tbuffer_load_dwordx4 %2, %1, 0 offen sc1 lds" ::"s"(lds), "s"(r), "v"(v) : "memory"); }
template <int N> DEV void wait_bar() { asm volatile("s_waitcnt vmcnt(%0)\n\ts_barrier" ::"n"(N) : "memory"); }
DEV void warm(unsigned lds, u32x4 r, unsigned v) { asm volatile("s_mov_b32 m0, %0\n\ts_nop 0\n\tbuffer_load_dword %2, %1, 0 offen lds" ::"s"(lds), "s"(r), "v"(v) : "memory"); }
constexpr unsigned WBIAS = 4096;
DEV u32x4 mkrs_b(const void* p, unsigned bytes) { return mkrs((const char*)p - WBIAS, bytes + WBIAS); }
constexpr int SC1 = 16;
DEV void st16(__amdgpu_buffer_rsrc_t r, unsigned off, u32x4 v) { __builtin_amdgcn_raw_buffer_store_b128(v, r, off, 0, SC1); }
DEV u32x4 ld16(__amdgpu_buffer_rsrc_t r, unsigned off) { return __builtin_amdgcn_raw_buffer_load_b128(r, off, 0, SC1); }
DEV f32x4 ld16f(__amdgpu_buffer_rsrc_t r, unsigned off) { return __builtin_bit_cast(f32x4, ld16(r, off)); }

template <int CTRL> DEV float dpp_add(float v) { return v + __builtin_bit_cast(float, __builtin_amdgcn_update_dpp(0, __builtin_bit_cast(int, v), CTRL, 0xf, 0xf, true)); }
DEV float xsum_16(float v) { v = dpp_add<0xB1>(v); v = dpp_add<0x4E>(v); v = dpp_add<0x141>(v); return dpp_add<0x140>(v); }
DEV float xsum_r16(float v) { auto r = __builtin_amdgcn_permlane16_swap(__float_as_uint(v), __float_as_uint(v), false, false); return __uint_as_float(r[0]) + __uint_as_float(r[1]); }
DEV float xsum_r32(float v) { auto r = __builtin_amdgcn_permlane32_swap(__float_as_uint(v), __float_as_uint(v), false, false); return __uint_as_float(r[0]) + __uint_as_float(r[1]); }
DEV float xsum_q(float v) { return xsum_r32(xsum_r16(v)); }
DEV float gelu_tanh(float x) {
  const float k1 = -2.0f * 0.7978845608028654f * 1.4426950408889634f;
  const float t = x * __builtin_fmaf(x * x, 0.044715f * k1, k1);
  return x * __builtin_amdgcn_rcpf(1.0f + __builtin_amdgcn_exp2f(t));
}
DEV f32x2 gelu2(f32x2 x) {
  const float k1 = -2.0f * 0.7978845608028654f * 1.4426950408889634f;
  const f32x2 t = x * ((x * x) * (0.044715f * k1) + k1);
  const f32x2 d = (f32x2){__builtin_amdgcn_exp2f(t[0]), __builtin_amdgcn_exp2f(t[1])} + 1.0f;
  return x * (f32x2){__builtin_amdgcn_rcpf(d[0]), __builtin_amdgcn_rcpf(d[1])};
}
DEV u32x4 swap_pair(u32x2 a, u32x2 b) {
  auto r0 = __builtin_amdgcn_permlane16_swap(a.x, b.x, false, false);
  auto r1 = __builtin_amdgcn_permlane16_swap(a.y, b.y, false, false);
  return (u32x4){r0[0], r1[0], r0[1], r1[1]};
}

DEV bool spin_fail(unsigned& spins, char* ws) {
  __builtin_amdgcn_s_sleep(1);
  if (++spins > (1u << 17)) { __hip_atomic_store((gu32*)(ws + WS_TMO), 1u, RLX_AGENT); return true; }
  return false;
}
DEV void set_flag(char* ws, unsigned off, unsigned tag) { __hip_atomic_store((gu32*)(ws + off), tag, RLX_AGENT); }
DEV void wait_flags8(char* ws, unsigned off, unsigned tag, int lane) {
  for (unsigned spins = 0;;) {
    const unsigned v = lane < 8 ? __hip_atomic_load((gu32*)(ws + off) + lane, RLX_AGENT) : tag;
    if (__all(v == tag)) break;
    if (spin_fail(spins, ws)) break;
  }
}
DEV bool flags_ok(u32x4 v, unsigned tag) { return __all(v.x == tag && v.y == tag && v.z == tag && v.w == tag); }
DEV u32x4 peek256(__amdgpu_buffer_rsrc_t rf, unsigned off, int lane) { return ld16(rf, off + (lane >> 1) * 128 + (lane & 1) * 16); }
DEV void wait256(__amdgpu_buffer_rsrc_t rf, char* ws, unsigned off, unsigned tag, int lane) {
  for (unsigned spins = 0;;) { if (flags_ok(peek256(rf, off, lane), tag)) break; if (spin_fail(spins, ws)) break; }
}

DEV void wait_panels(__amdgpu_buffer_rsrc_t rf, char* ws, unsigned off, int first, int count, unsigned tag, int lane) {
  const unsigned o = lane < 2 * count ? off + (((lane >> 1) + first) & 31) * 128 + (lane & 1) * 16 : off + (first & 31) * 128;
  for (unsigned spins = 0;;) { if (flags_ok(ld16(rf, o), tag)) break; if (spin_fail(spins, ws)) break; }
}

template <int NS, int PF, int S_, int CW, int CX, class IW, class IX>
DEV void loader_run(IW&& issueW, IX&& issueX) {
  constexpr auto cnt = [](int s) { return (s >= PF ? CW : 0) + CX; };
  constexpr auto span = [=](int a, int b) { int c = 0; for (int s = a; s <= b; ++s) c += (s >= PF ? CW : 0) + CX; return c; };
  auto step = [&](auto ic) { if constexpr (decltype(ic)::value >= PF) issueW(ic); issueX(ic); };
  static_for<0, NS>(step);
  wait_bar<span(1, NS - 1)>();
  static_for<0, S_ - 1>([&](auto ic) { constexpr int n = decltype(ic)::value;
    constexpr int last = n + NS - 1 < S_ - 1 ? n + NS - 1 : S_ - 1;
    wait_bar<span(n + 2, last)>();
    if constexpr (n + NS < S_) step(std::integral_constant<int, n + NS>{}); });
  (void)cnt;
}

template <int NS, int S_, int NA, int NB, int NM, class Rd, class Mm, class Hook, class Post>
DEV void ksplit_steps(bf16x8 (&A0)[NA], bf16x8 (&B0)[NB], bf16x8 (&A1)[NA], bf16x8 (&B1)[NB], int& slot, int n0, int n1, Rd&& rd, Mm&& mm, Hook&& hook, Post&& post) {
  constexpr int NR = NA + NB;
#define KS_ILV() do { _Pragma("unroll") for (int z = 0; z < NR; ++z) { __builtin_amdgcn_sched_group_barrier(0x8, 1, 0); __builtin_amdgcn_sched_group_barrier(0x100, 1, 0); } __builtin_amdgcn_sched_group_barrier(0x8, NM - NR, 0); } while (0)
#pragma unroll 1
  for (int n = n0; n < n1; n += 2) {
    const int s1 = slot + 1 == NS ? 0 : slot + 1;
    hook(n); wait_lgkm0(); bar(); post(n); sched_fence();
    rd(A1, B1, s1, n + 1); mm(A0, B0, false); KS_ILV(); sched_fence();
    const int s2 = s1 + 1 == NS ? 0 : s1 + 1;
    hook(n + 1); wait_lgkm0(); bar(); post(n + 1); sched_fence();
    rd(A0, B0, s2, n + 2); mm(A1, B1, false); KS_ILV(); sched_fence();
    slot = s2;
  }
}
template <int NS, int S_, int NA, int NB, class Rd, class Mm>
DEV void ksplit_last(bf16x8 (&A0)[NA], bf16x8 (&B0)[NB], bf16x8 (&A1)[NA], bf16x8 (&B1)[NB], int& slot, Rd&& rd, Mm&& mm) {
  const int s1 = slot + 1 == NS ? 0 : slot + 1;
  wait_lgkm0(); bar(); sched_fence();
  rd(A1, B1, s1, S_ - 1); mm(A0, B0, false); mm(A1, B1, true);
  slot = s1;
}
template <int NS, int S_, int NA, int NB, class Rd, class Mm>
DEV void ksplit_loop(Rd&& rd, Mm&& mm) {
  static_assert((S_ & 1) == 0, "even step count");
  bf16x8 A0[NA], B0[NB], A1[NA], B1[NB];
  bar();
  rd(A0, B0, 0, 0);
  int slot = 0;
  ksplit_steps<NS, S_, NA, NB, NA * NB>(A0, B0, A1, B1, slot, 0, S_ - 2, rd, mm, [](int) {}, [](int) {});
  ksplit_last<NS, S_, NA, NB>(A0, B0, A1, B1, slot, rd, mm);
}
template <int N>
DEV void kreduce(f32x4 (&acc)[N][2], f32x4 (&res)[N], LAS char* scratch, int w, int lane) {
  const int kh = w & 1;
  LAS char* mine = scratch + w * (N * 1024) + lane * 16;
  LAS char* other = scratch + (w ^ 1) * (N * 1024) + lane * 16;
  if (kh) { asm volatile("");
#pragma unroll
    for (int i = 0; i < N; ++i) { *(LAS f32x4*)(mine + i * 1024) = acc[i][0]; res[i] = acc[i][1]; }
  } else { asm volatile("");
#pragma unroll
    for (int i = 0; i < N; ++i) { *(LAS f32x4*)(mine + i * 1024) = acc[i][1]; res[i] = acc[i][0]; }
  }
  wait_lgkm0(); bar();
#pragma unroll
  for (int i = 0; i < N; ++i) res[i] += *(LAS f32x4*)(other + i * 1024);
}

#define WARM 1
#define JROT 1
#define A_PRE 2
#define PFW 6
#define PRE1 2
#define PFC 5
#define A_NS_ 6
#define B_NS_ 6
#define D_NS_ 5
#define E_NS_ 6
constexpr int A_NS = A_NS_, A_SLOT = 26 * 1024, A_SCR = 52 * 1024;
constexpr int P0_T = 130 * 1024, P0_PT = 140 * 1024, P0_RED = 157 * 1024;
constexpr int B_NS = B_NS_, B_SLOT = 18 * 1024, B_SCR = (B_NS_ == 8 ? 0 : 36 * 1024);
constexpr int C_NS = 5, C_X = 0, C_W = 82 * 1024, C_WSLOT = 14 * 1024;
constexpr int C_SCR = C_W + 2 * C_WSLOT;
constexpr int D_NS = D_NS_, D_SLOT = 22 * 1024, D_SCR = (D_NS_ == 7 ? 0 : 110 * 1024);
constexpr int E_NS = E_NS_, E_SLOT = 18 * 1024, E_SCR = (E_NS_ == 8 ? 0 : 110 * 1024);
constexpr int T_MODB = 152 * 1024;
constexpr int T_CV = T_MODB + 3072 + 64;
constexpr int T_MOD = 158 * 1024;
constexpr int T_DUMMY = T_MOD + 1792;
constexpr int LDS_BYTES = 160 * 1024;
static_assert(T_CV + 1792 <= P0_RED && P0_RED + 256 <= T_MOD && C_W + C_NS * C_WSLOT <= T_MODB && A_NS * A_SLOT <= P0_RED && D_NS * D_SLOT <= T_CV && B_NS * B_SLOT <= T_MODB && E_NS * E_SLOT <= T_MODB, "LDS map");

#define IC(n) std::integral_constant<int, (n)>{}
DEV void loader_main(const Params& P, const unsigned lds0, LAS char* smem, const int lw, const int lane_, const int b, const int p, const int j, const int krot) {
  STAMP_DECL STAMP(0);
  char* const ws = P.ws;
  const int ft0 = 27 * (j >> 2) + 7 * (j & 3);
#define LLANE int lane = lane_; asm volatile("" : "+v"(lane)); const unsigned vx = lane * 16; (void)vx;
#define LV4K const unsigned v4k0 = (unsigned)(lane >> 3) * 4096u + (unsigned)(((lane & 7) ^ (lane >> 4)) * 16), v4k1 = (unsigned)(8 + (lane >> 3)) * 4096u + (unsigned)(((lane & 7) ^ (4 + (lane >> 4))) * 16);
  {
    LLANE LV4K
    const u32x4 rre = mkrs_b(P.wsr, S * S * 2), rim = mkrs_b(P.wsi, S * S * 2), rx = mkrs(ws + WS_XN, S * D * 2);
    const unsigned wrow = (unsigned)(64 * p + 16 * lw) * 4096u;
    const unsigned w0 = v4k0 + WBIAS, w1 = v4k1 + WBIAS - 1024, w2 = v4k0 + WBIAS - 2048, w3 = v4k1 + WBIAS - 3072;
    const unsigned lw_w = lds0 + lw * 4096, lw_x = lds0 + 16384 + lw * 2560;
    const unsigned xo = (unsigned)(((lw >> 1) * 40 + 5 * j) * 1024 + (lw & 1) * 2560);
    unsigned kw = krot * 128, kx = kw;
    auto issueW = [&](auto ic) { dma_w4_or<(decltype(ic)::value % A_NS) * A_SLOT>(lw_w, kw, wrow, rre, rim, w0, w1, w2, w3); kw = (kw + 128) & 0xf80; };
    auto issueX = [&](auto ic) { dma_x2h_mad640<(decltype(ic)::value % A_NS) * A_SLOT>(lw_x, kx, xo, rx, vx); kx = (kx + 128) & 0xf80; };
    static_for<0, A_PRE>(issueW);
    bar();
    bar();
    STAMP(30);
    const int idx = b >> 3, unit0 = (idx & 3) + 4 * ((b & 7) + 8 * (idx >> 2));
    const bool domod = unit0 < 240; const int unit = domod ? unit0 : 0;
    {
      u32x4 wm[5]; unsigned cs[5];
#pragma unroll
      for (int i = 0; i < 5; ++i) { const int row = lw * 160 + i * 32 + (lane >> 1);
        wm[i] = *(const u32x4*)(P.wmod + (size_t)row * (6 * D) + unit * 16 + (lane & 1) * 8); cs[i] = P.c[row]; }
      float a[8] = {0, 0, 0, 0, 0, 0, 0, 0};
#pragma unroll
      for (int i = 0; i < 5; ++i) { const float x = bflo(cs[i]); const float s = rbf(x * __builtin_amdgcn_rcpf(1.0f + __expf(-x)));
        a[0] += s * bflo(wm[i].x); a[1] += s * bfhi(wm[i].x); a[2] += s * bflo(wm[i].y); a[3] += s * bfhi(wm[i].y);
        a[4] += s * bflo(wm[i].z); a[5] += s * bfhi(wm[i].z); a[6] += s * bflo(wm[i].w); a[7] += s * bfhi(wm[i].w); }
#pragma unroll
      for (int e = 0; e < 8; ++e) { float v = dpp_add<0x4E>(a[e]);
        v = dpp_add<0x124>(v); v = dpp_add<0x128>(v);
        a[e] = xsum_q(v); }
      if (lane < 2) {
#pragma unroll
        for (int e = 0; e < 8; ++e) ((LAS float*)(smem + P0_RED))[lw * 16 + lane * 8 + e] = a[e];
      }
      wait_lgkm0();
    }
    STAMP(32);
    bar();
    STAMP(33);
    __builtin_amdgcn_s_setprio(3);
    auto istep = [&](auto ic) { if constexpr (decltype(ic)::value >= A_PRE) issueW(ic); issueX(ic); };
#define A_CNT(s_) (((s_) >= A_PRE ? 4 : 0) + 3)
    static_assert((A_NS == 5 || A_NS == 6) && A_PRE <= 2, "start-up schedule");
    istep(IC(0)); istep(IC(1));
    wait_bar<A_CNT(1)>();
    STAMP(34);
    istep(IC(2)); istep(IC(3));
    if (lw == 0 && domod && lane < 16) {
      const LAS float* red = (const LAS float*)(smem + P0_RED);
      float s = rbf((red[lane] + red[16 + lane]) + (red[32 + lane] + red[48 + lane]));
      const int n = unit * 16 + lane, seg = n / D;
      if (seg == 1 || seg == 4) s = rbf(1.0f + s);
      __hip_atomic_store((gu32*)(ws + WS_MOD) + n, __float_as_uint(s), RLX_AGENT);
      const unsigned hi = __float_as_uint(s) >> 16, nb = (unsigned)__builtin_amdgcn_update_dpp(0, (int)hi, 0xB1, 0xf, 0xf, true);
      if ((seg == 3 || seg == 4) && !(lane & 1)) __hip_atomic_store((gu32*)(ws + WS_MODB) + (((seg == 4 ? 0 : D) + n - seg * D) >> 1), hi | (nb << 16), RLX_AGENT);
    }
    wait_bar<14>();
    static_for<4, A_NS + 1>(istep);
    wait_bar<(A_NS - 2) * 7>();
    const u32x4 rmod = mkrs(ws + WS_MOD, 3840 * 4);
    const int tl = lane / 20, tc = lane - 20 * tl;
    const unsigned tv0 = lane < 60 ? (unsigned)((tl * D + 80 * j) * 4 + tc * 16) : 0x7ffffff0u;
    const unsigned tv1 = lane < 40 ? (unsigned)(((4 + tl) * D + 80 * j) * 4 + tc * 16) : 0x7ffffff0u;
    istep(IC(A_NS + 1));
    static_for<2, 32 - A_NS - 0>([&](auto ic) { constexpr int n = decltype(ic)::value;
      wait_bar<(A_NS - 2) * 7>(); if constexpr (n + A_NS < 32) istep(IC(n + A_NS));
      if constexpr (n == 20) { if (lw == 0) { dma_1(lds0 + T_MOD, rmod, tv0); dma_1(lds0 + T_MOD + 960, rmod, tv1); } }
    });
    if (WARM) {
      { const int row = (j * 4 + lw) * 2 + (lane >> 5); warm(lds0 + T_DUMMY, mkrs(P.wsp, S * S * 2), (unsigned)(64 * p + row) * 4096u + (lane & 31) * 128); }
      warm(lds0 + T_DUMMY, mkrs((lw & 1) ? P.wdi : P.wdr, D * D * 2), lw < 2 && lane < 25 ? (unsigned)(b * 25 + lane) * 128 : 0x7ffffff0u);
    }
    static_for<0, A_NS - 1>([&](auto ic) { constexpr int r = A_NS - 2 - decltype(ic)::value; wait_bar<r * 7 + (WARM ? 2 : 0)>(); });
    bar();
  }
  {
    LLANE
    const u32x4 rre = mkrs_b(P.wdr, D * D * 2), rim = mkrs_b(P.wdi, D * D * 2), rx = mkrs(ws + WS_UV, 32 * 2 * 20 * 4096);
    auto wv = [&](int k) { const int r = 20 * lw + 8 * k + (lane >> 3); return (unsigned)(r * 1280 + (((lane & 7) ^ ((r >> 1) & 7)) * 16)) + WBIAS - 1024 * k; };
    const unsigned v0 = wv(0), v1 = wv(1), v2 = wv(2);
    const unsigned wb = (unsigned)(80 * j) * 1280u, lw_w = lds0 + lw * 2560, lw_x = lds0 + 10240 + lw * 2048, xb = (unsigned)p * 40 * 4096u + lw * 2048;
    auto issueW = [&](auto ic) { constexpr int n = decltype(ic)::value; dma_w2h<(n % B_NS) * B_SLOT, (n % 10) * 128>(lw_w, wb, n >= 10 ? rim : rre, v0, v1, v2); };
    auto issueX = [&](auto ic) { constexpr int n = decltype(ic)::value; dma_x2<(n % B_NS) * B_SLOT, 2 * n * 4096>(lw_x, xb, rx, vx); };
    __builtin_amdgcn_s_setprio(0);
    static_for<0, (PRE1 < PFW ? PRE1 : PFW)>(issueW);
    bar();
    static_for<(PRE1 < PFW ? PRE1 : PFW), PFW>(issueW);
    bar();
    __builtin_amdgcn_s_setprio(3);
    loader_run<B_NS, PFW, 20, 3, 2>(issueW, issueX);
    bar();
  }
  {
    LLANE
    const u32x4 rw = mkrs(ws + WS_WIN, 20 * 109 * 1024), rx = mkrs(ws + WS_X1P, 32 * 20 * 4096), rmb = mkrs(ws + WS_MODB, 2560);
    const unsigned wo = (unsigned)(((lw >> 1) * 109 + ft0) * 1024 + (lw & 1) * 3584);
    const unsigned lw_w = lds0 + C_W + lw * 3584, lw_x = lds0 + C_X + lw * 2048, xb = (unsigned)p * 20 * 4096u + lw * 2048;
    auto issueW = [&](auto ic) { constexpr int n = decltype(ic)::value, z1 = n >= 10; dma_x3h<(n % C_NS) * C_WSLOT, (2 * (n - 10 * z1) * 109 + (z1 ? 0 : 54)) * 1024>(lw_w, wo, rw, vx); };
    auto issueX = [&](auto ic) { constexpr int n = decltype(ic)::value; dma_x2<2 * n * 4096, 2 * n * 4096>(lw_x, xb, rx, vx); };
    __builtin_amdgcn_s_setprio(0);
    static_for<0, (PRE1 < PFC ? PRE1 : PFC)>(issueW);
    bar();
    if (lw < 3) dma_1(lds0 + T_MODB + lw * 1024, rmb, vx + lw * 1024);
    static_for<(PRE1 < PFC ? PRE1 : PFC), PFC>(issueW);
    bar();
    __builtin_amdgcn_s_setprio(3);
    static_for<0, C_NS>([&](auto ic) { if constexpr (decltype(ic)::value >= PFC) issueW(ic); issueX(ic); });
    wait_bar<(PFC - 1) * 2 + (C_NS - PFC) * 6>();
    static_for<0, 9>([&](auto ic) {
      constexpr int n = decltype(ic)::value, need = n + 1;
      constexpr int last = n + C_NS - 1 < 11 ? n + C_NS - 1 : 11;
      constexpr int cnt = [] { int c = 0; for (int s = need + 1; s <= last; ++s) c += (s < PFC ? 2 : s < 10 ? 6 : 4); return c; }();
      wait_bar<cnt>();
      if constexpr (n + C_NS < 12) { issueW(IC(n + C_NS)); if constexpr (n + C_NS < 10) issueX(IC(n + C_NS)); }
    });
    bar();
    bar();
    issueW(IC(12)); issueW(IC(13)); issueW(IC(14));
    wait_bar<16>();
    static_for<10, 19>([&](auto ic) {
      constexpr int n = decltype(ic)::value;
      constexpr int last = n + 4 < 19 ? n + 4 : 19;
      wait_bar<(last - (n + 1)) * 4>();
      if constexpr (n + C_NS < 20) issueW(IC(n + C_NS));
    });
    bar();
  }
  {
    LLANE LV4K
    const u32x4 rw = mkrs_b(P.wsp, S * S * 2), rx = mkrs(ws + WS_Z2N, 64 * 55 * 1024);
    const unsigned wrow = (unsigned)(64 * p + 16 * lw) * 4096u, w0 = v4k0 + WBIAS, w1 = v4k1 + WBIAS - 1024;
    const unsigned xo = (unsigned)(((lw >> 1) * 55 + ft0) * 1024 + (lw & 1) * 3584), lw_w = lds0 + lw * 2048, lw_x = lds0 + 8192 + lw * 3584;
    unsigned kw = (b & 7) * 512, kx = kw;
    auto issueW = [&](auto ic) { dma_w2_or<(decltype(ic)::value % D_NS) * D_SLOT>(lw_w, kw, wrow, rw, w0, w1); kw = (kw + 128) & 0xf80; };
    auto issueX = [&](auto ic) { dma_x3h_mad880<(decltype(ic)::value % D_NS) * D_SLOT>(lw_x, kx, xo, rx, vx); kx = (kx + 128) & 0xf80; };
    static_assert(D_NS * D_SLOT <= C_SCR, "ring below the exchange buffer");
    static_for<0, D_NS>(issueW);
    bar();
    static_for<0, D_NS>(issueX);
    wait_bar<(D_NS - 1) * 4>();
    static_for<0, 31>([&](auto ic) { constexpr int n = decltype(ic)::value;
      constexpr int last = n + D_NS - 1 < 31 ? n + D_NS - 1 : 31;
      constexpr int cnt = n + 1 < D_NS ? (D_NS - 2 - n) * 4 + (last - (D_NS - 1)) * 6 : (last - (n + 1)) * 6;
      wait_bar<cnt>();
      if constexpr (n + D_NS < 32) { issueW(IC(n + D_NS)); issueX(IC(n + D_NS)); } });
    bar();
  }
  {
    LLANE
    const u32x4 rw = mkrs(ws + WS_WOUT, 28 * 40 * 1024), rx = mkrs(ws + WS_P, 32 * 28 * 4096);
    const unsigned wo = (unsigned)(((lw >> 1) * 40 + 5 * j) * 1024 + (lw & 1) * 2560), lw_w = lds0 + lw * 2560, lw_x = lds0 + 10240 + lw * 2048, xb = (unsigned)p * 28 * 4096u + lw * 2048;
    auto issueW = [&](auto ic) { constexpr int n = decltype(ic)::value; dma_x2h<(n % E_NS) * E_SLOT, n * 80 * 1024>(lw_w, wo, rw, vx); };
    auto issueX = [&](auto ic) { constexpr int n = decltype(ic)::value; dma_x2<(n % E_NS) * E_SLOT, 2 * n * 4096>(lw_x, xb, rx, vx); };
    __builtin_amdgcn_s_setprio(0);
    static_for<0, (PRE1 < PFW ? PRE1 : PFW)>(issueW);
    bar();
    static_for<(PRE1 < PFW ? PRE1 : PFW), PFW>(issueW);
    bar();
    __builtin_amdgcn_s_setprio(3);
    loader_run<E_NS, PFW, 14, 3, 2>(issueW, issueX);
    bar();
  }
  STAMP_FLUSH();
}

DEV void consumer_main(const Params& P, LAS char* smem, const int w, const int lane_, const int b, const int p, const int j) {
  STAMP_DECL STAMP(0);
  char* const ws = P.ws;
  unsigned tagv = __hip_atomic_load((gu32*)(ws + WS_EPOCH), RLX_AGENT) + 1u, tag;
  const int mh = w >> 1, kh = w & 1;
  const auto rws = mkrsrc(ws, WS_END);
  const int ft0 = 27 * (j >> 2) + 7 * (j & 3), nft = (j & 3) == 3 ? 6 : 7;
  auto rot = [&](int i) { return i < 3 ? i + 4 * mh : i - 3 * mh; };
#define LANE_IDS int lane = lane_; asm volatile("" : "+v"(lane)); const int q = lane >> 4, tk = lane & 15, tid = w * 64 + lane, token = 64 * p + 16 * w + tk; (void)q; (void)tk; (void)tid; (void)token;
  {
    LANE_IDS
    unsigned v[2][5];
#pragma unroll
    for (int t = 0; t < 2; ++t) { const unsigned* xr = (const unsigned*)(P.x + (size_t)(64 * p + 8 * j + 2 * w + t) * D);
#pragma unroll
      for (int i = 0; i < 5; ++i) v[t][i] = xr[lane + 64 * i]; }
    const int u0 = w * 256 + b; const bool dopt = u0 < 810; const int u = dopt ? u0 : 0; const bool isin = u < 540;
    const int uu = isin ? u : u - 540, ncb = isin ? 27 : 10, pkb = uu / ncb, pcb = uu - pkb * ncb, ld = isin ? F : D;
    u32x4 pv[4];
    { const bf16_t* src = (isin ? P.win : P.wout) + (size_t)(pkb * 32) * ld + pcb * 64;
#pragma unroll
      for (int i = 0; i < 4; ++i) pv[i] = *(const u32x4*)(src + (size_t)(i * 8 + (lane >> 3)) * ld + (lane & 7) * 8); }
    LAS unsigned* t32 = (LAS unsigned*)(smem + P0_T);
    unsigned pk[2][5];
#pragma unroll
    for (int t = 0; t < 2; ++t) {
      float s = 0.f;
#pragma unroll
      for (int i = 0; i < 5; ++i) s += bflo(v[t][i]) + bfhi(v[t][i]);
      const float mean = xsum_q(xsum_16(s)) * (1.0f / D);
      float q2 = 0.f;
#pragma unroll
      for (int i = 0; i < 5; ++i) { const float a = bflo(v[t][i]) - mean, c = bfhi(v[t][i]) - mean; q2 += a * a + c * c; }
      const float rs = 1.0f / sqrtf(xsum_q(xsum_16(q2)) * (1.0f / D) + 1e-5f);
#pragma unroll
      for (int i = 0; i < 5; ++i) pk[t][i] = pack2((bflo(v[t][i]) - mean) * rs, (bfhi(v[t][i]) - mean) * rs);
    }
#pragma unroll
    for (int i = 0; i < 5; ++i) { const int d = 2 * (lane + 64 * i);
      t32[d * 4 + w] = (pk[0][i] & 0xffffu) | (pk[1][i] << 16); t32[(d + 1) * 4 + w] = (pk[0][i] >> 16) | (pk[1][i] & 0xffff0000u); }
    asm volatile("" : "+v"(tagv)); tag = __builtin_amdgcn_readfirstlane(tagv);
    wait_lgkm0(); bar();
    const unsigned xb = WS_XN + (unsigned)((2 * p + (j >> 2)) * 40 * 1024 + (j & 3) * 256);
#pragma unroll
    for (int i = 0; i < 3; ++i) { const int d = tid + 256 * i;
      if (d < D) st16(rws, xb + (d >> 4) * 1024 + (d & 15) * 16, *(LAS u32x4*)(smem + P0_T + d * 16)); }
    wait_vm<0>();
    bar();
    STAMP(1);
    if (w == 0 && lane == 0) set_flag(ws, WS_FN + p * 128 + j * 4, tag);
    {
      LAS char* t = smem + P0_PT + w * 4096;
#pragma unroll
      for (int i = 0; i < 4; ++i) *(LAS u32x4*)(t + (i * 8 + (lane >> 3)) * 128 + (lane & 7) * 16) = pv[i];
      const unsigned dst = dopt ? (isin ? WS_WIN + (unsigned)(pkb * 109 + pcb * 4) * 1024 : WS_WOUT + (unsigned)(pkb * 40 + pcb * 4) * 1024) + lane * 16 : 0xfffff000u;
      const int rq = tk >> 2, pp = lane & 3;
#pragma unroll
      for (int c = 0; c < 4; ++c) {
        const u32x2 l2 = __builtin_bit_cast(u32x2, __builtin_amdgcn_ds_read_tr16_b64_v4i16((LAS s16x4*)(t + (8 * q + rq) * 128 + c * 32 + pp * 8)));
        const u32x2 h2 = __builtin_bit_cast(u32x2, __builtin_amdgcn_ds_read_tr16_b64_v4i16((LAS s16x4*)(t + (8 * q + 4 + rq) * 128 + c * 32 + pp * 8)));
        st16(rws, dst + (dopt ? c * 1024 : 0), (u32x4){l2.x, l2.y, h2.x, h2.y});
      }
    }
    if (w == 0) { wait256(rws, ws, WS_FN, tag, lane); STAMP(23); }
    bar();
    STAMP(2);
  }
  u32x2 xv[5];
  {
    LANE_IDS
    f32x4 ur[5], ui[5];
#pragma unroll
    for (int i = 0; i < 5; ++i) { ur[i] = (f32x4){0, 0, 0, 0}; ui[i] = (f32x4){0, 0, 0, 0}; }
    f32x4 rsum[2] = {(f32x4){0, 0, 0, 0}, (f32x4){0, 0, 0, 0}};
    bf16x8 ones = __builtin_bit_cast(bf16x8, (u32x4){0x3f803f80u, 0x3f803f80u, 0x3f803f80u, 0x3f803f80u});
    asm volatile("" : "+v"(ones));
    const unsigned wa0 = (unsigned)(w * 4096 + tk * 128 + ((q ^ ((lane >> 1) & 7)) * 16));
    const unsigned xa = 16384 + lane * 16;
    bf16x8 Wa[2], Xa[5], Wb[2], Xb[5];
    auto rdm = [&](bf16x8(&Wn)[2], bf16x8(&Xn)[5], int slot, int kk, bf16x8(&Wf)[2], bf16x8(&Xf)[5]) {
      LAS char* sb = smem + slot * A_SLOT; const unsigned wa = wa0 ^ (kk * 64);
      static_for<0, 12>([&](auto ic) { constexpr int z = decltype(ic)::value, i = z >> 1, t = z & 1;
        if constexpr (z < 10) { if constexpr (t == 0) ur[i] = mfma(Xf[i], Wf[0], ur[i]); else ui[i] = mfma(Xf[i], Wf[1], ui[i]); } else rsum[t] = mfma(ones, Wf[t], rsum[t]);
        if constexpr (z == 0) Wn[0] = *(LAS bf16x8*)(sb + wa);
        if constexpr (z == 1) Xn[0] = *(LAS bf16x8*)(sb + xa + (kk * 5) * 1024);
        if constexpr (z == 2) Wn[1] = *(LAS bf16x8*)(sb + wa + 2048);
        if constexpr (z >= 3 && z < 7) Xn[z - 2] = *(LAS bf16x8*)(sb + xa + (kk * 5 + z - 2) * 1024);
        sched_fence(); });
    };
    auto mmlast = [&](bf16x8(&Wf)[2], bf16x8(&Xf)[5]) {
#pragma unroll
      for (int i = 0; i < 5; ++i) { ur[i] = mfma(Xf[i], Wf[0], ur[i]); ui[i] = mfma(Xf[i], Wf[1], ui[i]); }
      rsum[0] = mfma(ones, Wf[0], rsum[0]); rsum[1] = mfma(ones, Wf[1], rsum[1]);
    };
    int slot = 0;
    auto steps = [&](int n0, int n1, bool hooks) {
#pragma unroll 1
      for (int n = n0; n < n1; ++n) {
        rdm(Wb, Xb, slot, 1, Wa, Xa);
        const int ns = slot + 1 == A_NS ? 0 : slot + 1;
        if (hooks && n == 8) wait_vm<0>();
        wait_lgkm0(); bar(); sched_fence();
        if (hooks && n == 8 && w == 0 && lane == 0) set_flag(ws, WS_F2 + p * 128 + j * 4, tag);
        rdm(Wa, Xa, ns, 0, Wb, Xb);
        slot = ns;
      }
    };
    bar();
    STAMP(3); CSTAMP(40);
    { LAS char* sb = smem; Wa[0] = *(LAS bf16x8*)(sb + wa0); Wa[1] = *(LAS bf16x8*)(sb + wa0 + 2048);
#pragma unroll
      for (int i = 0; i < 5; ++i) Xa[i] = *(LAS bf16x8*)(sb + xa + i * 1024); }
    steps(0, 12, true);
    const u32x4 f2 = peek256(rws, WS_F2, lane);
    steps(12, 18, false);
    if (!flags_ok(f2, tag)) wait256(rws, ws, WS_F2, tag, lane);
    if (b == 0 && w == 0 && lane == 0) set_flag(ws, WS_EPOCH, tag + 15);
#pragma unroll
    for (int i = 0; i < 5; ++i) xv[i] = *(const u32x2*)(P.x + (size_t)token * D + 80 * j + 16 * i + 4 * q);
    steps(18, 31, false);
    { LAS char* sb = smem + slot * A_SLOT; const unsigned wa = wa0 ^ 64;
      Wb[0] = *(LAS bf16x8*)(sb + wa); Wb[1] = *(LAS bf16x8*)(sb + wa + 2048);
#pragma unroll
      for (int i = 0; i < 5; ++i) Xb[i] = *(LAS bf16x8*)(sb + xa + (5 + i) * 1024); }
    mmlast(Wa, Xa);
    wait_lgkm0(); bar();
    mmlast(Wb, Xb);
    STAMP(4); CSTAMP(41);
    const float rs0 = rsum[0][0], rs1 = rsum[1][0];
#pragma unroll
    for (int i = 0; i < 5; ++i) {
      u32x2 a, c;
      const f32x4 sa = *(const LAS f32x4*)(smem + T_MOD + (16 * i + 4 * q) * 4), c1 = *(const LAS f32x4*)(smem + T_MOD + (80 + 16 * i + 4 * q) * 4);
      { const f32x4 u = ur[i] * c1 + rs0 * sa; a.x = pack2(u[0], u[1]); a.y = pack2(u[2], u[3]); }
      { const f32x4 v = -(ui[i] * c1 + rs1 * sa); c.x = pack2(v[0], v[1]); c.y = pack2(v[2], v[3]); }
      const int dt = 5 * j + i, kb = dt >> 1, qq = 2 * (dt & 1) + (q >> 1);
      st16(rws, WS_UV + (unsigned)(((p * 2 + (q & 1)) * 20 + kb) * 4 + w) * 1024 + (qq * 16 + tk) * 16, swap_pair(a, c));
    }
    wait_vm<0>(); bar();
    STAMP(5);
    if (w == 0) { if (lane == 0) set_flag(ws, WS_FU + p * 128 + j * 4, tag); wait_flags8(ws, WS_FU + p * 128, tag, lane); }
    bar();
    STAMP(6); CSTAMP(44);
  }
  u32x2 x1p[5];
  {
    LANE_IDS
    f32x4 acc[5][2];
#pragma unroll
    for (int i = 0; i < 5; ++i) { acc[i][0] = (f32x4){0, 0, 0, 0}; acc[i][1] = (f32x4){0, 0, 0, 0}; }
    const unsigned wa = (unsigned)(tk * 128 + (((q + 4 * kh) ^ ((lane >> 1) & 7)) * 16));
    const unsigned xa = 10240 + (kh * 4 + 2 * mh) * 1024 + lane * 16;
    ksplit_loop<B_NS, 20, 5, 2>(
        [&](bf16x8(&A)[5], bf16x8(&Bf)[2], int slot, int) { LAS char* sb = smem + slot * B_SLOT;
          A[0] = *(LAS bf16x8*)(sb + wa); Bf[0] = *(LAS bf16x8*)(sb + xa); Bf[1] = *(LAS bf16x8*)(sb + xa + 1024);
#pragma unroll
          for (int i = 1; i < 5; ++i) A[i] = *(LAS bf16x8*)(sb + wa + i * 2048); },
        [&](bf16x8(&A)[5], bf16x8(&Bf)[2], bool) {
#pragma unroll
          for (int i = 0; i < 5; ++i) { acc[i][0] = mfma(A[i], Bf[0], acc[i][0]); acc[i][1] = mfma(A[i], Bf[1], acc[i][1]); } });
    STAMP(7); CSTAMP(45);
    f32x4 y[5];
    kreduce<5>(acc, y, smem + B_SCR, w, lane);
    float xs[20]; u32x2 xp[5];
    float s = 0.f;
#pragma unroll
    for (int i = 0; i < 5; ++i) {
      const f32x4 ga = *(const LAS f32x4*)(smem + T_MOD + (160 + 16 * i + 4 * q) * 4), c1b = *(const LAS f32x4*)(smem + T_MOD + 960 + (16 * i + 4 * q) * 4);
      x1p[i].x = pack2(bflo(xv[i].x) + ga[0] * y[i][0], bfhi(xv[i].x) + ga[1] * y[i][1]);
      x1p[i].y = pack2(bflo(xv[i].y) + ga[2] * y[i][2], bfhi(xv[i].y) + ga[3] * y[i][3]);
      asm volatile("" : "+v"(x1p[i].x), "+v"(x1p[i].y));
      xs[4 * i] = bflo(x1p[i].x); xs[4 * i + 1] = bfhi(x1p[i].x); xs[4 * i + 2] = bflo(x1p[i].y); xs[4 * i + 3] = bfhi(x1p[i].y);
      s += (xs[4 * i] + xs[4 * i + 1]) + (xs[4 * i + 2] + xs[4 * i + 3]);
      xp[i].x = pack2(xs[4 * i] * c1b[0], xs[4 * i + 1] * c1b[1]); xp[i].y = pack2(xs[4 * i + 2] * c1b[2], xs[4 * i + 3] * c1b[3]);
    }
    const unsigned img = WS_X1P + (unsigned)p * 20 * 4096 + w * 1024;
#pragma unroll
    for (int i = 0; i < 6; i += 2) {
      const int i2 = i + 1 < 5 ? i + 1 : i;
      const int dt = 5 * j + ((q & 1) ? i2 : i), kb = dt >> 1, qq = 2 * (dt & 1) + (q >> 1);
      st16(rws, img + kb * 4096 + (qq * 16 + tk) * 16, swap_pair(xp[i], xp[i2]));
    }
    const float mean = xsum_q(s) * (1.0f / 80.0f);
    float m2 = 0.f;
#pragma unroll
    for (int e = 0; e < 20; ++e) { const float d = xs[e] - mean; m2 += d * d; }
    m2 = xsum_q(m2);
    if (q == 0) __hip_atomic_store((gu64*)(ws + WS_ST1) + token * 8 + j, ((unsigned long long)__float_as_uint(m2) << 32) | __float_as_uint(mean), RLX_AGENT);
    wait_vm<0>(); bar();
    STAMP(8);
    if (w == 0) { if (lane == 0) set_flag(ws, WS_FX + p * 128 + j * 4, tag); wait_flags8(ws, WS_FX + p * 128, tag, lane); }
    bar();
    STAMP(9);
  }
  u32x2 z1p[7]; float bias;
  {
    LANE_IDS
    u32x4 st[4];
#pragma unroll
    for (int k = 0; k < 4; ++k) st[k] = ld16(rws, WS_ST1 + (unsigned)token * 64 + k * 16);
    bias = bflo(P.bsp[token]);
    for (int i = tid; i < 448; i += 256) ((LAS float*)(smem + T_CV))[i] = 0.f;
    f32x4 acc[7][2], aux[4];
    auto clear = [&]() {
#pragma unroll
      for (int i = 0; i < 7; ++i) { acc[i][0] = (f32x4){0, 0, 0, 0}; acc[i][1] = (f32x4){0, 0, 0, 0}; }
#pragma unroll
      for (int i = 0; i < 4; ++i) aux[i] = (f32x4){0, 0, 0, 0};
    };
    const unsigned xa = C_X + (kh * 4 + 2 * mh) * 1024 + lane * 16, wlo = C_W + kh * 7168 + lane * 16 + mh * 4096, whi = C_W + kh * 7168 + lane * 16 - mh * 3072;
    const int r4 = tk & 3;
    const unsigned ma = T_MODB + (r4 == 0 ? q * 16 + kh * 64 : r4 == 1 ? 1280 + q * 16 + kh * 64 : 2560), mstride = r4 < 2 ? 128 : 0;
    int nbase = 0;
    auto rd = [&](bf16x8(&A)[7], bf16x8(&Bf)[3], int slot, int n) {
      LAS char* sl = smem + wlo + slot * C_WSLOT; LAS char* sh = smem + whi + slot * C_WSLOT; LAS char* xb = smem + xa + n * 8192;
      Bf[0] = *(LAS bf16x8*)(xb); A[0] = *(LAS bf16x8*)(sl); Bf[1] = *(LAS bf16x8*)(xb + 1024);
      A[1] = *(LAS bf16x8*)(sl + 1024); A[2] = *(LAS bf16x8*)(sl + 2048);
#pragma unroll
      for (int i = 3; i < 7; ++i) A[i] = *(LAS bf16x8*)(sh + i * 1024);
      Bf[2] = *(LAS bf16x8*)(smem + ma + n * mstride);
    };
    auto auxmm = [&](bf16x8(&A)[7], bf16x8(&Bf)[3]) {
#pragma unroll
      for (int i = 0; i < 4; ++i) aux[i] = mfma(Bf[2], A[i], aux[i]);
    };
    auto mm2 = [&](bf16x8(&A)[7], bf16x8(&Bf)[3], bool) {
#pragma unroll
      for (int i = 0; i < 7; ++i) { acc[i][0] = mfma(Bf[0], A[i], acc[i][0]); acc[i][1] = mfma(Bf[1], A[i], acc[i][1]); }
      auxmm(A, Bf); };
    auto mm1 = [&](bf16x8(&A)[7], bf16x8(&Bf)[3], bool) {
#pragma unroll
      for (int i = 0; i < 7; ++i) { acc[i][0] = mfma(A[i], Bf[0], acc[i][0]); acc[i][1] = mfma(A[i], Bf[1], acc[i][1]); }
      auxmm(A, Bf); };
    auto put_cv = [&](int pass_) {
      LAS float* cv = (LAS float*)(smem + T_CV) + pass_ * 224;
      if (q == 0) {
#pragma unroll
        for (int i = 0; i < 4; ++i) if (mh == 0 || i < 3) { const int t = rot(i);
          __hip_atomic_fetch_add(cv + 16 * t + tk, aux[i][0], __ATOMIC_RELAXED, __HIP_MEMORY_SCOPE_WORKGROUP);
          __hip_atomic_fetch_add(cv + 112 + 16 * t + tk, aux[i][1], __ATOMIC_RELAXED, __HIP_MEMORY_SCOPE_WORKGROUP); }
      }
    };
    float rmean, rrstd;
    { float mu[8], mean = 0.f;
#pragma unroll
      for (int k = 0; k < 4; ++k) { mu[2 * k] = __uint_as_float(st[k].x); mu[2 * k + 1] = __uint_as_float(st[k].z); mean += mu[2 * k] + mu[2 * k + 1]; }
      mean *= 0.125f; float M2 = 0.f;
#pragma unroll
      for (int k = 0; k < 4; ++k) { const float d0 = mu[2 * k] - mean, d1 = mu[2 * k + 1] - mean; M2 += __uint_as_float(st[k].y) + __uint_as_float(st[k].w) + 80.0f * (d0 * d0 + d1 * d1); }
      rmean = mean; rrstd = 1.0f / sqrtf(M2 * (1.0f / D) + 1e-5f); }
    asm volatile("" : "+v"(rmean), "+v"(rrstd));
    (void)nbase;
    bf16x8 A0[7], B0[3], A1[7], B1[3];
    int slot = 0;
    auto nohook = [](int) {};
    clear();
    wait_lgkm0(); bar();
    STAMP(10); CSTAMP(46);
    rd(A0, B0, 0, 0);
    ksplit_steps<C_NS, 10, 7, 3, 18>(A0, B0, A1, B1, slot, 0, 8, rd, mm2, nohook, nohook);
    ksplit_last<C_NS, 10, 7, 3>(A0, B0, A1, B1, slot, rd, mm2);
    STAMP(11); CSTAMP(47);
    put_cv(0);
    f32x4 zs[7];
    kreduce<7>(acc, zs, smem + C_SCR, w, lane);
    wait_lgkm0(); bar();
    f32x2 mr01, mr23, rr01, rr23;
    mr01 = (f32x2){__shfl(rmean, 4 * q), __shfl(rmean, 4 * q + 1)}; mr23 = (f32x2){__shfl(rmean, 4 * q + 2), __shfl(rmean, 4 * q + 3)};
    rr01 = (f32x2){__shfl(rrstd, 4 * q), __shfl(rrstd, 4 * q + 1)}; rr23 = (f32x2){__shfl(rrstd, 4 * q + 2), __shfl(rrstd, 4 * q + 3)};
    f32x2 z01[7], z23[7];
#pragma unroll
    for (int i = 0; i < 7; ++i) {
      const float c1 = ((const LAS float*)(smem + T_CV))[16 * rot(i) + tk], c2 = ((const LAS float*)(smem + T_CV))[112 + 16 * rot(i) + tk];
      z01[i] = gelu2(rr01 * ((f32x2){zs[i][0], zs[i][1]} - mr01 * c1) + c2);
      z23[i] = gelu2(rr23 * ((f32x2){zs[i][2], zs[i][3]} - mr23 * c1) + c2);
    }
    {
      const float inv = 1.0f / (16.0f * nft);
      const f32x2 zero2 = (f32x2){0.f, 0.f};
      f32x2 s01 = zero2, s23 = zero2;
#pragma unroll
      for (int i = 0; i < 7; ++i) { const bool ok = (i != 2 && i != 6) || rot(i) < nft; s01 += ok ? z01[i] : zero2; s23 += ok ? z23[i] : zero2; }
      const f32x2 m01 = (f32x2){xsum_16(s01[0]), xsum_16(s01[1])} * inv, m23 = (f32x2){xsum_16(s23[0]), xsum_16(s23[1])} * inv;
      f32x2 t01 = zero2, t23 = zero2;
#pragma unroll
      for (int i = 0; i < 7; ++i) { const bool ok = (i != 2 && i != 6) || rot(i) < nft; const f32x2 a = ok ? z01[i] - m01 : zero2, c = ok ? z23[i] - m23 : zero2; t01 += a * a; t23 += c * c; }
      const float q0 = xsum_16(t01[0]), q1 = xsum_16(t01[1]), q2 = xsum_16(t23[0]), q3 = xsum_16(t23[1]);
      if (tk < 8) { const int r = tk & 3; const float a = r == 0 ? m01[0] : r == 1 ? m01[1] : r == 2 ? m23[0] : m23[1], c = r == 0 ? q0 : r == 1 ? q1 : r == 2 ? q2 : q3;
        const int tok = 64 * p + 16 * w + 4 * q + r;
        __hip_atomic_store((gu64*)(ws + WS_ST2) + (tok * 8 + j) * 2 + (tk >> 2), ((unsigned long long)tag << 32) | __float_as_uint(tk < 4 ? a : c), RLX_AGENT); }
    }
    STAMP(12); CSTAMP(48);
    clear();
    slot = 0;
    bar();
    rd(A0, B0, 0, 0);
    ksplit_steps<C_NS, 10, 7, 3, 18>(A0, B0, A1, B1, slot, 0, 4, rd, mm1, nohook, nohook);
    STAMP(13);
    {
      u32x4 g0, g1;
      const unsigned go = WS_ST2 + (unsigned)(token * 8 + 2 * q) * 16;
      for (unsigned spins = 0;;) { g0 = ld16(rws, go); g1 = ld16(rws, go + 16);
        if (__all(g0.y == tag && g0.w == tag && g1.y == tag && g1.w == tag)) break; if (spin_fail(spins, ws)) break; }
      const float n0 = 112.0f, n1 = (q & 1) ? 96.0f : 112.0f;
      const float m0 = __uint_as_float(g0.x), m1 = __uint_as_float(g1.x);
      const float mean = xsum_q(n0 * m0 + n1 * m1) * (1.0f / FH);
      const float d0 = m0 - mean, d1 = m1 - mean;
      const float M2 = xsum_q(__uint_as_float(g0.z) + __uint_as_float(g1.z) + n0 * d0 * d0 + n1 * d1 * d1);
      const float rstd = 1.0f / sqrtf(M2 * (1.0f / FH) + 1e-5f);
      mr01 = (f32x2){__shfl(mean, 4 * q), __shfl(mean, 4 * q + 1)}; mr23 = (f32x2){__shfl(mean, 4 * q + 2), __shfl(mean, 4 * q + 3)};
      rr01 = (f32x2){__shfl(rstd, 4 * q), __shfl(rstd, 4 * q + 1)}; rr23 = (f32x2){__shfl(rstd, 4 * q + 2), __shfl(rstd, 4 * q + 3)};
    }
    {
      u32x2 pk[7];
#pragma unroll
      for (int i = 0; i < 7; ++i) { const f32x2 a = (z01[i] - mr01) * rr01, c = (z23[i] - mr23) * rr23; pk[i].x = pack2(a[0], a[1]); pk[i].y = pack2(c[0], c[1]); }
      const unsigned img = WS_Z2N + (unsigned)(2 * p + (w >> 1)) * 55 * 1024 + ((2 * (w & 1) + (q >> 1)) * 16 + tk) * 16;
#pragma unroll
      for (int i = 0; i < 8; i += 2) {
        const int i2 = i + 1 < 7 ? i + 1 : i;
        const u32x4 ch = swap_pair(pk[i], pk[i2]);
        const int ti = (q & 1) ? rot(i2) : rot(i);
        st16(rws, ti < nft ? img + (ft0 + ti) * 1024 : 0xfffff000u, ch);
      }
    }
    STAMP(14);
    ksplit_steps<C_NS, 10, 7, 3, 18>(A0, B0, A1, B1, slot, 4, 8, rd, mm1,
        [&](int n) { if (n == 5) wait_vm<0>(); },
        [&](int n) { if (n == 5 && w == 0 && lane == 0) set_flag(ws, WS_FZ + (j * 4 + (p >> 3)) * 128 + (p & 7) * 4, tag); });
    ksplit_last<C_NS, 10, 7, 3>(A0, B0, A1, B1, slot, rd, mm1);
    STAMP(15); CSTAMP(49);
    const unsigned fzo = WS_FZ + (j * 4 + ((lane & 7) >> 1)) * 128 + (lane & 1) * 16;
    const u32x4 fz = ld16(rws, fzo);
    put_cv(1);
    kreduce<7>(acc, zs, smem + C_SCR, w, lane);
    if (!flags_ok(fz, tag)) { for (unsigned spins = 0;;) { if (flags_ok(ld16(rws, fzo), tag)) break; if (spin_fail(spins, ws)) break; } }
    wait_lgkm0(); bar();
#pragma unroll
    for (int i = 0; i < 7; ++i) {
      const LAS float* cv = (const LAS float*)(smem + T_CV) + 224;
      const f32x4 c1 = *(const LAS f32x4*)(cv + 16 * rot(i) + 4 * q), c2 = *(const LAS f32x4*)(cv + 112 + 16 * rot(i) + 4 * q);
      const f32x2 a = gelu2(rrstd * ((f32x2){zs[i][0], zs[i][1]} - rmean * (f32x2){c1[0], c1[1]}) + (f32x2){c2[0], c2[1]});
      const f32x2 c = gelu2(rrstd * ((f32x2){zs[i][2], zs[i][3]} - rmean * (f32x2){c1[2], c1[3]}) + (f32x2){c2[2], c2[3]});
      z1p[i] = (u32x2){pack2(a[0], a[1]), pack2(c[0], c[1])};
      asm volatile("" : "+v"(z1p[i].x), "+v"(z1p[i].y));
    }
    STAMP(16); CSTAMP(42);
  }
  {
    LANE_IDS
    f32x4 acc[7][2];
#pragma unroll
    for (int i = 0; i < 7; ++i) { acc[i][0] = (f32x4){0, 0, 0, 0}; acc[i][1] = (f32x4){0, 0, 0, 0}; }
    const unsigned wa = (unsigned)((2 * mh) * 2048 + tk * 128 + (((q + 4 * kh) ^ ((lane >> 1) & 7)) * 16));
    const unsigned xlo = 8192 + kh * 7168 + lane * 16 + mh * 4096, xhi = 8192 + kh * 7168 + lane * 16 - mh * 3072;
    ksplit_loop<D_NS, 32, 7, 2>(
        [&](bf16x8(&A)[7], bf16x8(&Bf)[2], int slot, int) { LAS char* sb = smem + slot * D_SLOT;
          Bf[0] = *(LAS bf16x8*)(sb + wa); A[0] = *(LAS bf16x8*)(sb + xlo); Bf[1] = *(LAS bf16x8*)(sb + wa + 2048);
          A[1] = *(LAS bf16x8*)(sb + xlo + 1024); A[2] = *(LAS bf16x8*)(sb + xlo + 2048);
#pragma unroll
          for (int i = 3; i < 7; ++i) A[i] = *(LAS bf16x8*)(sb + xhi + i * 1024); },
        [&](bf16x8(&A)[7], bf16x8(&Bf)[2], bool) {
#pragma unroll
          for (int i = 0; i < 7; ++i) { acc[i][0] = mfma(A[i], Bf[0], acc[i][0]); acc[i][1] = mfma(A[i], Bf[1], acc[i][1]); } });
    STAMP(17); CSTAMP(43);
    f32x4 g[7];
    kreduce<7>(acc, g, smem + D_SCR, w, lane);
    u32x2 pk[7];
#pragma unroll
    for (int i = 0; i < 7; ++i) {
      pk[i].x = pack2(bflo(z1p[i].x) * (g[i][0] + bias), bfhi(z1p[i].x) * (g[i][1] + bias));
      pk[i].y = pack2(bflo(z1p[i].y) * (g[i][2] + bias), bfhi(z1p[i].y) * (g[i][3] + bias));
    }
    const unsigned img = WS_P + (unsigned)p * 28 * 4096 + w * 1024;
#pragma unroll
    for (int i = 0; i < 8; i += 2) {
      const int i2 = i + 1 < 7 ? i + 1 : i;
      const u32x4 ch = swap_pair(pk[i], pk[i2]);
      const int ti = (q & 1) ? rot(i2) : rot(i), ft = ft0 + ti, kb = ft >> 1, qq = 2 * (ft & 1) + (q >> 1);
      st16(rws, ti < nft ? img + kb * 4096 + (qq * 16 + tk) * 16 : 0xfffff000u, ch);
    }
    wait_vm<0>(); bar();
    STAMP(18);
    if (w == 0) { if (lane == 0) set_flag(ws, WS_FP + p * 128 + j * 4, tag); wait_flags8(ws, WS_FP + p * 128, tag, lane); }
    bar();
    STAMP(19);
  }
  {
    LANE_IDS
    f32x4 acc[5][2];
#pragma unroll
    for (int i = 0; i < 5; ++i) { acc[i][0] = (f32x4){0, 0, 0, 0}; acc[i][1] = (f32x4){0, 0, 0, 0}; }
    const unsigned wa = kh * 5120 + lane * 16, xa = 10240 + (kh * 4 + 2 * mh) * 1024 + lane * 16;
    ksplit_loop<E_NS, 14, 5, 2>(
        [&](bf16x8(&A)[5], bf16x8(&Bf)[2], int slot, int) { LAS char* sb = smem + slot * E_SLOT;
          A[0] = *(LAS bf16x8*)(sb + wa); Bf[0] = *(LAS bf16x8*)(sb + xa); Bf[1] = *(LAS bf16x8*)(sb + xa + 1024);
#pragma unroll
          for (int i = 1; i < 5; ++i) A[i] = *(LAS bf16x8*)(sb + wa + i * 1024); },
        [&](bf16x8(&A)[5], bf16x8(&Bf)[2], bool last) {
          if (!(last && kh)) {
#pragma unroll
            for (int i = 0; i < 5; ++i) { acc[i][0] = mfma(A[i], Bf[0], acc[i][0]); acc[i][1] = mfma(A[i], Bf[1], acc[i][1]); } } });
    STAMP(20);
    f32x4 o[5];
    kreduce<5>(acc, o, smem + E_SCR, w, lane);
    u32x2 pk[5];
#pragma unroll
    for (int i = 0; i < 5; ++i) {
      const f32x4 gb = *(const LAS f32x4*)(smem + T_MOD + 960 + (80 + 16 * i + 4 * q) * 4);
      pk[i].x = pack2(bflo(x1p[i].x) + gb[0] * o[i][0], bfhi(x1p[i].x) + gb[1] * o[i][1]);
      pk[i].y = pack2(bflo(x1p[i].y) + gb[2] * o[i][2], bfhi(x1p[i].y) + gb[3] * o[i][3]);
    }
    bf16_t* orow = P.out + (size_t)token * D + 80 * j;
#pragma unroll
    for (int i = 0; i < 6; i += 2) {
      const int i2 = i + 1 < 5 ? i + 1 : i;
      const u32x4 ch = swap_pair(pk[i], pk[i2]);
      *(u32x4*)(orow + 16 * ((q & 1) ? i2 : i) + 8 * (q >> 1)) = ch;
    }
    STAMP(21);
  }
  STAMP_FLUSH();
}

__global__ __launch_bounds__(NT, 2) void fused_block(Params P) {
  extern __shared__ __attribute__((aligned(16))) char smem_[];
  LAS char* smem = (LAS char*)smem_;
  const int tid = threadIdx.x, lane = tid & 63, wid = __builtin_amdgcn_readfirstlane(tid >> 6), b = blockIdx.x;
  const int xcd = b & 7, idx = b >> 3, p = xcd * 4 + (idx >> 3), j = idx & 7;
  if (wid >= 4) loader_main(P, (unsigned)(size_t)smem, smem, wid - 4, lane, b, p, j, JROT ? ((j + xcd) & 7) * 4 : xcd * 4);
  else consumer_main(P, smem, wid, lane, b, p, j);
}

extern "C" void kernel_launch(void* const* d_in, const int* in_sizes, int n_in, void* d_out, int out_size, void* d_ws, size_t ws_size, hipStream_t stream) {
  Params P;
  P.x = (const bf16_t*)d_in[0]; P.wsr = (const bf16_t*)d_in[1]; P.wsi = (const bf16_t*)d_in[2];
  P.wdr = (const bf16_t*)d_in[3]; P.wdi = (const bf16_t*)d_in[4]; P.win = (const bf16_t*)d_in[6];
  P.wsp = (const bf16_t*)d_in[7]; P.bsp = (const bf16_t*)d_in[8]; P.wout = (const bf16_t*)d_in[9];
  P.c = (const bf16_t*)d_in[10]; P.wmod = (const bf16_t*)d_in[11];
  P.out = (bf16_t*)d_out; P.ws = (char*)d_ws;
  (void)hipFuncSetAttribute((const void*)fused_block, hipFuncAttributeMaxDynamicSharedMemorySize, LDS_BYTES);
  fused_block<<<256, NT, LDS_BYTES, stream>>>(P);
}
```

```cpp
#include <hip/hip_runtime.h>
#include <stdint.h>
#include <type_traits>

typedef unsigned short bf16_t;
typedef short bf16x8 __attribute__((ext_vector_type(8)));
typedef short s16x4 __attribute__((ext_vector_type(4)));
typedef float f32x4 __attribute__((ext_vector_type(4)));
typedef float f32x2 __attribute__((ext_vector_type(2)));
typedef unsigned u32x4 __attribute__((ext_vector_type(4)));
typedef unsigned u32x2 __attribute__((ext_vector_type(2)));
typedef __bf16 bf2_t __attribute__((ext_vector_type(2)));
#define LAS __attribute__((address_space(3)))
#define GAS __attribute__((address_space(1)))
typedef GAS unsigned gu32;
typedef GAS unsigned long long gu64;
#define RLX_AGENT __ATOMIC_RELAXED, __HIP_MEMORY_SCOPE_AGENT
#define DEV __device__ __forceinline__

constexpr int S = 2048, D = 640, F = 1728, FH = 864;
constexpr int NT = 512;

constexpr unsigned WS_EPOCH = 0;
constexpr unsigned WS_TMO   = 256;
constexpr unsigned WS_FN = 4096, WS_F2 = 8192, WS_FU = 12288, WS_FX = 16384, WS_FP = 20480, WS_FZ = 24576;
constexpr unsigned WS_MOD  = 65536;
constexpr unsigned WS_MODB = WS_MOD + 16384;

constexpr unsigned WS_XN   = 131072;
constexpr unsigned WS_WIN  = WS_XN + 64 * 40 * 1024;
constexpr unsigned WS_WOUT = WS_WIN + 20 * 109 * 1024;
constexpr unsigned WS_UV   = WS_WOUT + 27 * 40 * 1024;
constexpr unsigned WS_X1P  = WS_UV + 32 * 2 * 20 * 4096;
constexpr unsigned WS_ST1  = WS_X1P + 32 * 20 * 4096;
constexpr unsigned WS_Z2N  = WS_ST1 + 2048 * 64;
constexpr unsigned WS_ST2  = WS_Z2N + 64 * 55 * 1024;
constexpr unsigned WS_P    = WS_ST2 + 2048 * 128;
constexpr unsigned WS_RS   = WS_P + 32 * 28 * 4096;
constexpr unsigned WS_END  = WS_RS + 2048 * 128;

struct Params {
  const bf16_t *x, *wsr, *wsi, *wdr, *wdi, *win, *wsp, *bsp, *wout, *c, *wmod;
  bf16_t* out;
  char* ws;
};

#define STAMP_DECL
#define STAMP(i) do {} while (0)
#define STAMP_FLUSH() do {} while (0)
#define CSTAMP(i) do {} while (0)

template <int N> DEV void wait_vm() { asm volatile("s_waitcnt vmcnt(%0)" ::"n"(N) : "memory"); }
DEV void wait_lgkm0() { asm volatile("s_waitcnt lgkmcnt(0)" ::: "memory"); }
DEV void bar() { asm volatile("" ::: "memory"); __builtin_amdgcn_s_barrier(); asm volatile("" ::: "memory"); }
DEV void sched_fence() { __builtin_amdgcn_sched_barrier(0); }
template <int I, int N, class Fn> DEV void static_for(Fn&& f) { if constexpr (I < N) { f(std::integral_constant<int, I>{}); static_for<I + 1, N>(f); } }

DEV float bflo(unsigned v) { return __uint_as_float(v << 16); }
DEV float bfhi(unsigned v) { return __uint_as_float(v & 0xffff0000u); }
DEV unsigned pack2(float lo, float hi) { bf2_t v; v[0] = (__bf16)lo; v[1] = (__bf16)hi; return __builtin_bit_cast(unsigned, v); }
DEV float rbf(float f) { return bflo(pack2(f, f)); }
DEV float dot2(unsigned a, unsigned b, float c) { return __builtin_amdgcn_fdot2_f32_bf16(__builtin_bit_cast(bf2_t, a), __builtin_bit_cast(bf2_t, b), c, false); }
DEV f32x4 mfma(bf16x8 a, bf16x8 b, f32x4 c) { return __builtin_amdgcn_mfma_f32_16x16x32_bf16(a, b, c, 0, 0, 0); }

DEV u32x4 mkrs(const void* p, unsigned bytes) {
  const unsigned long long a = (unsigned long long)p;
  u32x4 r; r.x = (unsigned)a; r.y = (unsigned)(a >> 32) & 0xffffu; r.z = bytes; r.w = 0x00020000u; return r;
}
DEV __amdgpu_buffer_rsrc_t mkrsrc(const void* p, unsigned bytes) { return __builtin_amdgcn_make_buffer_rsrc((void*)p, 0, bytes, 0x00020000); }
#define LD_P(v, r, off, sc) "buffer_load_dwordx4 " v ", " r ", %0 offen offset:" off sc " lds\n\t"
#define LD_H(v, r, off, sc) "s_mov_b32 exec_hi, 0\n\tbuffer_load_dwordx4 " v ", " r ", %0 offen offset:" off sc " lds\n\ts_mov_b32 exec_hi, -1"
#define HD_OR  "s_add_u32 m0, %1, %2\n\ts_or_b32 %0, %3, %4\n\t"
#define HD_ADD "s_add_u32 m0, %1, %2\n\ts_add_u32 %0, %3, %4\n\t"
#define HD_MAD(M) "s_mul_i32 %0, %3, " M "\n\ts_add_u32 m0, %1, %2\n\ts_add_u32 %0, %0, %4\n\t"
#define DMA_IN(IMM) "s"(lds), "n"(IMM), "s"(a), "s"(b)
template <int IMM> DEV void dma_w4_or(unsigned lds, unsigned a, unsigned b, u32x4 ra, u32x4 rb, unsigned v0, unsigned v1, unsigned v2, unsigned v3) { unsigned t;
  asm volatile(HD_OR LD_P("%7", "%5", "0", "") LD_P("%8", "%5", "1024", "") LD_P("%9", "%6", "2048", "") LD_P("%10", "%6", "3072", "")
               : "=&s"(t) : DMA_IN(IMM), "s"(ra), "s"(rb), "v"(v0), "v"(v1), "v"(v2), "v"(v3) : "memory", "scc"); }
template <int IMM> DEV void dma_w2_or(unsigned lds, unsigned a, unsigned b, u32x4 r, unsigned v0, unsigned v1) { unsigned t;
  asm volatile(HD_OR LD_P("%6", "%5", "0", "") LD_P("%7", "%5", "1024", "") : "=&s"(t) : DMA_IN(IMM), "s"(r), "v"(v0), "v"(v1) : "memory", "scc"); }
template <int IMM, int SOFF> DEV void dma_w2h(unsigned lds, unsigned a, u32x4 r, unsigned v0, unsigned v1, unsigned v2) { unsigned t;
  asm volatile("s_add_u32 m0, %1, %2\n\ts_add_u32 %0, %3, %4\n\t" LD_P("%6", "%5", "0", "") LD_P("%7", "%5", "1024", "") LD_H("%8", "%5", "2048", "")
               : "=&s"(t) : "s"(lds), "n"(IMM), "s"(a), "n"(SOFF), "s"(r), "v"(v0), "v"(v1), "v"(v2) : "memory", "scc"); }
template <int IMM, int SOFF> DEV void dma_x2(unsigned lds, unsigned a, u32x4 r, unsigned v) { unsigned t;
  asm volatile("s_add_u32 m0, %1, %2\n\ts_add_u32 %0, %3, %4\n\t" LD_P("%6", "%5", "0", " sc1") LD_P("%6", "%5", "1024", " sc1") : "=&s"(t) : "s"(lds), "n"(IMM), "s"(a), "n"(SOFF), "s"(r), "v"(v) : "memory", "scc"); }
template <int IMM, int SOFF> DEV void dma_x2h(unsigned lds, unsigned a, u32x4 r, unsigned v) { unsigned t;
  asm volatile("s_add_u32 m0, %1, %2\n\ts_add_u32 %0, %3, %4\n\t" LD_P("%6", "%5", "0", " sc1") LD_P("%6", "%5", "1024", " sc1") LD_H("%6", "%5", "2048", " sc1") : "=&s"(t) : "s"(lds), "n"(IMM), "s"(a), "n"(SOFF), "s"(r), "v"(v) : "memory", "scc"); }
template <int IMM, int SOFF> DEV void dma_x3h(unsigned lds, unsigned a, u32x4 r, unsigned v) { unsigned t;
  asm volatile("s_add_u32 m0, %1, %2\n\ts_add_u32 %0, %3, %4\n\t" LD_P("%6", "%5", "0", " sc1") LD_P("%6", "%5", "1024", " sc1") LD_P("%6", "%5", "2048", " sc1") LD_H("%6", "%5", "3072", " sc1") : "=&s"(t) : "s"(lds), "n"(IMM), "s"(a), "n"(SOFF), "s"(r), "v"(v) : "memory", "scc"); }
template <int IMM> DEV void dma_x2h_mad640(unsigned lds, unsigned a, unsigned b, u32x4 r, unsigned v) { unsigned t;
  asm volatile(HD_MAD("640") LD_P("%6", "%5", "0", " sc1") LD_P("%6", "%5", "1024", " sc1") LD_H("%6", "%5", "2048", " sc1") : "=&s"(t) : DMA_IN(IMM), "s"(r), "v"(v) : "memory", "scc"); }
template <int IMM> DEV void dma_x3h_mad880(unsigned lds, unsigned a, unsigned b, u32x4 r, unsigned v) { unsigned t;
  asm volatile(HD_MAD("880") LD_P("%6", "%5", "0", " sc1") LD_P("%6", "%5", "1024", " sc1") LD_P("%6", "%5", "2048", " sc1") LD_H("%6", "%5", "3072", " sc1") : "=&s"(t) : DMA_IN(IMM), "s"(r), "v"(v) : "memory", "scc"); }
DEV void dma_1(unsigned lds, u32x4 r, unsigned v) { asm volatile("s_mov_b32 m0, %0\n\ts_nop 0\n\tbuffer_load_dwordx4 %2, %1, 0 offen sc1 lds" ::"s"(lds), "s"(r), "v"(v) : "memory"); }
template <int N> DEV void wait_bar() { asm volatile("s_waitcnt vmcnt(%0)\n\ts_barrier" ::"n"(N) : "memory"); }
DEV void warm(unsigned lds, u32x4 r, unsigned v) { asm volatile("s_mov_b32 m0, %0\n\ts_nop 0\n\tbuffer_load_dword %2, %1, 0 offen lds" ::"s"(lds), "s"(r), "v"(v) : "memory"); }
constexpr unsigned WBIAS = 4096;
DEV u32x4 mkrs_b(const void* p, unsigned bytes) { return mkrs((const char*)p - WBIAS, bytes + WBIAS); }
constexpr int SC1 = 16;
DEV void st16(__amdgpu_buffer_rsrc_t r, unsigned off, u32x4 v) { __builtin_amdgcn_raw_buffer_store_b128(v, r, off, 0, SC1); }
DEV u32x4 ld16(__amdgpu_buffer_rsrc_t r, unsigned off) { return __builtin_amdgcn_raw_buffer_load_b128(r, off, 0, SC1); }
DEV f32x4 ld16f(__amdgpu_buffer_rsrc_t r, unsigned off) { return __builtin_bit_cast(f32x4, ld16(r, off)); }

template <int CTRL> DEV float dpp_add(float v) { return v + __builtin_bit_cast(float, __builtin_amdgcn_update_dpp(0, __builtin_bit_cast(int, v), CTRL, 0xf, 0xf, true)); }
DEV float xsum_16(float v) { v = dpp_add<0xB1>(v); v = dpp_add<0x4E>(v); v = dpp_add<0x141>(v); return dpp_add<0x140>(v); }
DEV float xsum_r16(float v) { auto r = __builtin_amdgcn_permlane16_swap(__float_as_uint(v), __float_as_uint(v), false, false); return __uint_as_float(r[0]) + __uint_as_float(r[1]); }
DEV float xsum_r32(float v) { auto r = __builtin_amdgcn_permlane32_swap(__float_as_uint(v), __float_as_uint(v), false, false); return __uint_as_float(r[0]) + __uint_as_float(r[1]); }
DEV float xsum_q(float v) { return xsum_r32(xsum_r16(v)); }
DEV float gelu_tanh(float x) {
  const float k1 = -2.0f * 0.7978845608028654f * 1.4426950408889634f;
  const float t = x * __builtin_fmaf(x * x, 0.044715f * k1, k1);
  return x * __builtin_amdgcn_rcpf(1.0f + __builtin_amdgcn_exp2f(t));
}
DEV f32x2 gelu2(f32x2 x) {
  const float k1 = -2.0f * 0.7978845608028654f * 1.4426950408889634f;
  const f32x2 t = x * ((x * x) * (0.044715f * k1) + k1);
  const f32x2 d = (f32x2){__builtin_amdgcn_exp2f(t[0]), __builtin_amdgcn_exp2f(t[1])} + 1.0f;
  return x * (f32x2){__builtin_amdgcn_rcpf(d[0]), __builtin_amdgcn_rcpf(d[1])};
}
DEV u32x4 swap_pair(u32x2 a, u32x2 b) {
  auto r0 = __builtin_amdgcn_permlane16_swap(a.x, b.x, false, false);
  auto r1 = __builtin_amdgcn_permlane16_swap(a.y, b.y, false, false);
  return (u32x4){r0[0], r1[0], r0[1], r1[1]};
}

DEV bool spin_fail(unsigned& spins, char* ws) {
  __builtin_amdgcn_s_sleep(1);
  if (++spins > (1u << 17)) { __hip_atomic_store((gu32*)(ws + WS_TMO), 1u, RLX_AGENT); return true; }
  return false;
}
DEV void set_flag(char* ws, unsigned off, unsigned tag) { __hip_atomic_store((gu32*)(ws + off), tag, RLX_AGENT); }
DEV void wait_flags8(char* ws, unsigned off, unsigned tag, int lane) {
  for (unsigned spins = 0;;) {
    const unsigned v = lane < 8 ? __hip_atomic_load((gu32*)(ws + off) + lane, RLX_AGENT) : tag;
    if (__all(v == tag)) break;
    if (spin_fail(spins, ws)) break;
  }
}
DEV bool flags_ok(u32x4 v, unsigned tag) { return __all(v.x == tag && v.y == tag && v.z == tag && v.w == tag); }
DEV u32x4 peek256(__amdgpu_buffer_rsrc_t rf, unsigned off, int lane) { return ld16(rf, off + (lane >> 1) * 128 + (lane & 1) * 16); }
DEV void wait256(__amdgpu_buffer_rsrc_t rf, char* ws, unsigned off, unsigned tag, int lane) {
  for (unsigned spins = 0;;) { if (flags_ok(peek256(rf, off, lane), tag)) break; if (spin_fail(spins, ws)) break; }
}

DEV void wait_panels(__amdgpu_buffer_rsrc_t rf, char* ws, unsigned off, int first, int count, unsigned tag, int lane) {
  const unsigned o = lane < 2 * count ? off + (((lane >> 1) + first) & 31) * 128 + (lane & 1) * 16 : off + (first & 31) * 128;
  for (unsigned spins = 0;;) { if (flags_ok(ld16(rf, o), tag)) break; if (spin_fail(spins, ws)) break; }
}

template <int NS, int PF, int S_, int CW, int CX, class IW, class IX>
DEV void loader_run(IW&& issueW, IX&& issueX) {
  constexpr auto cnt = [](int s) { return (s >= PF ? CW : 0) + CX; };
  constexpr auto span = [=](int a, int b) { int c = 0; for (int s = a; s <= b; ++s) c += (s >= PF ? CW : 0) + CX; return c; };
  auto step = [&](auto ic) { if constexpr (decltype(ic)::value >= PF) issueW(ic); issueX(ic); };
  static_for<0, NS>(step);
  wait_bar<span(1, NS - 1)>();
  static_for<0, S_ - 1>([&](auto ic) { constexpr int n = decltype(ic)::value;
    constexpr int last = n + NS - 1 < S_ - 1 ? n + NS - 1 : S_ - 1;
    wait_bar<span(n + 2, last)>();
    if constexpr (n + NS < S_) step(std::integral_constant<int, n + NS>{}); });
  (void)cnt;
}

template <int NS, int S_, int NA, int NB, int NM, class Rd, class Mm, class Hook, class Post>
DEV void ksplit_steps(bf16x8 (&A0)[NA], bf16x8 (&B0)[NB], bf16x8 (&A1)[NA], bf16x8 (&B1)[NB], int& slot, int n0, int n1, Rd&& rd, Mm&& mm, Hook&& hook, Post&& post) {
  constexpr int NR = NA + NB;
#define KS_ILV() do { _Pragma("unroll") for (int z = 0; z < NR; ++z) { __builtin_amdgcn_sched_group_barrier(0x8, 1, 0); __builtin_amdgcn_sched_group_barrier(0x100, 1, 0); } __builtin_amdgcn_sched_group_barrier(0x8, NM - NR, 0); } while (0)
#pragma unroll 1
  for (int n = n0; n < n1; n += 2) {
    const int s1 = slot + 1 == NS ? 0 : slot + 1;
    hook(n); wait_lgkm0(); bar(); post(n); sched_fence();
    rd(A1, B1, s1, n + 1); mm(A0, B0, false); KS_ILV(); sched_fence();
    const int s2 = s1 + 1 == NS ? 0 : s1 + 1;
    hook(n + 1); wait_lgkm0(); bar(); post(n + 1); sched_fence();
    rd(A0, B0, s2, n + 2); mm(A1, B1, false); KS_ILV(); sched_fence();
    slot = s2;
  }
}
template <int NS, int S_, int NA, int NB, class Rd, class Mm>
DEV void ksplit_last(bf16x8 (&A0)[NA], bf16x8 (&B0)[NB], bf16x8 (&A1)[NA], bf16x8 (&B1)[NB], int& slot, Rd&& rd, Mm&& mm) {
  const int s1 = slot + 1 == NS ? 0 : slot + 1;
  wait_lgkm0(); bar(); sched_fence();
  rd(A1, B1, s1, S_ - 1); mm(A0, B0, false); mm(A1, B1, true);
  slot = s1;
}
template <int NS, int S_, int NA, int NB, class Rd, class Mm>
DEV void ksplit_loop(Rd&& rd, Mm&& mm) {
  static_assert((S_ & 1) == 0, "even step count");
  bf16x8 A0[NA], B0[NB], A1[NA], B1[NB];
  bar();
  rd(A0, B0, 0, 0);
  int slot = 0;
  ksplit_steps<NS, S_, NA, NB, NA * NB>(A0, B0, A1, B1, slot, 0, S_ - 2, rd, mm, [](int) {}, [](int) {});
  ksplit_last<NS, S_, NA, NB>(A0, B0, A1, B1, slot, rd, mm);
}
template <int N>
DEV void kreduce(f32x4 (&acc)[N][2], f32x4 (&res)[N], LAS char* scratch, int w, int lane) {
  const int kh = w & 1;
  LAS char* mine = scratch + w * (N * 1024) + lane * 16;
  LAS char* other = scratch + (w ^ 1) * (N * 1024) + lane * 16;
  if (kh) { asm volatile("");
#pragma unroll
    for (int i = 0; i < N; ++i) { *(LAS f32x4*)(mine + i * 1024) = acc[i][0]; res[i] = acc[i][1]; }
  } else { asm volatile("");
#pragma unroll
    for (int i = 0; i < N; ++i) { *(LAS f32x4*)(mine + i * 1024) = acc[i][1]; res[i] = acc[i][0]; }
  }
  wait_lgkm0(); bar();
#pragma unroll
  for (int i = 0; i < N; ++i) res[i] += *(LAS f32x4*)(other + i * 1024);
}

#define WARM 1
#define JROT 1
#define A_PRE 2
#define PFW 6
#define PRE1 2
#define PFC 5
#define A_PRE2 2
#define LPRIO 0
#define CPRIO 2
#define A_NS_ 5
#define B_NS_ 6
#define D_NS_ 5
#define E_NS_ 6
constexpr int A_NS = A_NS_, A_SLOT = 26 * 1024, A_SCR = 52 * 1024;
constexpr int P0_T = 130 * 1024, P0_PT = 140 * 1024, P0_RED = 157 * 1024;
constexpr int B_NS = B_NS_, B_SLOT = 18 * 1024, B_SCR = (B_NS_ == 8 ? 0 : 36 * 1024);
constexpr int C_NS = 5, C_X = 0, C_W = 82 * 1024, C_WSLOT = 14 * 1024;
constexpr int C_SCR = C_W + 2 * C_WSLOT;
constexpr int D_NS = D_NS_, D_SLOT = 22 * 1024, D_SCR = (D_NS_ == 7 ? 0 : 110 * 1024);
constexpr int E_NS = E_NS_, E_SLOT = 18 * 1024, E_SCR = (E_NS_ == 8 ? 0 : 110 * 1024);
constexpr int T_MODB = 152 * 1024;
constexpr int T_CV = T_MODB + 3072 + 64;
constexpr int T_MOD = 158 * 1024;
constexpr int T_DUMMY = T_MOD + 1792;
constexpr int LDS_BYTES = 160 * 1024;
static_assert(T_CV + 1792 <= P0_RED && P0_RED + 256 <= T_MOD && C_W + C_NS * C_WSLOT <= T_MODB && A_NS * A_SLOT <= P0_RED && D_NS * D_SLOT <= T_CV && B_NS * B_SLOT <= T_MODB && E_NS * E_SLOT <= T_MODB, "LDS map");

#define IC(n) std::integral_constant<int, (n)>{}
DEV void loader_main(const Params& P, const unsigned lds0, LAS char* smem, const int lw, const int lane_, const int b, const int p, const int j, const int krot) {
  STAMP_DECL STAMP(0);
  char* const ws = P.ws;
  const int ft0 = 27 * (j >> 2) + 7 * (j & 3);
#define LLANE int lane = lane_; asm volatile("" : "+v"(lane)); const unsigned vx = lane * 16; (void)vx;
#define LV4K const unsigned v4k0 = (unsigned)(lane >> 3) * 4096u + (unsigned)(((lane & 7) ^ (lane >> 4)) * 16), v4k1 = (unsigned)(8 + (lane >> 3)) * 4096u + (unsigned)(((lane & 7) ^ (4 + (lane >> 4))) * 16);
  {
    LLANE LV4K
    const u32x4 rre = mkrs_b(P.wsr, S * S * 2), rim = mkrs_b(P.wsi, S * S * 2), rx = mkrs(ws + WS_XN, S * D * 2);
    const unsigned wrow = (unsigned)(64 * p + 16 * lw) * 4096u;
    const unsigned w0 = v4k0 + WBIAS, w1 = v4k1 + WBIAS - 1024, w2 = v4k0 + WBIAS - 2048, w3 = v4k1 + WBIAS - 3072;
    const unsigned lw_w = lds0 + lw * 4096, lw_x = lds0 + 16384 + lw * 2560;
    const unsigned xo = (unsigned)(((lw >> 1) * 40 + 5 * j) * 1024 + (lw & 1) * 2560);
    unsigned kw = krot * 128, kx = kw;
    auto issueW = [&](auto ic) { dma_w4_or<(decltype(ic)::value % A_NS) * A_SLOT>(lw_w, kw, wrow, rre, rim, w0, w1, w2, w3); kw = (kw + 128) & 0xf80; };
    auto issueX = [&](auto ic) { dma_x2h_mad640<(decltype(ic)::value % A_NS) * A_SLOT>(lw_x, kx, xo, rx, vx); kx = (kx + 128) & 0xf80; };
    static_for<0, A_PRE>(issueW);
    bar();
    bar();
    STAMP(30);
    static_for<A_PRE, A_PRE + A_PRE2>(issueW);
    const int idx = b >> 3, unit0 = (idx & 3) + 4 * ((b & 7) + 8 * (idx >> 2));
    const bool domod = unit0 < 240; const int unit = domod ? unit0 : 0;
    {
      u32x4 wm[5]; unsigned cs[5];
#pragma unroll
      for (int i = 0; i < 5; ++i) { const int row = lw * 160 + i * 32 + (lane >> 1);
        wm[i] = *(const u32x4*)(P.wmod + (size_t)row * (6 * D) + unit * 16 + (lane & 1) * 8); cs[i] = P.c[row]; }
      float a[8] = {0, 0, 0, 0, 0, 0, 0, 0};
#pragma unroll
      for (int i = 0; i < 5; ++i) { const float x = bflo(cs[i]); const float s = rbf(x * __builtin_amdgcn_rcpf(1.0f + __expf(-x)));
        a[0] += s * bflo(wm[i].x); a[1] += s * bfhi(wm[i].x); a[2] += s * bflo(wm[i].y); a[3] += s * bfhi(wm[i].y);
        a[4] += s * bflo(wm[i].z); a[5] += s * bfhi(wm[i].z); a[6] += s * bflo(wm[i].w); a[7] += s * bfhi(wm[i].w); }
#pragma unroll
      for (int e = 0; e < 8; ++e) { float v = dpp_add<0x4E>(a[e]);
        v = dpp_add<0x124>(v); v = dpp_add<0x128>(v);
        a[e] = xsum_q(v); }
      if (lane < 2) {
#pragma unroll
        for (int e = 0; e < 8; ++e) ((LAS float*)(smem + P0_RED))[lw * 16 + lane * 8 + e] = a[e];
      }
      wait_lgkm0();
    }
    STAMP(32);
    bar();
    STAMP(33);
    __builtin_amdgcn_s_setprio(LPRIO);
    auto istep = [&](auto ic) { if constexpr (decltype(ic)::value >= A_PRE + A_PRE2) issueW(ic); issueX(ic); };
#define A_CNT(s_) (((s_) >= A_PRE + A_PRE2 ? 4 : 0) + 3)
    static_assert((A_NS == 5 || A_NS == 6) && A_PRE + A_PRE2 <= A_NS, "start-up schedule");
    istep(IC(0)); istep(IC(1));
    wait_bar<A_CNT(1)>();
    STAMP(34);
    istep(IC(2)); istep(IC(3));
    if (lw == 0 && domod && lane < 16) {
      const LAS float* red = (const LAS float*)(smem + P0_RED);
      float s = rbf((red[lane] + red[16 + lane]) + (red[32 + lane] + red[48 + lane]));
      const int n = unit * 16 + lane, seg = n / D;
      if (seg == 1 || seg == 4) s = rbf(1.0f + s);
      __hip_atomic_store((gu32*)(ws + WS_MOD) + n, __float_as_uint(s), RLX_AGENT);
      const unsigned hi = __float_as_uint(s) >> 16, nb = (unsigned)__builtin_amdgcn_update_dpp(0, (int)hi, 0xB1, 0xf, 0xf, true);
      if ((seg == 3 || seg == 4) && !(lane & 1)) __hip_atomic_store((gu32*)(ws + WS_MODB) + (((seg == 4 ? 0 : D) + n - seg * D) >> 1), hi | (nb << 16), RLX_AGENT);
    }
    wait_bar<A_CNT(2) + A_CNT(3)>();
    static_for<4, A_NS + 1>(istep);
    wait_bar<A_CNT(3) + A_CNT(4) + A_CNT(5) + (A_NS == 6 ? A_CNT(6) : 0)>();
    const u32x4 rmod = mkrs(ws + WS_MOD, 3840 * 4);
    const int tl = lane / 20, tc = lane - 20 * tl;
    const unsigned tv0 = lane < 60 ? (unsigned)((tl * D + 80 * j) * 4 + tc * 16) : 0x7ffffff0u;
    const unsigned tv1 = lane < 40 ? (unsigned)(((4 + tl) * D + 80 * j) * 4 + tc * 16) : 0x7ffffff0u;
    istep(IC(A_NS + 1));
    static_for<2, 32 - A_NS - 0>([&](auto ic) { constexpr int n = decltype(ic)::value;
      constexpr int cnt = [] { int c = 0; for (int s_ = n + 2; s_ <= n + A_NS - 1; ++s_) c += A_CNT(s_); return c; }();
      wait_bar<cnt>(); if constexpr (n + A_NS < 32) istep(IC(n + A_NS));
      if constexpr (n == 20) { if (lw == 0) { dma_1(lds0 + T_MOD, rmod, tv0); dma_1(lds0 + T_MOD + 960, rmod, tv1); } }
    });
    if (WARM) {
      { const int row = (j * 4 + lw) * 2 + (lane >> 5); warm(lds0 + T_DUMMY, mkrs(P.wsp, S * S * 2), (unsigned)(64 * p + row) * 4096u + (lane & 31) * 128); }
      warm(lds0 + T_DUMMY, mkrs((lw & 1) ? P.wdi : P.wdr, D * D * 2), lw < 2 && lane < 25 ? (unsigned)(b * 25 + lane) * 128 : 0x7ffffff0u);
    }
    static_for<0, A_NS - 1>([&](auto ic) { constexpr int r = A_NS - 2 - decltype(ic)::value; wait_bar<r * 7 + (WARM ? 2 : 0)>(); });
    bar();
  }
  {
    LLANE
    const u32x4 rre = mkrs_b(P.wdr, D * D * 2), rim = mkrs_b(P.wdi, D * D * 2), rx = mkrs(ws + WS_UV, 32 * 2 * 20 * 4096);
    auto wv = [&](int k) { const int r = 20 * lw + 8 * k + (lane >> 3); return (unsigned)(r * 1280 + (((lane & 7) ^ ((r >> 1) & 7)) * 16)) + WBIAS - 1024 * k; };
    const unsigned v0 = wv(0), v1 = wv(1), v2 = wv(2);
    const unsigned wb = (unsigned)(80 * j) * 1280u, lw_w = lds0 + lw * 2560, lw_x = lds0 + 10240 + lw * 2048, xb = (unsigned)p * 40 * 4096u + lw * 2048;
    auto issueW = [&](auto ic) { constexpr int n = decltype(ic)::value; dma_w2h<(n % B_NS) * B_SLOT, (n % 10) * 128>(lw_w, wb, n >= 10 ? rim : rre, v0, v1, v2); };
    auto issueX = [&](auto ic) { constexpr int n = decltype(ic)::value; dma_x2<(n % B_NS) * B_SLOT, 2 * n * 4096>(lw_x, xb, rx, vx); };
    __builtin_amdgcn_s_setprio(0);
    static_for<0, (PRE1 < PFW ? PRE1 : PFW)>(issueW);
    bar();
    static_for<(PRE1 < PFW ? PRE1 : PFW), PFW>(issueW);
    bar();
    __builtin_amdgcn_s_setprio(LPRIO);
    loader_run<B_NS, PFW, 20, 3, 2>(issueW, issueX);
    bar();
  }
  {
    LLANE
    const u32x4 rw = mkrs(ws + WS_WIN, 20 * 109 * 1024), rx = mkrs(ws + WS_X1P, 32 * 20 * 4096), rmb = mkrs(ws + WS_MODB, 2560);
    const unsigned wo = (unsigned)(((lw >> 1) * 109 + ft0) * 1024 + (lw & 1) * 3584);
    const unsigned lw_w = lds0 + C_W + lw * 3584, lw_x = lds0 + C_X + lw * 2048, xb = (unsigned)p * 20 * 4096u + lw * 2048;
    auto issueW = [&](auto ic) { constexpr int n = decltype(ic)::value, z1 = n >= 10; dma_x3h<(n % C_NS) * C_WSLOT, (2 * (n - 10 * z1) * 109 + (z1 ? 0 : 54)) * 1024>(lw_w, wo, rw, vx); };
    auto issueX = [&](auto ic) { constexpr int n = decltype(ic)::value; dma_x2<2 * n * 4096, 2 * n * 4096>(lw_x, xb, rx, vx); };
    __builtin_amdgcn_s_setprio(0);
    static_for<0, (PRE1 < PFC ? PRE1 : PFC)>(issueW);
    bar();
    if (lw < 3) dma_1(lds0 + T_MODB + lw * 1024, rmb, vx + lw * 1024);
    static_for<(PRE1 < PFC ? PRE1 : PFC), PFC>(issueW);
    bar();
    __builtin_amdgcn_s_setprio(LPRIO);
    static_for<0, C_NS>([&](auto ic) { if constexpr (decltype(ic)::value >= PFC) issueW(ic); issueX(ic); });
    wait_bar<(PFC - 1) * 2 + (C_NS - PFC) * 6>();
    static_for<0, 9>([&](auto ic) {
      constexpr int n = decltype(ic)::value, need = n + 1;
      constexpr int last = n + C_NS - 1 < 11 ? n + C_NS - 1 : 11;
      constexpr int cnt = [] { int c = 0; for (int s = need + 1; s <= last; ++s) c += (s < PFC ? 2 : s < 10 ? 6 : 4); return c; }();
      wait_bar<cnt>();
      if constexpr (n + C_NS < 12) { issueW(IC(n + C_NS)); if constexpr (n + C_NS < 10) issueX(IC(n + C_NS)); }
    });
    bar();
    bar();
    issueW(IC(12)); issueW(IC(13)); issueW(IC(14));
    wait_bar<16>();
    static_for<10, 19>([&](auto ic) {
      constexpr int n = decltype(ic)::value;
      constexpr int last = n + 4 < 19 ? n + 4 : 19;
      wait_bar<(last - (n + 1)) * 4>();
      if constexpr (n + C_NS < 20) issueW(IC(n + C_NS));
    });
    bar();
  }
  {
    LLANE LV4K
    const u32x4 rw = mkrs_b(P.wsp, S * S * 2), rx = mkrs(ws + WS_Z2N, 64 * 55 * 1024);
    const unsigned wrow = (unsigned)(64 * p + 16 * lw) * 4096u, w0 = v4k0 + WBIAS, w1 = v4k1 + WBIAS - 1024;
    const unsigned xo = (unsigned)(((lw >> 1) * 55 + ft0) * 1024 + (lw & 1) * 3584), lw_w = lds0 + lw * 2048, lw_x = lds0 + 8192 + lw * 3584;
    unsigned kw = (b & 7) * 512, kx = kw;
    auto issueW = [&](auto ic) { dma_w2_or<(decltype(ic)::value % D_NS) * D_SLOT>(lw_w, kw, wrow, rw, w0, w1); kw = (kw + 128) & 0xf80; };
    auto issueX = [&](auto ic) { dma_x3h_mad880<(decltype(ic)::value % D_NS) * D_SLOT>(lw_x, kx, xo, rx, vx); kx = (kx + 128) & 0xf80; };
    static_assert(D_NS * D_SLOT <= C_SCR, "ring below the exchange buffer");
    static_for<0, D_NS>(issueW);
    bar();
    static_for<0, D_NS>(issueX);
    wait_bar<(D_NS - 1) * 4>();
    static_for<0, 31>([&](auto ic) { constexpr int n = decltype(ic)::value;
      constexpr int last = n + D_NS - 1 < 31 ? n + D_NS - 1 : 31;
      constexpr int cnt = n + 1 < D_NS ? (D_NS - 2 - n) * 4 + (last - (D_NS - 1)) * 6 : (last - (n + 1)) * 6;
      wait_bar<cnt>();
      if constexpr (n + D_NS < 32) { issueW(IC(n + D_NS)); issueX(IC(n + D_NS)); } });
    bar();
  }
  {
    LLANE
    const u32x4 rw = mkrs(ws + WS_WOUT, 28 * 40 * 1024), rx = mkrs(ws + WS_P, 32 * 28 * 4096);
    const unsigned wo = (unsigned)(((lw >> 1) * 40 + 5 * j) * 1024 + (lw & 1) * 2560), lw_w = lds0 + lw * 2560, lw_x = lds0 + 10240 + lw * 2048, xb = (unsigned)p * 28 * 4096u + lw * 2048;
    auto issueW = [&](auto ic) { constexpr int n = decltype(ic)::value; dma_x2h<(n % E_NS) * E_SLOT, n * 80 * 1024>(lw_w, wo, rw, vx); };
    auto issueX = [&](auto ic) { constexpr int n = decltype(ic)::value; dma_x2<(n % E_NS) * E_SLOT, 2 * n * 4096>(lw_x, xb, rx, vx); };
    __builtin_amdgcn_s_setprio(0);
    static_for<0, (PRE1 < PFW ? PRE1 : PFW)>(issueW);
    bar();
    static_for<(PRE1 < PFW ? PRE1 : PFW), PFW>(issueW);
    bar();
    __builtin_amdgcn_s_setprio(LPRIO);
    loader_run<E_NS, PFW, 14, 3, 2>(issueW, issueX);
    bar();
  }
  STAMP_FLUSH();
}

DEV void consumer_main(const Params& P, LAS char* smem, const int w, const int lane_, const int b, const int p, const int j) {
  const unsigned long long tstart_ = __builtin_amdgcn_s_memrealtime();
  STAMP_DECL STAMP(0);
  if (CPRIO) __builtin_amdgcn_s_setprio(CPRIO);
  char* const ws = P.ws;
  unsigned tagv = __hip_atomic_load((gu32*)(ws + WS_EPOCH), RLX_AGENT) + 1u, tag;
  const int mh = w >> 1, kh = w & 1;
  const auto rws = mkrsrc(ws, WS_END);
  const int ft0 = 27 * (j >> 2) + 7 * (j & 3), nft = (j & 3) == 3 ? 6 : 7;
  auto rot = [&](int i) { return i < 3 ? i + 4 * mh : i - 3 * mh; };
#define LANE_IDS int lane = lane_; asm volatile("" : "+v"(lane)); const int q = lane >> 4, tk = lane & 15, tid = w * 64 + lane, token = 64 * p + 16 * w + tk; (void)q; (void)tk; (void)tid; (void)token;
  {
    LANE_IDS
    unsigned v[2][5];
#pragma unroll
    for (int t = 0; t < 2; ++t) { const unsigned* xr = (const unsigned*)(P.x + (size_t)(64 * p + 8 * j + 2 * w + t) * D);
#pragma unroll
      for (int i = 0; i < 5; ++i) v[t][i] = xr[lane + 64 * i]; }
    const int u0 = w * 256 + b; const bool dopt = u0 < 810; const int u = dopt ? u0 : 0; const bool isin = u < 540;
    const int uu = isin ? u : u - 540, ncb = isin ? 27 : 10, pkb = uu / ncb, pcb = uu - pkb * ncb, ld = isin ? F : D;
    u32x4 pv[4];
    { const bf16_t* src = (isin ? P.win : P.wout) + (size_t)(pkb * 32) * ld + pcb * 64;
#pragma unroll
      for (int i = 0; i < 4; ++i) pv[i] = *(const u32x4*)(src + (size_t)(i * 8 + (lane >> 3)) * ld + (lane & 7) * 8); }
    LAS unsigned* t32 = (LAS unsigned*)(smem + P0_T);
    unsigned pk[2][5];
#pragma unroll
    for (int t = 0; t < 2; ++t) {
      float s = 0.f;
#pragma unroll
      for (int i = 0; i < 5; ++i) s += bflo(v[t][i]) + bfhi(v[t][i]);
      const float mean = xsum_q(xsum_16(s)) * (1.0f / D);
      float q2 = 0.f;
#pragma unroll
      for (int i = 0; i < 5; ++i) { const float a = bflo(v[t][i]) - mean, c = bfhi(v[t][i]) - mean; q2 += a * a + c * c; }
      const float rs = 1.0f / sqrtf(xsum_q(xsum_16(q2)) * (1.0f / D) + 1e-5f);
#pragma unroll
      for (int i = 0; i < 5; ++i) pk[t][i] = pack2((bflo(v[t][i]) - mean) * rs, (bfhi(v[t][i]) - mean) * rs);
    }
#pragma unroll
    for (int i = 0; i < 5; ++i) { const int d = 2 * (lane + 64 * i);
      t32[d * 4 + w] = (pk[0][i] & 0xffffu) | (pk[1][i] << 16); t32[(d + 1) * 4 + w] = (pk[0][i] >> 16) | (pk[1][i] & 0xffff0000u); }
    asm volatile("" : "+v"(tagv)); tag = __builtin_amdgcn_readfirstlane(tagv);
    wait_lgkm0(); bar();
    const unsigned xb = WS_XN + (unsigned)((2 * p + (j >> 2)) * 40 * 1024 + (j & 3) * 256);
#pragma unroll
    for (int i = 0; i < 3; ++i) { const int d = tid + 256 * i;
      if (d < D) st16(rws, xb + (d >> 4) * 1024 + (d & 15) * 16, *(LAS u32x4*)(smem + P0_T + d * 16)); }
    wait_vm<0>();
    bar();
    STAMP(1);
    if (w == 0 && lane == 0) set_flag(ws, WS_FN + p * 128 + j * 4, tag);
    {
      LAS char* t = smem + P0_PT + w * 4096;
#pragma unroll
      for (int i = 0; i < 4; ++i) *(LAS u32x4*)(t + (i * 8 + (lane >> 3)) * 128 + (lane & 7) * 16) = pv[i];
      const unsigned dst = dopt ? (isin ? WS_WIN + (unsigned)(pkb * 109 + pcb * 4) * 1024 : WS_WOUT + (unsigned)(pkb * 40 + pcb * 4) * 1024) + lane * 16 : 0xfffff000u;
      const int rq = tk >> 2, pp = lane & 3;
#pragma unroll
      for (int c = 0; c < 4; ++c) {
        const u32x2 l2 = __builtin_bit_cast(u32x2, __builtin_amdgcn_ds_read_tr16_b64_v4i16((LAS s16x4*)(t + (8 * q + rq) * 128 + c * 32 + pp * 8)));
        const u32x2 h2 = __builtin_bit_cast(u32x2, __builtin_amdgcn_ds_read_tr16_b64_v4i16((LAS s16x4*)(t + (8 * q + 4 + rq) * 128 + c * 32 + pp * 8)));
        st16(rws, dst + (dopt ? c * 1024 : 0), (u32x4){l2.x, l2.y, h2.x, h2.y});
      }
    }
    if (w == 0) { wait256(rws, ws, WS_FN, tag, lane); STAMP(23); }
    bar();
    STAMP(2);
  }
  u32x2 xv[5];
  {
    LANE_IDS
    f32x4 ur[5], ui[5];
#pragma unroll
    for (int i = 0; i < 5; ++i) { ur[i] = (f32x4){0, 0, 0, 0}; ui[i] = (f32x4){0, 0, 0, 0}; }
    f32x4 rsum[2] = {(f32x4){0, 0, 0, 0}, (f32x4){0, 0, 0, 0}};
    bf16x8 ones = __builtin_bit_cast(bf16x8, (u32x4){0x3f803f80u, 0x3f803f80u, 0x3f803f80u, 0x3f803f80u});
    asm volatile("" : "+v"(ones));
    const unsigned wa0 = (unsigned)(w * 4096 + tk * 128 + ((q ^ ((lane >> 1) & 7)) * 16));
    const unsigned xa = 16384 + lane * 16;
    bf16x8 Wa[2], Xa[5], Wb[2], Xb[5];
    auto rdm = [&](bf16x8(&Wn)[2], bf16x8(&Xn)[5], int slot, int kk, bf16x8(&Wf)[2], bf16x8(&Xf)[5]) {
      LAS char* sb = smem + slot * A_SLOT; const unsigned wa = wa0 ^ (kk * 64);
      static_for<0, 12>([&](auto ic) { constexpr int z = decltype(ic)::value, i = z >> 1, t = z & 1;
        if constexpr (z < 10) { if constexpr (t == 0) ur[i] = mfma(Xf[i], Wf[0], ur[i]); else ui[i] = mfma(Xf[i], Wf[1], ui[i]); } else rsum[t] = mfma(ones, Wf[t], rsum[t]);
        if constexpr (z == 0) Wn[0] = *(LAS bf16x8*)(sb + wa);
        if constexpr (z == 1) Xn[0] = *(LAS bf16x8*)(sb + xa + (kk * 5) * 1024);
        if constexpr (z == 2) Wn[1] = *(LAS bf16x8*)(sb + wa + 2048);
        if constexpr (z >= 3 && z < 7) Xn[z - 2] = *(LAS bf16x8*)(sb + xa + (kk * 5 + z - 2) * 1024);
        sched_fence(); });
    };
    auto mmlast = [&](bf16x8(&Wf)[2], bf16x8(&Xf)[5]) {
#pragma unroll
      for (int i = 0; i < 5; ++i) { ur[i] = mfma(Xf[i], Wf[0], ur[i]); ui[i] = mfma(Xf[i], Wf[1], ui[i]); }
      rsum[0] = mfma(ones, Wf[0], rsum[0]); rsum[1] = mfma(ones, Wf[1], rsum[1]);
    };
    int slot = 0;
    auto steps = [&](int n0, int n1, bool hooks) {
#pragma unroll 1
      for (int n = n0; n < n1; ++n) {
        rdm(Wb, Xb, slot, 1, Wa, Xa);
        const int ns = slot + 1 == A_NS ? 0 : slot + 1;
        if (hooks && n == 8) wait_vm<0>();
        wait_lgkm0(); bar(); sched_fence();
        if (hooks && n == 8 && w == 0 && lane == 0) set_flag(ws, WS_F2 + p * 128 + j * 4, tag);
        rdm(Wa, Xa, ns, 0, Wb, Xb);
        slot = ns;
      }
    };
    bar();
    STAMP(3); CSTAMP(40);
    { LAS char* sb = smem; Wa[0] = *(LAS bf16x8*)(sb + wa0); Wa[1] = *(LAS bf16x8*)(sb + wa0 + 2048);
#pragma unroll
      for (int i = 0; i < 5; ++i) Xa[i] = *(LAS bf16x8*)(sb + xa + i * 1024); }
    steps(0, 12, true);
    const u32x4 f2 = peek256(rws, WS_F2, lane);
    steps(12, 18, false);
    if (!flags_ok(f2, tag)) wait256(rws, ws, WS_F2, tag, lane);
    if (b == 0 && w == 0 && lane == 0) set_flag(ws, WS_EPOCH, tag + 15);
#pragma unroll
    for (int i = 0; i < 5; ++i) xv[i] = *(const u32x2*)(P.x + (size_t)token * D + 80 * j + 16 * i + 4 * q);
    steps(18, 31, false);
    { LAS char* sb = smem + slot * A_SLOT; const unsigned wa = wa0 ^ 64;
      Wb[0] = *(LAS bf16x8*)(sb + wa); Wb[1] = *(LAS bf16x8*)(sb + wa + 2048);
#pragma unroll
      for (int i = 0; i < 5; ++i) Xb[i] = *(LAS bf16x8*)(sb + xa + (5 + i) * 1024); }
    mmlast(Wa, Xa);
    wait_lgkm0(); bar();
    mmlast(Wb, Xb);
    STAMP(4); CSTAMP(41);
    const float rs0 = rsum[0][0], rs1 = rsum[1][0];
#pragma unroll
    for (int i = 0; i < 5; ++i) {
      u32x2 a, c;
      const f32x4 sa = *(const LAS f32x4*)(smem + T_MOD + (16 * i + 4 * q) * 4), c1 = *(const LAS f32x4*)(smem + T_MOD + (80 + 16 * i + 4 * q) * 4);
      { const f32x4 u = ur[i] * c1 + rs0 * sa; a.x = pack2(u[0], u[1]); a.y = pack2(u[2], u[3]); }
      { const f32x4 v = -(ui[i] * c1 + rs1 * sa); c.x = pack2(v[0], v[1]); c.y = pack2(v[2], v[3]); }
      const int dt = 5 * j + i, kb = dt >> 1, qq = 2 * (dt & 1) + (q >> 1);
      st16(rws, WS_UV + (unsigned)(((p * 2 + (q & 1)) * 20 + kb) * 4 + w) * 1024 + (qq * 16 + tk) * 16, swap_pair(a, c));
    }
    wait_vm<0>(); bar();
    STAMP(5);
    if (w == 0) { if (lane == 0) set_flag(ws, WS_FU + p * 128 + j * 4, tag); wait_flags8(ws, WS_FU + p * 128, tag, lane); }
    bar();
    STAMP(6); CSTAMP(44);
  }
  u32x2 x1p[5];
  {
    LANE_IDS
    f32x4 acc[5][2];
#pragma unroll
    for (int i = 0; i < 5; ++i) { acc[i][0] = (f32x4){0, 0, 0, 0}; acc[i][1] = (f32x4){0, 0, 0, 0}; }
    const unsigned wa = (unsigned)(tk * 128 + (((q + 4 * kh) ^ ((lane >> 1) & 7)) * 16));
    const unsigned xa = 10240 + (kh * 4 + 2 * mh) * 1024 + lane * 16;
    ksplit_loop<B_NS, 20, 5, 2>(
        [&](bf16x8(&A)[5], bf16x8(&Bf)[2], int slot, int) { LAS char* sb = smem + slot * B_SLOT;
          A[0] = *(LAS bf16x8*)(sb + wa); Bf[0] = *(LAS bf16x8*)(sb + xa); Bf[1] = *(LAS bf16x8*)(sb + xa + 1024);
#pragma unroll
          for (int i = 1; i < 5; ++i) A[i] = *(LAS bf16x8*)(sb + wa + i * 2048); },
        [&](bf16x8(&A)[5], bf16x8(&Bf)[2], bool) {
#pragma unroll
          for (int i = 0; i < 5; ++i) { acc[i][0] = mfma(A[i], Bf[0], acc[i][0]); acc[i][1] = mfma(A[i], Bf[1], acc[i][1]); } });
    STAMP(7); CSTAMP(45);
    f32x4 y[5];
    kreduce<5>(acc, y, smem + B_SCR, w, lane);
    float xs[20]; u32x2 xp[5];
    float s = 0.f;
#pragma unroll
    for (int i = 0; i < 5; ++i) {
      const f32x4 ga = *(const LAS f32x4*)(smem + T_MOD + (160 + 16 * i + 4 * q) * 4), c1b = *(const LAS f32x4*)(smem + T_MOD + 960 + (16 * i + 4 * q) * 4);
      x1p[i].x = pack2(bflo(xv[i].x) + ga[0] * y[i][0], bfhi(xv[i].x) + ga[1] * y[i][1]);
      x1p[i].y = pack2(bflo(xv[i].y) + ga[2] * y[i][2], bfhi(xv[i].y) + ga[3] * y[i][3]);
      asm volatile("" : "+v"(x1p[i].x), "+v"(x1p[i].y));
      xs[4 * i] = bflo(x1p[i].x); xs[4 * i + 1] = bfhi(x1p[i].x); xs[4 * i + 2] = bflo(x1p[i].y); xs[4 * i + 3] = bfhi(x1p[i].y);
      s += (xs[4 * i] + xs[4 * i + 1]) + (xs[4 * i + 2] + xs[4 * i + 3]);
      xp[i].x = pack2(xs[4 * i] * c1b[0], xs[4 * i + 1] * c1b[1]); xp[i].y = pack2(xs[4 * i + 2] * c1b[2], xs[4 * i + 3] * c1b[3]);
    }
    const unsigned img = WS_X1P + (unsigned)p * 20 * 4096 + w * 1024;
#pragma unroll
    for (int i = 0; i < 6; i += 2) {
      const int i2 = i + 1 < 5 ? i + 1 : i;
      const int dt = 5 * j + ((q & 1) ? i2 : i), kb = dt >> 1, qq = 2 * (dt & 1) + (q >> 1);
      st16(rws, img + kb * 4096 + (qq * 16 + tk) * 16, swap_pair(xp[i], xp[i2]));
    }
    const float mean = xsum_q(s) * (1.0f / 80.0f);
    float m2 = 0.f;
#pragma unroll
    for (int e = 0; e < 20; ++e) { const float d = xs[e] - mean; m2 += d * d; }
    m2 = xsum_q(m2);
    if (q == 0) __hip_atomic_store((gu64*)(ws + WS_ST1) + token * 8 + j, ((unsigned long long)__float_as_uint(m2) << 32) | __float_as_uint(mean), RLX_AGENT);
    wait_vm<0>(); bar();
    STAMP(8);
    if (w == 0) { if (lane == 0) set_flag(ws, WS_FX + p * 128 + j * 4, tag); wait_flags8(ws, WS_FX + p * 128, tag, lane); }
    bar();
    STAMP(9);
  }
  u32x2 z1p[7]; float bias;
  {
    LANE_IDS
    u32x4 st[4];
#pragma unroll
    for (int k = 0; k < 4; ++k) st[k] = ld16(rws, WS_ST1 + (unsigned)token * 64 + k * 16);
    bias = bflo(P.bsp[token]);
    for (int i = tid; i < 448; i += 256) ((LAS float*)(smem + T_CV))[i] = 0.f;
    f32x4 acc[7][2], aux[4];
    auto clear = [&]() {
#pragma unroll
      for (int i = 0; i < 7; ++i) { acc[i][0] = (f32x4){0, 0, 0, 0}; acc[i][1] = (f32x4){0, 0, 0, 0}; }
#pragma unroll
      for (int i = 0; i < 4; ++i) aux[i] = (f32x4){0, 0, 0, 0};
    };
    const unsigned xa = C_X + (kh * 4 + 2 * mh) * 1024 + lane * 16, wlo = C_W + kh * 7168 + lane * 16 + mh * 4096, whi = C_W + kh * 7168 + lane * 16 - mh * 3072;
    const int r4 = tk & 3;
    const unsigned ma = T_MODB + (r4 == 0 ? q * 16 + kh * 64 : r4 == 1 ? 1280 + q * 16 + kh * 64 : 2560), mstride = r4 < 2 ? 128 : 0;
    int nbase = 0;
    auto rd = [&](bf16x8(&A)[7], bf16x8(&Bf)[3], int slot, int n) {
      LAS char* sl = smem + wlo + slot * C_WSLOT; LAS char* sh = smem + whi + slot * C_WSLOT; LAS char* xb = smem + xa + n * 8192;
      Bf[0] = *(LAS bf16x8*)(xb); A[0] = *(LAS bf16x8*)(sl); Bf[1] = *(LAS bf16x8*)(xb + 1024);
      A[1] = *(LAS bf16x8*)(sl + 1024); A[2] = *(LAS bf16x8*)(sl + 2048);
#pragma unroll
      for (int i = 3; i < 7; ++i) A[i] = *(LAS bf16x8*)(sh + i * 1024);
      Bf[2] = *(LAS bf16x8*)(smem + ma + n * mstride);
    };
    auto auxmm = [&](bf16x8(&A)[7], bf16x8(&Bf)[3]) {
#pragma unroll
      for (int i = 0; i < 4; ++i) aux[i] = mfma(Bf[2], A[i], aux[i]);
    };
    auto mm2 = [&](bf16x8(&A)[7], bf16x8(&Bf)[3], bool) {
#pragma unroll
      for (int i = 0; i < 7; ++i) { acc[i][0] = mfma(Bf[0], A[i], acc[i][0]); acc[i][1] = mfma(Bf[1], A[i], acc[i][1]); }
      auxmm(A, Bf); };
    auto mm1 = [&](bf16x8(&A)[7], bf16x8(&Bf)[3], bool) {
#pragma unroll
      for (int i = 0; i < 7; ++i) { acc[i][0] = mfma(A[i], Bf[0], acc[i][0]); acc[i][1] = mfma(A[i], Bf[1], acc[i][1]); }
      auxmm(A, Bf); };
    auto put_cv = [&](int pass_) {
      LAS float* cv = (LAS float*)(smem + T_CV) + pass_ * 224;
      if (q == 0) {
#pragma unroll
        for (int i = 0; i < 4; ++i) if (mh == 0 || i < 3) { const int t = rot(i);
          __hip_atomic_fetch_add(cv + 16 * t + tk, aux[i][0], __ATOMIC_RELAXED, __HIP_MEMORY_SCOPE_WORKGROUP);
          __hip_atomic_fetch_add(cv + 112 + 16 * t + tk, aux[i][1], __ATOMIC_RELAXED, __HIP_MEMORY_SCOPE_WORKGROUP); }
      }
    };
    float rmean, rrstd;
    { float mu[8], mean = 0.f;
#pragma unroll
      for (int k = 0; k < 4; ++k) { mu[2 * k] = __uint_as_float(st[k].x); mu[2 * k + 1] = __uint_as_float(st[k].z); mean += mu[2 * k] + mu[2 * k + 1]; }
      mean *= 0.125f; float M2 = 0.f;
#pragma unroll
      for (int k = 0; k < 4; ++k) { const float d0 = mu[2 * k] - mean, d1 = mu[2 * k + 1] - mean; M2 += __uint_as_float(st[k].y) + __uint_as_float(st[k].w) + 80.0f * (d0 * d0 + d1 * d1); }
      rmean = mean; rrstd = 1.0f / sqrtf(M2 * (1.0f / D) + 1e-5f); }
    asm volatile("" : "+v"(rmean), "+v"(rrstd));
    (void)nbase;
    bf16x8 A0[7], B0[3], A1[7], B1[3];
    int slot = 0;
    auto nohook = [](int) {};
    clear();
    wait_lgkm0(); bar();
    STAMP(10); CSTAMP(46);
    rd(A0, B0, 0, 0);
    ksplit_steps<C_NS, 10, 7, 3, 18>(A0, B0, A1, B1, slot, 0, 8, rd, mm2, nohook, nohook);
    ksplit_last<C_NS, 10, 7, 3>(A0, B0, A1, B1, slot, rd, mm2);
    STAMP(11); CSTAMP(47);
    put_cv(0);
    f32x4 zs[7];
    kreduce<7>(acc, zs, smem + C_SCR, w, lane);
    wait_lgkm0(); bar();
    f32x2 mr01, mr23, rr01, rr23;
    mr01 = (f32x2){__shfl(rmean, 4 * q), __shfl(rmean, 4 * q + 1)}; mr23 = (f32x2){__shfl(rmean, 4 * q + 2), __shfl(rmean, 4 * q + 3)};
    rr01 = (f32x2){__shfl(rrstd, 4 * q), __shfl(rrstd, 4 * q + 1)}; rr23 = (f32x2){__shfl(rrstd, 4 * q + 2), __shfl(rrstd, 4 * q + 3)};
    f32x2 z01[7], z23[7];
#pragma unroll
    for (int i = 0; i < 7; ++i) {
      const float c1 = ((const LAS float*)(smem + T_CV))[16 * rot(i) + tk], c2 = ((const LAS float*)(smem + T_CV))[112 + 16 * rot(i) + tk];
      z01[i] = gelu2(rr01 * ((f32x2){zs[i][0], zs[i][1]} - mr01 * c1) + c2);
      z23[i] = gelu2(rr23 * ((f32x2){zs[i][2], zs[i][3]} - mr23 * c1) + c2);
    }
    {
      const float inv = 1.0f / (16.0f * nft);
      const f32x2 zero2 = (f32x2){0.f, 0.f};
      f32x2 s01 = zero2, s23 = zero2;
#pragma unroll
      for (int i = 0; i < 7; ++i) { const bool ok = (i != 2 && i != 6) || rot(i) < nft; s01 += ok ? z01[i] : zero2; s23 += ok ? z23[i] : zero2; }
      const f32x2 m01 = (f32x2){xsum_16(s01[0]), xsum_16(s01[1])} * inv, m23 = (f32x2){xsum_16(s23[0]), xsum_16(s23[1])} * inv;
      f32x2 t01 = zero2, t23 = zero2;
#pragma unroll
      for (int i = 0; i < 7; ++i) { const bool ok = (i != 2 && i != 6) || rot(i) < nft; const f32x2 a = ok ? z01[i] - m01 : zero2, c = ok ? z23[i] - m23 : zero2; t01 += a * a; t23 += c * c; }
      const float q0 = xsum_16(t01[0]), q1 = xsum_16(t01[1]), q2 = xsum_16(t23[0]), q3 = xsum_16(t23[1]);
      if (tk < 8) { const int r = tk & 3; const float a = r == 0 ? m01[0] : r == 1 ? m01[1] : r == 2 ? m23[0] : m23[1], c = r == 0 ? q0 : r == 1 ? q1 : r == 2 ? q2 : q3;
        const int tok = 64 * p + 16 * w + 4 * q + r;
        __hip_atomic_store((gu64*)(ws + WS_ST2) + (tok * 8 + j) * 2 + (tk >> 2), ((unsigned long long)(tag ^ __float_as_uint(tk < 4 ? c : a)) << 32) | __float_as_uint(tk < 4 ? a : c), RLX_AGENT); }
    }
    STAMP(12); CSTAMP(48);
    clear();
    slot = 0;
    bar();
    rd(A0, B0, 0, 0);
    ksplit_steps<C_NS, 10, 7, 3, 18>(A0, B0, A1, B1, slot, 0, 4, rd, mm1, nohook, nohook);
    STAMP(13);
    {
      u32x4 g0, g1;
      const unsigned go = WS_ST2 + (unsigned)(token * 8 + 2 * q) * 16;
      for (unsigned spins = 0;;) { g0 = ld16(rws, go); g1 = ld16(rws, go + 16);
        if (__all(g0.y == (tag ^ g0.z) && g0.w == (tag ^ g0.x) && g1.y == (tag ^ g1.z) && g1.w == (tag ^ g1.x))) break; if (spin_fail(spins, ws)) break; }
      const float n0 = 112.0f, n1 = (q & 1) ? 96.0f : 112.0f;
      const float m0 = __uint_as_float(g0.x), m1 = __uint_as_float(g1.x);
      const float mean = xsum_q(n0 * m0 + n1 * m1) * (1.0f / FH);
      const float d0 = m0 - mean, d1 = m1 - mean;
      const float M2 = xsum_q(__uint_as_float(g0.z) + __uint_as_float(g1.z) + n0 * d0 * d0 + n1 * d1 * d1);
      const float rstd = 1.0f / sqrtf(M2 * (1.0f / FH) + 1e-5f);
      mr01 = (f32x2){__shfl(mean, 4 * q), __shfl(mean, 4 * q + 1)}; mr23 = (f32x2){__shfl(mean, 4 * q + 2), __shfl(mean, 4 * q + 3)};
      rr01 = (f32x2){__shfl(rstd, 4 * q), __shfl(rstd, 4 * q + 1)}; rr23 = (f32x2){__shfl(rstd, 4 * q + 2), __shfl(rstd, 4 * q + 3)};
    }
    {
      u32x2 pk[7];
#pragma unroll
      for (int i = 0; i < 7; ++i) { const f32x2 a = (z01[i] - mr01) * rr01, c = (z23[i] - mr23) * rr23; pk[i].x = pack2(a[0], a[1]); pk[i].y = pack2(c[0], c[1]); }
      const unsigned img = WS_Z2N + (unsigned)(2 * p + (w >> 1)) * 55 * 1024 + ((2 * (w & 1) + (q >> 1)) * 16 + tk) * 16;
#pragma unroll
      for (int i = 0; i < 8; i += 2) {
        const int i2 = i + 1 < 7 ? i + 1 : i;
        const u32x4 ch = swap_pair(pk[i], pk[i2]);
        const int ti = (q & 1) ? rot(i2) : rot(i);
        st16(rws, ti < nft ? img + (ft0 + ti) * 1024 : 0xfffff000u, ch);
      }
    }
    STAMP(14);
    ksplit_steps<C_NS, 10, 7, 3, 18>(A0, B0, A1, B1, slot, 4, 8, rd, mm1,
        [&](int n) { if (n == 5) wait_vm<0>(); },
        [&](int n) { if (n == 5 && w == 0 && lane == 0) set_flag(ws, WS_FZ + (j * 4 + (p >> 3)) * 128 + (p & 7) * 4, tag); });
    ksplit_last<C_NS, 10, 7, 3>(A0, B0, A1, B1, slot, rd, mm1);
    STAMP(15); CSTAMP(49);
    const unsigned fzo = WS_FZ + (j * 4 + ((lane & 7) >> 1)) * 128 + (lane & 1) * 16;
    const u32x4 fz = ld16(rws, fzo);
    put_cv(1);
    kreduce<7>(acc, zs, smem + C_SCR, w, lane);
    if (!flags_ok(fz, tag)) { for (unsigned spins = 0;;) { if (flags_ok(ld16(rws, fzo), tag)) break; if (spin_fail(spins, ws)) break; } }
    wait_lgkm0(); bar();
#pragma unroll
    for (int i = 0; i < 7; ++i) {
      const LAS float* cv = (const LAS float*)(smem + T_CV) + 224;
      const f32x4 c1 = *(const LAS f32x4*)(cv + 16 * rot(i) + 4 * q), c2 = *(const LAS f32x4*)(cv + 112 + 16 * rot(i) + 4 * q);
      const f32x2 a = gelu2(rrstd * ((f32x2){zs[i][0], zs[i][1]} - rmean * (f32x2){c1[0], c1[1]}) + (f32x2){c2[0], c2[1]});
      const f32x2 c = gelu2(rrstd * ((f32x2){zs[i][2], zs[i][3]} - rmean * (f32x2){c1[2], c1[3]}) + (f32x2){c2[2], c2[3]});
      z1p[i] = (u32x2){pack2(a[0], a[1]), pack2(c[0], c[1])};
      asm volatile("" : "+v"(z1p[i].x), "+v"(z1p[i].y));
    }
    STAMP(16); CSTAMP(42);
  }
  {
    LANE_IDS
    f32x4 acc[7][2];
#pragma unroll
    for (int i = 0; i < 7; ++i) { acc[i][0] = (f32x4){0, 0, 0, 0}; acc[i][1] = (f32x4){0, 0, 0, 0}; }
    const unsigned wa = (unsigned)((2 * mh) * 2048 + tk * 128 + (((q + 4 * kh) ^ ((lane >> 1) & 7)) * 16));
    const unsigned xlo = 8192 + kh * 7168 + lane * 16 + mh * 4096, xhi = 8192 + kh * 7168 + lane * 16 - mh * 3072;
    ksplit_loop<D_NS, 32, 7, 2>(
        [&](bf16x8(&A)[7], bf16x8(&Bf)[2], int slot, int) { LAS char* sb = smem + slot * D_SLOT;
          Bf[0] = *(LAS bf16x8*)(sb + wa); A[0] = *(LAS bf16x8*)(sb + xlo); Bf[1] = *(LAS bf16x8*)(sb + wa + 2048);
          A[1] = *(LAS bf16x8*)(sb + xlo + 1024); A[2] = *(LAS bf16x8*)(sb + xlo + 2048);
#pragma unroll
          for (int i = 3; i < 7; ++i) A[i] = *(LAS bf16x8*)(sb + xhi + i * 1024); },
        [&](bf16x8(&A)[7], bf16x8(&Bf)[2], bool) {
#pragma unroll
          for (int i = 0; i < 7; ++i) { acc[i][0] = mfma(A[i], Bf[0], acc[i][0]); acc[i][1] = mfma(A[i], Bf[1], acc[i][1]); } });
    STAMP(17); CSTAMP(43);
    f32x4 g[7];
    kreduce<7>(acc, g, smem + D_SCR, w, lane);
    u32x2 pk[7];
#pragma unroll
    for (int i = 0; i < 7; ++i) {
      pk[i].x = pack2(bflo(z1p[i].x) * (g[i][0] + bias), bfhi(z1p[i].x) * (g[i][1] + bias));
      pk[i].y = pack2(bflo(z1p[i].y) * (g[i][2] + bias), bfhi(z1p[i].y) * (g[i][3] + bias));
    }
    const unsigned img = WS_P + (unsigned)p * 28 * 4096 + w * 1024;
#pragma unroll
    for (int i = 0; i < 8; i += 2) {
      const int i2 = i + 1 < 7 ? i + 1 : i;
      const u32x4 ch = swap_pair(pk[i], pk[i2]);
      const int ti = (q & 1) ? rot(i2) : rot(i), ft = ft0 + ti, kb = ft >> 1, qq = 2 * (ft & 1) + (q >> 1);
      st16(rws, ti < nft ? img + kb * 4096 + (qq * 16 + tk) * 16 : 0xfffff000u, ch);
    }
    wait_vm<0>(); bar();
    STAMP(18);
    if (w == 0) { if (lane == 0) set_flag(ws, WS_FP + p * 128 + j * 4, tag); wait_flags8(ws, WS_FP + p * 128, tag, lane); }
    bar();
    STAMP(19);
  }
  {
    LANE_IDS
    f32x4 acc[5][2];
#pragma unroll
    for (int i = 0; i < 5; ++i) { acc[i][0] = (f32x4){0, 0, 0, 0}; acc[i][1] = (f32x4){0, 0, 0, 0}; }
    const unsigned wa = kh * 5120 + lane * 16, xa = 10240 + (kh * 4 + 2 * mh) * 1024 + lane * 16;
    ksplit_loop<E_NS, 14, 5, 2>(
        [&](bf16x8(&A)[5], bf16x8(&Bf)[2], int slot, int) { LAS char* sb = smem + slot * E_SLOT;
          A[0] = *(LAS bf16x8*)(sb + wa); Bf[0] = *(LAS bf16x8*)(sb + xa); Bf[1] = *(LAS bf16x8*)(sb + xa + 1024);
#pragma unroll
          for (int i = 1; i < 5; ++i) A[i] = *(LAS bf16x8*)(sb + wa + i * 1024); },
        [&](bf16x8(&A)[5], bf16x8(&Bf)[2], bool last) {
          if (!(last && kh)) {
#pragma unroll
            for (int i = 0; i < 5; ++i) { acc[i][0] = mfma(A[i], Bf[0], acc[i][0]); acc[i][1] = mfma(A[i], Bf[1], acc[i][1]); } } });
    STAMP(20);
    f32x4 o[5];
    kreduce<5>(acc, o, smem + E_SCR, w, lane);
    u32x2 pk[5];
#pragma unroll
    for (int i = 0; i < 5; ++i) {
      const f32x4 gb = *(const LAS f32x4*)(smem + T_MOD + 960 + (80 + 16 * i + 4 * q) * 4);
      pk[i].x = pack2(bflo(x1p[i].x) + gb[0] * o[i][0], bfhi(x1p[i].x) + gb[1] * o[i][1]);
      pk[i].y = pack2(bflo(x1p[i].y) + gb[2] * o[i][2], bfhi(x1p[i].y) + gb[3] * o[i][3]);
    }
    bf16_t* orow = P.out + (size_t)token * D + 80 * j;
#pragma unroll
    for (int i = 0; i < 6; i += 2) {
      const int i2 = i + 1 < 5 ? i + 1 : i;
      const u32x4 ch = swap_pair(pk[i], pk[i2]);
      *(u32x4*)(orow + 16 * ((q & 1) ? i2 : i) + 8 * (q >> 1)) = ch;
    }
    while (__builtin_amdgcn_s_memrealtime() - tstart_ < 5000ull) __builtin_amdgcn_s_sleep(1);
    STAMP(21);
  }
  STAMP_FLUSH();
}

__global__ __launch_bounds__(NT, 2) void fused_block(Params P) {
  extern __shared__ __attribute__((aligned(16))) char smem_[];
  LAS char* smem = (LAS char*)smem_;
  const int tid = threadIdx.x, lane = tid & 63, wid = __builtin_amdgcn_readfirstlane(tid >> 6), b = blockIdx.x;
  const int xcd = b & 7, idx = b >> 3, p = xcd * 4 + (idx >> 3), j = idx & 7;
  if (wid >= 4) loader_main(P, (unsigned)(size_t)smem, smem, wid - 4, lane, b, p, j, JROT ? ((j + xcd) & 7) * 4 : xcd * 4);
  else consumer_main(P, smem, wid, lane, b, p, j);
}

extern "C" void kernel_launch(void* const* d_in, const int* in_sizes, int n_in, void* d_out, int out_size, void* d_ws, size_t ws_size, hipStream_t stream) {
  Params P;
  P.x = (const bf16_t*)d_in[0]; P.wsr = (const bf16_t*)d_in[1]; P.wsi = (const bf16_t*)d_in[2];
  P.wdr = (const bf16_t*)d_in[3]; P.wdi = (const bf16_t*)d_in[4]; P.win = (const bf16_t*)d_in[6];
  P.wsp = (const bf16_t*)d_in[7]; P.bsp = (const bf16_t*)d_in[8]; P.wout = (const bf16_t*)d_in[9];
  P.c = (const bf16_t*)d_in[10]; P.wmod = (const bf16_t*)d_in[11];
  P.out = (bf16_t*)d_out; P.ws = (char*)d_ws;
  (void)hipFuncSetAttribute((const void*)fused_block, hipFuncAttributeMaxDynamicSharedMemorySize, LDS_BYTES);
  fused_block<<<256, NT, LDS_BYTES, stream>>>(P);
}
```

```cpp
#include <hip/hip_runtime.h>
#include <stdint.h>
#include <type_traits>

typedef unsigned short bf16_t;
typedef short bf16x8 __attribute__((ext_vector_type(8)));
typedef short s16x4 __attribute__((ext_vector_type(4)));
typedef float f32x4 __attribute__((ext_vector_type(4)));
typedef float f32x2 __attribute__((ext_vector_type(2)));
typedef unsigned u32x4 __attribute__((ext_vector_type(4)));
typedef unsigned u32x2 __attribute__((ext_vector_type(2)));
typedef __bf16 bf2_t __attribute__((ext_vector_type(2)));
#define LAS __attribute__((address_space(3)))
#define GAS __attribute__((address_space(1)))
typedef GAS unsigned gu32;
typedef GAS unsigned long long gu64;
#define RLX_AGENT __ATOMIC_RELAXED, __HIP_MEMORY_SCOPE_AGENT
#define DEV __device__ __forceinline__

constexpr int S = 2048, D = 640, F = 1728, FH = 864;
constexpr int NT = 512;

constexpr unsigned WS_EPOCH = 0;
constexpr unsigned WS_TMO   = 256;
constexpr unsigned WS_FN = 4096, WS_F2 = 8192, WS_FU = 12288, WS_FX = 16384, WS_FP = 20480, WS_FZ = 24576;
constexpr unsigned WS_MOD  = 65536;
constexpr unsigned WS_MODB = WS_MOD + 16384;

constexpr unsigned WS_XN   = 131072;
constexpr unsigned WS_WIN  = WS_XN + 64 * 40 * 1024;
constexpr unsigned WS_WOUT = WS_WIN + 20 * 109 * 1024;
constexpr unsigned WS_UV   = WS_WOUT + 27 * 40 * 1024;
constexpr unsigned WS_X1P  = WS_UV + 32 * 2 * 20 * 4096;
constexpr unsigned WS_ST1  = WS_X1P + 32 * 20 * 4096;
constexpr unsigned WS_Z2N  = WS_ST1 + 2048 * 64;
constexpr unsigned WS_ST2  = WS_Z2N + 64 * 55 * 1024;
constexpr unsigned WS_P    = WS_ST2 + 2048 * 128;
constexpr unsigned WS_END  = WS_P + 32 * 28 * 4096;

struct Params {
  const bf16_t *x, *wsr, *wsi, *wdr, *wdi, *win, *wsp, *bsp, *wout, *c, *wmod;
  bf16_t* out;
  char* ws;
};

template <int N> DEV void wait_vm() { asm volatile("s_waitcnt vmcnt(%0)" ::"n"(N) : "memory"); }
DEV void wait_lgkm0() { asm volatile("s_waitcnt lgkmcnt(0)" ::: "memory"); }
DEV void bar() { asm volatile("" ::: "memory"); __builtin_amdgcn_s_barrier(); asm volatile("" ::: "memory"); }
DEV void sched_fence() { __builtin_amdgcn_sched_barrier(0); }
template <int I, int N, class Fn> DEV void static_for(Fn&& f) { if constexpr (I < N) { f(std::integral_constant<int, I>{}); static_for<I + 1, N>(f); } }

DEV float bflo(unsigned v) { return __uint_as_float(v << 16); }
DEV float bfhi(unsigned v) { return __uint_as_float(v & 0xffff0000u); }
DEV unsigned pack2(float lo, float hi) { bf2_t v; v[0] = (__bf16)lo; v[1] = (__bf16)hi; return __builtin_bit_cast(unsigned, v); }
DEV float rbf(float f) { return bflo(pack2(f, f)); }
DEV f32x4 mfma(bf16x8 a, bf16x8 b, f32x4 c) { return __builtin_amdgcn_mfma_f32_16x16x32_bf16(a, b, c, 0, 0, 0); }

DEV u32x4 mkrs(const void* p, unsigned bytes) {
  const unsigned long long a = (unsigned long long)p;
  u32x4 r; r.x = (unsigned)a; r.y = (unsigned)(a >> 32) & 0xffffu; r.z = bytes; r.w = 0x00020000u; return r;
}
DEV __amdgpu_buffer_rsrc_t mkrsrc(const void* p, unsigned bytes) { return __builtin_amdgcn_make_buffer_rsrc((void*)p, 0, bytes, 0x00020000); }
#define LD_P(v, r, off, sc) "buffer_load_dwordx4 " v ", " r ", %0 offen offset:" off sc " lds\n\t"
#define LD_H(v, r, off, sc) "s_mov_b32 exec_hi, 0\n\tbuffer_load_dwordx4 " v ", " r ", %0 offen offset:" off sc " lds\n\ts_mov_b32 exec_hi, -1"
#define HD_OR  "s_add_u32 m0, %1, %2\n\ts_or_b32 %0, %3, %4\n\t"
#define HD_MAD(M) "s_mul_i32 %0, %3, " M "\n\ts_add_u32 m0, %1, %2\n\ts_add_u32 %0, %0, %4\n\t"
#define DMA_IN(IMM) "s"(lds), "n"(IMM), "s"(a), "s"(b)
template <int IMM> DEV void dma_w4_or(unsigned lds, unsigned a, unsigned b, u32x4 ra, u32x4 rb, unsigned v0, unsigned v1, unsigned v2, unsigned v3) { unsigned t;
  asm volatile(HD_OR LD_P("%7", "%5", "0", "") LD_P("%8", "%5", "1024", "") LD_P("%9", "%6", "2048", "") LD_P("%10", "%6", "3072", "")
               : "=&s"(t) : DMA_IN(IMM), "s"(ra), "s"(rb), "v"(v0), "v"(v1), "v"(v2), "v"(v3) : "memory", "scc"); }
template <int IMM> DEV void dma_w2_or(unsigned lds, unsigned a, unsigned b, u32x4 r, unsigned v0, unsigned v1) { unsigned t;
  asm volatile(HD_OR LD_P("%6", "%5", "0", "") LD_P("%7", "%5", "1024", "") : "=&s"(t) : DMA_IN(IMM), "s"(r), "v"(v0), "v"(v1) : "memory", "scc"); }
template <int IMM, int SOFF> DEV void dma_w2h(unsigned lds, unsigned a, u32x4 r, unsigned v0, unsigned v1, unsigned v2) { unsigned t;
  asm volatile("s_add_u32 m0, %1, %2\n\ts_add_u32 %0, %3, %4\n\t" LD_P("%6", "%5", "0", "") LD_P("%7", "%5", "1024", "") LD_H("%8", "%5", "2048", "")
               : "=&s"(t) : "s"(lds), "n"(IMM), "s"(a), "n"(SOFF), "s"(r), "v"(v0), "v"(v1), "v"(v2) : "memory", "scc"); }
template <int IMM, int SOFF> DEV void dma_x2(unsigned lds, unsigned a, u32x4 r, unsigned v) { unsigned t;
  asm volatile("s_add_u32 m0, %1, %2\n\ts_add_u32 %0, %3, %4\n\t" LD_P("%6", "%5", "0", " sc1") LD_P("%6", "%5", "1024", " sc1") : "=&s"(t) : "s"(lds), "n"(IMM), "s"(a), "n"(SOFF), "s"(r), "v"(v) : "memory", "scc"); }
template <int IMM, int SOFF> DEV void dma_x2h(unsigned lds, unsigned a, u32x4 r, unsigned v) { unsigned t;
  asm volatile("s_add_u32 m0, %1, %2\n\ts_add_u32 %0, %3, %4\n\t" LD_P("%6", "%5", "0", " sc1") LD_P("%6", "%5", "1024", " sc1") LD_H("%6", "%5", "2048", " sc1") : "=&s"(t) : "s"(lds), "n"(IMM), "s"(a), "n"(SOFF), "s"(r), "v"(v) : "memory", "scc"); }
template <int IMM, int SOFF> DEV void dma_x3h(unsigned lds, unsigned a, u32x4 r, unsigned v) { unsigned t;
  asm volatile("s_add_u32 m0, %1, %2\n\ts_add_u32 %0, %3, %4\n\t" LD_P("%6", "%5", "0", " sc1") LD_P("%6", "%5", "1024", " sc1") LD_P("%6", "%5", "2048", " sc1") LD_H("%6", "%5", "3072", " sc1") : "=&s"(t) : "s"(lds), "n"(IMM), "s"(a), "n"(SOFF), "s"(r), "v"(v) : "memory", "scc"); }
template <int IMM> DEV void dma_x2h_mad640(unsigned lds, unsigned a, unsigned b, u32x4 r, unsigned v) { unsigned t;
  asm volatile(HD_MAD("640") LD_P("%6", "%5", "0", " sc1") LD_P("%6", "%5", "1024", " sc1") LD_H("%6", "%5", "2048", " sc1") : "=&s"(t) : DMA_IN(IMM), "s"(r), "v"(v) : "memory", "scc"); }
template <int IMM> DEV void dma_x3h_mad880(unsigned lds, unsigned a, unsigned b, u32x4 r, unsigned v) { unsigned t;
  asm volatile(HD_MAD("880") LD_P("%6", "%5", "0", " sc1") LD_P("%6", "%5", "1024", " sc1") LD_P("%6", "%5", "2048", " sc1") LD_H("%6", "%5", "3072", " sc1") : "=&s"(t) : DMA_IN(IMM), "s"(r), "v"(v) : "memory", "scc"); }
DEV void dma_1(unsigned lds, u32x4 r, unsigned v) { asm volatile("s_mov_b32 m0, %0\n\ts_nop 0\n\tbuffer_load_dwordx4 %2, %1, 0 offen sc1 lds" ::"s"(lds), "s"(r), "v"(v) : "memory"); }
template <int N> DEV void wait_bar() { asm volatile("s_waitcnt vmcnt(%0)\n\ts_barrier" ::"n"(N) : "memory"); }
DEV void warm(unsigned lds, u32x4 r, unsigned v) { asm volatile("s_mov_b32 m0, %0\n\ts_nop 0\n\tbuffer_load_dword %2, %1, 0 offen lds" ::"s"(lds), "s"(r), "v"(v) : "memory"); }
constexpr unsigned WBIAS = 4096;
DEV u32x4 mkrs_b(const void* p, unsigned bytes) { return mkrs((const char*)p - WBIAS, bytes + WBIAS); }
constexpr int SC1 = 16;
DEV void st16(__amdgpu_buffer_rsrc_t r, unsigned off, u32x4 v) { __builtin_amdgcn_raw_buffer_store_b128(v, r, off, 0, 17); }
DEV u32x4 ld16(__amdgpu_buffer_rsrc_t r, unsigned off) { return __builtin_amdgcn_raw_buffer_load_b128(r, off, 0, SC1); }
DEV f32x4 ld16f(__amdgpu_buffer_rsrc_t r, unsigned off) { return __builtin_bit_cast(f32x4, ld16(r, off)); }

template <int CTRL> DEV float dpp_add(float v) { return v + __builtin_bit_cast(float, __builtin_amdgcn_update_dpp(0, __builtin_bit_cast(int, v), CTRL, 0xf, 0xf, true)); }
DEV float xsum_16(float v) { v = dpp_add<0xB1>(v); v = dpp_add<0x4E>(v); v = dpp_add<0x141>(v); return dpp_add<0x140>(v); }
DEV float xsum_r16(float v) { auto r = __builtin_amdgcn_permlane16_swap(__float_as_uint(v), __float_as_uint(v), false, false); return __uint_as_float(r[0]) + __uint_as_float(r[1]); }
DEV float xsum_r32(float v) { auto r = __builtin_amdgcn_permlane32_swap(__float_as_uint(v), __float_as_uint(v), false, false); return __uint_as_float(r[0]) + __uint_as_float(r[1]); }
DEV float xsum_q(float v) { return xsum_r32(xsum_r16(v)); }
DEV f32x2 gelu2(f32x2 x) {
  const float k1 = -2.0f * 0.7978845608028654f * 1.4426950408889634f;
  const f32x2 t = x * ((x * x) * (0.044715f * k1) + k1);
  const f32x2 d = (f32x2){__builtin_amdgcn_exp2f(t[0]), __builtin_amdgcn_exp2f(t[1])} + 1.0f;
  return x * (f32x2){__builtin_amdgcn_rcpf(d[0]), __builtin_amdgcn_rcpf(d[1])};
}
DEV u32x4 swap_pair(u32x2 a, u32x2 b) {
  auto r0 = __builtin_amdgcn_permlane16_swap(a.x, b.x, false, false);
  auto r1 = __builtin_amdgcn_permlane16_swap(a.y, b.y, false, false);
  return (u32x4){r0[0], r1[0], r0[1], r1[1]};
}

DEV bool spin_fail(unsigned& spins, char* ws) {
  __builtin_amdgcn_s_sleep(1);
  if (++spins > (1u << 17)) { __hip_atomic_store((gu32*)(ws + WS_TMO), 1u, RLX_AGENT); return true; }
  return false;
}
DEV void set_flag(char* ws, unsigned off, unsigned tag) { __hip_atomic_store((gu32*)(ws + off), tag, RLX_AGENT); }
DEV void wait_flags8(char* ws, unsigned off, unsigned tag, int lane) {
  for (unsigned spins = 0;;) {
    const unsigned v = lane < 8 ? __hip_atomic_load((gu32*)(ws + off) + lane, RLX_AGENT) : tag;
    if (__all(v == tag)) break;
    if (spin_fail(spins, ws)) break;
  }
}
DEV bool flags_ok(u32x4 v, unsigned tag) { return __all(v.x == tag && v.y == tag && v.z == tag && v.w == tag); }
DEV u32x4 peek256(__amdgpu_buffer_rsrc_t rf, unsigned off, int lane) { return ld16(rf, off + (lane >> 1) * 128 + (lane & 1) * 16); }
DEV void wait256(__amdgpu_buffer_rsrc_t rf, char* ws, unsigned off, unsigned tag, int lane) {
  for (unsigned spins = 0;;) { if (flags_ok(peek256(rf, off, lane), tag)) break; if (spin_fail(spins, ws)) break; }
}

template <int NS, int PF, int S_, int CW, int CX, class IW, class IX>
DEV void loader_run(IW&& issueW, IX&& issueX) {
  constexpr auto span = [=](int a, int b) { int c = 0; for (int s = a; s <= b; ++s) c += (s >= PF ? CW : 0) + CX; return c; };
  auto step = [&](auto ic) { if constexpr (decltype(ic)::value >= PF) issueW(ic); issueX(ic); };
  static_for<0, NS>(step);
  wait_bar<span(1, NS - 1)>();
  static_for<0, S_ - 1>([&](auto ic) { constexpr int n = decltype(ic)::value;
    constexpr int last = n + NS - 1 < S_ - 1 ? n + NS - 1 : S_ - 1;
    wait_bar<span(n + 2, last)>();
    if constexpr (n + NS < S_) step(std::integral_constant<int, n + NS>{}); });
}

template <int NS, int S_, int NA, int NB, int NM, class Rd, class Mm, class Hook, class Post>
DEV void ksplit_steps(bf16x8 (&A0)[NA], bf16x8 (&B0)[NB], bf16x8 (&A1)[NA], bf16x8 (&B1)[NB], int& slot, int n0, int n1, Rd&& rd, Mm&& mm, Hook&& hook, Post&& post) {
  constexpr int NR = NA + NB;
#define KS_ILV() do { _Pragma("unroll") for (int z = 0; z < NR; ++z) { __builtin_amdgcn_sched_group_barrier(0x8, 1, 0); __builtin_amdgcn_sched_group_barrier(0x100, 1, 0); } __builtin_amdgcn_sched_group_barrier(0x8, NM - NR, 0); } while (0)
#pragma unroll 1
  for (int n = n0; n < n1; n += 2) {
    const int s1 = slot + 1 == NS ? 0 : slot + 1;
    hook(n); wait_lgkm0(); bar(); post(n); sched_fence();
    rd(A1, B1, s1, n + 1); mm(A0, B0, false); KS_ILV(); sched_fence();
    const int s2 = s1 + 1 == NS ? 0 : s1 + 1;
    hook(n + 1); wait_lgkm0(); bar(); post(n + 1); sched_fence();
    rd(A0, B0, s2, n + 2); mm(A1, B1, false); KS_ILV(); sched_fence();
    slot = s2;
  }
}
template <int NS, int S_, int NA, int NB, class Rd, class Mm>
DEV void ksplit_last(bf16x8 (&A0)[NA], bf16x8 (&B0)[NB], bf16x8 (&A1)[NA], bf16x8 (&B1)[NB], int& slot, Rd&& rd, Mm&& mm) {
  const int s1 = slot + 1 == NS ? 0 : slot + 1;
  wait_lgkm0(); bar(); sched_fence();
  rd(A1, B1, s1, S_ - 1); mm(A0, B0, false); mm(A1, B1, true);
  slot = s1;
}
template <int NS, int S_, int NA, int NB, class Rd, class Mm>
DEV void ksplit_loop(Rd&& rd, Mm&& mm) {
  static_assert((S_ & 1) == 0, "even step count");
  bf16x8 A0[NA], B0[NB], A1[NA], B1[NB];
  bar();
  rd(A0, B0, 0, 0);
  int slot = 0;
  ksplit_steps<NS, S_, NA, NB, NA * NB>(A0, B0, A1, B1, slot, 0, S_ - 2, rd, mm, [](int) {}, [](int) {});
  ksplit_last<NS, S_, NA, NB>(A0, B0, A1, B1, slot, rd, mm);
}
template <int N>
DEV void kreduce(f32x4 (&acc)[N][2], f32x4 (&res)[N], LAS char* scratch, int w, int lane) {
  const int kh = w & 1;
  LAS char* mine = scratch + w * (N * 1024) + lane * 16;
  LAS char* other = scratch + (w ^ 1) * (N * 1024) + lane * 16;
  if (kh) { asm volatile("");
#pragma unroll
    for (int i = 0; i < N; ++i) { *(LAS f32x4*)(mine + i * 1024) = acc[i][0]; res[i] = acc[i][1]; }
  } else { asm volatile("");
#pragma unroll
    for (int i = 0; i < N; ++i) { *(LAS f32x4*)(mine + i * 1024) = acc[i][1]; res[i] = acc[i][0]; }
  }
  wait_lgkm0(); bar();
#pragma unroll
  for (int i = 0; i < N; ++i) res[i] += *(LAS f32x4*)(other + i * 1024);
}

constexpr int A_PRE = 2;
constexpr int PFW = 6, PFC = 5, PRE1 = 2;
constexpr int LPRIO = 0, CPRIO = 2;
constexpr int A_NS = 5, A_SLOT = 26 * 1024, A_SCR = 52 * 1024;
constexpr int P0_T = 130 * 1024, P0_PT = 140 * 1024, P0_RED = 157 * 1024;
constexpr int B_NS = 6, B_SLOT = 18 * 1024, B_SCR = 36 * 1024;
constexpr int C_NS = 5, C_X = 0, C_W = 82 * 1024, C_WSLOT = 14 * 1024;
constexpr int C_SCR = C_W + 2 * C_WSLOT;
constexpr int D_NS = 5, D_SLOT = 22 * 1024, D_SCR = 110 * 1024;
constexpr int E_NS = 6, E_SLOT = 18 * 1024, E_SCR = 110 * 1024;
constexpr int T_MODB = 152 * 1024;
constexpr int T_CV = T_MODB + 3072 + 64;
constexpr int T_MOD = 158 * 1024;
constexpr int T_DUMMY = T_MOD + 1792;
constexpr int LDS_BYTES = 160 * 1024;
static_assert(T_CV + 1792 <= P0_RED && P0_RED + 256 <= T_MOD && C_W + C_NS * C_WSLOT <= T_MODB && A_NS * A_SLOT <= P0_RED && D_NS * D_SLOT <= T_CV && B_NS * B_SLOT <= T_MODB && E_NS * E_SLOT <= T_MODB, "LDS map");

#define IC(n) std::integral_constant<int, (n)>{}
DEV void loader_main(const Params& P, const unsigned lds0, LAS char* smem, const int lw, const int lane_, const int b, const int p, const int j) {
  char* const ws = P.ws;
  const int ft0 = 27 * (j >> 2) + 7 * (j & 3);
#define LLANE int lane = lane_; asm volatile("" : "+v"(lane)); const unsigned vx = lane * 16; (void)vx;
#define LV4K const unsigned v4k0 = (unsigned)(lane >> 3) * 4096u + (unsigned)(((lane & 7) ^ (lane >> 4)) * 16), v4k1 = (unsigned)(8 + (lane >> 3)) * 4096u + (unsigned)(((lane & 7) ^ (4 + (lane >> 4))) * 16);
  {
    LLANE LV4K
    const u32x4 rre = mkrs_b(P.wsr, S * S * 2), rim = mkrs_b(P.wsi, S * S * 2), rx = mkrs(ws + WS_XN, S * D * 2);
    const unsigned wrow = (unsigned)(64 * p + 16 * lw) * 4096u;
    const unsigned w0 = v4k0 + WBIAS, w1 = v4k1 + WBIAS - 1024, w2 = v4k0 + WBIAS - 2048, w3 = v4k1 + WBIAS - 3072;
    const unsigned lw_w = lds0 + lw * 4096, lw_x = lds0 + 16384 + lw * 2560;
    const unsigned xo = (unsigned)(((lw >> 1) * 40 + 5 * j) * 1024 + (lw & 1) * 2560);
    const unsigned rot = (unsigned)j * 512, xrot = (unsigned)(b & 7) * 512;
    auto kof = [&](auto ic) { constexpr int n = decltype(ic)::value;
      const unsigned k = n < 4 ? rot + n * 128 : (n - 4) * 128 + ((unsigned)((n - 4) * 128) >= rot ? 512u : 0u); return (k + xrot) & 0xf80u; };
    auto issueW = [&](auto ic) { dma_w4_or<(decltype(ic)::value % A_NS) * A_SLOT>(lw_w, kof(ic), wrow, rre, rim, w0, w1, w2, w3); };
    auto issueX = [&](auto ic) { dma_x2h_mad640<(decltype(ic)::value % A_NS) * A_SLOT>(lw_x, kof(ic), xo, rx, vx); };
    static_for<0, A_PRE>(issueW);
    bar();
    bar();
    const int idx = b >> 3, unit0 = (idx & 3) + 4 * ((b & 7) + 8 * (idx >> 2));
    const bool domod = unit0 < 240; const int unit = domod ? unit0 : 0;
    {
      u32x4 wm[5]; unsigned cs[5];
#pragma unroll
      for (int i = 0; i < 5; ++i) { const int row = lw * 160 + i * 32 + (lane >> 1);
        wm[i] = *(const u32x4*)(P.wmod + (size_t)row * (6 * D) + unit * 16 + (lane & 1) * 8); cs[i] = P.c[row]; }
      float a[8] = {0, 0, 0, 0, 0, 0, 0, 0};
#pragma unroll
      for (int i = 0; i < 5; ++i) { const float x = bflo(cs[i]); const float s = rbf(x * __builtin_amdgcn_rcpf(1.0f + __expf(-x)));
        a[0] += s * bflo(wm[i].x); a[1] += s * bfhi(wm[i].x); a[2] += s * bflo(wm[i].y); a[3] += s * bfhi(wm[i].y);
        a[4] += s * bflo(wm[i].z); a[5] += s * bfhi(wm[i].z); a[6] += s * bflo(wm[i].w); a[7] += s * bfhi(wm[i].w); }
#pragma unroll
      for (int e = 0; e < 8; ++e) { float v = dpp_add<0x4E>(a[e]);
        v = dpp_add<0x124>(v); v = dpp_add<0x128>(v);
        a[e] = xsum_q(v); }
      if (lane < 2) {
#pragma unroll
        for (int e = 0; e < 8; ++e) ((LAS float*)(smem + P0_RED))[lw * 16 + lane * 8 + e] = a[e];
      }
      wait_lgkm0();
    }
    bar();
    __builtin_amdgcn_s_setprio(LPRIO);
    auto istep = [&](auto ic) { if constexpr (decltype(ic)::value >= A_PRE) issueW(ic); issueX(ic); };
#define A_CNT(s_) (((s_) >= A_PRE ? 4 : 0) + 3)
    static_assert((A_NS == 5 || A_NS == 6) && A_PRE <= A_NS, "start-up schedule");
    istep(IC(0)); istep(IC(1));
    wait_bar<A_CNT(1)>();
    istep(IC(2)); istep(IC(3));
    if (lw == 0 && domod && lane < 16) {
      const LAS float* red = (const LAS float*)(smem + P0_RED);
      float s = rbf((red[lane] + red[16 + lane]) + (red[32 + lane] + red[48 + lane]));
      const int n = unit * 16 + lane, seg = n / D;
      if (seg == 1 || seg == 4) s = rbf(1.0f + s);
      __hip_atomic_store((gu32*)(ws + WS_MOD) + n, __float_as_uint(s), RLX_AGENT);
      const unsigned hi = __float_as_uint(s) >> 16, nb = (unsigned)__builtin_amdgcn_update_dpp(0, (int)hi, 0xB1, 0xf, 0xf, true);
      if ((seg == 3 || seg == 4) && !(lane & 1)) __hip_atomic_store((gu32*)(ws + WS_MODB) + (((seg == 4 ? 0 : D) + n - seg * D) >> 1), hi | (nb << 16), RLX_AGENT);
    }
    wait_bar<A_CNT(2) + A_CNT(3)>();
    static_for<4, A_NS + 1>(istep);
    wait_bar<A_CNT(3) + A_CNT(4) + A_CNT(5) + (A_NS == 6 ? A_CNT(6) : 0)>();
    const u32x4 rmod = mkrs(ws + WS_MOD, 3840 * 4);
    const int tl = lane / 20, tc = lane - 20 * tl;
    const unsigned tv0 = lane < 60 ? (unsigned)((tl * D + 80 * j) * 4 + tc * 16) : 0x7ffffff0u;
    const unsigned tv1 = lane < 40 ? (unsigned)(((4 + tl) * D + 80 * j) * 4 + tc * 16) : 0x7ffffff0u;
    istep(IC(A_NS + 1));
    static_for<2, 32 - A_NS - 0>([&](auto ic) { constexpr int n = decltype(ic)::value;
      constexpr int cnt = [] { int c = 0; for (int s_ = n + 2; s_ <= n + A_NS - 1; ++s_) c += A_CNT(s_); return c; }();
      wait_bar<cnt>(); if constexpr (n + A_NS < 32) istep(IC(n + A_NS));
      if constexpr (n == 20) { if (lw == 0) { dma_1(lds0 + T_MOD, rmod, tv0); dma_1(lds0 + T_MOD + 960, rmod, tv1); } }
    });
    {
      { const int row = (j * 4 + lw) * 2 + (lane >> 5); warm(lds0 + T_DUMMY, mkrs(P.wsp, S * S * 2), (unsigned)(64 * p + row) * 4096u + (lane & 31) * 128); }
      warm(lds0 + T_DUMMY, mkrs((lw & 1) ? P.wdi : P.wdr, D * D * 2), lw < 2 && lane < 25 ? (unsigned)(b * 25 + lane) * 128 : 0x7ffffff0u);
    }
    static_for<0, A_NS - 1>([&](auto ic) { constexpr int r = A_NS - 2 - decltype(ic)::value; wait_bar<r * 7 + 2>(); });
    bar();
  }
  {
    LLANE
    const u32x4 rre = mkrs_b(P.wdr, D * D * 2), rim = mkrs_b(P.wdi, D * D * 2), rx = mkrs(ws + WS_UV, 32 * 2 * 20 * 4096);
    auto wv = [&](int k) { const int r = 20 * lw + 8 * k + (lane >> 3); return (unsigned)(r * 1280 + (((lane & 7) ^ ((r >> 1) & 7)) * 16)) + WBIAS - 1024 * k; };
    const unsigned v0 = wv(0), v1 = wv(1), v2 = wv(2);
    const unsigned wb = (unsigned)(80 * j) * 1280u, lw_w = lds0 + lw * 2560, lw_x = lds0 + 10240 + lw * 2048, xb = (unsigned)p * 40 * 4096u + lw * 2048;
    auto issueW = [&](auto ic) { constexpr int n = decltype(ic)::value; dma_w2h<(n % B_NS) * B_SLOT, (n % 10) * 128>(lw_w, wb, n >= 10 ? rim : rre, v0, v1, v2); };
    auto issueX = [&](auto ic) { constexpr int n = decltype(ic)::value; dma_x2<(n % B_NS) * B_SLOT, 2 * n * 4096>(lw_x, xb, rx, vx); };
    __builtin_amdgcn_s_setprio(0);
    static_for<0, (PRE1 < PFW ? PRE1 : PFW)>(issueW);
    bar();
    static_for<(PRE1 < PFW ? PRE1 : PFW), PFW>(issueW);
    bar();
    __builtin_amdgcn_s_setprio(LPRIO);
    loader_run<B_NS, PFW, 20, 3, 2>(issueW, issueX);
    bar();
  }
  {
    LLANE
    const u32x4 rw = mkrs(ws + WS_WIN, 20 * 109 * 1024), rx = mkrs(ws + WS_X1P, 32 * 20 * 4096), rmb = mkrs(ws + WS_MODB, 2560);
    const unsigned wo = (unsigned)(((lw >> 1) * 109 + ft0) * 1024 + (lw & 1) * 3584);
    const unsigned lw_w = lds0 + C_W + lw * 3584, lw_x = lds0 + C_X + lw * 2048, xb = (unsigned)p * 20 * 4096u + lw * 2048;
    auto issueW = [&](auto ic) { constexpr int n = decltype(ic)::value, z1 = n >= 10; dma_x3h<(n % C_NS) * C_WSLOT, (2 * (n - 10 * z1) * 109 + (z1 ? 0 : 54)) * 1024>(lw_w, wo, rw, vx); };
    auto issueX = [&](auto ic) { constexpr int n = decltype(ic)::value; dma_x2<2 * n * 4096, 2 * n * 4096>(lw_x, xb, rx, vx); };
    __builtin_amdgcn_s_setprio(0);
    static_for<0, (PRE1 < PFC ? PRE1 : PFC)>(issueW);
    bar();
    if (lw < 3) dma_1(lds0 + T_MODB + lw * 1024, rmb, vx + lw * 1024);
    static_for<(PRE1 < PFC ? PRE1 : PFC), PFC>(issueW);
    bar();
    __builtin_amdgcn_s_setprio(LPRIO);
    static_for<0, C_NS>([&](auto ic) { if constexpr (decltype(ic)::value >= PFC) issueW(ic); issueX(ic); });
    wait_bar<(PFC - 1) * 2 + (C_NS - PFC) * 6>();
    static_for<0, 9>([&](auto ic) {
      constexpr int n = decltype(ic)::value, need = n + 1;
      constexpr int last = n + C_NS - 1 < 11 ? n + C_NS - 1 : 11;
      constexpr int cnt = [] { int c = 0; for (int s = need + 1; s <= last; ++s) c += (s < PFC ? 2 : s < 10 ? 6 : 4); return c; }();
      wait_bar<cnt>();
      if constexpr (n + C_NS < 12) { issueW(IC(n + C_NS)); if constexpr (n + C_NS < 10) issueX(IC(n + C_NS)); }
    });
    bar();
    bar();
    issueW(IC(12)); issueW(IC(13)); issueW(IC(14));
    wait_bar<16>();
    static_for<10, 19>([&](auto ic) {
      constexpr int n = decltype(ic)::value;
      constexpr int last = n + 4 < 19 ? n + 4 : 19;
      wait_bar<(last - (n + 1)) * 4>();
      if constexpr (n + C_NS < 20) issueW(IC(n + C_NS));
    });
    bar();
  }
  {
    LLANE LV4K
    const u32x4 rw = mkrs_b(P.wsp, S * S * 2), rx = mkrs(ws + WS_Z2N, 64 * 55 * 1024);
    const unsigned wrow = (unsigned)(64 * p + 16 * lw) * 4096u, w0 = v4k0 + WBIAS, w1 = v4k1 + WBIAS - 1024;
    const unsigned xo = (unsigned)(((lw >> 1) * 55 + ft0) * 1024 + (lw & 1) * 3584), lw_w = lds0 + lw * 2048, lw_x = lds0 + 8192 + lw * 3584;
    unsigned kw = (b & 7) * 512, kx = kw;
    auto issueW = [&](auto ic) { dma_w2_or<(decltype(ic)::value % D_NS) * D_SLOT>(lw_w, kw, wrow, rw, w0, w1); kw = (kw + 128) & 0xf80; };
    auto issueX = [&](auto ic) { dma_x3h_mad880<(decltype(ic)::value % D_NS) * D_SLOT>(lw_x, kx, xo, rx, vx); kx = (kx + 128) & 0xf80; };
    static_assert(D_NS * D_SLOT <= C_SCR, "ring below the exchange buffer");
    static_for<0, D_NS>(issueW);
    bar();
    static_for<0, D_NS>(issueX);
    wait_bar<(D_NS - 1) * 4>();
    static_for<0, 31>([&](auto ic) { constexpr int n = decltype(ic)::value;
      constexpr int last = n + D_NS - 1 < 31 ? n + D_NS - 1 : 31;
      constexpr int cnt = n + 1 < D_NS ? (D_NS - 2 - n) * 4 + (last - (D_NS - 1)) * 6 : (last - (n + 1)) * 6;
      wait_bar<cnt>();
      if constexpr (n + D_NS < 32) { issueW(IC(n + D_NS)); issueX(IC(n + D_NS)); } });
    bar();
  }
  {
    LLANE
    const u32x4 rw = mkrs(ws + WS_WOUT, 28 * 40 * 1024), rx = mkrs(ws + WS_P, 32 * 28 * 4096);
    const unsigned wo = (unsigned)(((lw >> 1) * 40 + 5 * j) * 1024 + (lw & 1) * 2560), lw_w = lds0 + lw * 2560, lw_x = lds0 + 10240 + lw * 2048, xb = (unsigned)p * 28 * 4096u + lw * 2048;
    auto issueW = [&](auto ic) { constexpr int n = decltype(ic)::value; dma_x2h<(n % E_NS) * E_SLOT, n * 80 * 1024>(lw_w, wo, rw, vx); };
    auto issueX = [&](auto ic) { constexpr int n = decltype(ic)::value; dma_x2<(n % E_NS) * E_SLOT, 2 * n * 4096>(lw_x, xb, rx, vx); };
    __builtin_amdgcn_s_setprio(0);
    static_for<0, (PRE1 < PFW ? PRE1 : PFW)>(issueW);
    bar();
    static_for<(PRE1 < PFW ? PRE1 : PFW), PFW>(issueW);
    bar();
    __builtin_amdgcn_s_setprio(LPRIO);
    loader_run<E_NS, PFW, 14, 3, 2>(issueW, issueX);
    bar();
  }
}

DEV void consumer_main(const Params& P, LAS char* smem, const int w, const int lane_, const int b, const int p, const int j) {
  __builtin_amdgcn_s_setprio(CPRIO);
  char* const ws = P.ws;
  unsigned tagv = __hip_atomic_load((gu32*)(ws + WS_EPOCH), RLX_AGENT) + 1u, tag;
  const int mh = w >> 1, kh = w & 1;
  const auto rws = mkrsrc(ws, WS_END);
  const int ft0 = 27 * (j >> 2) + 7 * (j & 3), nft = (j & 3) == 3 ? 6 : 7;
  auto rot = [&](int i) { return i < 3 ? i + 4 * mh : i - 3 * mh; };
#define LANE_IDS int lane = lane_; asm volatile("" : "+v"(lane)); const int q = lane >> 4, tk = lane & 15, tid = w * 64 + lane, token = 64 * p + 16 * w + tk; (void)q; (void)tk; (void)tid; (void)token;
  {
    LANE_IDS
    unsigned v[2][5];
#pragma unroll
    for (int t = 0; t < 2; ++t) { const unsigned* xr = (const unsigned*)(P.x + (size_t)(64 * p + 8 * j + 2 * w + t) * D);
#pragma unroll
      for (int i = 0; i < 5; ++i) v[t][i] = xr[lane + 64 * i]; }
    const int u0 = w * 256 + b; const bool dopt = u0 < 810; const int u = dopt ? u0 : 0; const bool isin = u < 540;
    const int uu = isin ? u : u - 540, ncb = isin ? 27 : 10, pkb = uu / ncb, pcb = uu - pkb * ncb, ld = isin ? F : D;
    u32x4 pv[4];
    { const bf16_t* src = (isin ? P.win : P.wout) + (size_t)(pkb * 32) * ld + pcb * 64;
#pragma unroll
      for (int i = 0; i < 4; ++i) pv[i] = *(const u32x4*)(src + (size_t)(i * 8 + (lane >> 3)) * ld + (lane & 7) * 8); }
    LAS unsigned* t32 = (LAS unsigned*)(smem + P0_T);
    unsigned pk[2][5];
#pragma unroll
    for (int t = 0; t < 2; ++t) {
      float s = 0.f;
#pragma unroll
      for (int i = 0; i < 5; ++i) s += bflo(v[t][i]) + bfhi(v[t][i]);
      const float mean = xsum_q(xsum_16(s)) * (1.0f / D);
      float q2 = 0.f;
#pragma unroll
      for (int i = 0; i < 5; ++i) { const float a = bflo(v[t][i]) - mean, c = bfhi(v[t][i]) - mean; q2 += a * a + c * c; }
      const float rs = 1.0f / sqrtf(xsum_q(xsum_16(q2)) * (1.0f / D) + 1e-5f);
#pragma unroll
      for (int i = 0; i < 5; ++i) pk[t][i] = pack2((bflo(v[t][i]) - mean) * rs, (bfhi(v[t][i]) - mean) * rs);
    }
#pragma unroll
    for (int i = 0; i < 5; ++i) { const int d = 2 * (lane + 64 * i);
      t32[d * 4 + w] = (pk[0][i] & 0xffffu) | (pk[1][i] << 16); t32[(d + 1) * 4 + w] = (pk[0][i] >> 16) | (pk[1][i] & 0xffff0000u); }
    asm volatile("" : "+v"(tagv)); tag = __builtin_amdgcn_readfirstlane(tagv);
    wait_lgkm0(); bar();
    const unsigned xb = WS_XN + (unsigned)((2 * p + (j >> 2)) * 40 * 1024 + (j & 3) * 256);
#pragma unroll
    for (int i = 0; i < 3; ++i) { const int d = tid + 256 * i;
      if (d < D) st16(rws, xb + (d >> 4) * 1024 + (d & 15) * 16, *(LAS u32x4*)(smem + P0_T + d * 16)); }
    wait_vm<0>();
    bar();
    if (w == 0 && lane == 0) set_flag(ws, WS_FN + p * 128 + j * 4, tag);
    {
      LAS char* t = smem + P0_PT + w * 4096;
#pragma unroll
      for (int i = 0; i < 4; ++i) *(LAS u32x4*)(t + (i * 8 + (lane >> 3)) * 128 + (lane & 7) * 16) = pv[i];
      const unsigned dst = dopt ? (isin ? WS_WIN + (unsigned)(pkb * 109 + pcb * 4) * 1024 : WS_WOUT + (unsigned)(pkb * 40 + pcb * 4) * 1024) + lane * 16 : 0xfffff000u;
      const int rq = tk >> 2, pp = lane & 3;
#pragma unroll
      for (int c = 0; c < 4; ++c) {
        const u32x2 l2 = __builtin_bit_cast(u32x2, __builtin_amdgcn_ds_read_tr16_b64_v4i16((LAS s16x4*)(t + (8 * q + rq) * 128 + c * 32 + pp * 8)));
        const u32x2 h2 = __builtin_bit_cast(u32x2, __builtin_amdgcn_ds_read_tr16_b64_v4i16((LAS s16x4*)(t + (8 * q + 4 + rq) * 128 + c * 32 + pp * 8)));
        st16(rws, dst + (dopt ? c * 1024 : 0), (u32x4){l2.x, l2.y, h2.x, h2.y});
      }
    }
    if (w == 0) { wait256(rws, ws, WS_FN, tag, lane); }
    bar();
  }
  u32x2 xv[5];
  {
    LANE_IDS
    f32x4 ur[5], ui[5];
#pragma unroll
    for (int i = 0; i < 5; ++i) { ur[i] = (f32x4){0, 0, 0, 0}; ui[i] = (f32x4){0, 0, 0, 0}; }
    f32x4 rsum[2] = {(f32x4){0, 0, 0, 0}, (f32x4){0, 0, 0, 0}};
    bf16x8 ones = __builtin_bit_cast(bf16x8, (u32x4){0x3f803f80u, 0x3f803f80u, 0x3f803f80u, 0x3f803f80u});
    asm volatile("" : "+v"(ones));
    const unsigned wa0 = (unsigned)(w * 4096 + tk * 128 + ((q ^ ((lane >> 1) & 7)) * 16));
    const unsigned xa = 16384 + lane * 16;
    bf16x8 Wa[2], Xa[5], Wb[2], Xb[5];
    auto rdm = [&](bf16x8(&Wn)[2], bf16x8(&Xn)[5], int slot, int kk, bf16x8(&Wf)[2], bf16x8(&Xf)[5]) {
      LAS char* sb = smem + slot * A_SLOT; const unsigned wa = wa0 ^ (kk * 64);
      static_for<0, 12>([&](auto ic) { constexpr int z = decltype(ic)::value, i = z >> 1, t = z & 1;
        if constexpr (z < 10) { if constexpr (t == 0) ur[i] = mfma(Xf[i], Wf[0], ur[i]); else ui[i] = mfma(Xf[i], Wf[1], ui[i]); } else rsum[t] = mfma(ones, Wf[t], rsum[t]);
        if constexpr (z == 0) Wn[0] = *(LAS bf16x8*)(sb + wa);
        if constexpr (z == 1) Xn[0] = *(LAS bf16x8*)(sb + xa + (kk * 5) * 1024);
        if constexpr (z == 2) Wn[1] = *(LAS bf16x8*)(sb + wa + 2048);
        if constexpr (z >= 3 && z < 7) Xn[z - 2] = *(LAS bf16x8*)(sb + xa + (kk * 5 + z - 2) * 1024);
        sched_fence(); });
    };
    auto mmlast = [&](bf16x8(&Wf)[2], bf16x8(&Xf)[5]) {
#pragma unroll
      for (int i = 0; i < 5; ++i) { ur[i] = mfma(Xf[i], Wf[0], ur[i]); ui[i] = mfma(Xf[i], Wf[1], ui[i]); }
      rsum[0] = mfma(ones, Wf[0], rsum[0]); rsum[1] = mfma(ones, Wf[1], rsum[1]);
    };
    int slot = 0;
    auto steps = [&](int n0, int n1, bool hooks) {
#pragma unroll 1
      for (int n = n0; n < n1; ++n) {
        rdm(Wb, Xb, slot, 1, Wa, Xa);
        const int ns = slot + 1 == A_NS ? 0 : slot + 1;
        if (hooks && n == 8) wait_vm<0>();
        wait_lgkm0(); bar(); sched_fence();
        if (hooks && n == 8 && w == 0 && lane == 0) set_flag(ws, WS_F2 + p * 128 + j * 4, tag);
        rdm(Wa, Xa, ns, 0, Wb, Xb);
        slot = ns;
      }
    };
    bar();
    { LAS char* sb = smem; Wa[0] = *(LAS bf16x8*)(sb + wa0); Wa[1] = *(LAS bf16x8*)(sb + wa0 + 2048);
#pragma unroll
      for (int i = 0; i < 5; ++i) Xa[i] = *(LAS bf16x8*)(sb + xa + i * 1024); }
    steps(0, 12, true);
    const u32x4 f2 = peek256(rws, WS_F2, lane);
    steps(12, 18, false);
    if (!flags_ok(f2, tag)) wait256(rws, ws, WS_F2, tag, lane);
    if (b == 0 && w == 0 && lane == 0) set_flag(ws, WS_EPOCH, tag + 15);
#pragma unroll
    for (int i = 0; i < 5; ++i) xv[i] = *(const u32x2*)(P.x + (size_t)token * D + 80 * j + 16 * i + 4 * q);
    steps(18, 31, false);
    { LAS char* sb = smem + slot * A_SLOT; const unsigned wa = wa0 ^ 64;
      Wb[0] = *(LAS bf16x8*)(sb + wa); Wb[1] = *(LAS bf16x8*)(sb + wa + 2048);
#pragma unroll
      for (int i = 0; i < 5; ++i) Xb[i] = *(LAS bf16x8*)(sb + xa + (5 + i) * 1024); }
    mmlast(Wa, Xa);
    wait_lgkm0(); bar();
    mmlast(Wb, Xb);
    const float rs0 = rsum[0][0], rs1 = rsum[1][0];
#pragma unroll
    for (int i = 0; i < 5; ++i) {
      u32x2 a, c;
      const f32x4 sa = *(const LAS f32x4*)(smem + T_MOD + (16 * i + 4 * q) * 4), c1 = *(const LAS f32x4*)(smem + T_MOD + (80 + 16 * i + 4 * q) * 4);
      { const f32x4 u = ur[i] * c1 + rs0 * sa; a.x = pack2(u[0], u[1]); a.y = pack2(u[2], u[3]); }
      { const f32x4 v = -(ui[i] * c1 + rs1 * sa); c.x = pack2(v[0], v[1]); c.y = pack2(v[2], v[3]); }
      const int dt = 5 * j + i, kb = dt >> 1, qq = 2 * (dt & 1) + (q >> 1);
      st16(rws, WS_UV + (unsigned)(((p * 2 + (q & 1)) * 20 + kb) * 4 + w) * 1024 + (qq * 16 + tk) * 16, swap_pair(a, c));
    }
    wait_vm<0>(); bar();
    if (w == 0) { if (lane == 0) set_flag(ws, WS_FU + p * 128 + j * 4, tag); wait_flags8(ws, WS_FU + p * 128, tag, lane); }
    bar();
  }
  u32x2 x1p[5];
  {
    LANE_IDS
    f32x4 acc[5][2];
#pragma unroll
    for (int i = 0; i < 5; ++i) { acc[i][0] = (f32x4){0, 0, 0, 0}; acc[i][1] = (f32x4){0, 0, 0, 0}; }
    const unsigned wa = (unsigned)(tk * 128 + (((q + 4 * kh) ^ ((lane >> 1) & 7)) * 16));
    const unsigned xa = 10240 + (kh * 4 + 2 * mh) * 1024 + lane * 16;
    ksplit_loop<B_NS, 20, 5, 2>(
        [&](bf16x8(&A)[5], bf16x8(&Bf)[2], int slot, int) { LAS char* sb = smem + slot * B_SLOT;
          A[0] = *(LAS bf16x8*)(sb + wa); Bf[0] = *(LAS bf16x8*)(sb + xa); Bf[1] = *(LAS bf16x8*)(sb + xa + 1024);
#pragma unroll
          for (int i = 1; i < 5; ++i) A[i] = *(LAS bf16x8*)(sb + wa + i * 2048); },
        [&](bf16x8(&A)[5], bf16x8(&Bf)[2], bool) {
#pragma unroll
          for (int i = 0; i < 5; ++i) { acc[i][0] = mfma(A[i], Bf[0], acc[i][0]); acc[i][1] = mfma(A[i], Bf[1], acc[i][1]); } });
    f32x4 y[5];
    kreduce<5>(acc, y, smem + B_SCR, w, lane);
    float xs[20]; u32x2 xp[5];
    float s = 0.f;
#pragma unroll
    for (int i = 0; i < 5; ++i) {
      const f32x4 ga = *(const LAS f32x4*)(smem + T_MOD + (160 + 16 * i + 4 * q) * 4), c1b = *(const LAS f32x4*)(smem + T_MOD + 960 + (16 * i + 4 * q) * 4);
      x1p[i].x = pack2(bflo(xv[i].x) + ga[0] * y[i][0], bfhi(xv[i].x) + ga[1] * y[i][1]);
      x1p[i].y = pack2(bflo(xv[i].y) + ga[2] * y[i][2], bfhi(xv[i].y) + ga[3] * y[i][3]);
      asm volatile("" : "+v"(x1p[i].x), "+v"(x1p[i].y));
      xs[4 * i] = bflo(x1p[i].x); xs[4 * i + 1] = bfhi(x1p[i].x); xs[4 * i + 2] = bflo(x1p[i].y); xs[4 * i + 3] = bfhi(x1p[i].y);
      s += (xs[4 * i] + xs[4 * i + 1]) + (xs[4 * i + 2] + xs[4 * i + 3]);
      xp[i].x = pack2(xs[4 * i] * c1b[0], xs[4 * i + 1] * c1b[1]); xp[i].y = pack2(xs[4 * i + 2] * c1b[2], xs[4 * i + 3] * c1b[3]);
    }
    const unsigned img = WS_X1P + (unsigned)p * 20 * 4096 + w * 1024;
#pragma unroll
    for (int i = 0; i < 6; i += 2) {
      const int i2 = i + 1 < 5 ? i + 1 : i;
      const int dt = 5 * j + ((q & 1) ? i2 : i), kb = dt >> 1, qq = 2 * (dt & 1) + (q >> 1);
      st16(rws, img + kb * 4096 + (qq * 16 + tk) * 16, swap_pair(xp[i], xp[i2]));
    }
    const float mean = xsum_q(s) * (1.0f / 80.0f);
    float m2 = 0.f;
#pragma unroll
    for (int e = 0; e < 20; ++e) { const float d = xs[e] - mean; m2 += d * d; }
    m2 = xsum_q(m2);
    if (q == 0) __hip_atomic_store((gu64*)(ws + WS_ST1) + token * 8 + j, ((unsigned long long)__float_as_uint(m2) << 32) | __float_as_uint(mean), RLX_AGENT);
    wait_vm<0>(); bar();
    if (w == 0) { if (lane == 0) set_flag(ws, WS_FX + p * 128 + j * 4, tag); wait_flags8(ws, WS_FX + p * 128, tag, lane); }
    bar();
  }
  u32x2 z1p[7]; float bias;
  {
    LANE_IDS
    u32x4 st[4];
#pragma unroll
    for (int k = 0; k < 4; ++k) st[k] = ld16(rws, WS_ST1 + (unsigned)token * 64 + k * 16);
    bias = bflo(P.bsp[token]);
    for (int i = tid; i < 448; i += 256) ((LAS float*)(smem + T_CV))[i] = 0.f;
    f32x4 acc[7][2], aux[4];
    auto clear = [&]() {
#pragma unroll
      for (int i = 0; i < 7; ++i) { acc[i][0] = (f32x4){0, 0, 0, 0}; acc[i][1] = (f32x4){0, 0, 0, 0}; }
#pragma unroll
      for (int i = 0; i < 4; ++i) aux[i] = (f32x4){0, 0, 0, 0};
    };
    const unsigned xa = C_X + (kh * 4 + 2 * mh) * 1024 + lane * 16, wlo = C_W + kh * 7168 + lane * 16 + mh * 4096, whi = C_W + kh * 7168 + lane * 16 - mh * 3072;
    const int r4 = tk & 3;
    const unsigned ma = T_MODB + (r4 == 0 ? q * 16 + kh * 64 : r4 == 1 ? 1280 + q * 16 + kh * 64 : 2560), mstride = r4 < 2 ? 128 : 0;
    auto rd = [&](bf16x8(&A)[7], bf16x8(&Bf)[3], int slot, int n) {
      LAS char* sl = smem + wlo + slot * C_WSLOT; LAS char* sh = smem + whi + slot * C_WSLOT; LAS char* xb = smem + xa + n * 8192;
      Bf[0] = *(LAS bf16x8*)(xb); A[0] = *(LAS bf16x8*)(sl); Bf[1] = *(LAS bf16x8*)(xb + 1024);
      A[1] = *(LAS bf16x8*)(sl + 1024); A[2] = *(LAS bf16x8*)(sl + 2048);
#pragma unroll
      for (int i = 3; i < 7; ++i) A[i] = *(LAS bf16x8*)(sh + i * 1024);
      Bf[2] = *(LAS bf16x8*)(smem + ma + n * mstride);
    };
    auto auxmm = [&](bf16x8(&A)[7], bf16x8(&Bf)[3]) {
#pragma unroll
      for (int i = 0; i < 4; ++i) aux[i] = mfma(Bf[2], A[i], aux[i]);
    };
    auto mm2 = [&](bf16x8(&A)[7], bf16x8(&Bf)[3], bool) {
#pragma unroll
      for (int i = 0; i < 7; ++i) { acc[i][0] = mfma(Bf[0], A[i], acc[i][0]); acc[i][1] = mfma(Bf[1], A[i], acc[i][1]); }
      auxmm(A, Bf); };
    auto mm1 = [&](bf16x8(&A)[7], bf16x8(&Bf)[3], bool) {
#pragma unroll
      for (int i = 0; i < 7; ++i) { acc[i][0] = mfma(A[i], Bf[0], acc[i][0]); acc[i][1] = mfma(A[i], Bf[1], acc[i][1]); }
      auxmm(A, Bf); };
    auto put_cv = [&](int pass_) {
      LAS float* cv = (LAS float*)(smem + T_CV) + pass_ * 224;
      if (q == 0) {
#pragma unroll
        for (int i = 0; i < 4; ++i) if (mh == 0 || i < 3) { const int t = rot(i);
          __hip_atomic_fetch_add(cv + 16 * t + tk, aux[i][0], __ATOMIC_RELAXED, __HIP_MEMORY_SCOPE_WORKGROUP);
          __hip_atomic_fetch_add(cv + 112 + 16 * t + tk, aux[i][1], __ATOMIC_RELAXED, __HIP_MEMORY_SCOPE_WORKGROUP); }
      }
    };
    float rmean, rrstd;
    { float mu[8], mean = 0.f;
#pragma unroll
      for (int k = 0; k < 4; ++k) { mu[2 * k] = __uint_as_float(st[k].x); mu[2 * k + 1] = __uint_as_float(st[k].z); mean += mu[2 * k] + mu[2 * k + 1]; }
      mean *= 0.125f; float M2 = 0.f;
#pragma unroll
      for (int k = 0; k < 4; ++k) { const float d0 = mu[2 * k] - mean, d1 = mu[2 * k + 1] - mean; M2 += __uint_as_float(st[k].y) + __uint_as_float(st[k].w) + 80.0f * (d0 * d0 + d1 * d1); }
      rmean = mean; rrstd = 1.0f / sqrtf(M2 * (1.0f / D) + 1e-5f); }
    asm volatile("" : "+v"(rmean), "+v"(rrstd));
    bf16x8 A0[7], B0[3], A1[7], B1[3];
    int slot = 0;
    auto nohook = [](int) {};
    clear();
    wait_lgkm0(); bar();
    rd(A0, B0, 0, 0);
    ksplit_steps<C_NS, 10, 7, 3, 18>(A0, B0, A1, B1, slot, 0, 8, rd, mm2, nohook, nohook);
    ksplit_last<C_NS, 10, 7, 3>(A0, B0, A1, B1, slot, rd, mm2);
    put_cv(0);
    f32x4 zs[7];
    kreduce<7>(acc, zs, smem + C_SCR, w, lane);
    wait_lgkm0(); bar();
    f32x2 mr01, mr23, rr01, rr23;
    mr01 = (f32x2){__shfl(rmean, 4 * q), __shfl(rmean, 4 * q + 1)}; mr23 = (f32x2){__shfl(rmean, 4 * q + 2), __shfl(rmean, 4 * q + 3)};
    rr01 = (f32x2){__shfl(rrstd, 4 * q), __shfl(rrstd, 4 * q + 1)}; rr23 = (f32x2){__shfl(rrstd, 4 * q + 2), __shfl(rrstd, 4 * q + 3)};
    f32x2 z01[7], z23[7];
#pragma unroll
    for (int i = 0; i < 7; ++i) {
      const float c1 = ((const LAS float*)(smem + T_CV))[16 * rot(i) + tk], c2 = ((const LAS float*)(smem + T_CV))[112 + 16 * rot(i) + tk];
      z01[i] = gelu2(rr01 * ((f32x2){zs[i][0], zs[i][1]} - mr01 * c1) + c2);
      z23[i] = gelu2(rr23 * ((f32x2){zs[i][2], zs[i][3]} - mr23 * c1) + c2);
    }
    {
      const float inv = 1.0f / (16.0f * nft);
      const f32x2 zero2 = (f32x2){0.f, 0.f};
      f32x2 s01 = zero2, s23 = zero2;
#pragma unroll
      for (int i = 0; i < 7; ++i) { const bool ok = (i != 2 && i != 6) || rot(i) < nft; s01 += ok ? z01[i] : zero2; s23 += ok ? z23[i] : zero2; }
      const f32x2 m01 = (f32x2){xsum_16(s01[0]), xsum_16(s01[1])} * inv, m23 = (f32x2){xsum_16(s23[0]), xsum_16(s23[1])} * inv;
      f32x2 t01 = zero2, t23 = zero2;
#pragma unroll
      for (int i = 0; i < 7; ++i) { const bool ok = (i != 2 && i != 6) || rot(i) < nft; const f32x2 a = ok ? z01[i] - m01 : zero2, c = ok ? z23[i] - m23 : zero2; t01 += a * a; t23 += c * c; }
      const float q0 = xsum_16(t01[0]), q1 = xsum_16(t01[1]), q2 = xsum_16(t23[0]), q3 = xsum_16(t23[1]);
      if (tk < 8) { const int r = tk & 3; const float a = r == 0 ? m01[0] : r == 1 ? m01[1] : r == 2 ? m23[0] : m23[1], c = r == 0 ? q0 : r == 1 ? q1 : r == 2 ? q2 : q3;
        const int tok = 64 * p + 16 * w + 4 * q + r;
        __hip_atomic_store((gu64*)(ws + WS_ST2) + (tok * 8 + j) * 2 + (tk >> 2), ((unsigned long long)(tag ^ __float_as_uint(tk < 4 ? c : a)) << 32) | __float_as_uint(tk < 4 ? a : c), RLX_AGENT); }
    }
    clear();
    slot = 0;
    bar();
    rd(A0, B0, 0, 0);
    ksplit_steps<C_NS, 10, 7, 3, 18>(A0, B0, A1, B1, slot, 0, 4, rd, mm1, nohook, nohook);
    {
      u32x4 g0, g1;
      const unsigned go = WS_ST2 + (unsigned)(token * 8 + 2 * q) * 16;
      for (unsigned spins = 0;;) { g0 = ld16(rws, go); g1 = ld16(rws, go + 16);
        if (__all(g0.y == (tag ^ g0.z) && g0.w == (tag ^ g0.x) && g1.y == (tag ^ g1.z) && g1.w == (tag ^ g1.x))) break; if (spin_fail(spins, ws)) break; }
      const float n0 = 112.0f, n1 = (q & 1) ? 96.0f : 112.0f;
      const float m0 = __uint_as_float(g0.x), m1 = __uint_as_float(g1.x);
      const float mean = xsum_q(n0 * m0 + n1 * m1) * (1.0f / FH);
      const float d0 = m0 - mean, d1 = m1 - mean;
      const float M2 = xsum_q(__uint_as_float(g0.z) + __uint_as_float(g1.z) + n0 * d0 * d0 + n1 * d1 * d1);
      const float rstd = 1.0f / sqrtf(M2 * (1.0f / FH) + 1e-5f);
      mr01 = (f32x2){__shfl(mean, 4 * q), __shfl(mean, 4 * q + 1)}; mr23 = (f32x2){__shfl(mean, 4 * q + 2), __shfl(mean, 4 * q + 3)};
      rr01 = (f32x2){__shfl(rstd, 4 * q), __shfl(rstd, 4 * q + 1)}; rr23 = (f32x2){__shfl(rstd, 4 * q + 2), __shfl(rstd, 4 * q + 3)};
    }
    {
      u32x2 pk[7];
#pragma unroll
      for (int i = 0; i < 7; ++i) { const f32x2 a = (z01[i] - mr01) * rr01, c = (z23[i] - mr23) * rr23; pk[i].x = pack2(a[0], a[1]); pk[i].y = pack2(c[0], c[1]); }
      const unsigned img = WS_Z2N + (unsigned)(2 * p + (w >> 1)) * 55 * 1024 + ((2 * (w & 1) + (q >> 1)) * 16 + tk) * 16;
#pragma unroll
      for (int i = 0; i < 8; i += 2) {
        const int i2 = i + 1 < 7 ? i + 1 : i;
        const u32x4 ch = swap_pair(pk[i], pk[i2]);
        const int ti = (q & 1) ? rot(i2) : rot(i);
        st16(rws, ti < nft ? img + (ft0 + ti) * 1024 : 0xfffff000u, ch);
      }
    }
    ksplit_steps<C_NS, 10, 7, 3, 18>(A0, B0, A1, B1, slot, 4, 8, rd, mm1,
        [&](int n) { if (n == 5) wait_vm<0>(); },
        [&](int n) { if (n == 5 && w == 0 && lane == 0) set_flag(ws, WS_FZ + (j * 4 + (p >> 3)) * 128 + (p & 7) * 4, tag); });
    ksplit_last<C_NS, 10, 7, 3>(A0, B0, A1, B1, slot, rd, mm1);
    const unsigned fzo = WS_FZ + (j * 4 + ((lane & 7) >> 1)) * 128 + (lane & 1) * 16;
    const u32x4 fz = ld16(rws, fzo);
    put_cv(1);
    kreduce<7>(acc, zs, smem + C_SCR, w, lane);
    if (!flags_ok(fz, tag)) { for (unsigned spins = 0;;) { if (flags_ok(ld16(rws, fzo), tag)) break; if (spin_fail(spins, ws)) break; } }
    wait_lgkm0(); bar();
#pragma unroll
    for (int i = 0; i < 7; ++i) {
      const LAS float* cv = (const LAS float*)(smem + T_CV) + 224;
      const f32x4 c1 = *(const LAS f32x4*)(cv + 16 * rot(i) + 4 * q), c2 = *(const LAS f32x4*)(cv + 112 + 16 * rot(i) + 4 * q);
      const f32x2 a = gelu2(rrstd * ((f32x2){zs[i][0], zs[i][1]} - rmean * (f32x2){c1[0], c1[1]}) + (f32x2){c2[0], c2[1]});
      const f32x2 c = gelu2(rrstd * ((f32x2){zs[i][2], zs[i][3]} - rmean * (f32x2){c1[2], c1[3]}) + (f32x2){c2[2], c2[3]});
      z1p[i] = (u32x2){pack2(a[0], a[1]), pack2(c[0], c[1])};
      asm volatile("" : "+v"(z1p[i].x), "+v"(z1p[i].y));
    }
  }
  {
    LANE_IDS
    f32x4 acc[7][2];
#pragma unroll
    for (int i = 0; i < 7; ++i) { acc[i][0] = (f32x4){0, 0, 0, 0}; acc[i][1] = (f32x4){0, 0, 0, 0}; }
    const unsigned wa = (unsigned)((2 * mh) * 2048 + tk * 128 + (((q + 4 * kh) ^ ((lane >> 1) & 7)) * 16));
    const unsigned xlo = 8192 + kh * 7168 + lane * 16 + mh * 4096, xhi = 8192 + kh * 7168 + lane * 16 - mh * 3072;
    ksplit_loop<D_NS, 32, 7, 2>(
        [&](bf16x8(&A)[7], bf16x8(&Bf)[2], int slot, int) { LAS char* sb = smem + slot * D_SLOT;
          Bf[0] = *(LAS bf16x8*)(sb + wa); A[0] = *(LAS bf16x8*)(sb + xlo); Bf[1] = *(LAS bf16x8*)(sb + wa + 2048);
          A[1] = *(LAS bf16x8*)(sb + xlo + 1024); A[2] = *(LAS bf16x8*)(sb + xlo + 2048);
#pragma unroll
          for (int i = 3; i < 7; ++i) A[i] = *(LAS bf16x8*)(sb + xhi + i * 1024); },
        [&](bf16x8(&A)[7], bf16x8(&Bf)[2], bool) {
#pragma unroll
          for (int i = 0; i < 7; ++i) { acc[i][0] = mfma(A[i], Bf[0], acc[i][0]); acc[i][1] = mfma(A[i], Bf[1], acc[i][1]); } });
    f32x4 g[7];
    kreduce<7>(acc, g, smem + D_SCR, w, lane);
    u32x2 pk[7];
#pragma unroll
    for (int i = 0; i < 7; ++i) {
      pk[i].x = pack2(bflo(z1p[i].x) * (g[i][0] + bias), bfhi(z1p[i].x) * (g[i][1] + bias));
      pk[i].y = pack2(bflo(z1p[i].y) * (g[i][2] + bias), bfhi(z1p[i].y) * (g[i][3] + bias));
    }
    const unsigned img = WS_P + (unsigned)p * 28 * 4096 + w * 1024;
#pragma unroll
    for (int i = 0; i < 8; i += 2) {
      const int i2 = i + 1 < 7 ? i + 1 : i;
      const u32x4 ch = swap_pair(pk[i], pk[i2]);
      const int ti = (q & 1) ? rot(i2) : rot(i), ft = ft0 + ti, kb = ft >> 1, qq = 2 * (ft & 1) + (q >> 1);
      st16(rws, ti < nft ? img + kb * 4096 + (qq * 16 + tk) * 16 : 0xfffff000u, ch);
    }
    wait_vm<0>(); bar();
    if (w == 0) { if (lane == 0) set_flag(ws, WS_FP + p * 128 + j * 4, tag); wait_flags8(ws, WS_FP + p * 128, tag, lane); }
    bar();
  }
  {
    LANE_IDS
    f32x4 acc[5][2];
#pragma unroll
    for (int i = 0; i < 5; ++i) { acc[i][0] = (f32x4){0, 0, 0, 0}; acc[i][1] = (f32x4){0, 0, 0, 0}; }
    const unsigned wa = kh * 5120 + lane * 16, xa = 10240 + (kh * 4 + 2 * mh) * 1024 + lane * 16;
    ksplit_loop<E_NS, 14, 5, 2>(
        [&](bf16x8(&A)[5], bf16x8(&Bf)[2], int slot, int) { LAS char* sb = smem + slot * E_SLOT;
          A[0] = *(LAS bf16x8*)(sb + wa); Bf[0] = *(LAS bf16x8*)(sb + xa); Bf[1] = *(LAS bf16x8*)(sb + xa + 1024);
#pragma unroll
          for (int i = 1; i < 5; ++i) A[i] = *(LAS bf16x8*)(sb + wa + i * 1024); },
        [&](bf16x8(&A)[5], bf16x8(&Bf)[2], bool last) {
          if (!(last && kh)) {
#pragma unroll
            for (int i = 0; i < 5; ++i) { acc[i][0] = mfma(A[i], Bf[0], acc[i][0]); acc[i][1] = mfma(A[i], Bf[1], acc[i][1]); } } });
    f32x4 o[5];
    kreduce<5>(acc, o, smem + E_SCR, w, lane);
    u32x2 pk[5];
#pragma unroll
    for (int i = 0; i < 5; ++i) {
      const f32x4 gb = *(const LAS f32x4*)(smem + T_MOD + 960 + (80 + 16 * i + 4 * q) * 4);
      pk[i].x = pack2(bflo(x1p[i].x) + gb[0] * o[i][0], bfhi(x1p[i].x) + gb[1] * o[i][1]);
      pk[i].y = pack2(bflo(x1p[i].y) + gb[2] * o[i][2], bfhi(x1p[i].y) + gb[3] * o[i][3]);
    }
    bf16_t* orow = P.out + (size_t)token * D + 80 * j;
#pragma unroll
    for (int i = 0; i < 6; i += 2) {
      const int i2 = i + 1 < 5 ? i + 1 : i;
      const u32x4 ch = swap_pair(pk[i], pk[i2]);
      *(u32x4*)(orow + 16 * ((q & 1) ? i2 : i) + 8 * (q >> 1)) = ch;
    }
  }
}

__global__ __launch_bounds__(NT, 2) void fused_block(Params P) {
  extern __shared__ __attribute__((aligned(16))) char smem_[];
  LAS char* smem = (LAS char*)smem_;
  const int tid = threadIdx.x, lane = tid & 63, wid = __builtin_amdgcn_readfirstlane(tid >> 6), b = blockIdx.x;
  const int xcd = b & 7, idx = b >> 3, p = xcd * 4 + (idx >> 3), j = idx & 7;
  if (wid >= 4) loader_main(P, (unsigned)(size_t)smem, smem, wid - 4, lane, b, p, j);
  else consumer_main(P, smem, wid, lane, b, p, j);
}

extern "C" void kernel_launch(void* const* d_in, const int* in_sizes, int n_in, void* d_out, int out_size, void* d_ws, size_t ws_size, hipStream_t stream) {
  Params P;
  P.x = (const bf16_t*)d_in[0]; P.wsr = (const bf16_t*)d_in[1]; P.wsi = (const bf16_t*)d_in[2];
  P.wdr = (const bf16_t*)d_in[3]; P.wdi = (const bf16_t*)d_in[4]; P.win = (const bf16_t*)d_in[6];
  P.wsp = (const bf16_t*)d_in[7]; P.bsp = (const bf16_t*)d_in[8]; P.wout = (const bf16_t*)d_in[9];
  P.c = (const bf16_t*)d_in[10]; P.wmod = (const bf16_t*)d_in[11];
  P.out = (bf16_t*)d_out; P.ws = (char*)d_ws;
  (void)hipFuncSetAttribute((const void*)fused_block, hipFuncAttributeMaxDynamicSharedMemorySize, LDS_BYTES);
  fused_block<<<256, NT, LDS_BYTES, stream>>>(P);
}
```

```cpp
#include <hip/hip_runtime.h>
#include <stdint.h>
#include <type_traits>

typedef unsigned short bf16_t;
typedef short bf16x8 __attribute__((ext_vector_type(8)));
typedef short s16x4 __attribute__((ext_vector_type(4)));
typedef float f32x4 __attribute__((ext_vector_type(4)));
typedef float f32x2 __attribute__((ext_vector_type(2)));
typedef unsigned u32x4 __attribute__((ext_vector_type(4)));
typedef unsigned u32x2 __attribute__((ext_vector_type(2)));
typedef __bf16 bf2_t __attribute__((ext_vector_type(2)));
#define LAS __attribute__((address_space(3)))
#define GAS __attribute__((address_space(1)))
typedef GAS unsigned gu32;
typedef GAS unsigned long long gu64;
#define RLX_AGENT __ATOMIC_RELAXED, __HIP_MEMORY_SCOPE_AGENT
#define DEV __device__ __forceinline__

constexpr int S = 2048, D = 640, F = 1728, FH = 864;
constexpr int NT = 512;

constexpr unsigned WS_EPOCH = 0;
constexpr unsigned WS_TMO   = 256;
constexpr unsigned WS_FN = 4096, WS_F2 = 8192, WS_FU = 12288, WS_FX = 16384, WS_FP = 20480, WS_FZ = 24576;
constexpr unsigned WS_MOD  = 65536;
constexpr unsigned WS_MODB = WS_MOD + 16384;

constexpr unsigned WS_XN   = 131072;
constexpr unsigned WS_WIN  = WS_XN + 64 * 40 * 1024;
constexpr unsigned WS_WOUT = WS_WIN + 20 * 109 * 1024;
constexpr unsigned WS_UV   = WS_WOUT + 27 * 40 * 1024;
constexpr unsigned WS_X1P  = WS_UV + 32 * 2 * 20 * 4096;
constexpr unsigned WS_ST1  = WS_X1P + 32 * 20 * 4096;
constexpr unsigned WS_Z2N  = WS_ST1 + 2048 * 64;
constexpr unsigned WS_ST2  = WS_Z2N + 64 * 55 * 1024;
constexpr unsigned WS_P    = WS_ST2 + 2048 * 128;
constexpr unsigned WS_RS   = WS_P + 32 * 28 * 4096;
constexpr unsigned WS_END  = WS_RS + 2048 * 128;

struct Params {
  const bf16_t *x, *wsr, *wsi, *wdr, *wdi, *win, *wsp, *bsp, *wout, *c, *wmod;
  bf16_t* out;
  char* ws;
};

#define STAMP_DECL
#define STAMP(i) do {} while (0)
#define STAMP_FLUSH() do {} while (0)
#define CSTAMP(i) do {} while (0)

template <int N> DEV void wait_vm() { asm volatile("s_waitcnt vmcnt(%0)" ::"n"(N) : "memory"); }
DEV void wait_lgkm0() { asm volatile("s_waitcnt lgkmcnt(0)" ::: "memory"); }
DEV void bar() { asm volatile("" ::: "memory"); __builtin_amdgcn_s_barrier(); asm volatile("" ::: "memory"); }
DEV void sched_fence() { __builtin_amdgcn_sched_barrier(0); }
template <int I, int N, class Fn> DEV void static_for(Fn&& f) { if constexpr (I < N) { f(std::integral_constant<int, I>{}); static_for<I + 1, N>(f); } }

DEV float bflo(unsigned v) { return __uint_as_float(v << 16); }
DEV float bfhi(unsigned v) { return __uint_as_float(v & 0xffff0000u); }
DEV unsigned pack2(float lo, float hi) { bf2_t v; v[0] = (__bf16)lo; v[1] = (__bf16)hi; return __builtin_bit_cast(unsigned, v); }
DEV float rbf(float f) { return bflo(pack2(f, f)); }
DEV float dot2(unsigned a, unsigned b, float c) { return __builtin_amdgcn_fdot2_f32_bf16(__builtin_bit_cast(bf2_t, a), __builtin_bit_cast(bf2_t, b), c, false); }
DEV f32x4 mfma(bf16x8 a, bf16x8 b, f32x4 c) { return __builtin_amdgcn_mfma_f32_16x16x32_bf16(a, b, c, 0, 0, 0); }

DEV u32x4 mkrs(const void* p, unsigned bytes) {
  const unsigned long long a = (unsigned long long)p;
  u32x4 r; r.x = (unsigned)a; r.y = (unsigned)(a >> 32) & 0xffffu; r.z = bytes; r.w = 0x00020000u; return r;
}
DEV __amdgpu_buffer_rsrc_t mkrsrc(const void* p, unsigned bytes) { return __builtin_amdgcn_make_buffer_rsrc((void*)p, 0, bytes, 0x00020000); }
#define LD_P(v, r, off, sc) "buffer_load_dwordx4 " v ", " r ", %0 offen offset:" off sc " lds\n\t"
#define LD_H(v, r, off, sc) "s_mov_b32 exec_hi, 0\n\tbuffer_load_dwordx4 " v ", " r ", %0 offen offset:" off sc " lds\n\ts_mov_b32 exec_hi, -1"
#define HD_OR  "s_add_u32 m0, %1, %2\n\ts_or_b32 %0, %3, %4\n\t"
#define HD_ADD "s_add_u32 m0, %1, %2\n\ts_add_u32 %0, %3, %4\n\t"
#define HD_MAD(M) "s_mul_i32 %0, %3, " M "\n\ts_add_u32 m0, %1, %2\n\ts_add_u32 %0, %0, %4\n\t"
#define DMA_IN(IMM) "s"(lds), "n"(IMM), "s"(a), "s"(b)
template <int IMM> DEV void dma_w4_or(unsigned lds, unsigned a, unsigned b, u32x4 ra, u32x4 rb, unsigned v0, unsigned v1, unsigned v2, unsigned v3) { unsigned t;
  asm volatile(HD_OR LD_P("%7", "%5", "0", "") LD_P("%8", "%5", "1024", "") LD_P("%9", "%6", "2048", "") LD_P("%10", "%6", "3072", "")
               : "=&s"(t) : DMA_IN(IMM), "s"(ra), "s"(rb), "v"(v0), "v"(v1), "v"(v2), "v"(v3) : "memory", "scc"); }
template <int IMM> DEV void dma_w2_or(unsigned lds, unsigned a, unsigned b, u32x4 r, unsigned v0, unsigned v1) { unsigned t;
  asm volatile(HD_OR LD_P("%6", "%5", "0", "") LD_P("%7", "%5", "1024", "") : "=&s"(t) : DMA_IN(IMM), "s"(r), "v"(v0), "v"(v1) : "memory", "scc"); }
template <int IMM, int SOFF> DEV void dma_w2h(unsigned lds, unsigned a, u32x4 r, unsigned v0, unsigned v1, unsigned v2) { unsigned t;
  asm volatile("s_add_u32 m0, %1, %2\n\ts_add_u32 %0, %3, %4\n\t" LD_P("%6", "%5", "0", "") LD_P("%7", "%5", "1024", "") LD_H("%8", "%5", "2048", "")
               : "=&s"(t) : "s"(lds), "n"(IMM), "s"(a), "n"(SOFF), "s"(r), "v"(v0), "v"(v1), "v"(v2) : "memory", "scc"); }
template <int IMM, int SOFF> DEV void dma_x2(unsigned lds, unsigned a, u32x4 r, unsigned v) { unsigned t;
  asm volatile("s_add_u32 m0, %1, %2\n\ts_add_u32 %0, %3, %4\n\t" LD_P("%6", "%5", "0", " sc1") LD_P("%6", "%5", "1024", " sc1") : "=&s"(t) : "s"(lds), "n"(IMM), "s"(a), "n"(SOFF), "s"(r), "v"(v) : "memory", "scc"); }
template <int IMM, int SOFF> DEV void dma_x2h(unsigned lds, unsigned a, u32x4 r, unsigned v) { unsigned t;
  asm volatile("s_add_u32 m0, %1, %2\n\ts_add_u32 %0, %3, %4\n\t" LD_P("%6", "%5", "0", " sc1") LD_P("%6", "%5", "1024", " sc1") LD_H("%6", "%5", "2048", " sc1") : "=&s"(t) : "s"(lds), "n"(IMM), "s"(a), "n"(SOFF), "s"(r), "v"(v) : "memory", "scc"); }
template <int IMM, int SOFF> DEV void dma_x3h(unsigned lds, unsigned a, u32x4 r, unsigned v) { unsigned t;
  asm volatile("s_add_u32 m0, %1, %2\n\ts_add_u32 %0, %3, %4\n\t" LD_P("%6", "%5", "0", " sc1") LD_P("%6", "%5", "1024", " sc1") LD_P("%6", "%5", "2048", " sc1") LD_H("%6", "%5", "3072", " sc1") : "=&s"(t) : "s"(lds), "n"(IMM), "s"(a), "n"(SOFF), "s"(r), "v"(v) : "memory", "scc"); }
template <int IMM> DEV void dma_x2h_mad640(unsigned lds, unsigned a, unsigned b, u32x4 r, unsigned v) { unsigned t;
  asm volatile(HD_MAD("640") LD_P("%6", "%5", "0", " sc1") LD_P("%6", "%5", "1024", " sc1") LD_H("%6", "%5", "2048", " sc1") : "=&s"(t) : DMA_IN(IMM), "s"(r), "v"(v) : "memory", "scc"); }
template <int IMM> DEV void dma_x3h_mad880(unsigned lds, unsigned a, unsigned b, u32x4 r, unsigned v) { unsigned t;
  asm volatile(HD_MAD("880") LD_P("%6", "%5", "0", " sc1") LD_P("%6", "%5", "1024", " sc1") LD_P("%6", "%5", "2048", " sc1") LD_H("%6", "%5", "3072", " sc1") : "=&s"(t) : DMA_IN(IMM), "s"(r), "v"(v) : "memory", "scc"); }
DEV void dma_1(unsigned lds, u32x4 r, unsigned v) { asm volatile("s_mov_b32 m0, %0\n\ts_nop 0\n\tbuffer_load_dwordx4 %2, %1, 0 offen sc1 lds" ::"s"(lds), "s"(r), "v"(v) : "memory"); }
template <int N> DEV void wait_bar() { asm volatile("s_waitcnt vmcnt(%0)\n\ts_barrier" ::"n"(N) : "memory"); }
DEV void warm(unsigned lds, u32x4 r, unsigned v) { asm volatile("s_mov_b32 m0, %0\n\ts_nop 0\n\tbuffer_load_dword %2, %1, 0 offen lds" ::"s"(lds), "s"(r), "v"(v) : "memory"); }
constexpr unsigned WBIAS = 4096;
DEV u32x4 mkrs_b(const void* p, unsigned bytes) { return mkrs((const char*)p - WBIAS, bytes + WBIAS); }
constexpr int SC1 = 16;
DEV void st16(__amdgpu_buffer_rsrc_t r, unsigned off, u32x4 v) { __builtin_amdgcn_raw_buffer_store_b128(v, r, off, 0, SC1); }
DEV u32x4 ld16(__amdgpu_buffer_rsrc_t r, unsigned off) { return __builtin_amdgcn_raw_buffer_load_b128(r, off, 0, SC1); }
DEV f32x4 ld16f(__amdgpu_buffer_rsrc_t r, unsigned off) { return __builtin_bit_cast(f32x4, ld16(r, off)); }

template <int CTRL> DEV float dpp_add(float v) { return v + __builtin_bit_cast(float, __builtin_amdgcn_update_dpp(0, __builtin_bit_cast(int, v), CTRL, 0xf, 0xf, true)); }
DEV float xsum_16(float v) { v = dpp_add<0xB1>(v); v = dpp_add<0x4E>(v); v = dpp_add<0x141>(v); return dpp_add<0x140>(v); }
DEV float xsum_r16(float v) { auto r = __builtin_amdgcn_permlane16_swap(__float_as_uint(v), __float_as_uint(v), false, false); return __uint_as_float(r[0]) + __uint_as_float(r[1]); }
DEV float xsum_r32(float v) { auto r = __builtin_amdgcn_permlane32_swap(__float_as_uint(v), __float_as_uint(v), false, false); return __uint_as_float(r[0]) + __uint_as_float(r[1]); }
DEV float xsum_q(float v) { return xsum_r32(xsum_r16(v)); }
DEV float gelu_tanh(float x) {
  const float k1 = -2.0f * 0.7978845608028654f * 1.4426950408889634f;
  const float t = x * __builtin_fmaf(x * x, 0.044715f * k1, k1);
  return x * __builtin_amdgcn_rcpf(1.0f + __builtin_amdgcn_exp2f(t));
}
DEV f32x2 gelu2(f32x2 x) {
  const float k1 = -2.0f * 0.7978845608028654f * 1.4426950408889634f;
  const f32x2 t = x * ((x * x) * (0.044715f * k1) + k1);
  const f32x2 d = (f32x2){__builtin_amdgcn_exp2f(t[0]), __builtin_amdgcn_exp2f(t[1])} + 1.0f;
  return x * (f32x2){__builtin_amdgcn_rcpf(d[0]), __builtin_amdgcn_rcpf(d[1])};
}
DEV u32x4 swap_pair(u32x2 a, u32x2 b) {
  auto r0 = __builtin_amdgcn_permlane16_swap(a.x, b.x, false, false);
  auto r1 = __builtin_amdgcn_permlane16_swap(a.y, b.y, false, false);
  return (u32x4){r0[0], r1[0], r0[1], r1[1]};
}

DEV bool spin_fail(unsigned& spins, char* ws) {
  __builtin_amdgcn_s_sleep(1);
  if (++spins > (1u << 17)) { __hip_atomic_store((gu32*)(ws + WS_TMO), 1u, RLX_AGENT); return true; }
  return false;
}
DEV void set_flag(char* ws, unsigned off, unsigned tag) { __hip_atomic_store((gu32*)(ws + off), tag, RLX_AGENT); }
DEV void wait_flags8(char* ws, unsigned off, unsigned tag, int lane) {
  for (unsigned spins = 0;;) {
    const unsigned v = lane < 8 ? __hip_atomic_load((gu32*)(ws + off) + lane, RLX_AGENT) : tag;
    if (__all(v == tag)) break;
    if (spin_fail(spins, ws)) break;
  }
}
DEV bool flags_ok(u32x4 v, unsigned tag) { return __all(v.x == tag && v.y == tag && v.z == tag && v.w == tag); }
DEV u32x4 peek256(__amdgpu_buffer_rsrc_t rf, unsigned off, int lane) { return ld16(rf, off + (lane >> 1) * 128 + (lane & 1) * 16); }
DEV void wait256(__amdgpu_buffer_rsrc_t rf, char* ws, unsigned off, unsigned tag, int lane) {
  for (unsigned spins = 0;;) { if (flags_ok(peek256(rf, off, lane), tag)) break; if (spin_fail(spins, ws)) break; }
}

DEV void wait_panels(__amdgpu_buffer_rsrc_t rf, char* ws, unsigned off, int first, int count, unsigned tag, int lane) {
  const unsigned o = lane < 2 * count ? off + (((lane >> 1) + first) & 31) * 128 + (lane & 1) * 16 : off + (first & 31) * 128;
  for (unsigned spins = 0;;) { if (flags_ok(ld16(rf, o), tag)) break; if (spin_fail(spins, ws)) break; }
}

template <int NS, int PF, int S_, int CW, int CX, class IW, class IX>
DEV void loader_run(IW&& issueW, IX&& issueX) {
  constexpr auto cnt = [](int s) { return (s >= PF ? CW : 0) + CX; };
  constexpr auto span = [=](int a, int b) { int c = 0; for (int s = a; s <= b; ++s) c += (s >= PF ? CW : 0) + CX; return c; };
  auto step = [&](auto ic) { if constexpr (decltype(ic)::value >= PF) issueW(ic); issueX(ic); };
  static_for<0, NS>(step);
  wait_bar<span(1, NS - 1)>();
  static_for<0, S_ - 1>([&](auto ic) { constexpr int n = decltype(ic)::value;
    constexpr int last = n + NS - 1 < S_ - 1 ? n + NS - 1 : S_ - 1;
    wait_bar<span(n + 2, last)>();
    if constexpr (n + NS < S_) step(std::integral_constant<int, n + NS>{}); });
  (void)cnt;
}

template <int NS, int S_, int NA, int NB, int NM, class Rd, class Mm, class Hook, class Post>
DEV void ksplit_steps(bf16x8 (&A0)[NA], bf16x8 (&B0)[NB], bf16x8 (&A1)[NA], bf16x8 (&B1)[NB], int& slot, int n0, int n1, Rd&& rd, Mm&& mm, Hook&& hook, Post&& post) {
  constexpr int NR = NA + NB;
#define KS_ILV() do { _Pragma("unroll") for (int z = 0; z < NR; ++z) { __builtin_amdgcn_sched_group_barrier(0x8, 1, 0); __builtin_amdgcn_sched_group_barrier(0x100, 1, 0); } __builtin_amdgcn_sched_group_barrier(0x8, NM - NR, 0); } while (0)
#pragma unroll 1
  for (int n = n0; n < n1; n += 2) {
    const int s1 = slot + 1 == NS ? 0 : slot + 1;
    hook(n); wait_lgkm0(); bar(); post(n); sched_fence();
    rd(A1, B1, s1, n + 1); mm(A0, B0, false); KS_ILV(); sched_fence();
    const int s2 = s1 + 1 == NS ? 0 : s1 + 1;
    hook(n + 1); wait_lgkm0(); bar(); post(n + 1); sched_fence();
    rd(A0, B0, s2, n + 2); mm(A1, B1, false); KS_ILV(); sched_fence();
    slot = s2;
  }
}
template <int NS, int S_, int NA, int NB, class Rd, class Mm>
DEV void ksplit_last(bf16x8 (&A0)[NA], bf16x8 (&B0)[NB], bf16x8 (&A1)[NA], bf16x8 (&B1)[NB], int& slot, Rd&& rd, Mm&& mm) {
  const int s1 = slot + 1 == NS ? 0 : slot + 1;
  wait_lgkm0(); bar(); sched_fence();
  rd(A1, B1, s1, S_ - 1); mm(A0, B0, false); mm(A1, B1, true);
  slot = s1;
}
template <int NS, int S_, int NA, int NB, class Rd, class Mm>
DEV void ksplit_loop(Rd&& rd, Mm&& mm) {
  static_assert((S_ & 1) == 0, "even step count");
  bf16x8 A0[NA], B0[NB], A1[NA], B1[NB];
  bar();
  rd(A0, B0, 0, 0);
  int slot = 0;
  ksplit_steps<NS, S_, NA, NB, NA * NB>(A0, B0, A1, B1, slot, 0, S_ - 2, rd, mm, [](int) {}, [](int) {});
  ksplit_last<NS, S_, NA, NB>(A0, B0, A1, B1, slot, rd, mm);
}
template <int N>
DEV void kreduce(f32x4 (&acc)[N][2], f32x4 (&res)[N], LAS char* scratch, int w, int lane) {
  const int kh = w & 1;
  LAS char* mine = scratch + w * (N * 1024) + lane * 16;
  LAS char* other = scratch + (w ^ 1) * (N * 1024) + lane * 16;
  if (kh) { asm volatile("");
#pragma unroll
    for (int i = 0; i < N; ++i) { *(LAS f32x4*)(mine + i * 1024) = acc[i][0]; res[i] = acc[i][1]; }
  } else { asm volatile("");
#pragma unroll
    for (int i = 0; i < N; ++i) { *(LAS f32x4*)(mine + i * 1024) = acc[i][1]; res[i] = acc[i][0]; }
  }
  wait_lgkm0(); bar();
#pragma unroll
  for (int i = 0; i < N; ++i) res[i] += *(LAS f32x4*)(other + i * 1024);
}

#define WARM 1
#define JROT 2
#define A_PRE 2
#define PFW 6
#define PRE1 2
#define PFC 5
#define A_PRE2 0
#define WARMW 0
#define LPRIO 0
#define CPRIO 2
#define A_NS_ 5
#define B_NS_ 6
#define D_NS_ 5
#define E_NS_ 6
constexpr int A_NS = A_NS_, A_SLOT = 26 * 1024, A_SCR = 52 * 1024;
constexpr int P0_T = 130 * 1024, P0_PT = 140 * 1024, P0_RED = 157 * 1024;
constexpr int B_NS = B_NS_, B_SLOT = 18 * 1024, B_SCR = (B_NS_ == 8 ? 0 : 36 * 1024);
constexpr int C_NS = 5, C_X = 0, C_W = 82 * 1024, C_WSLOT = 14 * 1024;
constexpr int C_SCR = C_W + 2 * C_WSLOT;
constexpr int D_NS = D_NS_, D_SLOT = 22 * 1024, D_SCR = (D_NS_ == 7 ? 0 : 110 * 1024);
constexpr int E_NS = E_NS_, E_SLOT = 18 * 1024, E_SCR = (E_NS_ == 8 ? 0 : 110 * 1024);
constexpr int T_MODB = 152 * 1024;
constexpr int T_CV = T_MODB + 3072 + 64;
constexpr int T_MOD = 158 * 1024;
constexpr int T_DUMMY = T_MOD + 1792;
constexpr int LDS_BYTES = 160 * 1024;
static_assert(T_CV + 1792 <= P0_RED && P0_RED + 256 <= T_MOD && C_W + C_NS * C_WSLOT <= T_MODB && A_NS * A_SLOT <= P0_RED && D_NS * D_SLOT <= T_CV && B_NS * B_SLOT <= T_MODB && E_NS * E_SLOT <= T_MODB, "LDS map");

#define IC(n) std::integral_constant<int, (n)>{}
DEV void loader_main(const Params& P, const unsigned lds0, LAS char* smem, const int lw, const int lane_, const int b, const int p, const int j, const int krot) {
  STAMP_DECL STAMP(0);
  char* const ws = P.ws;
  const int ft0 = 27 * (j >> 2) + 7 * (j & 3);
#define LLANE int lane = lane_; asm volatile("" : "+v"(lane)); const unsigned vx = lane * 16; (void)vx;
#define LV4K const unsigned v4k0 = (unsigned)(lane >> 3) * 4096u + (unsigned)(((lane & 7) ^ (lane >> 4)) * 16), v4k1 = (unsigned)(8 + (lane >> 3)) * 4096u + (unsigned)(((lane & 7) ^ (4 + (lane >> 4))) * 16);
  {
    LLANE LV4K
    const u32x4 rre = mkrs_b(P.wsr, S * S * 2), rim = mkrs_b(P.wsi, S * S * 2), rx = mkrs(ws + WS_XN, S * D * 2);
    const unsigned wrow = (unsigned)(64 * p + 16 * lw) * 4096u;
    const unsigned w0 = v4k0 + WBIAS, w1 = v4k1 + WBIAS - 1024, w2 = v4k0 + WBIAS - 2048, w3 = v4k1 + WBIAS - 3072;
    const unsigned lw_w = lds0 + lw * 4096, lw_x = lds0 + 16384 + lw * 2560;
    const unsigned xo = (unsigned)(((lw >> 1) * 40 + 5 * j) * 1024 + (lw & 1) * 2560);
    const unsigned rot = (unsigned)j * 512, xrot = (unsigned)(b & 7) * 512;
    auto kof = [&](auto ic) { constexpr int n = decltype(ic)::value;
      if constexpr (JROT == 2) { const unsigned k = n < 4 ? rot + n * 128 : (n - 4) * 128 + ((unsigned)((n - 4) * 128) >= rot ? 512u : 0u); return (k + xrot) & 0xf80u; }
      else return (unsigned)((krot + n) * 128) & 0xf80u; };
    auto issueW = [&](auto ic) { dma_w4_or<(decltype(ic)::value % A_NS) * A_SLOT>(lw_w, kof(ic), wrow, rre, rim, w0, w1, w2, w3); };
    auto issueX = [&](auto ic) { dma_x2h_mad640<(decltype(ic)::value % A_NS) * A_SLOT>(lw_x, kof(ic), xo, rx, vx); };
    static_for<0, A_PRE>(issueW);
    bar();
    bar();
    STAMP(30);
    static_for<A_PRE, A_PRE + A_PRE2>(issueW);
    if (WARMW) {
#pragma unroll
      for (int u = 0; u < 2; ++u) { const int row = (j * 4 + lw) * 4 + 2 * u + (lane >> 5);
        warm(lds0 + T_DUMMY, j * 4 + lw < 16 ? rre : rim, (unsigned)(64 * p + (row & 63)) * 4096u + (lane & 31) * 128 + WBIAS); }
    }
    const int idx = b >> 3, unit0 = (idx & 3) + 4 * ((b & 7) + 8 * (idx >> 2));
    const bool domod = unit0 < 240; const int unit = domod ? unit0 : 0;
    {
      u32x4 wm[5]; unsigned cs[5];
#pragma unroll
      for (int i = 0; i < 5; ++i) { const int row = lw * 160 + i * 32 + (lane >> 1);
        wm[i] = *(const u32x4*)(P.wmod + (size_t)row * (6 * D) + unit * 16 + (lane & 1) * 8); cs[i] = P.c[row]; }
      float a[8] = {0, 0, 0, 0, 0, 0, 0, 0};
#pragma unroll
      for (int i = 0; i < 5; ++i) { const float x = bflo(cs[i]); const float s = rbf(x * __builtin_amdgcn_rcpf(1.0f + __expf(-x)));
        a[0] += s * bflo(wm[i].x); a[1] += s * bfhi(wm[i].x); a[2] += s * bflo(wm[i].y); a[3] += s * bfhi(wm[i].y);
        a[4] += s * bflo(wm[i].z); a[5] += s * bfhi(wm[i].z); a[6] += s * bflo(wm[i].w); a[7] += s * bfhi(wm[i].w); }
#pragma unroll
      for (int e = 0; e < 8; ++e) { float v = dpp_add<0x4E>(a[e]);
        v = dpp_add<0x124>(v); v = dpp_add<0x128>(v);
        a[e] = xsum_q(v); }
      if (lane < 2) {
#pragma unroll
        for (int e = 0; e < 8; ++e) ((LAS float*)(smem + P0_RED))[lw * 16 + lane * 8 + e] = a[e];
      }
      wait_lgkm0();
    }
    STAMP(32);
    bar();
    STAMP(33);
    __builtin_amdgcn_s_setprio(LPRIO);
    auto istep = [&](auto ic) { if constexpr (decltype(ic)::value >= A_PRE + A_PRE2) issueW(ic); issueX(ic); };
#define A_CNT(s_) (((s_) >= A_PRE + A_PRE2 ? 4 : 0) + 3)
    static_assert((A_NS == 5 || A_NS == 6) && A_PRE + A_PRE2 <= A_NS, "start-up schedule");
    istep(IC(0)); istep(IC(1));
    wait_bar<A_CNT(1)>();
    STAMP(34);
    istep(IC(2)); istep(IC(3));
    if (lw == 0 && domod && lane < 16) {
      const LAS float* red = (const LAS float*)(smem + P0_RED);
      float s = rbf((red[lane] + red[16 + lane]) + (red[32 + lane] + red[48 + lane]));
      const int n = unit * 16 + lane, seg = n / D;
      if (seg == 1 || seg == 4) s = rbf(1.0f + s);
      __hip_atomic_store((gu32*)(ws + WS_MOD) + n, __float_as_uint(s), RLX_AGENT);
      const unsigned hi = __float_as_uint(s) >> 16, nb = (unsigned)__builtin_amdgcn_update_dpp(0, (int)hi, 0xB1, 0xf, 0xf, true);
      if ((seg == 3 || seg == 4) && !(lane & 1)) __hip_atomic_store((gu32*)(ws + WS_MODB) + (((seg == 4 ? 0 : D) + n - seg * D) >> 1), hi | (nb << 16), RLX_AGENT);
    }
    wait_bar<A_CNT(2) + A_CNT(3)>();
    static_for<4, A_NS + 1>(istep);
    wait_bar<A_CNT(3) + A_CNT(4) + A_CNT(5) + (A_NS == 6 ? A_CNT(6) : 0)>();
    const u32x4 rmod = mkrs(ws + WS_MOD, 3840 * 4);
    const int tl = lane / 20, tc = lane - 20 * tl;
    const unsigned tv0 = lane < 60 ? (unsigned)((tl * D + 80 * j) * 4 + tc * 16) : 0x7ffffff0u;
    const unsigned tv1 = lane < 40 ? (unsigned)(((4 + tl) * D + 80 * j) * 4 + tc * 16) : 0x7ffffff0u;
    istep(IC(A_NS + 1));
    static_for<2, 32 - A_NS - 0>([&](auto ic) { constexpr int n = decltype(ic)::value;
      constexpr int cnt = [] { int c = 0; for (int s_ = n + 2; s_ <= n + A_NS - 1; ++s_) c += A_CNT(s_); return c; }();
      wait_bar<cnt>(); if constexpr (n + A_NS < 32) istep(IC(n + A_NS));
      if constexpr (n == 20) { if (lw == 0) { dma_1(lds0 + T_MOD, rmod, tv0); dma_1(lds0 + T_MOD + 960, rmod, tv1); } }
    });
    if (WARM) {
      { const int row = (j * 4 + lw) * 2 + (lane >> 5); warm(lds0 + T_DUMMY, mkrs(P.wsp, S * S * 2), (unsigned)(64 * p + row) * 4096u + (lane & 31) * 128); }
      warm(lds0 + T_DUMMY, mkrs((lw & 1) ? P.wdi : P.wdr, D * D * 2), lw < 2 && lane < 25 ? (unsigned)(b * 25 + lane) * 128 : 0x7ffffff0u);
    }
    static_for<0, A_NS - 1>([&](auto ic) { constexpr int r = A_NS - 2 - decltype(ic)::value; wait_bar<r * 7 + (WARM ? 2 : 0)>(); });
    bar();
  }
  {
    LLANE
    const u32x4 rre = mkrs_b(P.wdr, D * D * 2), rim = mkrs_b(P.wdi, D * D * 2), rx = mkrs(ws + WS_UV, 32 * 2 * 20 * 4096);
    auto wv = [&](int k) { const int r = 20 * lw + 8 * k + (lane >> 3); return (unsigned)(r * 1280 + (((lane & 7) ^ ((r >> 1) & 7)) * 16)) + WBIAS - 1024 * k; };
    const unsigned v0 = wv(0), v1 = wv(1), v2 = wv(2);
    const unsigned wb = (unsigned)(80 * j) * 1280u, lw_w = lds0 + lw * 2560, lw_x = lds0 + 10240 + lw * 2048, xb = (unsigned)p * 40 * 4096u + lw * 2048;
    auto issueW = [&](auto ic) { constexpr int n = decltype(ic)::value; dma_w2h<(n % B_NS) * B_SLOT, (n % 10) * 128>(lw_w, wb, n >= 10 ? rim : rre, v0, v1, v2); };
    auto issueX = [&](auto ic) { constexpr int n = decltype(ic)::value; dma_x2<(n % B_NS) * B_SLOT, 2 * n * 4096>(lw_x, xb, rx, vx); };
    __builtin_amdgcn_s_setprio(0);
    static_for<0, (PRE1 < PFW ? PRE1 : PFW)>(issueW);
    bar();
    static_for<(PRE1 < PFW ? PRE1 : PFW), PFW>(issueW);
    bar();
    __builtin_amdgcn_s_setprio(LPRIO);
    loader_run<B_NS, PFW, 20, 3, 2>(issueW, issueX);
    bar();
  }
  {
    LLANE
    const u32x4 rw = mkrs(ws + WS_WIN, 20 * 109 * 1024), rx = mkrs(ws + WS_X1P, 32 * 20 * 4096), rmb = mkrs(ws + WS_MODB, 2560);
    const unsigned wo = (unsigned)(((lw >> 1) * 109 + ft0) * 1024 + (lw & 1) * 3584);
    const unsigned lw_w = lds0 + C_W + lw * 3584, lw_x = lds0 + C_X + lw * 2048, xb = (unsigned)p * 20 * 4096u + lw * 2048;
    auto issueW = [&](auto ic) { constexpr int n = decltype(ic)::value, z1 = n >= 10; dma_x3h<(n % C_NS) * C_WSLOT, (2 * (n - 10 * z1) * 109 + (z1 ? 0 : 54)) * 1024>(lw_w, wo, rw, vx); };
    auto issueX = [&](auto ic) { constexpr int n = decltype(ic)::value; dma_x2<2 * n * 4096, 2 * n * 4096>(lw_x, xb, rx, vx); };
    __builtin_amdgcn_s_setprio(0);
    static_for<0, (PRE1 < PFC ? PRE1 : PFC)>(issueW);
    bar();
    if (lw < 3) dma_1(lds0 + T_MODB + lw * 1024, rmb, vx + lw * 1024);
    static_for<(PRE1 < PFC ? PRE1 : PFC), PFC>(issueW);
    bar();
    __builtin_amdgcn_s_setprio(LPRIO);
    static_for<0, C_NS>([&](auto ic) { if constexpr (decltype(ic)::value >= PFC) issueW(ic); issueX(ic); });
    wait_bar<(PFC - 1) * 2 + (C_NS - PFC) * 6>();
    static_for<0, 9>([&](auto ic) {
      constexpr int n = decltype(ic)::value, need = n + 1;
      constexpr int last = n + C_NS - 1 < 11 ? n + C_NS - 1 : 11;
      constexpr int cnt = [] { int c = 0; for (int s = need + 1; s <= last; ++s) c += (s < PFC ? 2 : s < 10 ? 6 : 4); return c; }();
      wait_bar<cnt>();
      if constexpr (n + C_NS < 12) { issueW(IC(n + C_NS)); if constexpr (n + C_NS < 10) issueX(IC(n + C_NS)); }
    });
    bar();
    bar();
    issueW(IC(12)); issueW(IC(13)); issueW(IC(14));
    wait_bar<16>();
    static_for<10, 19>([&](auto ic) {
      constexpr int n = decltype(ic)::value;
      constexpr int last = n + 4 < 19 ? n + 4 : 19;
      wait_bar<(last - (n + 1)) * 4>();
      if constexpr (n + C_NS < 20) issueW(IC(n + C_NS));
    });
    bar();
  }
  {
    LLANE LV4K
    const u32x4 rw = mkrs_b(P.wsp, S * S * 2), rx = mkrs(ws + WS_Z2N, 64 * 55 * 1024);
    const unsigned wrow = (unsigned)(64 * p + 16 * lw) * 4096u, w0 = v4k0 + WBIAS, w1 = v4k1 + WBIAS - 1024;
    const unsigned xo = (unsigned)(((lw >> 1) * 55 + ft0) * 1024 + (lw & 1) * 3584), lw_w = lds0 + lw * 2048, lw_x = lds0 + 8192 + lw * 3584;
    unsigned kw = (b & 7) * 512, kx = kw;
    auto issueW = [&](auto ic) { dma_w2_or<(decltype(ic)::value % D_NS) * D_SLOT>(lw_w, kw, wrow, rw, w0, w1); kw = (kw + 128) & 0xf80; };
    auto issueX = [&](auto ic) { dma_x3h_mad880<(decltype(ic)::value % D_NS) * D_SLOT>(lw_x, kx, xo, rx, vx); kx = (kx + 128) & 0xf80; };
    static_assert(D_NS * D_SLOT <= C_SCR, "ring below the exchange buffer");
    static_for<0, D_NS>(issueW);
    bar();
    static_for<0, D_NS>(issueX);
    wait_bar<(D_NS - 1) * 4>();
    static_for<0, 31>([&](auto ic) { constexpr int n = decltype(ic)::value;
      constexpr int last = n + D_NS - 1 < 31 ? n + D_NS - 1 : 31;
      constexpr int cnt = n + 1 < D_NS ? (D_NS - 2 - n) * 4 + (last - (D_NS - 1)) * 6 : (last - (n + 1)) * 6;
      wait_bar<cnt>();
      if constexpr (n + D_NS < 32) { issueW(IC(n + D_NS)); issueX(IC(n + D_NS)); } });
    bar();
  }
  {
    LLANE
    const u32x4 rw = mkrs(ws + WS_WOUT, 28 * 40 * 1024), rx = mkrs(ws + WS_P, 32 * 28 * 4096);
    const unsigned wo = (unsigned)(((lw >> 1) * 40 + 5 * j) * 1024 + (lw & 1) * 2560), lw_w = lds0 + lw * 2560, lw_x = lds0 + 10240 + lw * 2048, xb = (unsigned)p * 28 * 4096u + lw * 2048;
    auto issueW = [&](auto ic) { constexpr int n = decltype(ic)::value; dma_x2h<(n % E_NS) * E_SLOT, n * 80 * 1024>(lw_w, wo, rw, vx); };
    auto issueX = [&](auto ic) { constexpr int n = decltype(ic)::value; dma_x2<(n % E_NS) * E_SLOT, 2 * n * 4096>(lw_x, xb, rx, vx); };
    __builtin_amdgcn_s_setprio(0);
    static_for<0, (PRE1 < PFW ? PRE1 : PFW)>(issueW);
    bar();
    static_for<(PRE1 < PFW ? PRE1 : PFW), PFW>(issueW);
    bar();
    __builtin_amdgcn_s_setprio(LPRIO);
    loader_run<E_NS, PFW, 14, 3, 2>(issueW, issueX);
    bar();
  }
  STAMP_FLUSH();
}

DEV void consumer_main(const Params& P, LAS char* smem, const int w, const int lane_, const int b, const int p, const int j) {
  STAMP_DECL STAMP(0);
  if (CPRIO) __builtin_amdgcn_s_setprio(CPRIO);
  char* const ws = P.ws;
  unsigned tagv = __hip_atomic_load((gu32*)(ws + WS_EPOCH), RLX_AGENT) + 1u, tag;
  const int mh = w >> 1, kh = w & 1;
  const auto rws = mkrsrc(ws, WS_END);
  const int ft0 = 27 * (j >> 2) + 7 * (j & 3), nft = (j & 3) == 3 ? 6 : 7;
  auto rot = [&](int i) { return i < 3 ? i + 4 * mh : i - 3 * mh; };
#define LANE_IDS int lane = lane_; asm volatile("" : "+v"(lane)); const int q = lane >> 4, tk = lane & 15, tid = w * 64 + lane, token = 64 * p + 16 * w + tk; (void)q; (void)tk; (void)tid; (void)token;
  {
    LANE_IDS
    unsigned v[2][5];
#pragma unroll
    for (int t = 0; t < 2; ++t) { const unsigned* xr = (const unsigned*)(P.x + (size_t)(64 * p + 8 * j + 2 * w + t) * D);
#pragma unroll
      for (int i = 0; i < 5; ++i) v[t][i] = xr[lane + 64 * i]; }
    const int u0 = w * 256 + b; const bool dopt = u0 < 810; const int u = dopt ? u0 : 0; const bool isin = u < 540;
    const int uu = isin ? u : u - 540, ncb = isin ? 27 : 10, pkb = uu / ncb, pcb = uu - pkb * ncb, ld = isin ? F : D;
    u32x4 pv[4];
    { const bf16_t* src = (isin ? P.win : P.wout) + (size_t)(pkb * 32) * ld + pcb * 64;
#pragma unroll
      for (int i = 0; i < 4; ++i) pv[i] = *(const u32x4*)(src + (size_t)(i * 8 + (lane >> 3)) * ld + (lane & 7) * 8); }
    LAS unsigned* t32 = (LAS unsigned*)(smem + P0_T);
    unsigned pk[2][5];
#pragma unroll
    for (int t = 0; t < 2; ++t) {
      float s = 0.f;
#pragma unroll
      for (int i = 0; i < 5; ++i) s += bflo(v[t][i]) + bfhi(v[t][i]);
      const float mean = xsum_q(xsum_16(s)) * (1.0f / D);
      float q2 = 0.f;
#pragma unroll
      for (int i = 0; i < 5; ++i) { const float a = bflo(v[t][i]) - mean, c = bfhi(v[t][i]) - mean; q2 += a * a + c * c; }
      const float rs = 1.0f / sqrtf(xsum_q(xsum_16(q2)) * (1.0f / D) + 1e-5f);
#pragma unroll
      for (int i = 0; i < 5; ++i) pk[t][i] = pack2((bflo(v[t][i]) - mean) * rs, (bfhi(v[t][i]) - mean) * rs);
    }
#pragma unroll
    for (int i = 0; i < 5; ++i) { const int d = 2 * (lane + 64 * i);
      t32[d * 4 + w] = (pk[0][i] & 0xffffu) | (pk[1][i] << 16); t32[(d + 1) * 4 + w] = (pk[0][i] >> 16) | (pk[1][i] & 0xffff0000u); }
    asm volatile("" : "+v"(tagv)); tag = __builtin_amdgcn_readfirstlane(tagv);
    wait_lgkm0(); bar();
    const unsigned xb = WS_XN + (unsigned)((2 * p + (j >> 2)) * 40 * 1024 + (j & 3) * 256);
#pragma unroll
    for (int i = 0; i < 3; ++i) { const int d = tid + 256 * i;
      if (d < D) st16(rws, xb + (d >> 4) * 1024 + (d & 15) * 16, *(LAS u32x4*)(smem + P0_T + d * 16)); }
    wait_vm<0>();
    bar();
    STAMP(1);
    if (w == 0 && lane == 0) set_flag(ws, WS_FN + p * 128 + j * 4, tag);
    {
      LAS char* t = smem + P0_PT + w * 4096;
#pragma unroll
      for (int i = 0; i < 4; ++i) *(LAS u32x4*)(t + (i * 8 + (lane >> 3)) * 128 + (lane & 7) * 16) = pv[i];
      const unsigned dst = dopt ? (isin ? WS_WIN + (unsigned)(pkb * 109 + pcb * 4) * 1024 : WS_WOUT + (unsigned)(pkb * 40 + pcb * 4) * 1024) + lane * 16 : 0xfffff000u;
      const int rq = tk >> 2, pp = lane & 3;
#pragma unroll
      for (int c = 0; c < 4; ++c) {
        const u32x2 l2 = __builtin_bit_cast(u32x2, __builtin_amdgcn_ds_read_tr16_b64_v4i16((LAS s16x4*)(t + (8 * q + rq) * 128 + c * 32 + pp * 8)));
        const u32x2 h2 = __builtin_bit_cast(u32x2, __builtin_amdgcn_ds_read_tr16_b64_v4i16((LAS s16x4*)(t + (8 * q + 4 + rq) * 128 + c * 32 + pp * 8)));
        st16(rws, dst + (dopt ? c * 1024 : 0), (u32x4){l2.x, l2.y, h2.x, h2.y});
      }
    }
    if (w == 0) { wait256(rws, ws, WS_FN, tag, lane); STAMP(23); }
    bar();
    STAMP(2);
  }
  u32x2 xv[5];
  {
    LANE_IDS
    f32x4 ur[5], ui[5];
#pragma unroll
    for (int i = 0; i < 5; ++i) { ur[i] = (f32x4){0, 0, 0, 0}; ui[i] = (f32x4){0, 0, 0, 0}; }
    f32x4 rsum[2] = {(f32x4){0, 0, 0, 0}, (f32x4){0, 0, 0, 0}};
    bf16x8 ones = __builtin_bit_cast(bf16x8, (u32x4){0x3f803f80u, 0x3f803f80u, 0x3f803f80u, 0x3f803f80u});
    asm volatile("" : "+v"(ones));
    const unsigned wa0 = (unsigned)(w * 4096 + tk * 128 + ((q ^ ((lane >> 1) & 7)) * 16));
    const unsigned xa = 16384 + lane * 16;
    bf16x8 Wa[2], Xa[5], Wb[2], Xb[5];
    auto rdm = [&](bf16x8(&Wn)[2], bf16x8(&Xn)[5], int slot, int kk, bf16x8(&Wf)[2], bf16x8(&Xf)[5]) {
      LAS char* sb = smem + slot * A_SLOT; const unsigned wa = wa0 ^ (kk * 64);
      static_for<0, 12>([&](auto ic) { constexpr int z = decltype(ic)::value, i = z >> 1, t = z & 1;
        if constexpr (z < 10) { if constexpr (t == 0) ur[i] = mfma(Xf[i], Wf[0], ur[i]); else ui[i] = mfma(Xf[i], Wf[1], ui[i]); } else rsum[t] = mfma(ones, Wf[t], rsum[t]);
        if constexpr (z == 0) Wn[0] = *(LAS bf16x8*)(sb + wa);
        if constexpr (z == 1) Xn[0] = *(LAS bf16x8*)(sb + xa + (kk * 5) * 1024);
        if constexpr (z == 2) Wn[1] = *(LAS bf16x8*)(sb + wa + 2048);
        if constexpr (z >= 3 && z < 7) Xn[z - 2] = *(LAS bf16x8*)(sb + xa + (kk * 5 + z - 2) * 1024);
        sched_fence(); });
    };
    auto mmlast = [&](bf16x8(&Wf)[2], bf16x8(&Xf)[5]) {
#pragma unroll
      for (int i = 0; i < 5; ++i) { ur[i] = mfma(Xf[i], Wf[0], ur[i]); ui[i] = mfma(Xf[i], Wf[1], ui[i]); }
      rsum[0] = mfma(ones, Wf[0], rsum[0]); rsum[1] = mfma(ones, Wf[1], rsum[1]);
    };
    int slot = 0;
    auto steps = [&](int n0, int n1, bool hooks) {
#pragma unroll 1
      for (int n = n0; n < n1; ++n) {
        rdm(Wb, Xb, slot, 1, Wa, Xa);
        const int ns = slot + 1 == A_NS ? 0 : slot + 1;
        if (hooks && n == 8) wait_vm<0>();
        wait_lgkm0(); bar(); sched_fence();
        if (hooks && n == 8 && w == 0 && lane == 0) set_flag(ws, WS_F2 + p * 128 + j * 4, tag);
        rdm(Wa, Xa, ns, 0, Wb, Xb);
        slot = ns;
      }
    };
    bar();
    STAMP(3); CSTAMP(40);
    { LAS char* sb = smem; Wa[0] = *(LAS bf16x8*)(sb + wa0); Wa[1] = *(LAS bf16x8*)(sb + wa0 + 2048);
#pragma unroll
      for (int i = 0; i < 5; ++i) Xa[i] = *(LAS bf16x8*)(sb + xa + i * 1024); }
    steps(0, 12, true);
    const u32x4 f2 = peek256(rws, WS_F2, lane);
    steps(12, 18, false);
    if (!flags_ok(f2, tag)) wait256(rws, ws, WS_F2, tag, lane);
    if (b == 0 && w == 0 && lane == 0) set_flag(ws, WS_EPOCH, tag + 15);
#pragma unroll
    for (int i = 0; i < 5; ++i) xv[i] = *(const u32x2*)(P.x + (size_t)token * D + 80 * j + 16 * i + 4 * q);
    steps(18, 31, false);
    { LAS char* sb = smem + slot * A_SLOT; const unsigned wa = wa0 ^ 64;
      Wb[0] = *(LAS bf16x8*)(sb + wa); Wb[1] = *(LAS bf16x8*)(sb + wa + 2048);
#pragma unroll
      for (int i = 0; i < 5; ++i) Xb[i] = *(LAS bf16x8*)(sb + xa + (5 + i) * 1024); }
    mmlast(Wa, Xa);
    wait_lgkm0(); bar();
    mmlast(Wb, Xb);
    STAMP(4); CSTAMP(41);
    const float rs0 = rsum[0][0], rs1 = rsum[1][0];
#pragma unroll
    for (int i = 0; i < 5; ++i) {
      u32x2 a, c;
      const f32x4 sa = *(const LAS f32x4*)(smem + T_MOD + (16 * i + 4 * q) * 4), c1 = *(const LAS f32x4*)(smem + T_MOD + (80 + 16 * i + 4 * q) * 4);
      { const f32x4 u = ur[i] * c1 + rs0 * sa; a.x = pack2(u[0], u[1]); a.y = pack2(u[2], u[3]); }
      { const f32x4 v = -(ui[i] * c1 + rs1 * sa); c.x = pack2(v[0], v[1]); c.y = pack2(v[2], v[3]); }
      const int dt = 5 * j + i, kb = dt >> 1, qq = 2 * (dt & 1) + (q >> 1);
      st16(rws, WS_UV + (unsigned)(((p * 2 + (q & 1)) * 20 + kb) * 4 + w) * 1024 + (qq * 16 + tk) * 16, swap_pair(a, c));
    }
    wait_vm<0>(); bar();
    STAMP(5);
    if (w == 0) { if (lane == 0) set_flag(ws, WS_FU + p * 128 + j * 4, tag); wait_flags8(ws, WS_FU + p * 128, tag, lane); }
    bar();
    STAMP(6); CSTAMP(44);
  }
  u32x2 x1p[5];
  {
    LANE_IDS
    f32x4 acc[5][2];
#pragma unroll
    for (int i = 0; i < 5; ++i) { acc[i][0] = (f32x4){0, 0, 0, 0}; acc[i][1] = (f32x4){0, 0, 0, 0}; }
    const unsigned wa = (unsigned)(tk * 128 + (((q + 4 * kh) ^ ((lane >> 1) & 7)) * 16));
    const unsigned xa = 10240 + (kh * 4 + 2 * mh) * 1024 + lane * 16;
    ksplit_loop<B_NS, 20, 5, 2>(
        [&](bf16x8(&A)[5], bf16x8(&Bf)[2], int slot, int) { LAS char* sb = smem + slot * B_SLOT;
          A[0] = *(LAS bf16x8*)(sb + wa); Bf[0] = *(LAS bf16x8*)(sb + xa); Bf[1] = *(LAS bf16x8*)(sb + xa + 1024);
#pragma unroll
          for (int i = 1; i < 5; ++i) A[i] = *(LAS bf16x8*)(sb + wa + i * 2048); },
        [&](bf16x8(&A)[5], bf16x8(&Bf)[2], bool) {
#pragma unroll
          for (int i = 0; i < 5; ++i) { acc[i][0] = mfma(A[i], Bf[0], acc[i][0]); acc[i][1] = mfma(A[i], Bf[1], acc[i][1]); } });
    STAMP(7); CSTAMP(45);
    f32x4 y[5];
    kreduce<5>(acc, y, smem + B_SCR, w, lane);
    float xs[20]; u32x2 xp[5];
    float s = 0.f;
#pragma unroll
    for (int i = 0; i < 5; ++i) {
      const f32x4 ga = *(const LAS f32x4*)(smem + T_MOD + (160 + 16 * i + 4 * q) * 4), c1b = *(const LAS f32x4*)(smem + T_MOD + 960 + (16 * i + 4 * q) * 4);
      x1p[i].x = pack2(bflo(xv[i].x) + ga[0] * y[i][0], bfhi(xv[i].x) + ga[1] * y[i][1]);
      x1p[i].y = pack2(bflo(xv[i].y) + ga[2] * y[i][2], bfhi(xv[i].y) + ga[3] * y[i][3]);
      asm volatile("" : "+v"(x1p[i].x), "+v"(x1p[i].y));
      xs[4 * i] = bflo(x1p[i].x); xs[4 * i + 1] = bfhi(x1p[i].x); xs[4 * i + 2] = bflo(x1p[i].y); xs[4 * i + 3] = bfhi(x1p[i].y);
      s += (xs[4 * i] + xs[4 * i + 1]) + (xs[4 * i + 2] + xs[4 * i + 3]);
      xp[i].x = pack2(xs[4 * i] * c1b[0], xs[4 * i + 1] * c1b[1]); xp[i].y = pack2(xs[4 * i + 2] * c1b[2], xs[4 * i + 3] * c1b[3]);
    }
    const unsigned img = WS_X1P + (unsigned)p * 20 * 4096 + w * 1024;
#pragma unroll
    for (int i = 0; i < 6; i += 2) {
      const int i2 = i + 1 < 5 ? i + 1 : i;
      const int dt = 5 * j + ((q & 1) ? i2 : i), kb = dt >> 1, qq = 2 * (dt & 1) + (q >> 1);
      st16(rws, img + kb * 4096 + (qq * 16 + tk) * 16, swap_pair(xp[i], xp[i2]));
    }
    const float mean = xsum_q(s) * (1.0f / 80.0f);
    float m2 = 0.f;
#pragma unroll
    for (int e = 0; e < 20; ++e) { const float d = xs[e] - mean; m2 += d * d; }
    m2 = xsum_q(m2);
    if (q == 0) __hip_atomic_store((gu64*)(ws + WS_ST1) + token * 8 + j, ((unsigned long long)__float_as_uint(m2) << 32) | __float_as_uint(mean), RLX_AGENT);
    wait_vm<0>(); bar();
    STAMP(8);
    if (w == 0) { if (lane == 0) set_flag(ws, WS_FX + p * 128 + j * 4, tag); wait_flags8(ws, WS_FX + p * 128, tag, lane); }
    bar();
    STAMP(9);
  }
  u32x2 z1p[7]; float bias;
  {
    LANE_IDS
    u32x4 st[4];
#pragma unroll
    for (int k = 0; k < 4; ++k) st[k] = ld16(rws, WS_ST1 + (unsigned)token * 64 + k * 16);
    bias = bflo(P.bsp[token]);
    for (int i = tid; i < 448; i += 256) ((LAS float*)(smem + T_CV))[i] = 0.f;
    f32x4 acc[7][2], aux[4];
    auto clear = [&]() {
#pragma unroll
      for (int i = 0; i < 7; ++i) { acc[i][0] = (f32x4){0, 0, 0, 0}; acc[i][1] = (f32x4){0, 0, 0, 0}; }
#pragma unroll
      for (int i = 0; i < 4; ++i) aux[i] = (f32x4){0, 0, 0, 0};
    };
    const unsigned xa = C_X + (kh * 4 + 2 * mh) * 1024 + lane * 16, wlo = C_W + kh * 7168 + lane * 16 + mh * 4096, whi = C_W + kh * 7168 + lane * 16 - mh * 3072;
    const int r4 = tk & 3;
    const unsigned ma = T_MODB + (r4 == 0 ? q * 16 + kh * 64 : r4 == 1 ? 1280 + q * 16 + kh * 64 : 2560), mstride = r4 < 2 ? 128 : 0;
    int nbase = 0;
    auto rd = [&](bf16x8(&A)[7], bf16x8(&Bf)[3], int slot, int n) {
      LAS char* sl = smem + wlo + slot * C_WSLOT; LAS char* sh = smem + whi + slot * C_WSLOT; LAS char* xb = smem + xa + n * 8192;
      Bf[0] = *(LAS bf16x8*)(xb); A[0] = *(LAS bf16x8*)(sl); Bf[1] = *(LAS bf16x8*)(xb + 1024);
      A[1] = *(LAS bf16x8*)(sl + 1024); A[2] = *(LAS bf16x8*)(sl + 2048);
#pragma unroll
      for (int i = 3; i < 7; ++i) A[i] = *(LAS bf16x8*)(sh + i * 1024);
      Bf[2] = *(LAS bf16x8*)(smem + ma + n * mstride);
    };
    auto auxmm = [&](bf16x8(&A)[7], bf16x8(&Bf)[3]) {
#pragma unroll
      for (int i = 0; i < 4; ++i) aux[i] = mfma(Bf[2], A[i], aux[i]);
    };
    auto mm2 = [&](bf16x8(&A)[7], bf16x8(&Bf)[3], bool) {
#pragma unroll
      for (int i = 0; i < 7; ++i) { acc[i][0] = mfma(Bf[0], A[i], acc[i][0]); acc[i][1] = mfma(Bf[1], A[i], acc[i][1]); }
      auxmm(A, Bf); };
    auto mm1 = [&](bf16x8(&A)[7], bf16x8(&Bf)[3], bool) {
#pragma unroll
      for (int i = 0; i < 7; ++i) { acc[i][0] = mfma(A[i], Bf[0], acc[i][0]); acc[i][1] = mfma(A[i], Bf[1], acc[i][1]); }
      auxmm(A, Bf); };
    auto put_cv = [&](int pass_) {
      LAS float* cv = (LAS float*)(smem + T_CV) + pass_ * 224;
      if (q == 0) {
#pragma unroll
        for (int i = 0; i < 4; ++i) if (mh == 0 || i < 3) { const int t = rot(i);
          __hip_atomic_fetch_add(cv + 16 * t + tk, aux[i][0], __ATOMIC_RELAXED, __HIP_MEMORY_SCOPE_WORKGROUP);
          __hip_atomic_fetch_add(cv + 112 + 16 * t + tk, aux[i][1], __ATOMIC_RELAXED, __HIP_MEMORY_SCOPE_WORKGROUP); }
      }
    };
    float rmean, rrstd;
    { float mu[8], mean = 0.f;
#pragma unroll
      for (int k = 0; k < 4; ++k) { mu[2 * k] = __uint_as_float(st[k].x); mu[2 * k + 1] = __uint_as_float(st[k].z); mean += mu[2 * k] + mu[2 * k + 1]; }
      mean *= 0.125f; float M2 = 0.f;
#pragma unroll
      for (int k = 0; k < 4; ++k) { const float d0 = mu[2 * k] - mean, d1 = mu[2 * k + 1] - mean; M2 += __uint_as_float(st[k].y) + __uint_as_float(st[k].w) + 80.0f * (d0 * d0 + d1 * d1); }
      rmean = mean; rrstd = 1.0f / sqrtf(M2 * (1.0f / D) + 1e-5f); }
    asm volatile("" : "+v"(rmean), "+v"(rrstd));
    (void)nbase;
    bf16x8 A0[7], B0[3], A1[7], B1[3];
    int slot = 0;
    auto nohook = [](int) {};
    clear();
    wait_lgkm0(); bar();
    STAMP(10); CSTAMP(46);
    rd(A0, B0, 0, 0);
    ksplit_steps<C_NS, 10, 7, 3, 18>(A0, B0, A1, B1, slot, 0, 8, rd, mm2, nohook, nohook);
    ksplit_last<C_NS, 10, 7, 3>(A0, B0, A1, B1, slot, rd, mm2);
    STAMP(11); CSTAMP(47);
    put_cv(0);
    f32x4 zs[7];
    kreduce<7>(acc, zs, smem + C_SCR, w, lane);
    wait_lgkm0(); bar();
    f32x2 mr01, mr23, rr01, rr23;
    mr01 = (f32x2){__shfl(rmean, 4 * q), __shfl(rmean, 4 * q + 1)}; mr23 = (f32x2){__shfl(rmean, 4 * q + 2), __shfl(rmean, 4 * q + 3)};
    rr01 = (f32x2){__shfl(rrstd, 4 * q), __shfl(rrstd, 4 * q + 1)}; rr23 = (f32x2){__shfl(rrstd, 4 * q + 2), __shfl(rrstd, 4 * q + 3)};
    f32x2 z01[7], z23[7];
#pragma unroll
    for (int i = 0; i < 7; ++i) {
      const float c1 = ((const LAS float*)(smem + T_CV))[16 * rot(i) + tk], c2 = ((const LAS float*)(smem + T_CV))[112 + 16 * rot(i) + tk];
      z01[i] = gelu2(rr01 * ((f32x2){zs[i][0], zs[i][1]} - mr01 * c1) + c2);
      z23[i] = gelu2(rr23 * ((f32x2){zs[i][2], zs[i][3]} - mr23 * c1) + c2);
    }
    {
      const float inv = 1.0f / (16.0f * nft);
      const f32x2 zero2 = (f32x2){0.f, 0.f};
      f32x2 s01 = zero2, s23 = zero2;
#pragma unroll
      for (int i = 0; i < 7; ++i) { const bool ok = (i != 2 && i != 6) || rot(i) < nft; s01 += ok ? z01[i] : zero2; s23 += ok ? z23[i] : zero2; }
      const f32x2 m01 = (f32x2){xsum_16(s01[0]), xsum_16(s01[1])} * inv, m23 = (f32x2){xsum_16(s23[0]), xsum_16(s23[1])} * inv;
      f32x2 t01 = zero2, t23 = zero2;
#pragma unroll
      for (int i = 0; i < 7; ++i) { const bool ok = (i != 2 && i != 6) || rot(i) < nft; const f32x2 a = ok ? z01[i] - m01 : zero2, c = ok ? z23[i] - m23 : zero2; t01 += a * a; t23 += c * c; }
      const float q0 = xsum_16(t01[0]), q1 = xsum_16(t01[1]), q2 = xsum_16(t23[0]), q3 = xsum_16(t23[1]);
      if (tk < 8) { const int r = tk & 3; const float a = r == 0 ? m01[0] : r == 1 ? m01[1] : r == 2 ? m23[0] : m23[1], c = r == 0 ? q0 : r == 1 ? q1 : r == 2 ? q2 : q3;
        const int tok = 64 * p + 16 * w + 4 * q + r;
        __hip_atomic_store((gu64*)(ws + WS_ST2) + (tok * 8 + j) * 2 + (tk >> 2), ((unsigned long long)(tag ^ __float_as_uint(tk < 4 ? c : a)) << 32) | __float_as_uint(tk < 4 ? a : c), RLX_AGENT); }
    }
    STAMP(12); CSTAMP(48);
    clear();
    slot = 0;
    bar();
    rd(A0, B0, 0, 0);
    ksplit_steps<C_NS, 10, 7, 3, 18>(A0, B0, A1, B1, slot, 0, 4, rd, mm1, nohook, nohook);
    STAMP(13);
    {
      u32x4 g0, g1;
      const unsigned go = WS_ST2 + (unsigned)(token * 8 + 2 * q) * 16;
      for (unsigned spins = 0;;) { g0 = ld16(rws, go); g1 = ld16(rws, go + 16);
        if (__all(g0.y == (tag ^ g0.z) && g0.w == (tag ^ g0.x) && g1.y == (tag ^ g1.z) && g1.w == (tag ^ g1.x))) break; if (spin_fail(spins, ws)) break; }
      const float n0 = 112.0f, n1 = (q & 1) ? 96.0f : 112.0f;
      const float m0 = __uint_as_float(g0.x), m1 = __uint_as_float(g1.x);
      const float mean = xsum_q(n0 * m0 + n1 * m1) * (1.0f / FH);
      const float d0 = m0 - mean, d1 = m1 - mean;
      const float M2 = xsum_q(__uint_as_float(g0.z) + __uint_as_float(g1.z) + n0 * d0 * d0 + n1 * d1 * d1);
      const float rstd = 1.0f / sqrtf(M2 * (1.0f / FH) + 1e-5f);
      mr01 = (f32x2){__shfl(mean, 4 * q), __shfl(mean, 4 * q + 1)}; mr23 = (f32x2){__shfl(mean, 4 * q + 2), __shfl(mean, 4 * q + 3)};
      rr01 = (f32x2){__shfl(rstd, 4 * q), __shfl(rstd, 4 * q + 1)}; rr23 = (f32x2){__shfl(rstd, 4 * q + 2), __shfl(rstd, 4 * q + 3)};
    }
    {
      u32x2 pk[7];
#pragma unroll
      for (int i = 0; i < 7; ++i) { const f32x2 a = (z01[i] - mr01) * rr01, c = (z23[i] - mr23) * rr23; pk[i].x = pack2(a[0], a[1]); pk[i].y = pack2(c[0], c[1]); }
      const unsigned img = WS_Z2N + (unsigned)(2 * p + (w >> 1)) * 55 * 1024 + ((2 * (w & 1) + (q >> 1)) * 16 + tk) * 16;
#pragma unroll
      for (int i = 0; i < 8; i += 2) {
        const int i2 = i + 1 < 7 ? i + 1 : i;
        const u32x4 ch = swap_pair(pk[i], pk[i2]);
        const int ti = (q & 1) ? rot(i2) : rot(i);
        st16(rws, ti < nft ? img + (ft0 + ti) * 1024 : 0xfffff000u, ch);
      }
    }
    STAMP(14);
    ksplit_steps<C_NS, 10, 7, 3, 18>(A0, B0, A1, B1, slot, 4, 8, rd, mm1,
        [&](int n) { if (n == 5) wait_vm<0>(); },
        [&](int n) { if (n == 5 && w == 0 && lane == 0) set_flag(ws, WS_FZ + (j * 4 + (p >> 3)) * 128 + (p & 7) * 4, tag); });
    ksplit_last<C_NS, 10, 7, 3>(A0, B0, A1, B1, slot, rd, mm1);
    STAMP(15); CSTAMP(49);
    const unsigned fzo = WS_FZ + (j * 4 + ((lane & 7) >> 1)) * 128 + (lane & 1) * 16;
    const u32x4 fz = ld16(rws, fzo);
    put_cv(1);
    kreduce<7>(acc, zs, smem + C_SCR, w, lane);
    if (!flags_ok(fz, tag)) { for (unsigned spins = 0;;) { if (flags_ok(ld16(rws, fzo), tag)) break; if (spin_fail(spins, ws)) break; } }
    wait_lgkm0(); bar();
#pragma unroll
    for (int i = 0; i < 7; ++i) {
      const LAS float* cv = (const LAS float*)(smem + T_CV) + 224;
      const f32x4 c1 = *(const LAS f32x4*)(cv + 16 * rot(i) + 4 * q), c2 = *(const LAS f32x4*)(cv + 112 + 16 * rot(i) + 4 * q);
      const f32x2 a = gelu2(rrstd * ((f32x2){zs[i][0], zs[i][1]} - rmean * (f32x2){c1[0], c1[1]}) + (f32x2){c2[0], c2[1]});
      const f32x2 c = gelu2(rrstd * ((f32x2){zs[i][2], zs[i][3]} - rmean * (f32x2){c1[2], c1[3]}) + (f32x2){c2[2], c2[3]});
      z1p[i] = (u32x2){pack2(a[0], a[1]), pack2(c[0], c[1])};
      asm volatile("" : "+v"(z1p[i].x), "+v"(z1p[i].y));
    }
    STAMP(16); CSTAMP(42);
  }
  {
    LANE_IDS
    f32x4 acc[7][2];
#pragma unroll
    for (int i = 0; i < 7; ++i) { acc[i][0] = (f32x4){0, 0, 0, 0}; acc[i][1] = (f32x4){0, 0, 0, 0}; }
    const unsigned wa = (unsigned)((2 * mh) * 2048 + tk * 128 + (((q + 4 * kh) ^ ((lane >> 1) & 7)) * 16));
    const unsigned xlo = 8192 + kh * 7168 + lane * 16 + mh * 4096, xhi = 8192 + kh * 7168 + lane * 16 - mh * 3072;
    ksplit_loop<D_NS, 32, 7, 2>(
        [&](bf16x8(&A)[7], bf16x8(&Bf)[2], int slot, int) { LAS char* sb = smem + slot * D_SLOT;
          Bf[0] = *(LAS bf16x8*)(sb + wa); A[0] = *(LAS bf16x8*)(sb + xlo); Bf[1] = *(LAS bf16x8*)(sb + wa + 2048);
          A[1] = *(LAS bf16x8*)(sb + xlo + 1024); A[2] = *(LAS bf16x8*)(sb + xlo + 2048);
#pragma unroll
          for (int i = 3; i < 7; ++i) A[i] = *(LAS bf16x8*)(sb + xhi + i * 1024); },
        [&](bf16x8(&A)[7], bf16x8(&Bf)[2], bool) {
#pragma unroll
          for (int i = 0; i < 7; ++i) { acc[i][0] = mfma(A[i], Bf[0], acc[i][0]); acc[i][1] = mfma(A[i], Bf[1], acc[i][1]); } });
    STAMP(17); CSTAMP(43);
    f32x4 g[7];
    kreduce<7>(acc, g, smem + D_SCR, w, lane);
    u32x2 pk[7];
#pragma unroll
    for (int i = 0; i < 7; ++i) {
      pk[i].x = pack2(bflo(z1p[i].x) * (g[i][0] + bias), bfhi(z1p[i].x) * (g[i][1] + bias));
      pk[i].y = pack2(bflo(z1p[i].y) * (g[i][2] + bias), bfhi(z1p[i].y) * (g[i][3] + bias));
    }
    const unsigned img = WS_P + (unsigned)p * 28 * 4096 + w * 1024;
#pragma unroll
    for (int i = 0; i < 8; i += 2) {
      const int i2 = i + 1 < 7 ? i + 1 : i;
      const u32x4 ch = swap_pair(pk[i], pk[i2]);
      const int ti = (q & 1) ? rot(i2) : rot(i), ft = ft0 + ti, kb = ft >> 1, qq = 2 * (ft & 1) + (q >> 1);
      st16(rws, ti < nft ? img + kb * 4096 + (qq * 16 + tk) * 16 : 0xfffff000u, ch);
    }
    wait_vm<0>(); bar();
    STAMP(18);
    if (w == 0) { if (lane == 0) set_flag(ws, WS_FP + p * 128 + j * 4, tag); wait_flags8(ws, WS_FP + p * 128, tag, lane); }
    bar();
    STAMP(19);
  }
  {
    LANE_IDS
    f32x4 acc[5][2];
#pragma unroll
    for (int i = 0; i < 5; ++i) { acc[i][0] = (f32x4){0, 0, 0, 0}; acc[i][1] = (f32x4){0, 0, 0, 0}; }
    const unsigned wa = kh * 5120 + lane * 16, xa = 10240 + (kh * 4 + 2 * mh) * 1024 + lane * 16;
    ksplit_loop<E_NS, 14, 5, 2>(
        [&](bf16x8(&A)[5], bf16x8(&Bf)[2], int slot, int) { LAS char* sb = smem + slot * E_SLOT;
          A[0] = *(LAS bf16x8*)(sb + wa); Bf[0] = *(LAS bf16x8*)(sb + xa); Bf[1] = *(LAS bf16x8*)(sb + xa + 1024);
#pragma unroll
          for (int i = 1; i < 5; ++i) A[i] = *(LAS bf16x8*)(sb + wa + i * 1024); },
        [&](bf16x8(&A)[5], bf16x8(&Bf)[2], bool last) {
          if (!(last && kh)) {
#pragma unroll
            for (int i = 0; i < 5; ++i) { acc[i][0] = mfma(A[i], Bf[0], acc[i][0]); acc[i][1] = mfma(A[i], Bf[1], acc[i][1]); } } });
    STAMP(20);
    f32x4 o[5];
    kreduce<5>(acc, o, smem + E_SCR, w, lane);
    u32x2 pk[5];
#pragma unroll
    for (int i = 0; i < 5; ++i) {
      const f32x4 gb = *(const LAS f32x4*)(smem + T_MOD + 960 + (80 + 16 * i + 4 * q) * 4);
      pk[i].x = pack2(bflo(x1p[i].x) + gb[0] * o[i][0], bfhi(x1p[i].x) + gb[1] * o[i][1]);
      pk[i].y = pack2(bflo(x1p[i].y) + gb[2] * o[i][2], bfhi(x1p[i].y) + gb[3] * o[i][3]);
    }
    bf16_t* orow = P.out + (size_t)token * D + 80 * j;
#pragma unroll
    for (int i = 0; i < 6; i += 2) {
      const int i2 = i + 1 < 5 ? i + 1 : i;
      const u32x4 ch = swap_pair(pk[i], pk[i2]);
      *(u32x4*)(orow + 16 * ((q & 1) ? i2 : i) + 8 * (q >> 1)) = ch;
    }
    STAMP(21);
  }
  STAMP_FLUSH();
}

__global__ __launch_bounds__(NT, 2) void fused_block(Params P) {
  extern __shared__ __attribute__((aligned(16))) char smem_[];
  LAS char* smem = (LAS char*)smem_;
  const int tid = threadIdx.x, lane = tid & 63, wid = __builtin_amdgcn_readfirstlane(tid >> 6), b = blockIdx.x;
  const int xcd = b & 7, idx = b >> 3, p = xcd * 4 + (idx >> 3), j = idx & 7;
  if (wid >= 4) loader_main(P, (unsigned)(size_t)smem, smem, wid - 4, lane, b, p, j, JROT ? ((j + xcd) & 7) * 4 : xcd * 4);
  else consumer_main(P, smem, wid, lane, b, p, j);
}

extern "C" void kernel_launch(void* const* d_in, const int* in_sizes, int n_in, void* d_out, int out_size, void* d_ws, size_t ws_size, hipStream_t stream) {
  Params P;
  P.x = (const bf16_t*)d_in[0]; P.wsr = (const bf16_t*)d_in[1]; P.wsi = (const bf16_t*)d_in[2];
  P.wdr = (const bf16_t*)d_in[3]; P.wdi = (const bf16_t*)d_in[4]; P.win = (const bf16_t*)d_in[6];
  P.wsp = (const bf16_t*)d_in[7]; P.bsp = (const bf16_t*)d_in[8]; P.wout = (const bf16_t*)d_in[9];
  P.c = (const bf16_t*)d_in[10]; P.wmod = (const bf16_t*)d_in[11];
  P.out = (bf16_t*)d_out; P.ws = (char*)d_ws;
  (void)hipFuncSetAttribute((const void*)fused_block, hipFuncAttributeMaxDynamicSharedMemorySize, LDS_BYTES);
  fused_block<<<256, NT, LDS_BYTES, stream>>>(P);
}
```

```cpp
#include <hip/hip_runtime.h>
#include <stdint.h>
#include <type_traits>

typedef unsigned short bf16_t;
typedef short bf16x8 __attribute__((ext_vector_type(8)));
typedef short s16x4 __attribute__((ext_vector_type(4)));
typedef float f32x4 __attribute__((ext_vector_type(4)));
typedef float f32x2 __attribute__((ext_vector_type(2)));
typedef unsigned u32x4 __attribute__((ext_vector_type(4)));
typedef unsigned u32x2 __attribute__((ext_vector_type(2)));
typedef __bf16 bf2_t __attribute__((ext_vector_type(2)));
#define LAS __attribute__((address_space(3)))
#define GAS __attribute__((address_space(1)))
typedef GAS unsigned gu32;
typedef GAS unsigned long long gu64;
#define RLX_AGENT __ATOMIC_RELAXED, __HIP_MEMORY_SCOPE_AGENT
#define DEV __device__ __forceinline__

constexpr int S = 2048, D = 640, F = 1728, FH = 864;
constexpr int NT = 512;

constexpr unsigned WS_EPOCH = 0;
constexpr unsigned WS_TMO   = 256;
constexpr unsigned WS_FN = 4096, WS_F2 = 8192, WS_FU = 12288, WS_FX = 16384, WS_FP = 20480, WS_FZ = 24576;
constexpr unsigned WS_MOD  = 65536;
constexpr unsigned WS_MODB = WS_MOD + 16384;

constexpr unsigned WS_XN   = 131072;
constexpr unsigned WS_WIN  = WS_XN + 64 * 40 * 1024;
constexpr unsigned WS_WOUT = WS_WIN + 20 * 109 * 1024;
constexpr unsigned WS_UV   = WS_WOUT + 27 * 40 * 1024;
constexpr unsigned WS_X1P  = WS_UV + 32 * 2 * 20 * 4096;
constexpr unsigned WS_ST1  = WS_X1P + 32 * 20 * 4096;
constexpr unsigned WS_Z2N  = WS_ST1 + 2048 * 64;
constexpr unsigned WS_ST2  = WS_Z2N + 64 * 55 * 1024;
constexpr unsigned WS_P    = WS_ST2 + 2048 * 128;
constexpr unsigned WS_END  = WS_P + 32 * 28 * 4096;

struct Params {
  const bf16_t *x, *wsr, *wsi, *wdr, *wdi, *win, *wsp, *bsp, *wout, *c, *wmod;
  bf16_t* out;
  char* ws;
};

template <int N> DEV void wait_vm() { asm volatile("s_waitcnt vmcnt(%0)" ::"n"(N) : "memory"); }
DEV void wait_lgkm0() { asm volatile("s_waitcnt lgkmcnt(0)" ::: "memory"); }
DEV void bar() { asm volatile("" ::: "memory"); __builtin_amdgcn_s_barrier(); asm volatile("" ::: "memory"); }
DEV void sched_fence() { __builtin_amdgcn_sched_barrier(0); }
template <int I, int N, class Fn> DEV void static_for(Fn&& f) { if constexpr (I < N) { f(std::integral_constant<int, I>{}); static_for<I + 1, N>(f); } }

DEV float bflo(unsigned v) { return __uint_as_float(v << 16); }
DEV float bfhi(unsigned v) { return __uint_as_float(v & 0xffff0000u); }
DEV unsigned pack2(float lo, float hi) { bf2_t v; v[0] = (__bf16)lo; v[1] = (__bf16)hi; return __builtin_bit_cast(unsigned, v); }
DEV float rbf(float f) { return bflo(pack2(f, f)); }
DEV f32x4 mfma(bf16x8 a, bf16x8 b, f32x4 c) { return __builtin_amdgcn_mfma_f32_16x16x32_bf16(a, b, c, 0, 0, 0); }

DEV u32x4 mkrs(const void* p, unsigned bytes) {
  const unsigned long long a = (unsigned long long)p;
  u32x4 r; r.x = (unsigned)a; r.y = (unsigned)(a >> 32) & 0xffffu; r.z = bytes; r.w = 0x00020000u; return r;
}
DEV __amdgpu_buffer_rsrc_t mkrsrc(const void* p, unsigned bytes) { return __builtin_amdgcn_make_buffer_rsrc((void*)p, 0, bytes, 0x00020000); }
#define LD_P(v, r, off, sc) "buffer_load_dwordx4 " v ", " r ", %0 offen offset:" off sc " lds\n\t"
#define LD_H(v, r, off, sc) "s_mov_b32 exec_hi, 0\n\tbuffer_load_dwordx4 " v ", " r ", %0 offen offset:" off sc " lds\n\ts_mov_b32 exec_hi, -1"
#define HD_OR  "s_add_u32 m0, %1, %2\n\ts_or_b32 %0, %3, %4\n\t"
#define HD_MAD(M) "s_mul_i32 %0, %3, " M "\n\ts_add_u32 m0, %1, %2\n\ts_add_u32 %0, %0, %4\n\t"
#define DMA_IN(IMM) "s"(lds), "n"(IMM), "s"(a), "s"(b)
template <int IMM> DEV void dma_w4_or(unsigned lds, unsigned a, unsigned b, u32x4 ra, u32x4 rb, unsigned v0, unsigned v1, unsigned v2, unsigned v3) { unsigned t;
  asm volatile(HD_OR LD_P("%7", "%5", "0", "") LD_P("%8", "%5", "1024", "") LD_P("%9", "%6", "2048", "") LD_P("%10", "%6", "3072", "")
               : "=&s"(t) : DMA_IN(IMM), "s"(ra), "s"(rb), "v"(v0), "v"(v1), "v"(v2), "v"(v3) : "memory", "scc"); }
template <int IMM> DEV void dma_w2_or(unsigned lds, unsigned a, unsigned b, u32x4 r, unsigned v0, unsigned v1) { unsigned t;
  asm volatile(HD_OR LD_P("%6", "%5", "0", "") LD_P("%7", "%5", "1024", "") : "=&s"(t) : DMA_IN(IMM), "s"(r), "v"(v0), "v"(v1) : "memory", "scc"); }
template <int IMM, int SOFF> DEV void dma_w2h(unsigned lds, unsigned a, u32x4 r, unsigned v0, unsigned v1, unsigned v2) { unsigned t;
  asm volatile("s_add_u32 m0, %1, %2\n\ts_add_u32 %0, %3, %4\n\t" LD_P("%6", "%5", "0", "") LD_P("%7", "%5", "1024", "") LD_H("%8", "%5", "2048", "")
               : "=&s"(t) : "s"(lds), "n"(IMM), "s"(a), "n"(SOFF), "s"(r), "v"(v0), "v"(v1), "v"(v2) : "memory", "scc"); }
template <int IMM, int SOFF> DEV void dma_x2(unsigned lds, unsigned a, u32x4 r, unsigned v) { unsigned t;
  asm volatile("s_add_u32 m0, %1, %2\n\ts_add_u32 %0, %3, %4\n\t" LD_P("%6", "%5", "0", " sc1") LD_P("%6", "%5", "1024", " sc1") : "=&s"(t) : "s"(lds), "n"(IMM), "s"(a), "n"(SOFF), "s"(r), "v"(v) : "memory", "scc"); }
template <int IMM, int SOFF> DEV void dma_x2h(unsigned lds, unsigned a, u32x4 r, unsigned v) { unsigned t;
  asm volatile("s_add_u32 m0, %1, %2\n\ts_add_u32 %0, %3, %4\n\t" LD_P("%6", "%5", "0", " sc1") LD_P("%6", "%5", "1024", " sc1") LD_H("%6", "%5", "2048", " sc1") : "=&s"(t) : "s"(lds), "n"(IMM), "s"(a), "n"(SOFF), "s"(r), "v"(v) : "memory", "scc"); }
template <int IMM, int SOFF> DEV void dma_x3h(unsigned lds, unsigned a, u32x4 r, unsigned v) { unsigned t;
  asm volatile("s_add_u32 m0, %1, %2\n\ts_add_u32 %0, %3, %4\n\t" LD_P("%6", "%5", "0", " sc1") LD_P("%6", "%5", "1024", " sc1") LD_P("%6", "%5", "2048", " sc1") LD_H("%6", "%5", "3072", " sc1") : "=&s"(t) : "s"(lds), "n"(IMM), "s"(a), "n"(SOFF), "s"(r), "v"(v) : "memory", "scc"); }
template <int IMM> DEV void dma_x2h_mad640(unsigned lds, unsigned a, unsigned b, u32x4 r, unsigned v) { unsigned t;
  asm volatile(HD_MAD("640") LD_P("%6", "%5", "0", " sc1") LD_P("%6", "%5", "1024", " sc1") LD_H("%6", "%5", "2048", " sc1") : "=&s"(t) : DMA_IN(IMM), "s"(r), "v"(v) : "memory", "scc"); }
template <int IMM> DEV void dma_x3h_mad880(unsigned lds, unsigned a, unsigned b, u32x4 r, unsigned v) { unsigned t;
  asm volatile(HD_MAD("880") LD_P("%6", "%5", "0", " sc1") LD_P("%6", "%5", "1024", " sc1") LD_P("%6", "%5", "2048", " sc1") LD_H("%6", "%5", "3072", " sc1") : "=&s"(t) : DMA_IN(IMM), "s"(r), "v"(v) : "memory", "scc"); }
DEV void dma_1(unsigned lds, u32x4 r, unsigned v) { asm volatile("s_mov_b32 m0, %0\n\ts_nop 0\n\tbuffer_load_dwordx4 %2, %1, 0 offen sc1 lds" ::"s"(lds), "s"(r), "v"(v) : "memory"); }
template <int N> DEV void wait_bar() { asm volatile("s_waitcnt vmcnt(%0)\n\ts_barrier" ::"n"(N) : "memory"); }
DEV void warm(unsigned lds, u32x4 r, unsigned v) { asm volatile("s_mov_b32 m0, %0\n\ts_nop 0\n\tbuffer_load_dword %2, %1, 0 offen lds" ::"s"(lds), "s"(r), "v"(v) : "memory"); }
constexpr unsigned WBIAS = 4096;
DEV u32x4 mkrs_b(const void* p, unsigned bytes) { return mkrs((const char*)p - WBIAS, bytes + WBIAS); }
constexpr int SC1 = 16;
DEV void st16(__amdgpu_buffer_rsrc_t r, unsigned off, u32x4 v) { __builtin_amdgcn_raw_buffer_store_b128(v, r, off, 0, SC1); }
DEV u32x4 ld16(__amdgpu_buffer_rsrc_t r, unsigned off) { return __builtin_amdgcn_raw_buffer_load_b128(r, off, 0, SC1); }
DEV f32x4 ld16f(__amdgpu_buffer_rsrc_t r, unsigned off) { return __builtin_bit_cast(f32x4, ld16(r, off)); }

template <int CTRL> DEV float dpp_add(float v) { return v + __builtin_bit_cast(float, __builtin_amdgcn_update_dpp(0, __builtin_bit_cast(int, v), CTRL, 0xf, 0xf, true)); }
DEV float xsum_16(float v) { v = dpp_add<0xB1>(v); v = dpp_add<0x4E>(v); v = dpp_add<0x141>(v); return dpp_add<0x140>(v); }
DEV float xsum_r16(float v) { auto r = __builtin_amdgcn_permlane16_swap(__float_as_uint(v), __float_as_uint(v), false, false); return __uint_as_float(r[0]) + __uint_as_float(r[1]); }
DEV float xsum_r32(float v) { auto r = __builtin_amdgcn_permlane32_swap(__float_as_uint(v), __float_as_uint(v), false, false); return __uint_as_float(r[0]) + __uint_as_float(r[1]); }
DEV float xsum_q(float v) { return xsum_r32(xsum_r16(v)); }
DEV f32x2 gelu2(f32x2 x) {
  const float k1 = -2.0f * 0.7978845608028654f * 1.4426950408889634f;
  const f32x2 t = x * ((x * x) * (0.044715f * k1) + k1);
  const f32x2 d = (f32x2){__builtin_amdgcn_exp2f(t[0]), __builtin_amdgcn_exp2f(t[1])} + 1.0f;
  return x * (f32x2){__builtin_amdgcn_rcpf(d[0]), __builtin_amdgcn_rcpf(d[1])};
}
DEV u32x4 swap_pair(u32x2 a, u32x2 b) {
  auto r0 = __builtin_amdgcn_permlane16_swap(a.x, b.x, false, false);
  auto r1 = __builtin_amdgcn_permlane16_swap(a.y, b.y, false, false);
  return (u32x4){r0[0], r1[0], r0[1], r1[1]};
}

DEV bool spin_fail(unsigned& spins, char* ws) {
  __builtin_amdgcn_s_sleep(1);
  if (++spins > (1u << 17)) { __hip_atomic_store((gu32*)(ws + WS_TMO), 1u, RLX_AGENT); return true; }
  return false;
}
DEV void set_flag(char* ws, unsigned off, unsigned tag) { __hip_atomic_store((gu32*)(ws + off), tag, RLX_AGENT); }
DEV void wait_flags8(char* ws, unsigned off, unsigned tag, int lane) {
  for (unsigned spins = 0;;) {
    const unsigned v = lane < 8 ? __hip_atomic_load((gu32*)(ws + off) + lane, RLX_AGENT) : tag;
    if (__all(v == tag)) break;
    if (spin_fail(spins, ws)) break;
  }
}
DEV bool flags_ok(u32x4 v, unsigned tag) { return __all(v.x == tag && v.y == tag && v.z == tag && v.w == tag); }
DEV u32x4 peek256(__amdgpu_buffer_rsrc_t rf, unsigned off, int lane) { return ld16(rf, off + (lane >> 1) * 128 + (lane & 1) * 16); }
DEV void wait256(__amdgpu_buffer_rsrc_t rf, char* ws, unsigned off, unsigned tag, int lane) {
  for (unsigned spins = 0;;) { if (flags_ok(peek256(rf, off, lane), tag)) break; if (spin_fail(spins, ws)) break; }
}

template <int NS, int PF, int S_, int CW, int CX, class IW, class IX>
DEV void loader_run(IW&& issueW, IX&& issueX) {
  constexpr auto span = [=](int a, int b) { int c = 0; for (int s = a; s <= b; ++s) c += (s >= PF ? CW : 0) + CX; return c; };
  auto step = [&](auto ic) { if constexpr (decltype(ic)::value >= PF) issueW(ic); issueX(ic); };
  static_for<0, NS>(step);
  wait_bar<span(1, NS - 1)>();
  static_for<0, S_ - 1>([&](auto ic) { constexpr int n = decltype(ic)::value;
    constexpr int last = n + NS - 1 < S_ - 1 ? n + NS - 1 : S_ - 1;
    wait_bar<span(n + 2, last)>();
    if constexpr (n + NS < S_) step(std::integral_constant<int, n + NS>{}); });
}

template <int NS, int S_, int NA, int NB, int NM, class Rd, class Mm, class Hook, class Post>
DEV void ksplit_steps(bf16x8 (&A0)[NA], bf16x8 (&B0)[NB], bf16x8 (&A1)[NA], bf16x8 (&B1)[NB], int& slot, int n0, int n1, Rd&& rd, Mm&& mm, Hook&& hook, Post&& post) {
  constexpr int NR = NA + NB;
#define KS_ILV() do { __builtin_amdgcn_sched_group_barrier(0x8, 1, 0); __builtin_amdgcn_sched_group_barrier(0x2, 4, 0); __builtin_amdgcn_sched_group_barrier(0x100, 1, 0); \
    _Pragma("unroll") for (int z = 1; z < NR; ++z) { __builtin_amdgcn_sched_group_barrier(0x8, 1, 0); __builtin_amdgcn_sched_group_barrier(0x100, 1, 0); } __builtin_amdgcn_sched_group_barrier(0x8, NM - NR, 0); } while (0)
#pragma unroll 1
  for (int n = n0; n < n1; n += 2) {
    int s1 = slot + 1 == NS ? 0 : slot + 1, s2 = s1 + 1 == NS ? 0 : s1 + 1;
    asm volatile("" : "+s"(s1), "+s"(s2));
    hook(n); wait_lgkm0(); bar(); post(n); sched_fence();
    rd(A1, B1, s1, n + 1); mm(A0, B0, false); KS_ILV(); sched_fence();
    hook(n + 1); wait_lgkm0(); bar(); post(n + 1); sched_fence();
    rd(A0, B0, s2, n + 2); mm(A1, B1, false); KS_ILV(); sched_fence();
    slot = s2;
  }
}
template <int NS, int S_, int NA, int NB, class Rd, class Mm>
DEV void ksplit_last(bf16x8 (&A0)[NA], bf16x8 (&B0)[NB], bf16x8 (&A1)[NA], bf16x8 (&B1)[NB], int& slot, Rd&& rd, Mm&& mm) {
  const int s1 = slot + 1 == NS ? 0 : slot + 1;
  wait_lgkm0(); bar(); sched_fence();
  rd(A1, B1, s1, S_ - 1); mm(A0, B0, false); mm(A1, B1, true);
  slot = s1;
}
template <int NS, int S_, int NA, int NB, class Rd, class Mm>
DEV void ksplit_loop(Rd&& rd, Mm&& mm) {
  static_assert((S_ & 1) == 0, "even step count");
  bf16x8 A0[NA], B0[NB], A1[NA], B1[NB];
  bar();
  rd(A0, B0, 0, 0);
  int slot = 0;
  ksplit_steps<NS, S_, NA, NB, NA * NB>(A0, B0, A1, B1, slot, 0, S_ - 2, rd, mm, [](int) {}, [](int) {});
  ksplit_last<NS, S_, NA, NB>(A0, B0, A1, B1, slot, rd, mm);
}
template <int N>
DEV void kreduce(f32x4 (&acc)[N][2], f32x4 (&res)[N], LAS char* scratch, int w, int lane) {
  const int kh = w & 1;
  LAS char* mine = scratch + w * (N * 1024) + lane * 16;
  LAS char* other = scratch + (w ^ 1) * (N * 1024) + lane * 16;
  if (kh) { asm volatile("");
#pragma unroll
    for (int i = 0; i < N; ++i) { *(LAS f32x4*)(mine + i * 1024) = acc[i][0]; res[i] = acc[i][1]; }
  } else { asm volatile("");
#pragma unroll
    for (int i = 0; i < N; ++i) { *(LAS f32x4*)(mine + i * 1024) = acc[i][1]; res[i] = acc[i][0]; }
  }
  wait_lgkm0(); bar();
#pragma unroll
  for (int i = 0; i < N; ++i) res[i] += *(LAS f32x4*)(other + i * 1024);
}

constexpr int A_PRE = 2;
constexpr int PFW = 6, PFC = 5, PRE1 = 2;
constexpr int LPRIO = 0, CPRIO = 2;
constexpr int A_NS = 5, A_SLOT = 26 * 1024, A_SCR = 52 * 1024;
constexpr int P0_T = 130 * 1024, P0_PT = 140 * 1024, P0_RED = 157 * 1024;
constexpr int B_NS = 6, B_SLOT = 18 * 1024, B_SCR = 36 * 1024;
constexpr int C_NS = 5, C_X = 0, C_W = 82 * 1024, C_WSLOT = 14 * 1024;
constexpr int C_SCR = C_W + 2 * C_WSLOT;
constexpr int D_NS = 5, D_SLOT = 22 * 1024, D_SCR = 110 * 1024;
constexpr int E_NS = 6, E_SLOT = 18 * 1024, E_SCR = 110 * 1024;
constexpr int T_MODB = 152 * 1024;
constexpr int T_CV = T_MODB + 3072 + 64;
constexpr int T_MOD = 158 * 1024;
constexpr int T_DUMMY = T_MOD + 1792;
constexpr int LDS_BYTES = 160 * 1024;
static_assert(T_CV + 1792 <= P0_RED && P0_RED + 256 <= T_MOD && C_W + C_NS * C_WSLOT <= T_MODB && A_NS * A_SLOT <= P0_RED && D_NS * D_SLOT <= T_CV && B_NS * B_SLOT <= T_MODB && E_NS * E_SLOT <= T_MODB, "LDS map");

#define IC(n) std::integral_constant<int, (n)>{}
DEV void loader_main(const Params& P, const unsigned lds0, LAS char* smem, const int lw, const int lane_, const int b, const int p, const int j) {
  char* const ws = P.ws;
  const int ft0 = 27 * (j >> 2) + 7 * (j & 3);
#define LLANE int lane = lane_; asm volatile("" : "+v"(lane)); const unsigned vx = lane * 16; (void)vx;
#define LV4K const unsigned v4k0 = (unsigned)(lane >> 3) * 4096u + (unsigned)(((lane & 7) ^ (lane >> 4)) * 16), v4k1 = (unsigned)(8 + (lane >> 3)) * 4096u + (unsigned)(((lane & 7) ^ (4 + (lane >> 4))) * 16);
  {
    LLANE LV4K
    const u32x4 rre = mkrs_b(P.wsr, S * S * 2), rim = mkrs_b(P.wsi, S * S * 2), rx = mkrs(ws + WS_XN, S * D * 2);
    const unsigned wrow = (unsigned)(64 * p + 16 * lw) * 4096u;
    const unsigned w0 = v4k0 + WBIAS, w1 = v4k1 + WBIAS - 1024, w2 = v4k0 + WBIAS - 2048, w3 = v4k1 + WBIAS - 3072;
    const unsigned lw_w = lds0 + lw * 4096, lw_x = lds0 + 16384 + lw * 2560;
    const unsigned xo = (unsigned)(((lw >> 1) * 40 + 5 * j) * 1024 + (lw & 1) * 2560);
    const unsigned rot = (unsigned)j * 512, xrot = (unsigned)(b & 7) * 512;
    auto kof = [&](auto ic) { constexpr int n = decltype(ic)::value;
      const unsigned k = n < 4 ? rot + n * 128 : (n - 4) * 128 + ((unsigned)((n - 4) * 128) >= rot ? 512u : 0u); return (k + xrot) & 0xf80u; };
    auto issueW = [&](auto ic) { dma_w4_or<(decltype(ic)::value % A_NS) * A_SLOT>(lw_w, kof(ic), wrow, rre, rim, w0, w1, w2, w3); };
    auto issueX = [&](auto ic) { dma_x2h_mad640<(decltype(ic)::value % A_NS) * A_SLOT>(lw_x, kof(ic), xo, rx, vx); };
    static_for<0, A_PRE>(issueW);
    bar();
    bar();
    const int idx = b >> 3, unit0 = (idx & 3) + 4 * ((b & 7) + 8 * (idx >> 2));
    const bool domod = unit0 < 240; const int unit = domod ? unit0 : 0;
    {
      u32x4 wm[5]; unsigned cs[5];
#pragma unroll
      for (int i = 0; i < 5; ++i) { const int row = lw * 160 + i * 32 + (lane >> 1);
        wm[i] = *(const u32x4*)(P.wmod + (size_t)row * (6 * D) + unit * 16 + (lane & 1) * 8); cs[i] = P.c[row]; }
      float a[8] = {0, 0, 0, 0, 0, 0, 0, 0};
#pragma unroll
      for (int i = 0; i < 5; ++i) { const float x = bflo(cs[i]); const float s = rbf(x * __builtin_amdgcn_rcpf(1.0f + __expf(-x)));
        a[0] += s * bflo(wm[i].x); a[1] += s * bfhi(wm[i].x); a[2] += s * bflo(wm[i].y); a[3] += s * bfhi(wm[i].y);
        a[4] += s * bflo(wm[i].z); a[5] += s * bfhi(wm[i].z); a[6] += s * bflo(wm[i].w); a[7] += s * bfhi(wm[i].w); }
#pragma unroll
      for (int e = 0; e < 8; ++e) { float v = dpp_add<0x4E>(a[e]);
        v = dpp_add<0x124>(v); v = dpp_add<0x128>(v);
        a[e] = xsum_q(v); }
      if (lane < 2) {
#pragma unroll
        for (int e = 0; e < 8; ++e) ((LAS float*)(smem + P0_RED))[lw * 16 + lane * 8 + e] = a[e];
      }
      wait_lgkm0();
    }
    bar();
    __builtin_amdgcn_s_setprio(LPRIO);
    auto istep = [&](auto ic) { if constexpr (decltype(ic)::value >= A_PRE) issueW(ic); issueX(ic); };
#define A_CNT(s_) (((s_) >= A_PRE ? 4 : 0) + 3)
    static_assert((A_NS == 5 || A_NS == 6) && A_PRE <= A_NS, "start-up schedule");
    istep(IC(0)); istep(IC(1));
    wait_bar<A_CNT(1)>();
    istep(IC(2)); istep(IC(3));
    if (lw == 0 && domod && lane < 16) {
      const LAS float* red = (const LAS float*)(smem + P0_RED);
      float s = rbf((red[lane] + red[16 + lane]) + (red[32 + lane] + red[48 + lane]));
      const int n = unit * 16 + lane, seg = n / D;
      if (seg == 1 || seg == 4) s = rbf(1.0f + s);
      __hip_atomic_store((gu32*)(ws + WS_MOD) + n, __float_as_uint(s), RLX_AGENT);
      const unsigned hi = __float_as_uint(s) >> 16, nb = (unsigned)__builtin_amdgcn_update_dpp(0, (int)hi, 0xB1, 0xf, 0xf, true);
      if ((seg == 3 || seg == 4) && !(lane & 1)) __hip_atomic_store((gu32*)(ws + WS_MODB) + (((seg == 4 ? 0 : D) + n - seg * D) >> 1), hi | (nb << 16), RLX_AGENT);
    }
    wait_bar<A_CNT(2) + A_CNT(3)>();
    static_for<4, A_NS + 1>(istep);
    wait_bar<A_CNT(3) + A_CNT(4) + A_CNT(5) + (A_NS == 6 ? A_CNT(6) : 0)>();
    const u32x4 rmod = mkrs(ws + WS_MOD, 3840 * 4);
    const int tl = lane / 20, tc = lane - 20 * tl;
    const unsigned tv0 = lane < 60 ? (unsigned)((tl * D + 80 * j) * 4 + tc * 16) : 0x7ffffff0u;
    const unsigned tv1 = lane < 40 ? (unsigned)(((4 + tl) * D + 80 * j) * 4 + tc * 16) : 0x7ffffff0u;
    istep(IC(A_NS + 1));
    static_for<2, 32 - A_NS - 0>([&](auto ic) { constexpr int n = decltype(ic)::value;
      constexpr int cnt = [] { int c = 0; for (int s_ = n + 2; s_ <= n + A_NS - 1; ++s_) c += A_CNT(s_); return c; }();
      wait_bar<cnt>(); if constexpr (n + A_NS < 32) istep(IC(n + A_NS));
      if constexpr (n == 20) { if (lw == 0) { dma_1(lds0 + T_MOD, rmod, tv0); dma_1(lds0 + T_MOD + 960, rmod, tv1); } }
    });
    {
      { const int row = (j * 4 + lw) * 2 + (lane >> 5); warm(lds0 + T_DUMMY, mkrs(P.wsp, S * S * 2), (unsigned)(64 * p + row) * 4096u + (lane & 31) * 128); }
      warm(lds0 + T_DUMMY, mkrs((lw & 1) ? P.wdi : P.wdr, D * D * 2), lw < 2 && lane < 25 ? (unsigned)(b * 25 + lane) * 128 : 0x7ffffff0u);
    }
    static_for<0, A_NS - 1>([&](auto ic) { constexpr int r = A_NS - 2 - decltype(ic)::value; wait_bar<r * 7 + 2>(); });
    bar();
  }
  {
    LLANE
    const u32x4 rre = mkrs_b(P.wdr, D * D * 2), rim = mkrs_b(P.wdi, D * D * 2), rx = mkrs(ws + WS_UV, 32 * 2 * 20 * 4096);
    auto wv = [&](int k) { const int r = 20 * lw + 8 * k + (lane >> 3); return (unsigned)(r * 1280 + (((lane & 7) ^ ((r >> 1) & 7)) * 16)) + WBIAS - 1024 * k; };
    const unsigned v0 = wv(0), v1 = wv(1), v2 = wv(2);
    const unsigned wb = (unsigned)(80 * j) * 1280u, lw_w = lds0 + lw * 2560, lw_x = lds0 + 10240 + lw * 2048, xb = (unsigned)p * 40 * 4096u + lw * 2048;
    auto issueW = [&](auto ic) { constexpr int n = decltype(ic)::value; dma_w2h<(n % B_NS) * B_SLOT, (n % 10) * 128>(lw_w, wb, n >= 10 ? rim : rre, v0, v1, v2); };
    auto issueX = [&](auto ic) { constexpr int n = decltype(ic)::value; dma_x2<(n % B_NS) * B_SLOT, 2 * n * 4096>(lw_x, xb, rx, vx); };
    __builtin_amdgcn_s_setprio(0);
    static_for<0, (PRE1 < PFW ? PRE1 : PFW)>(issueW);
    bar();
    static_for<(PRE1 < PFW ? PRE1 : PFW), PFW>(issueW);
    bar();
    __builtin_amdgcn_s_setprio(LPRIO);
    loader_run<B_NS, PFW, 20, 3, 2>(issueW, issueX);
    bar();
  }
  {
    LLANE
    const u32x4 rw = mkrs(ws + WS_WIN, 20 * 109 * 1024), rx = mkrs(ws + WS_X1P, 32 * 20 * 4096), rmb = mkrs(ws + WS_MODB, 2560);
    const unsigned wo = (unsigned)(((lw >> 1) * 109 + ft0) * 1024 + (lw & 1) * 3584);
    const unsigned lw_w = lds0 + C_W + lw * 3584, lw_x = lds0 + C_X + lw * 2048, xb = (unsigned)p * 20 * 4096u + lw * 2048;
    auto issueW = [&](auto ic) { constexpr int n = decltype(ic)::value, z1 = n >= 10; dma_x3h<(n % C_NS) * C_WSLOT, (2 * (n - 10 * z1) * 109 + (z1 ? 0 : 54)) * 1024>(lw_w, wo, rw, vx); };
    auto issueX = [&](auto ic) { constexpr int n = decltype(ic)::value; dma_x2<2 * n * 4096, 2 * n * 4096>(lw_x, xb, rx, vx); };
    __builtin_amdgcn_s_setprio(0);
    static_for<0, (PRE1 < PFC ? PRE1 : PFC)>(issueW);
    bar();
    if (lw < 3) dma_1(lds0 + T_MODB + lw * 1024, rmb, vx + lw * 1024);
    static_for<(PRE1 < PFC ? PRE1 : PFC), PFC>(issueW);
    bar();
    __builtin_amdgcn_s_setprio(LPRIO);
    static_for<0, C_NS>([&](auto ic) { if constexpr (decltype(ic)::value >= PFC) issueW(ic); issueX(ic); });
    wait_bar<(PFC - 1) * 2 + (C_NS - PFC) * 6>();
    static_for<0, 9>([&](auto ic) {
      constexpr int n = decltype(ic)::value, need = n + 1;
      constexpr int last = n + C_NS - 1 < 11 ? n + C_NS - 1 : 11;
      constexpr int cnt = [] { int c = 0; for (int s = need + 1; s <= last; ++s) c += (s < PFC ? 2 : s < 10 ? 6 : 4); return c; }();
      wait_bar<cnt>();
      if constexpr (n + C_NS < 12) { issueW(IC(n + C_NS)); if constexpr (n + C_NS < 10) issueX(IC(n + C_NS)); }
    });
    bar();
    bar();
    issueW(IC(12)); issueW(IC(13)); issueW(IC(14));
    wait_bar<16>();
    static_for<10, 19>([&](auto ic) {
      constexpr int n = decltype(ic)::value;
      constexpr int last = n + 4 < 19 ? n + 4 : 19;
      wait_bar<(last - (n + 1)) * 4>();
      if constexpr (n + C_NS < 20) issueW(IC(n + C_NS));
    });
    bar();
  }
  {
    LLANE LV4K
    const u32x4 rw = mkrs_b(P.wsp, S * S * 2), rx = mkrs(ws + WS_Z2N, 64 * 55 * 1024);
    const unsigned wrow = (unsigned)(64 * p + 16 * lw) * 4096u, w0 = v4k0 + WBIAS, w1 = v4k1 + WBIAS - 1024;
    const unsigned xo = (unsigned)(((lw >> 1) * 55 + ft0) * 1024 + (lw & 1) * 3584), lw_w = lds0 + lw * 2048, lw_x = lds0 + 8192 + lw * 3584;
    unsigned kw = (b & 7) * 512, kx = kw;
    auto issueW = [&](auto ic) { dma_w2_or<(decltype(ic)::value % D_NS) * D_SLOT>(lw_w, kw, wrow, rw, w0, w1); kw = (kw + 128) & 0xf80; };
    auto issueX = [&](auto ic) { dma_x3h_mad880<(decltype(ic)::value % D_NS) * D_SLOT>(lw_x, kx, xo, rx, vx); kx = (kx + 128) & 0xf80; };
    static_assert(D_NS * D_SLOT <= C_SCR, "ring below the exchange buffer");
    static_for<0, D_NS>(issueW);
    bar();
    static_for<0, D_NS>(issueX);
    wait_bar<(D_NS - 1) * 4>();
    static_for<0, 31>([&](auto ic) { constexpr int n = decltype(ic)::value;
      constexpr int last = n + D_NS - 1 < 31 ? n + D_NS - 1 : 31;
      constexpr int cnt = n + 1 < D_NS ? (D_NS - 2 - n) * 4 + (last - (D_NS - 1)) * 6 : (last - (n + 1)) * 6;
      wait_bar<cnt>();
      if constexpr (n + D_NS < 32) { issueW(IC(n + D_NS)); issueX(IC(n + D_NS)); } });
    bar();
  }
  {
    LLANE
    const u32x4 rw = mkrs(ws + WS_WOUT, 28 * 40 * 1024), rx = mkrs(ws + WS_P, 32 * 28 * 4096);
    const unsigned wo = (unsigned)(((lw >> 1) * 40 + 5 * j) * 1024 + (lw & 1) * 2560), lw_w = lds0 + lw * 2560, lw_x = lds0 + 10240 + lw * 2048, xb = (unsigned)p * 28 * 4096u + lw * 2048;
    auto issueW = [&](auto ic) { constexpr int n = decltype(ic)::value; dma_x2h<(n % E_NS) * E_SLOT, n * 80 * 1024>(lw_w, wo, rw, vx); };
    auto issueX = [&](auto ic) { constexpr int n = decltype(ic)::value; dma_x2<(n % E_NS) * E_SLOT, 2 * n * 4096>(lw_x, xb, rx, vx); };
    __builtin_amdgcn_s_setprio(0);
    static_for<0, (PRE1 < PFW ? PRE1 : PFW)>(issueW);
    bar();
    static_for<(PRE1 < PFW ? PRE1 : PFW), PFW>(issueW);
    bar();
    __builtin_amdgcn_s_setprio(LPRIO);
    loader_run<E_NS, PFW, 14, 3, 2>(issueW, issueX);
    bar();
  }
}

DEV void consumer_main(const Params& P, LAS char* smem, const int w, const int lane_, const int b, const int p, const int j) {
  __builtin_amdgcn_s_setprio(CPRIO);
  char* const ws = P.ws;
  unsigned tagv = __hip_atomic_load((gu32*)(ws + WS_EPOCH), RLX_AGENT) + 1u, tag;
  const int mh = w >> 1, kh = w & 1;
  const auto rws = mkrsrc(ws, WS_END);
  const int ft0 = 27 * (j >> 2) + 7 * (j & 3), nft = (j & 3) == 3 ? 6 : 7;
  auto rot = [&](int i) { return i < 3 ? i + 4 * mh : i - 3 * mh; };
#define LANE_IDS int lane = lane_; asm volatile("" : "+v"(lane)); const int q = lane >> 4, tk = lane & 15, tid = w * 64 + lane, token = 64 * p + 16 * w + tk; (void)q; (void)tk; (void)tid; (void)token;
  {
    LANE_IDS
    unsigned v[2][5];
#pragma unroll
    for (int t = 0; t < 2; ++t) { const unsigned* xr = (const unsigned*)(P.x + (size_t)(64 * p + 8 * j + 2 * w + t) * D);
#pragma unroll
      for (int i = 0; i < 5; ++i) v[t][i] = xr[lane + 64 * i]; }
    const int u0 = w * 256 + b; const bool dopt = u0 < 810; const int u = dopt ? u0 : 0; const bool isin = u < 540;
    const int uu = isin ? u : u - 540, ncb = isin ? 27 : 10, pkb = uu / ncb, pcb = uu - pkb * ncb, ld = isin ? F : D;
    u32x4 pv[4];
    { const bf16_t* src = (isin ? P.win : P.wout) + (size_t)(pkb * 32) * ld + pcb * 64;
#pragma unroll
      for (int i = 0; i < 4; ++i) pv[i] = *(const u32x4*)(src + (size_t)(i * 8 + (lane >> 3)) * ld + (lane & 7) * 8); }
    LAS unsigned* t32 = (LAS unsigned*)(smem + P0_T);
    unsigned pk[2][5];
#pragma unroll
    for (int t = 0; t < 2; ++t) {
      float s = 0.f;
#pragma unroll
      for (int i = 0; i < 5; ++i) s += bflo(v[t][i]) + bfhi(v[t][i]);
      const float mean = xsum_q(xsum_16(s)) * (1.0f / D);
      float q2 = 0.f;
#pragma unroll
      for (int i = 0; i < 5; ++i) { const float a = bflo(v[t][i]) - mean, c = bfhi(v[t][i]) - mean; q2 += a * a + c * c; }
      const float rs = 1.0f / sqrtf(xsum_q(xsum_16(q2)) * (1.0f / D) + 1e-5f);
#pragma unroll
      for (int i = 0; i < 5; ++i) pk[t][i] = pack2((bflo(v[t][i]) - mean) * rs, (bfhi(v[t][i]) - mean) * rs);
    }
#pragma unroll
    for (int i = 0; i < 5; ++i) { const int d = 2 * (lane + 64 * i);
      t32[d * 4 + w] = (pk[0][i] & 0xffffu) | (pk[1][i] << 16); t32[(d + 1) * 4 + w] = (pk[0][i] >> 16) | (pk[1][i] & 0xffff0000u); }
    asm volatile("" : "+v"(tagv)); tag = __builtin_amdgcn_readfirstlane(tagv);
    wait_lgkm0(); bar();
    const unsigned xb = WS_XN + (unsigned)((2 * p + (j >> 2)) * 40 * 1024 + (j & 3) * 256);
#pragma unroll
    for (int i = 0; i < 3; ++i) { const int d = tid + 256 * i;
      if (d < D) st16(rws, xb + (d >> 4) * 1024 + (d & 15) * 16, *(LAS u32x4*)(smem + P0_T + d * 16)); }
    wait_vm<0>();
    bar();
    if (w == 0 && lane == 0) set_flag(ws, WS_FN + p * 128 + j * 4, tag);
    {
      LAS char* t = smem + P0_PT + w * 4096;
#pragma unroll
      for (int i = 0; i < 4; ++i) *(LAS u32x4*)(t + (i * 8 + (lane >> 3)) * 128 + (lane & 7) * 16) = pv[i];
      const unsigned dst = dopt ? (isin ? WS_WIN + (unsigned)(pkb * 109 + pcb * 4) * 1024 : WS_WOUT + (unsigned)(pkb * 40 + pcb * 4) * 1024) + lane * 16 : 0xfffff000u;
      const int rq = tk >> 2, pp = lane & 3;
#pragma unroll
      for (int c = 0; c < 4; ++c) {
        const u32x2 l2 = __builtin_bit_cast(u32x2, __builtin_amdgcn_ds_read_tr16_b64_v4i16((LAS s16x4*)(t + (8 * q + rq) * 128 + c * 32 + pp * 8)));
        const u32x2 h2 = __builtin_bit_cast(u32x2, __builtin_amdgcn_ds_read_tr16_b64_v4i16((LAS s16x4*)(t + (8 * q + 4 + rq) * 128 + c * 32 + pp * 8)));
        st16(rws, dst + (dopt ? c * 1024 : 0), (u32x4){l2.x, l2.y, h2.x, h2.y});
      }
    }
    if (w == 0) { wait256(rws, ws, WS_FN, tag, lane); }
    bar();
  }
  u32x2 xv[5];
  {
    LANE_IDS
    f32x4 ur[5], ui[5];
#pragma unroll
    for (int i = 0; i < 5; ++i) { ur[i] = (f32x4){0, 0, 0, 0}; ui[i] = (f32x4){0, 0, 0, 0}; }
    f32x4 rsum[2] = {(f32x4){0, 0, 0, 0}, (f32x4){0, 0, 0, 0}};
    bf16x8 ones = __builtin_bit_cast(bf16x8, (u32x4){0x3f803f80u, 0x3f803f80u, 0x3f803f80u, 0x3f803f80u});
    asm volatile("" : "+v"(ones));
    const unsigned wa0 = (unsigned)(w * 4096 + tk * 128 + ((q ^ ((lane >> 1) & 7)) * 16));
    const unsigned xa = 16384 + lane * 16;
    bf16x8 Wa[2], Xa[5], Wb[2], Xb[5];
    auto rdm = [&](bf16x8(&Wn)[2], bf16x8(&Xn)[5], int slot, int kk, bf16x8(&Wf)[2], bf16x8(&Xf)[5]) {
      LAS char* sb = smem + slot * A_SLOT; const unsigned wa = wa0 ^ (kk * 64);
      static_for<0, 12>([&](auto ic) { constexpr int z = decltype(ic)::value, i = z >> 1, t = z & 1;
        if constexpr (z < 10) { if constexpr (t == 0) ur[i] = mfma(Xf[i], Wf[0], ur[i]); else ui[i] = mfma(Xf[i], Wf[1], ui[i]); } else rsum[t] = mfma(ones, Wf[t], rsum[t]);
        if constexpr (z == 0) Wn[0] = *(LAS bf16x8*)(sb + wa);
        if constexpr (z == 1) Xn[0] = *(LAS bf16x8*)(sb + xa + (kk * 5) * 1024);
        if constexpr (z == 2) Wn[1] = *(LAS bf16x8*)(sb + wa + 2048);
        if constexpr (z >= 3 && z < 7) Xn[z - 2] = *(LAS bf16x8*)(sb + xa + (kk * 5 + z - 2) * 1024);
        sched_fence(); });
    };
    auto mmlast = [&](bf16x8(&Wf)[2], bf16x8(&Xf)[5]) {
#pragma unroll
      for (int i = 0; i < 5; ++i) { ur[i] = mfma(Xf[i], Wf[0], ur[i]); ui[i] = mfma(Xf[i], Wf[1], ui[i]); }
      rsum[0] = mfma(ones, Wf[0], rsum[0]); rsum[1] = mfma(ones, Wf[1], rsum[1]);
    };
    int slot = 0;
    auto steps = [&](int n0, int n1, bool hooks) {
#pragma unroll 1
      for (int n = n0; n < n1; ++n) {
        rdm(Wb, Xb, slot, 1, Wa, Xa);
        const int ns = slot + 1 == A_NS ? 0 : slot + 1;
        if (hooks && n == 8) wait_vm<0>();
        wait_lgkm0(); bar(); sched_fence();
        if (hooks && n == 8 && w == 0 && lane == 0) set_flag(ws, WS_F2 + p * 128 + j * 4, tag);
        rdm(Wa, Xa, ns, 0, Wb, Xb);
        slot = ns;
      }
    };
    bar();
    { LAS char* sb = smem; Wa[0] = *(LAS bf16x8*)(sb + wa0); Wa[1] = *(LAS bf16x8*)(sb + wa0 + 2048);
#pragma unroll
      for (int i = 0; i < 5; ++i) Xa[i] = *(LAS bf16x8*)(sb + xa + i * 1024); }
    steps(0, 12, true);
    const u32x4 f2 = peek256(rws, WS_F2, lane);
    steps(12, 18, false);
    if (!flags_ok(f2, tag)) wait256(rws, ws, WS_F2, tag, lane);
    if (b == 0 && w == 0 && lane == 0) set_flag(ws, WS_EPOCH, tag + 15);
#pragma unroll
    for (int i = 0; i < 5; ++i) xv[i] = *(const u32x2*)(P.x + (size_t)token * D + 80 * j + 16 * i + 4 * q);
    steps(18, 31, false);
    { LAS char* sb = smem + slot * A_SLOT; const unsigned wa = wa0 ^ 64;
      Wb[0] = *(LAS bf16x8*)(sb + wa); Wb[1] = *(LAS bf16x8*)(sb + wa + 2048);
#pragma unroll
      for (int i = 0; i < 5; ++i) Xb[i] = *(LAS bf16x8*)(sb + xa + (5 + i) * 1024); }
    mmlast(Wa, Xa);
    wait_lgkm0(); bar();
    mmlast(Wb, Xb);
    const float rs0 = rsum[0][0], rs1 = rsum[1][0];
#pragma unroll
    for (int i = 0; i < 5; ++i) {
      u32x2 a, c;
      const f32x4 sa = *(const LAS f32x4*)(smem + T_MOD + (16 * i + 4 * q) * 4), c1 = *(const LAS f32x4*)(smem + T_MOD + (80 + 16 * i + 4 * q) * 4);
      { const f32x4 u = ur[i] * c1 + rs0 * sa; a.x = pack2(u[0], u[1]); a.y = pack2(u[2], u[3]); }
      { const f32x4 v = -(ui[i] * c1 + rs1 * sa); c.x = pack2(v[0], v[1]); c.y = pack2(v[2], v[3]); }
      const int dt = 5 * j + i, kb = dt >> 1, qq = 2 * (dt & 1) + (q >> 1);
      st16(rws, WS_UV + (unsigned)(((p * 2 + (q & 1)) * 20 + kb) * 4 + w) * 1024 + (qq * 16 + tk) * 16, swap_pair(a, c));
    }
    wait_vm<0>(); bar();
    if (w == 0) { if (lane == 0) set_flag(ws, WS_FU + p * 128 + j * 4, tag); wait_flags8(ws, WS_FU + p * 128, tag, lane); }
    bar();
  }
  u32x2 x1p[5];
  {
    LANE_IDS
    f32x4 acc[5][2];
#pragma unroll
    for (int i = 0; i < 5; ++i) { acc[i][0] = (f32x4){0, 0, 0, 0}; acc[i][1] = (f32x4){0, 0, 0, 0}; }
    const unsigned wa = (unsigned)(tk * 128 + (((q + 4 * kh) ^ ((lane >> 1) & 7)) * 16));
    const unsigned xa = 10240 + (kh * 4 + 2 * mh) * 1024 + lane * 16;
    ksplit_loop<B_NS, 20, 5, 2>(
        [&](bf16x8(&A)[5], bf16x8(&Bf)[2], int slot, int) { LAS char* sb = smem + slot * B_SLOT;
          A[0] = *(LAS bf16x8*)(sb + wa); Bf[0] = *(LAS bf16x8*)(sb + xa); Bf[1] = *(LAS bf16x8*)(sb + xa + 1024);
#pragma unroll
          for (int i = 1; i < 5; ++i) A[i] = *(LAS bf16x8*)(sb + wa + i * 2048); },
        [&](bf16x8(&A)[5], bf16x8(&Bf)[2], bool) {
#pragma unroll
          for (int i = 0; i < 5; ++i) { acc[i][0] = mfma(A[i], Bf[0], acc[i][0]); acc[i][1] = mfma(A[i], Bf[1], acc[i][1]); } });
    f32x4 y[5];
    kreduce<5>(acc, y, smem + B_SCR, w, lane);
    float xs[20]; u32x2 xp[5];
    float s = 0.f;
#pragma unroll
    for (int i = 0; i < 5; ++i) {
      const f32x4 ga = *(const LAS f32x4*)(smem + T_MOD + (160 + 16 * i + 4 * q) * 4), c1b = *(const LAS f32x4*)(smem + T_MOD + 960 + (16 * i + 4 * q) * 4);
      x1p[i].x = pack2(bflo(xv[i].x) + ga[0] * y[i][0], bfhi(xv[i].x) + ga[1] * y[i][1]);
      x1p[i].y = pack2(bflo(xv[i].y) + ga[2] * y[i][2], bfhi(xv[i].y) + ga[3] * y[i][3]);
      asm volatile("" : "+v"(x1p[i].x), "+v"(x1p[i].y));
      xs[4 * i] = bflo(x1p[i].x); xs[4 * i + 1] = bfhi(x1p[i].x); xs[4 * i + 2] = bflo(x1p[i].y); xs[4 * i + 3] = bfhi(x1p[i].y);
      s += (xs[4 * i] + xs[4 * i + 1]) + (xs[4 * i + 2] + xs[4 * i + 3]);
      xp[i].x = pack2(xs[4 * i] * c1b[0], xs[4 * i + 1] * c1b[1]); xp[i].y = pack2(xs[4 * i + 2] * c1b[2], xs[4 * i + 3] * c1b[3]);
    }
    const unsigned img = WS_X1P + (unsigned)p * 20 * 4096 + w * 1024;
#pragma unroll
    for (int i = 0; i < 6; i += 2) {
      const int i2 = i + 1 < 5 ? i + 1 : i;
      const int dt = 5 * j + ((q & 1) ? i2 : i), kb = dt >> 1, qq = 2 * (dt & 1) + (q >> 1);
      st16(rws, img + kb * 4096 + (qq * 16 + tk) * 16, swap_pair(xp[i], xp[i2]));
    }
    const float mean = xsum_q(s) * (1.0f / 80.0f);
    float m2 = 0.f;
#pragma unroll
    for (int e = 0; e < 20; ++e) { const float d = xs[e] - mean; m2 += d * d; }
    m2 = xsum_q(m2);
    if (q == 0) __hip_atomic_store((gu64*)(ws + WS_ST1) + token * 8 + j, ((unsigned long long)__float_as_uint(m2) << 32) | __float_as_uint(mean), RLX_AGENT);
    wait_vm<0>(); bar();
    if (w == 0) { if (lane == 0) set_flag(ws, WS_FX + p * 128 + j * 4, tag); wait_flags8(ws, WS_FX + p * 128, tag, lane); }
    bar();
  }
  u32x2 z1p[7]; float bias;
  {
    LANE_IDS
    u32x4 st[4];
#pragma unroll
    for (int k = 0; k < 4; ++k) st[k] = ld16(rws, WS_ST1 + (unsigned)token * 64 + k * 16);
    bias = bflo(P.bsp[token]);
    for (int i = tid; i < 448; i += 256) ((LAS float*)(smem + T_CV))[i] = 0.f;
    f32x4 acc[7][2], aux[4];
    auto clear = [&]() {
#pragma unroll
      for (int i = 0; i < 7; ++i) { acc[i][0] = (f32x4){0, 0, 0, 0}; acc[i][1] = (f32x4){0, 0, 0, 0}; }
#pragma unroll
      for (int i = 0; i < 4; ++i) aux[i] = (f32x4){0, 0, 0, 0};
    };
    const unsigned xa = C_X + (kh * 4 + 2 * mh) * 1024 + lane * 16, wlo = C_W + kh * 7168 + lane * 16 + mh * 4096, whi = C_W + kh * 7168 + lane * 16 - mh * 3072;
    const int r4 = tk & 3;
    const unsigned ma = T_MODB + (r4 == 0 ? q * 16 + kh * 64 : r4 == 1 ? 1280 + q * 16 + kh * 64 : 2560), mstride = r4 < 2 ? 128 : 0;
    auto rd = [&](bf16x8(&A)[7], bf16x8(&Bf)[3], int slot, int n) {
      LAS char* sl = smem + wlo + slot * C_WSLOT; LAS char* sh = smem + whi + slot * C_WSLOT; LAS char* xb = smem + xa + n * 8192;
      Bf[0] = *(LAS bf16x8*)(xb); A[0] = *(LAS bf16x8*)(sl); Bf[1] = *(LAS bf16x8*)(xb + 1024);
      A[1] = *(LAS bf16x8*)(sl + 1024); A[2] = *(LAS bf16x8*)(sl + 2048);
#pragma unroll
      for (int i = 3; i < 7; ++i) A[i] = *(LAS bf16x8*)(sh + i * 1024);
      Bf[2] = *(LAS bf16x8*)(smem + ma + n * mstride);
    };
    auto auxmm = [&](bf16x8(&A)[7], bf16x8(&Bf)[3]) {
#pragma unroll
      for (int i = 0; i < 4; ++i) aux[i] = mfma(Bf[2], A[i], aux[i]);
    };
    auto mm2 = [&](bf16x8(&A)[7], bf16x8(&Bf)[3], bool) {
#pragma unroll
      for (int i = 0; i < 7; ++i) { acc[i][0] = mfma(Bf[0], A[i], acc[i][0]); acc[i][1] = mfma(Bf[1], A[i], acc[i][1]); }
      auxmm(A, Bf); };
    auto mm1 = [&](bf16x8(&A)[7], bf16x8(&Bf)[3], bool) {
#pragma unroll
      for (int i = 0; i < 7; ++i) { acc[i][0] = mfma(A[i], Bf[0], acc[i][0]); acc[i][1] = mfma(A[i], Bf[1], acc[i][1]); }
      auxmm(A, Bf); };
    auto put_cv = [&](int pass_) {
      LAS float* cv = (LAS float*)(smem + T_CV) + pass_ * 224;
      if (q == 0) {
#pragma unroll
        for (int i = 0; i < 4; ++i) if (mh == 0 || i < 3) { const int t = rot(i);
          __hip_atomic_fetch_add(cv + 16 * t + tk, aux[i][0], __ATOMIC_RELAXED, __HIP_MEMORY_SCOPE_WORKGROUP);
          __hip_atomic_fetch_add(cv + 112 + 16 * t + tk, aux[i][1], __ATOMIC_RELAXED, __HIP_MEMORY_SCOPE_WORKGROUP); }
      }
    };
    float rmean, rrstd;
    { float mu[8], mean = 0.f;
#pragma unroll
      for (int k = 0; k < 4; ++k) { mu[2 * k] = __uint_as_float(st[k].x); mu[2 * k + 1] = __uint_as_float(st[k].z); mean += mu[2 * k] + mu[2 * k + 1]; }
      mean *= 0.125f; float M2 = 0.f;
#pragma unroll
      for (int k = 0; k < 4; ++k) { const float d0 = mu[2 * k] - mean, d1 = mu[2 * k + 1] - mean; M2 += __uint_as_float(st[k].y) + __uint_as_float(st[k].w) + 80.0f * (d0 * d0 + d1 * d1); }
      rmean = mean; rrstd = 1.0f / sqrtf(M2 * (1.0f / D) + 1e-5f); }
    asm volatile("" : "+v"(rmean), "+v"(rrstd));
    bf16x8 A0[7], B0[3], A1[7], B1[3];
    int slot = 0;
    auto nohook = [](int) {};
    clear();
    wait_lgkm0(); bar();
    rd(A0, B0, 0, 0);
    ksplit_steps<C_NS, 10, 7, 3, 18>(A0, B0, A1, B1, slot, 0, 8, rd, mm2, nohook, nohook);
    ksplit_last<C_NS, 10, 7, 3>(A0, B0, A1, B1, slot, rd, mm2);
    put_cv(0);
    f32x4 zs[7];
    kreduce<7>(acc, zs, smem + C_SCR, w, lane);
    wait_lgkm0(); bar();
    f32x2 mr01, mr23, rr01, rr23;
    mr01 = (f32x2){__shfl(rmean, 4 * q), __shfl(rmean, 4 * q + 1)}; mr23 = (f32x2){__shfl(rmean, 4 * q + 2), __shfl(rmean, 4 * q + 3)};
    rr01 = (f32x2){__shfl(rrstd, 4 * q), __shfl(rrstd, 4 * q + 1)}; rr23 = (f32x2){__shfl(rrstd, 4 * q + 2), __shfl(rrstd, 4 * q + 3)};
    f32x2 z01[7], z23[7];
#pragma unroll
    for (int i = 0; i < 7; ++i) {
      const float c1 = ((const LAS float*)(smem + T_CV))[16 * rot(i) + tk], c2 = ((const LAS float*)(smem + T_CV))[112 + 16 * rot(i) + tk];
      z01[i] = gelu2(rr01 * ((f32x2){zs[i][0], zs[i][1]} - mr01 * c1) + c2);
      z23[i] = gelu2(rr23 * ((f32x2){zs[i][2], zs[i][3]} - mr23 * c1) + c2);
    }
    {
      const float inv = 1.0f / (16.0f * nft);
      const f32x2 zero2 = (f32x2){0.f, 0.f};
      f32x2 s01 = zero2, s23 = zero2;
#pragma unroll
      for (int i = 0; i < 7; ++i) { const bool ok = (i != 2 && i != 6) || rot(i) < nft; s01 += ok ? z01[i] : zero2; s23 += ok ? z23[i] : zero2; }
      const f32x2 m01 = (f32x2){xsum_16(s01[0]), xsum_16(s01[1])} * inv, m23 = (f32x2){xsum_16(s23[0]), xsum_16(s23[1])} * inv;
      f32x2 t01 = zero2, t23 = zero2;
#pragma unroll
      for (int i = 0; i < 7; ++i) { const bool ok = (i != 2 && i != 6) || rot(i) < nft; const f32x2 a = ok ? z01[i] - m01 : zero2, c = ok ? z23[i] - m23 : zero2; t01 += a * a; t23 += c * c; }
      const float q0 = xsum_16(t01[0]), q1 = xsum_16(t01[1]), q2 = xsum_16(t23[0]), q3 = xsum_16(t23[1]);
      if (tk < 8) { const int r = tk & 3; const float a = r == 0 ? m01[0] : r == 1 ? m01[1] : r == 2 ? m23[0] : m23[1], c = r == 0 ? q0 : r == 1 ? q1 : r == 2 ? q2 : q3;
        const int tok = 64 * p + 16 * w + 4 * q + r;
        __hip_atomic_store((gu64*)(ws + WS_ST2) + (tok * 8 + j) * 2 + (tk >> 2), ((unsigned long long)(tag ^ __float_as_uint(tk < 4 ? c : a)) << 32) | __float_as_uint(tk < 4 ? a : c), RLX_AGENT); }
    }
    clear();
    slot = 0;
    bar();
    rd(A0, B0, 0, 0);
    ksplit_steps<C_NS, 10, 7, 3, 18>(A0, B0, A1, B1, slot, 0, 4, rd, mm1, nohook, nohook);
    {
      u32x4 g0, g1;
      const unsigned go = WS_ST2 + (unsigned)(token * 8 + 2 * q) * 16;
      for (unsigned spins = 0;;) { g0 = ld16(rws, go); g1 = ld16(rws, go + 16);
        if (__all(g0.y == (tag ^ g0.z) && g0.w == (tag ^ g0.x) && g1.y == (tag ^ g1.z) && g1.w == (tag ^ g1.x))) break; if (spin_fail(spins, ws)) break; }
      const float n0 = 112.0f, n1 = (q & 1) ? 96.0f : 112.0f;
      const float m0 = __uint_as_float(g0.x), m1 = __uint_as_float(g1.x);
      const float mean = xsum_q(n0 * m0 + n1 * m1) * (1.0f / FH);
      const float d0 = m0 - mean, d1 = m1 - mean;
      const float M2 = xsum_q(__uint_as_float(g0.z) + __uint_as_float(g1.z) + n0 * d0 * d0 + n1 * d1 * d1);
      const float rstd = 1.0f / sqrtf(M2 * (1.0f / FH) + 1e-5f);
      mr01 = (f32x2){__shfl(mean, 4 * q), __shfl(mean, 4 * q + 1)}; mr23 = (f32x2){__shfl(mean, 4 * q + 2), __shfl(mean, 4 * q + 3)};
      rr01 = (f32x2){__shfl(rstd, 4 * q), __shfl(rstd, 4 * q + 1)}; rr23 = (f32x2){__shfl(rstd, 4 * q + 2), __shfl(rstd, 4 * q + 3)};
    }
    {
      u32x2 pk[7];
#pragma unroll
      for (int i = 0; i < 7; ++i) { const f32x2 a = (z01[i] - mr01) * rr01, c = (z23[i] - mr23) * rr23; pk[i].x = pack2(a[0], a[1]); pk[i].y = pack2(c[0], c[1]); }
      const unsigned img = WS_Z2N + (unsigned)(2 * p + (w >> 1)) * 55 * 1024 + ((2 * (w & 1) + (q >> 1)) * 16 + tk) * 16;
#pragma unroll
      for (int i = 0; i < 8; i += 2) {
        const int i2 = i + 1 < 7 ? i + 1 : i;
        const u32x4 ch = swap_pair(pk[i], pk[i2]);
        const int ti = (q & 1) ? rot(i2) : rot(i);
        st16(rws, ti < nft ? img + (ft0 + ti) * 1024 : 0xfffff000u, ch);
      }
    }
    ksplit_steps<C_NS, 10, 7, 3, 18>(A0, B0, A1, B1, slot, 4, 8, rd, mm1,
        [&](int n) { if (n == 5) wait_vm<0>(); },
        [&](int n) { if (n == 5 && w == 0 && lane == 0) set_flag(ws, WS_FZ + (j * 4 + (p >> 3)) * 128 + (p & 7) * 4, tag); });
    ksplit_last<C_NS, 10, 7, 3>(A0, B0, A1, B1, slot, rd, mm1);
    const unsigned fzo = WS_FZ + (j * 4 + ((lane & 7) >> 1)) * 128 + (lane & 1) * 16;
    const u32x4 fz = ld16(rws, fzo);
    put_cv(1);
    kreduce<7>(acc, zs, smem + C_SCR, w, lane);
    if (!flags_ok(fz, tag)) { for (unsigned spins = 0;;) { if (flags_ok(ld16(rws, fzo), tag)) break; if (spin_fail(spins, ws)) break; } }
    wait_lgkm0(); bar();
#pragma unroll
    for (int i = 0; i < 7; ++i) {
      const LAS float* cv = (const LAS float*)(smem + T_CV) + 224;
      const f32x4 c1 = *(const LAS f32x4*)(cv + 16 * rot(i) + 4 * q), c2 = *(const LAS f32x4*)(cv + 112 + 16 * rot(i) + 4 * q);
      const f32x2 a = gelu2(rrstd * ((f32x2){zs[i][0], zs[i][1]} - rmean * (f32x2){c1[0], c1[1]}) + (f32x2){c2[0], c2[1]});
      const f32x2 c = gelu2(rrstd * ((f32x2){zs[i][2], zs[i][3]} - rmean * (f32x2){c1[2], c1[3]}) + (f32x2){c2[2], c2[3]});
      z1p[i] = (u32x2){pack2(a[0], a[1]), pack2(c[0], c[1])};
      asm volatile("" : "+v"(z1p[i].x), "+v"(z1p[i].y));
    }
  }
  {
    LANE_IDS
    f32x4 acc[7][2];
#pragma unroll
    for (int i = 0; i < 7; ++i) { acc[i][0] = (f32x4){0, 0, 0, 0}; acc[i][1] = (f32x4){0, 0, 0, 0}; }
    const unsigned wa = (unsigned)((2 * mh) * 2048 + tk * 128 + (((q + 4 * kh) ^ ((lane >> 1) & 7)) * 16));
    const unsigned xlo = 8192 + kh * 7168 + lane * 16 + mh * 4096, xhi = 8192 + kh * 7168 + lane * 16 - mh * 3072;
    ksplit_loop<D_NS, 32, 7, 2>(
        [&](bf16x8(&A)[7], bf16x8(&Bf)[2], int slot, int) { LAS char* sb = smem + slot * D_SLOT;
          Bf[0] = *(LAS bf16x8*)(sb + wa); A[0] = *(LAS bf16x8*)(sb + xlo); Bf[1] = *(LAS bf16x8*)(sb + wa + 2048);
          A[1] = *(LAS bf16x8*)(sb + xlo + 1024); A[2] = *(LAS bf16x8*)(sb + xlo + 2048);
#pragma unroll
          for (int i = 3; i < 7; ++i) A[i] = *(LAS bf16x8*)(sb + xhi + i * 1024); },
        [&](bf16x8(&A)[7], bf16x8(&Bf)[2], bool) {
#pragma unroll
          for (int i = 0; i < 7; ++i) { acc[i][0] = mfma(A[i], Bf[0], acc[i][0]); acc[i][1] = mfma(A[i], Bf[1], acc[i][1]); } });
    f32x4 g[7];
    kreduce<7>(acc, g, smem + D_SCR, w, lane);
    u32x2 pk[7];
#pragma unroll
    for (int i = 0; i < 7; ++i) {
      pk[i].x = pack2(bflo(z1p[i].x) * (g[i][0] + bias), bfhi(z1p[i].x) * (g[i][1] + bias));
      pk[i].y = pack2(bflo(z1p[i].y) * (g[i][2] + bias), bfhi(z1p[i].y) * (g[i][3] + bias));
    }
    const unsigned img = WS_P + (unsigned)p * 28 * 4096 + w * 1024;
#pragma unroll
    for (int i = 0; i < 8; i += 2) {
      const int i2 = i + 1 < 7 ? i + 1 : i;
      const u32x4 ch = swap_pair(pk[i], pk[i2]);
      const int ti = (q & 1) ? rot(i2) : rot(i), ft = ft0 + ti, kb = ft >> 1, qq = 2 * (ft & 1) + (q >> 1);
      st16(rws, ti < nft ? img + kb * 4096 + (qq * 16 + tk) * 16 : 0xfffff000u, ch);
    }
    wait_vm<0>(); bar();
    if (w == 0) { if (lane == 0) set_flag(ws, WS_FP + p * 128 + j * 4, tag); wait_flags8(ws, WS_FP + p * 128, tag, lane); }
    bar();
  }
  {
    LANE_IDS
    f32x4 acc[5][2];
#pragma unroll
    for (int i = 0; i < 5; ++i) { acc[i][0] = (f32x4){0, 0, 0, 0}; acc[i][1] = (f32x4){0, 0, 0, 0}; }
    const unsigned wa = kh * 5120 + lane * 16, xa = 10240 + (kh * 4 + 2 * mh) * 1024 + lane * 16;
    ksplit_loop<E_NS, 14, 5, 2>(
        [&](bf16x8(&A)[5], bf16x8(&Bf)[2], int slot, int) { LAS char* sb = smem + slot * E_SLOT;
          A[0] = *(LAS bf16x8*)(sb + wa); Bf[0] = *(LAS bf16x8*)(sb + xa); Bf[1] = *(LAS bf16x8*)(sb + xa + 1024);
#pragma unroll
          for (int i = 1; i < 5; ++i) A[i] = *(LAS bf16x8*)(sb + wa + i * 1024); },
        [&](bf16x8(&A)[5], bf16x8(&Bf)[2], bool last) {
          if (!(last && kh)) {
#pragma unroll
            for (int i = 0; i < 5; ++i) { acc[i][0] = mfma(A[i], Bf[0], acc[i][0]); acc[i][1] = mfma(A[i], Bf[1], acc[i][1]); } } });
    f32x4 o[5];
    kreduce<5>(acc, o, smem + E_SCR, w, lane);
    u32x2 pk[5];
#pragma unroll
    for (int i = 0; i < 5; ++i) {
      const f32x4 gb = *(const LAS f32x4*)(smem + T_MOD + 960 + (80 + 16 * i + 4 * q) * 4);
      pk[i].x = pack2(bflo(x1p[i].x) + gb[0] * o[i][0], bfhi(x1p[i].x) + gb[1] * o[i][1]);
      pk[i].y = pack2(bflo(x1p[i].y) + gb[2] * o[i][2], bfhi(x1p[i].y) + gb[3] * o[i][3]);
    }
    bf16_t* orow = P.out + (size_t)token * D + 80 * j;
#pragma unroll
    for (int i = 0; i < 6; i += 2) {
      const int i2 = i + 1 < 5 ? i + 1 : i;
      const u32x4 ch = swap_pair(pk[i], pk[i2]);
      *(u32x4*)(orow + 16 * ((q & 1) ? i2 : i) + 8 * (q >> 1)) = ch;
    }
  }
}

__global__ __launch_bounds__(NT, 2) void fused_block(Params P) {
  extern __shared__ __attribute__((aligned(16))) char smem_[];
  LAS char* smem = (LAS char*)smem_;
  const int tid = threadIdx.x, lane = tid & 63, wid = __builtin_amdgcn_readfirstlane(tid >> 6), b = blockIdx.x;
  const int xcd = b & 7, idx = b >> 3, p = xcd * 4 + (idx >> 3), j = idx & 7;
  if (wid >= 4) loader_main(P, (unsigned)(size_t)smem, smem, wid - 4, lane, b, p, j);
  else consumer_main(P, smem, wid, lane, b, p, j);
}

extern "C" void kernel_launch(void* const* d_in, const int* in_sizes, int n_in, void* d_out, int out_size, void* d_ws, size_t ws_size, hipStream_t stream) {
  Params P;
  P.x = (const bf16_t*)d_in[0]; P.wsr = (const bf16_t*)d_in[1]; P.wsi = (const bf16_t*)d_in[2];
  P.wdr = (const bf16_t*)d_in[3]; P.wdi = (const bf16_t*)d_in[4]; P.win = (const bf16_t*)d_in[6];
  P.wsp = (const bf16_t*)d_in[7]; P.bsp = (const bf16_t*)d_in[8]; P.wout = (const bf16_t*)d_in[9];
  P.c = (const bf16_t*)d_in[10]; P.wmod = (const bf16_t*)d_in[11];
  P.out = (bf16_t*)d_out; P.ws = (char*)d_ws;
  (void)hipFuncSetAttribute((const void*)fused_block, hipFuncAttributeMaxDynamicSharedMemorySize, LDS_BYTES);
  fused_block<<<256, NT, LDS_BYTES, stream>>>(P);
}
```

```cpp
#include <hip/hip_runtime.h>
#include <stdint.h>
#include <type_traits>

typedef unsigned short bf16_t;
typedef short bf16x8 __attribute__((ext_vector_type(8)));
typedef short s16x4 __attribute__((ext_vector_type(4)));
typedef float f32x4 __attribute__((ext_vector_type(4)));
typedef float f32x2 __attribute__((ext_vector_type(2)));
typedef unsigned u32x4 __attribute__((ext_vector_type(4)));
typedef unsigned u32x2 __attribute__((ext_vector_type(2)));
typedef __bf16 bf2_t __attribute__((ext_vector_type(2)));
#define LAS __attribute__((address_space(3)))
#define GAS __attribute__((address_space(1)))
typedef GAS unsigned gu32;
typedef GAS unsigned long long gu64;
#define RLX_AGENT __ATOMIC_RELAXED, __HIP_MEMORY_SCOPE_AGENT
#define DEV __device__ __forceinline__

constexpr int S = 2048, D = 640, F = 1728, FH = 864;
constexpr int NT = 512;

constexpr unsigned WS_EPOCH = 0;
constexpr unsigned WS_TMO   = 256;
constexpr unsigned WS_FN = 4096, WS_F2 = 8192, WS_FU = 12288, WS_FX = 16384, WS_FP = 20480, WS_FZ = 24576;
constexpr unsigned WS_MOD  = 65536;
constexpr unsigned WS_MODB = WS_MOD + 16384;

constexpr unsigned WS_XN   = 131072;
constexpr unsigned WS_WIN  = WS_XN + 64 * 40 * 1024;
constexpr unsigned WS_WOUT = WS_WIN + 20 * 109 * 1024;
constexpr unsigned WS_UV   = WS_WOUT + 27 * 40 * 1024;
constexpr unsigned WS_X1P  = WS_UV + 32 * 2 * 20 * 4096;
constexpr unsigned WS_ST1  = WS_X1P + 32 * 20 * 4096;
constexpr unsigned WS_Z2N  = WS_ST1 + 2048 * 64;
constexpr unsigned WS_ST2  = WS_Z2N + 64 * 55 * 1024;
constexpr unsigned WS_P    = WS_ST2 + 2048 * 128;
constexpr unsigned WS_END  = WS_P + 32 * 28 * 4096;

struct Params {
  const bf16_t *x, *wsr, *wsi, *wdr, *wdi, *win, *wsp, *bsp, *wout, *c, *wmod;
  bf16_t* out;
  char* ws;
};

template <int N> DEV void wait_vm() { asm volatile("s_waitcnt vmcnt(%0)" ::"n"(N) : "memory"); }
DEV void wait_lgkm0() { asm volatile("s_waitcnt lgkmcnt(0)" ::: "memory"); }
DEV void bar() { asm volatile("" ::: "memory"); __builtin_amdgcn_s_barrier(); asm volatile("" ::: "memory"); }
DEV void sched_fence() { __builtin_amdgcn_sched_barrier(0); }
template <int I, int N, class Fn> DEV void static_for(Fn&& f) { if constexpr (I < N) { f(std::integral_constant<int, I>{}); static_for<I + 1, N>(f); } }

DEV float bflo(unsigned v) { return __uint_as_float(v << 16); }
DEV float bfhi(unsigned v) { return __uint_as_float(v & 0xffff0000u); }
DEV unsigned pack2(float lo, float hi) { bf2_t v; v[0] = (__bf16)lo; v[1] = (__bf16)hi; return __builtin_bit_cast(unsigned, v); }
DEV float rbf(float f) { return bflo(pack2(f, f)); }
DEV f32x4 mfma(bf16x8 a, bf16x8 b, f32x4 c) { return __builtin_amdgcn_mfma_f32_16x16x32_bf16(a, b, c, 0, 0, 0); }

DEV u32x4 mkrs(const void* p, unsigned bytes) {
  const unsigned long long a = (unsigned long long)p;
  u32x4 r; r.x = (unsigned)a; r.y = (unsigned)(a >> 32) & 0xffffu; r.z = bytes; r.w = 0x00020000u; return r;
}
DEV __amdgpu_buffer_rsrc_t mkrsrc(const void* p, unsigned bytes) { return __builtin_amdgcn_make_buffer_rsrc((void*)p, 0, bytes, 0x00020000); }
#define LD_P(v, r, off, sc) "buffer_load_dwordx4 " v ", " r ", %0 offen offset:" off sc " lds\n\t"
#define LD_H(v, r, off, sc) "s_mov_b32 exec_hi, 0\n\tbuffer_load_dwordx4 " v ", " r ", %0 offen offset:" off sc " lds\n\ts_mov_b32 exec_hi, -1"
#define HD_OR  "s_add_u32 m0, %1, %2\n\ts_or_b32 %0, %3, %4\n\t"
#define HD_MAD(M) "s_mul_i32 %0, %3, " M "\n\ts_add_u32 m0, %1, %2\n\ts_add_u32 %0, %0, %4\n\t"
#define DMA_IN(IMM) "s"(lds), "n"(IMM), "s"(a), "s"(b)
template <int IMM> DEV void dma_w4_or(unsigned lds, unsigned a, unsigned b, u32x4 ra, u32x4 rb, unsigned v0, unsigned v1, unsigned v2, unsigned v3) { unsigned t;
  asm volatile(HD_OR LD_P("%7", "%5", "0", "") LD_P("%8", "%5", "1024", "") LD_P("%9", "%6", "2048", "") LD_P("%10", "%6", "3072", "")
               : "=&s"(t) : DMA_IN(IMM), "s"(ra), "s"(rb), "v"(v0), "v"(v1), "v"(v2), "v"(v3) : "memory", "scc"); }
template <int IMM> DEV void dma_w2_or(unsigned lds, unsigned a, unsigned b, u32x4 r, unsigned v0, unsigned v1) { unsigned t;
  asm volatile(HD_OR LD_P("%6", "%5", "0", "") LD_P("%7", "%5", "1024", "") : "=&s"(t) : DMA_IN(IMM), "s"(r), "v"(v0), "v"(v1) : "memory", "scc"); }
template <int IMM, int SOFF> DEV void dma_w2h(unsigned lds, unsigned a, u32x4 r, unsigned v0, unsigned v1, unsigned v2) { unsigned t;
  asm volatile("s_add_u32 m0, %1, %2\n\ts_add_u32 %0, %3, %4\n\t" LD_P("%6", "%5", "0", "") LD_P("%7", "%5", "1024", "") LD_H("%8", "%5", "2048", "")
               : "=&s"(t) : "s"(lds), "n"(IMM), "s"(a), "n"(SOFF), "s"(r), "v"(v0), "v"(v1), "v"(v2) : "memory", "scc"); }
template <int IMM, int SOFF> DEV void dma_x2(unsigned lds, unsigned a, u32x4 r, unsigned v) { unsigned t;
  asm volatile("s_add_u32 m0, %1, %2\n\ts_add_u32 %0, %3, %4\n\t" LD_P("%6", "%5", "0", " sc1") LD_P("%6", "%5", "1024", " sc1") : "=&s"(t) : "s"(lds), "n"(IMM), "s"(a), "n"(SOFF), "s"(r), "v"(v) : "memory", "scc"); }
template <int IMM, int SOFF> DEV void dma_x2h(unsigned lds, unsigned a, u32x4 r, unsigned v) { unsigned t;
  asm volatile("s_add_u32 m0, %1, %2\n\ts_add_u32 %0, %3, %4\n\t" LD_P("%6", "%5", "0", " sc1") LD_P("%6", "%5", "1024", " sc1") LD_H("%6", "%5", "2048", " sc1") : "=&s"(t) : "s"(lds), "n"(IMM), "s"(a), "n"(SOFF), "s"(r), "v"(v) : "memory", "scc"); }
template <int IMM, int SOFF> DEV void dma_x3h(unsigned lds, unsigned a, u32x4 r, unsigned v) { unsigned t;
  asm volatile("s_add_u32 m0, %1, %2\n\ts_add_u32 %0, %3, %4\n\t" LD_P("%6", "%5", "0", " sc1") LD_P("%6", "%5", "1024", " sc1") LD_P("%6", "%5", "2048", " sc1") LD_H("%6", "%5", "3072", " sc1") : "=&s"(t) : "s"(lds), "n"(IMM), "s"(a), "n"(SOFF), "s"(r), "v"(v) : "memory", "scc"); }
template <int IMM> DEV void dma_x2h_mad640(unsigned lds, unsigned a, unsigned b, u32x4 r, unsigned v) { unsigned t;
  asm volatile(HD_MAD("640") LD_P("%6", "%5", "0", " sc1") LD_P("%6", "%5", "1024", " sc1") LD_H("%6", "%5", "2048", " sc1") : "=&s"(t) : DMA_IN(IMM), "s"(r), "v"(v) : "memory", "scc"); }
template <int IMM> DEV void dma_x3h_mad880(unsigned lds, unsigned a, unsigned b, u32x4 r, unsigned v) { unsigned t;
  asm volatile(HD_MAD("880") LD_P("%6", "%5", "0", " sc1") LD_P("%6", "%5", "1024", " sc1") LD_P("%6", "%5", "2048", " sc1") LD_H("%6", "%5", "3072", " sc1") : "=&s"(t) : DMA_IN(IMM), "s"(r), "v"(v) : "memory", "scc"); }
DEV void dma_1(unsigned lds, u32x4 r, unsigned v) { asm volatile("s_mov_b32 m0, %0\n\ts_nop 0\n\tbuffer_load_dwordx4 %2, %1, 0 offen sc1 lds" ::"s"(lds), "s"(r), "v"(v) : "memory"); }
template <int N> DEV void wait_bar() { asm volatile("s_waitcnt vmcnt(%0)\n\ts_barrier" ::"n"(N) : "memory"); }
DEV void warm(unsigned lds, u32x4 r, unsigned v) { asm volatile("s_mov_b32 m0, %0\n\ts_nop 0\n\tbuffer_load_dword %2, %1, 0 offen lds" ::"s"(lds), "s"(r), "v"(v) : "memory"); }
constexpr unsigned WBIAS = 4096;
DEV u32x4 mkrs_b(const void* p, unsigned bytes) { return mkrs((const char*)p - WBIAS, bytes + WBIAS); }
constexpr int SC1 = 16;
DEV void st16(__amdgpu_buffer_rsrc_t r, unsigned off, u32x4 v) { __builtin_amdgcn_raw_buffer_store_b128(v, r, off, 0, SC1); }
DEV u32x4 ld16(__amdgpu_buffer_rsrc_t r, unsigned off) { return __builtin_amdgcn_raw_buffer_load_b128(r, off, 0, SC1); }
DEV f32x4 ld16f(__amdgpu_buffer_rsrc_t r, unsigned off) { return __builtin_bit_cast(f32x4, ld16(r, off)); }

template <int CTRL> DEV float dpp_add(float v) { return v + __builtin_bit_cast(float, __builtin_amdgcn_update_dpp(0, __builtin_bit_cast(int, v), CTRL, 0xf, 0xf, true)); }
DEV float xsum_16(float v) { v = dpp_add<0xB1>(v); v = dpp_add<0x4E>(v); v = dpp_add<0x141>(v); return dpp_add<0x140>(v); }
DEV float xsum_r16(float v) { auto r = __builtin_amdgcn_permlane16_swap(__float_as_uint(v), __float_as_uint(v), false, false); return __uint_as_float(r[0]) + __uint_as_float(r[1]); }
DEV float xsum_r32(float v) { auto r = __builtin_amdgcn_permlane32_swap(__float_as_uint(v), __float_as_uint(v), false, false); return __uint_as_float(r[0]) + __uint_as_float(r[1]); }
DEV float xsum_q(float v) { return xsum_r32(xsum_r16(v)); }
DEV f32x2 gelu2(f32x2 x) {
  const float k1 = -2.0f * 0.7978845608028654f * 1.4426950408889634f;
  const f32x2 t = x * ((x * x) * (0.044715f * k1) + k1);
  const f32x2 d = (f32x2){__builtin_amdgcn_exp2f(t[0]), __builtin_amdgcn_exp2f(t[1])} + 1.0f;
  return x * (f32x2){__builtin_amdgcn_rcpf(d[0]), __builtin_amdgcn_rcpf(d[1])};
}
DEV u32x4 swap_pair(u32x2 a, u32x2 b) {
  auto r0 = __builtin_amdgcn_permlane16_swap(a.x, b.x, false, false);
  auto r1 = __builtin_amdgcn_permlane16_swap(a.y, b.y, false, false);
  return (u32x4){r0[0], r1[0], r0[1], r1[1]};
}

DEV bool spin_fail(unsigned& spins, char* ws) {
  __builtin_amdgcn_s_sleep(1);
  if (++spins > (1u << 17)) { __hip_atomic_store((gu32*)(ws + WS_TMO), 1u, RLX_AGENT); return true; }
  return false;
}
DEV void set_flag(char* ws, unsigned off, unsigned tag) { __hip_atomic_store((gu32*)(ws + off), tag, RLX_AGENT); }
DEV void wait_flags8(char* ws, unsigned off, unsigned tag, int lane) {
  for (unsigned spins = 0;;) {
    const unsigned v = lane < 8 ? __hip_atomic_load((gu32*)(ws + off) + lane, RLX_AGENT) : tag;
    if (__all(v == tag)) break;
    if (spin_fail(spins, ws)) break;
  }
}
DEV bool flags_ok(u32x4 v, unsigned tag) { return __all(v.x == tag && v.y == tag && v.z == tag && v.w == tag); }
DEV u32x4 peek256(__amdgpu_buffer_rsrc_t rf, unsigned off, int lane) { return ld16(rf, off + (lane >> 1) * 128 + (lane & 1) * 16); }
DEV void wait256(__amdgpu_buffer_rsrc_t rf, char* ws, unsigned off, unsigned tag, int lane) {
  for (unsigned spins = 0;;) { if (flags_ok(peek256(rf, off, lane), tag)) break; if (spin_fail(spins, ws)) break; }
}

template <int NS, int PF, int S_, int CW, int CX, class IW, class IX>
DEV void loader_run(IW&& issueW, IX&& issueX) {
  constexpr auto span = [=](int a, int b) { int c = 0; for (int s = a; s <= b; ++s) c += (s >= PF ? CW : 0) + CX; return c; };
  auto step = [&](auto ic) { if constexpr (decltype(ic)::value >= PF) issueW(ic); issueX(ic); };
  static_for<0, NS>(step);
  wait_bar<span(1, NS - 1)>();
  static_for<0, S_ - 1>([&](auto ic) { constexpr int n = decltype(ic)::value;
    constexpr int last = n + NS - 1 < S_ - 1 ? n + NS - 1 : S_ - 1;
    wait_bar<span(n + 2, last)>();
    if constexpr (n + NS < S_) step(std::integral_constant<int, n + NS>{}); });
}

template <int NS, int S_, int NA, int NB, int NM, class Rd, class Mm, class Hook, class Post>
DEV void ksplit_steps(bf16x8 (&A0)[NA], bf16x8 (&B0)[NB], bf16x8 (&A1)[NA], bf16x8 (&B1)[NB], int& slot, int n0, int n1, Rd&& rd, Mm&& mm, Hook&& hook, Post&& post) {
  constexpr int NR = NA + NB;
#define KS_ILV() do { _Pragma("unroll") for (int z = 0; z < NR; ++z) { __builtin_amdgcn_sched_group_barrier(0x8, 1, 0); __builtin_amdgcn_sched_group_barrier(0x100, 1, 0); } __builtin_amdgcn_sched_group_barrier(0x8, NM - NR, 0); } while (0)
#pragma unroll 1
  for (int n = n0; n < n1; n += 2) {
    const int s1 = slot + 1 == NS ? 0 : slot + 1;
    hook(n); wait_lgkm0(); bar(); post(n); sched_fence();
    rd(A1, B1, s1, n + 1); mm(A0, B0, false); KS_ILV(); sched_fence();
    const int s2 = s1 + 1 == NS ? 0 : s1 + 1;
    hook(n + 1); wait_lgkm0(); bar(); post(n + 1); sched_fence();
    rd(A0, B0, s2, n + 2); mm(A1, B1, false); KS_ILV(); sched_fence();
    slot = s2;
  }
}
template <int NS, int S_, int NA, int NB, class Rd, class Mm>
DEV void ksplit_last(bf16x8 (&A0)[NA], bf16x8 (&B0)[NB], bf16x8 (&A1)[NA], bf16x8 (&B1)[NB], int& slot, Rd&& rd, Mm&& mm) {
  const int s1 = slot + 1 == NS ? 0 : slot + 1;
  wait_lgkm0(); bar(); sched_fence();
  rd(A1, B1, s1, S_ - 1); mm(A0, B0, false); mm(A1, B1, true);
  slot = s1;
}
template <int NS, int S_, int NA, int NB, class Rd, class Mm>
DEV void ksplit_loop(Rd&& rd, Mm&& mm) {
  static_assert((S_ & 1) == 0, "even step count");
  bf16x8 A0[NA], B0[NB], A1[NA], B1[NB];
  bar();
  rd(A0, B0, 0, 0);
  int slot = 0;
  ksplit_steps<NS, S_, NA, NB, NA * NB>(A0, B0, A1, B1, slot, 0, S_ - 2, rd, mm, [](int) {}, [](int) {});
  ksplit_last<NS, S_, NA, NB>(A0, B0, A1, B1, slot, rd, mm);
}
template <int N>
DEV void kreduce(f32x4 (&acc)[N][2], f32x4 (&res)[N], LAS char* scratch, int w, int lane) {
  const int kh = w & 1;
  LAS char* mine = scratch + w * (N * 1024) + lane * 16;
  LAS char* other = scratch + (w ^ 1) * (N * 1024) + lane * 16;
  if (kh) { asm volatile("");
#pragma unroll
    for (int i = 0; i < N; ++i) { *(LAS f32x4*)(mine + i * 1024) = acc[i][0]; res[i] = acc[i][1]; }
  } else { asm volatile("");
#pragma unroll
    for (int i = 0; i < N; ++i) { *(LAS f32x4*)(mine + i * 1024) = acc[i][1]; res[i] = acc[i][0]; }
  }
  wait_lgkm0(); bar();
#pragma unroll
  for (int i = 0; i < N; ++i) res[i] += *(LAS f32x4*)(other + i * 1024);
}

constexpr int A_PRE = 2;
constexpr int PFW = 6, PFC = 5, PRE1 = 2;
constexpr int LPRIO = 0, CPRIO = 2;
constexpr int A_NS = 5, A_SLOT = 26 * 1024, A_SCR = 52 * 1024;
constexpr int P0_T = 130 * 1024, P0_PT = 140 * 1024, P0_RED = 157 * 1024;
constexpr int B_NS = 6, B_SLOT = 18 * 1024, B_SCR = 36 * 1024;
constexpr int C_NS = 5, C_X = 0, C_W = 82 * 1024, C_WSLOT = 14 * 1024;
constexpr int C_SCR = C_W + 2 * C_WSLOT;
constexpr int D_NS = 5, D_SLOT = 22 * 1024, D_SCR = 110 * 1024;
constexpr int E_NS = 6, E_SLOT = 18 * 1024, E_SCR = 110 * 1024;
constexpr int T_MODB = 152 * 1024;
constexpr int T_CV = T_MODB + 3072 + 64;
constexpr int T_MOD = 158 * 1024;
constexpr int T_DUMMY = T_MOD + 1792;
constexpr int LDS_BYTES = 160 * 1024;
static_assert(T_CV + 1792 <= P0_RED && P0_RED + 256 <= T_MOD && C_W + C_NS * C_WSLOT <= T_MODB && A_NS * A_SLOT <= P0_RED && D_NS * D_SLOT <= T_CV && B_NS * B_SLOT <= T_MODB && E_NS * E_SLOT <= T_MODB, "LDS map");

#define IC(n) std::integral_constant<int, (n)>{}
DEV void loader_main(const Params& P, const unsigned lds0, LAS char* smem, const int lw, const int lane_, const int b, const int p, const int j) {
  char* const ws = P.ws;
  const int ft0 = 27 * (j >> 2) + 7 * (j & 3);
#define LLANE int lane = lane_; asm volatile("" : "+v"(lane)); const unsigned vx = lane * 16; (void)vx;
#define LV4K const unsigned v4k0 = (unsigned)(lane >> 3) * 4096u + (unsigned)(((lane & 7) ^ (lane >> 4)) * 16), v4k1 = (unsigned)(8 + (lane >> 3)) * 4096u + (unsigned)(((lane & 7) ^ (4 + (lane >> 4))) * 16);
  {
    LLANE LV4K
    const u32x4 rre = mkrs_b(P.wsr, S * S * 2), rim = mkrs_b(P.wsi, S * S * 2), rx = mkrs(ws + WS_XN, S * D * 2);
    const unsigned wrow = (unsigned)(64 * p + 16 * lw) * 4096u;
    const unsigned w0 = v4k0 + WBIAS, w1 = v4k1 + WBIAS - 1024, w2 = v4k0 + WBIAS - 2048, w3 = v4k1 + WBIAS - 3072;
    const unsigned lw_w = lds0 + lw * 4096, lw_x = lds0 + 16384 + lw * 2560;
    const unsigned xo = (unsigned)(((lw >> 1) * 40 + 5 * j) * 1024 + (lw & 1) * 2560);
    const unsigned rot = (unsigned)j * 512, xrot = (unsigned)(b & 7) * 512;
    auto kof = [&](auto ic) { constexpr int n = decltype(ic)::value;
      const unsigned k = n < 4 ? rot + n * 128 : (n - 4) * 128 + ((unsigned)((n - 4) * 128) >= rot ? 512u : 0u); return (k + xrot) & 0xf80u; };
    auto issueW = [&](auto ic) { dma_w4_or<(decltype(ic)::value % A_NS) * A_SLOT>(lw_w, kof(ic), wrow, rre, rim, w0, w1, w2, w3); };
    auto issueX = [&](auto ic) { dma_x2h_mad640<(decltype(ic)::value % A_NS) * A_SLOT>(lw_x, kof(ic), xo, rx, vx); };
    static_for<0, A_PRE>(issueW);
    bar();
    bar();
    const int idx = b >> 3, unit0 = (idx & 3) + 4 * ((b & 7) + 8 * (idx >> 2));
    const bool domod = unit0 < 240; const int unit = domod ? unit0 : 0;
    {
      u32x4 wm[5]; unsigned cs[5];
#pragma unroll
      for (int i = 0; i < 5; ++i) { const int row = lw * 160 + i * 32 + (lane >> 1);
        wm[i] = *(const u32x4*)(P.wmod + (size_t)row * (6 * D) + unit * 16 + (lane & 1) * 8); cs[i] = P.c[row]; }
      float a[8] = {0, 0, 0, 0, 0, 0, 0, 0};
#pragma unroll
      for (int i = 0; i < 5; ++i) { const float x = bflo(cs[i]); const float s = rbf(x * __builtin_amdgcn_rcpf(1.0f + __expf(-x)));
        a[0] += s * bflo(wm[i].x); a[1] += s * bfhi(wm[i].x); a[2] += s * bflo(wm[i].y); a[3] += s * bfhi(wm[i].y);
        a[4] += s * bflo(wm[i].z); a[5] += s * bfhi(wm[i].z); a[6] += s * bflo(wm[i].w); a[7] += s * bfhi(wm[i].w); }
#pragma unroll
      for (int e = 0; e < 8; ++e) { float v = dpp_add<0x4E>(a[e]);
        v = dpp_add<0x124>(v); v = dpp_add<0x128>(v);
        a[e] = xsum_q(v); }
      if (lane < 2) {
#pragma unroll
        for (int e = 0; e < 8; ++e) ((LAS float*)(smem + P0_RED))[lw * 16 + lane * 8 + e] = a[e];
      }
      wait_lgkm0();
    }
    bar();
    __builtin_amdgcn_s_setprio(LPRIO);
    auto istep = [&](auto ic) { if constexpr (decltype(ic)::value >= A_PRE) issueW(ic); issueX(ic); };
#define A_CNT(s_) (((s_) >= A_PRE ? 4 : 0) + 3)
    static_assert((A_NS == 5 || A_NS == 6) && A_PRE <= A_NS, "start-up schedule");
    istep(IC(0)); istep(IC(1));
    wait_bar<A_CNT(1)>();
    istep(IC(2)); istep(IC(3));
    if (lw == 0 && domod && lane < 16) {
      const LAS float* red = (const LAS float*)(smem + P0_RED);
      float s = rbf((red[lane] + red[16 + lane]) + (red[32 + lane] + red[48 + lane]));
      const int n = unit * 16 + lane, seg = n / D;
      if (seg == 1 || seg == 4) s = rbf(1.0f + s);
      __hip_atomic_store((gu32*)(ws + WS_MOD) + n, __float_as_uint(s), RLX_AGENT);
      const unsigned hi = __float_as_uint(s) >> 16, nb = (unsigned)__builtin_amdgcn_update_dpp(0, (int)hi, 0xB1, 0xf, 0xf, true);
      if ((seg == 3 || seg == 4) && !(lane & 1)) __hip_atomic_store((gu32*)(ws + WS_MODB) + (((seg == 4 ? 0 : D) + n - seg * D) >> 1), hi | (nb << 16), RLX_AGENT);
    }
    wait_bar<A_CNT(2) + A_CNT(3)>();
    static_for<4, A_NS + 1>(istep);
    wait_bar<A_CNT(3) + A_CNT(4) + A_CNT(5) + (A_NS == 6 ? A_CNT(6) : 0)>();
    const u32x4 rmod = mkrs(ws + WS_MOD, 3840 * 4);
    const int tl = lane / 20, tc = lane - 20 * tl;
    const unsigned tv0 = lane < 60 ? (unsigned)((tl * D + 80 * j) * 4 + tc * 16) : 0x7ffffff0u;
    const unsigned tv1 = lane < 40 ? (unsigned)(((4 + tl) * D + 80 * j) * 4 + tc * 16) : 0x7ffffff0u;
    istep(IC(A_NS + 1));
    static_for<2, 32 - A_NS - 0>([&](auto ic) { constexpr int n = decltype(ic)::value;
      constexpr int cnt = [] { int c = 0; for (int s_ = n + 2; s_ <= n + A_NS - 1; ++s_) c += A_CNT(s_); return c; }();
      wait_bar<cnt>(); if constexpr (n + A_NS < 32) istep(IC(n + A_NS));
      if constexpr (n == 20) { if (lw == 0) { dma_1(lds0 + T_MOD, rmod, tv0); dma_1(lds0 + T_MOD + 960, rmod, tv1); } }
    });
    {
      { const int row = (j * 4 + lw) * 2 + (lane >> 5); warm(lds0 + T_DUMMY, mkrs(P.wsp, S * S * 2), (unsigned)(64 * p + row) * 4096u + (lane & 31) * 128); }
      warm(lds0 + T_DUMMY, mkrs((lw & 1) ? P.wdi : P.wdr, D * D * 2), lw < 2 && lane < 25 ? (unsigned)(b * 25 + lane) * 128 : 0x7ffffff0u);
    }
    static_for<0, A_NS - 1>([&](auto ic) { constexpr int r = A_NS - 2 - decltype(ic)::value; wait_bar<r * 7 + 2>(); });
    bar();
  }
  {
    LLANE
    const u32x4 rre = mkrs_b(P.wdr, D * D * 2), rim = mkrs_b(P.wdi, D * D * 2), rx = mkrs(ws + WS_UV, 32 * 2 * 20 * 4096);
    auto wv = [&](int k) { const int r = 20 * lw + 8 * k + (lane >> 3); return (unsigned)(r * 1280 + (((lane & 7) ^ ((r >> 1) & 7)) * 16)) + WBIAS - 1024 * k; };
    const unsigned v0 = wv(0), v1 = wv(1), v2 = wv(2);
    const unsigned wb = (unsigned)(80 * j) * 1280u, lw_w = lds0 + lw * 2560, lw_x = lds0 + 10240 + lw * 2048, xb = (unsigned)p * 40 * 4096u + lw * 2048;
    auto issueW = [&](auto ic) { constexpr int n = decltype(ic)::value; dma_w2h<(n % B_NS) * B_SLOT, (n % 10) * 128>(lw_w, wb, n >= 10 ? rim : rre, v0, v1, v2); };
    auto issueX = [&](auto ic) { constexpr int n = decltype(ic)::value; dma_x2<(n % B_NS) * B_SLOT, 2 * n * 4096>(lw_x, xb, rx, vx); };
    __builtin_amdgcn_s_setprio(0);
    static_for<0, (PRE1 < PFW ? PRE1 : PFW)>(issueW);
    bar();
    static_for<(PRE1 < PFW ? PRE1 : PFW), PFW>(issueW);
    bar();
    __builtin_amdgcn_s_setprio(LPRIO);
    loader_run<B_NS, PFW, 20, 3, 2>(issueW, issueX);
    bar();
  }
  {
    LLANE
    const u32x4 rw = mkrs(ws + WS_WIN, 20 * 109 * 1024), rx = mkrs(ws + WS_X1P, 32 * 20 * 4096), rmb = mkrs(ws + WS_MODB, 2560);
    const unsigned wo = (unsigned)(((lw >> 1) * 109 + ft0) * 1024 + (lw & 1) * 3584);
    const unsigned lw_w = lds0 + C_W + lw * 3584, lw_x = lds0 + C_X + lw * 2048, xb = (unsigned)p * 20 * 4096u + lw * 2048;
    auto issueW = [&](auto ic) { constexpr int n = decltype(ic)::value, z1 = n >= 10; dma_x3h<(n % C_NS) * C_WSLOT, (2 * (n - 10 * z1) * 109 + (z1 ? 0 : 54)) * 1024>(lw_w, wo, rw, vx); };
    auto issueX = [&](auto ic) { constexpr int n = decltype(ic)::value; dma_x2<2 * n * 4096, 2 * n * 4096>(lw_x, xb, rx, vx); };
    __builtin_amdgcn_s_setprio(0);
    static_for<0, (PRE1 < PFC ? PRE1 : PFC)>(issueW);
    bar();
    if (lw < 3) dma_1(lds0 + T_MODB + lw * 1024, rmb, vx + lw * 1024);
    static_for<(PRE1 < PFC ? PRE1 : PFC), PFC>(issueW);
    bar();
    __builtin_amdgcn_s_setprio(LPRIO);
    static_for<0, C_NS>([&](auto ic) { if constexpr (decltype(ic)::value >= PFC) issueW(ic); issueX(ic); });
    wait_bar<(PFC - 1) * 2 + (C_NS - PFC) * 6>();
    static_for<0, 9>([&](auto ic) {
      constexpr int n = decltype(ic)::value, need = n + 1;
      constexpr int last = n + C_NS - 1 < 11 ? n + C_NS - 1 : 11;
      constexpr int cnt = [] { int c = 0; for (int s = need + 1; s <= last; ++s) c += (s < PFC ? 2 : s < 10 ? 6 : 4); return c; }();
      wait_bar<cnt>();
      if constexpr (n + C_NS < 12) { issueW(IC(n + C_NS)); if constexpr (n + C_NS < 10) issueX(IC(n + C_NS)); }
    });
    bar();
    bar();
    issueW(IC(12)); issueW(IC(13)); issueW(IC(14));
    wait_bar<16>();
    static_for<10, 19>([&](auto ic) {
      constexpr int n = decltype(ic)::value;
      constexpr int last = n + 4 < 19 ? n + 4 : 19;
      wait_bar<(last - (n + 1)) * 4>();
      if constexpr (n + C_NS < 20) issueW(IC(n + C_NS));
    });
    bar();
  }
  {
    LLANE LV4K
    const u32x4 rw = mkrs_b(P.wsp, S * S * 2), rx = mkrs(ws + WS_Z2N, 64 * 55 * 1024);
    const unsigned wrow = (unsigned)(64 * p + 16 * lw) * 4096u, w0 = v4k0 + WBIAS, w1 = v4k1 + WBIAS - 1024;
    const unsigned xo = (unsigned)(((lw >> 1) * 55 + ft0) * 1024 + (lw & 1) * 3584), lw_w = lds0 + lw * 2048, lw_x = lds0 + 8192 + lw * 3584;
    unsigned kw = (b & 7) * 512, kx = kw;
    auto issueW = [&](auto ic) { dma_w2_or<(decltype(ic)::value % D_NS) * D_SLOT>(lw_w, kw, wrow, rw, w0, w1); kw = (kw + 128) & 0xf80; };
    auto issueX = [&](auto ic) { dma_x3h_mad880<(decltype(ic)::value % D_NS) * D_SLOT>(lw_x, kx, xo, rx, vx); kx = (kx + 128) & 0xf80; };
    static_assert(D_NS * D_SLOT <= C_SCR, "ring below the exchange buffer");
    static_for<0, D_NS>(issueW);
    bar();
    static_for<0, D_NS>(issueX);
    wait_bar<(D_NS - 1) * 4>();
    static_for<0, 31>([&](auto ic) { constexpr int n = decltype(ic)::value;
      constexpr int last = n + D_NS - 1 < 31 ? n + D_NS - 1 : 31;
      constexpr int cnt = n + 1 < D_NS ? (D_NS - 2 - n) * 4 + (last - (D_NS - 1)) * 6 : (last - (n + 1)) * 6;
      wait_bar<cnt>();
      if constexpr (n + D_NS < 32) { issueW(IC(n + D_NS)); issueX(IC(n + D_NS)); } });
    bar();
  }
  {
    LLANE
    const u32x4 rw = mkrs(ws + WS_WOUT, 28 * 40 * 1024), rx = mkrs(ws + WS_P, 32 * 28 * 4096);
    const unsigned wo = (unsigned)(((lw >> 1) * 40 + 5 * j) * 1024 + (lw & 1) * 2560), lw_w = lds0 + lw * 2560, lw_x = lds0 + 10240 + lw * 2048, xb = (unsigned)p * 28 * 4096u + lw * 2048;
    auto issueW = [&](auto ic) { constexpr int n = decltype(ic)::value; dma_x2h<(n % E_NS) * E_SLOT, n * 80 * 1024>(lw_w, wo, rw, vx); };
    auto issueX = [&](auto ic) { constexpr int n = decltype(ic)::value; dma_x2<(n % E_NS) * E_SLOT, 2 * n * 4096>(lw_x, xb, rx, vx); };
    __builtin_amdgcn_s_setprio(0);
    static_for<0, (PRE1 < PFW ? PRE1 : PFW)>(issueW);
    bar();
    static_for<(PRE1 < PFW ? PRE1 : PFW), PFW>(issueW);
    bar();
    __builtin_amdgcn_s_setprio(LPRIO);
    loader_run<E_NS, PFW, 14, 3, 2>(issueW, issueX);
    bar();
  }
}

DEV void consumer_main(const Params& P, LAS char* smem, const int w, const int lane_, const int b, const int p, const int j) {
  __builtin_amdgcn_s_setprio(CPRIO);
  char* const ws = P.ws;
  unsigned tagv = __hip_atomic_load((gu32*)(ws + WS_EPOCH), RLX_AGENT) + 1u, tag;
  const int mh = w >> 1, kh = w & 1;
  const auto rws = mkrsrc(ws, WS_END);
  const int ft0 = 27 * (j >> 2) + 7 * (j & 3), nft = (j & 3) == 3 ? 6 : 7;
  auto rot = [&](int i) { return i < 3 ? i + 4 * mh : i - 3 * mh; };
#define LANE_IDS int lane = lane_; asm volatile("" : "+v"(lane)); const int q = lane >> 4, tk = lane & 15, tid = w * 64 + lane, token = 64 * p + 16 * w + tk; (void)q; (void)tk; (void)tid; (void)token;
  {
    LANE_IDS
    unsigned v[2][5];
#pragma unroll
    for (int t = 0; t < 2; ++t) { const unsigned* xr = (const unsigned*)(P.x + (size_t)(64 * p + 8 * j + 2 * w + t) * D);
#pragma unroll
      for (int i = 0; i < 5; ++i) v[t][i] = xr[lane + 64 * i]; }
    const int u0 = w * 256 + b; const bool dopt = u0 < 810; const int u = dopt ? u0 : 0; const bool isin = u < 540;
    const int uu = isin ? u : u - 540, ncb = isin ? 27 : 10, pkb = uu / ncb, pcb = uu - pkb * ncb, ld = isin ? F : D;
    u32x4 pv[4];
    { const bf16_t* src = (isin ? P.win : P.wout) + (size_t)(pkb * 32) * ld + pcb * 64;
#pragma unroll
      for (int i = 0; i < 4; ++i) pv[i] = *(const u32x4*)(src + (size_t)(i * 8 + (lane >> 3)) * ld + (lane & 7) * 8); }
    LAS unsigned* t32 = (LAS unsigned*)(smem + P0_T);
    unsigned pk[2][5];
#pragma unroll
    for (int t = 0; t < 2; ++t) {
      float s = 0.f;
#pragma unroll
      for (int i = 0; i < 5; ++i) s += bflo(v[t][i]) + bfhi(v[t][i]);
      const float mean = xsum_q(xsum_16(s)) * (1.0f / D);
      float q2 = 0.f;
#pragma unroll
      for (int i = 0; i < 5; ++i) { const float a = bflo(v[t][i]) - mean, c = bfhi(v[t][i]) - mean; q2 += a * a + c * c; }
      const float rs = __builtin_amdgcn_rsqf(xsum_q(xsum_16(q2)) * (1.0f / D) + 1e-5f);
#pragma unroll
      for (int i = 0; i < 5; ++i) pk[t][i] = pack2((bflo(v[t][i]) - mean) * rs, (bfhi(v[t][i]) - mean) * rs);
    }
#pragma unroll
    for (int i = 0; i < 5; ++i) { const int d = 2 * (lane + 64 * i);
      *(LAS u32x2*)(t32 + w * D + d) = (u32x2){(pk[0][i] & 0xffffu) | (pk[1][i] << 16), (pk[0][i] >> 16) | (pk[1][i] & 0xffff0000u)}; }
    asm volatile("" : "+v"(tagv)); tag = __builtin_amdgcn_readfirstlane(tagv);
    wait_lgkm0(); bar();
    const unsigned xb = WS_XN + (unsigned)((2 * p + (j >> 2)) * 40 * 1024 + (j & 3) * 256);
#pragma unroll
    for (int i = 0; i < 3; ++i) { const int d = tid + 256 * i;
      if (d < D) st16(rws, xb + (d >> 4) * 1024 + (d & 15) * 16, (u32x4){t32[d], t32[D + d], t32[2 * D + d], t32[3 * D + d]}); }
    wait_vm<0>();
    bar();
    if (w == 0 && lane == 0) set_flag(ws, WS_FN + p * 128 + j * 4, tag);
    {
      LAS char* t = smem + P0_PT + w * 4096;
#pragma unroll
      for (int i = 0; i < 4; ++i) *(LAS u32x4*)(t + (i * 8 + (lane >> 3)) * 128 + (lane & 7) * 16) = pv[i];
      const unsigned dst = dopt ? (isin ? WS_WIN + (unsigned)(pkb * 109 + pcb * 4) * 1024 : WS_WOUT + (unsigned)(pkb * 40 + pcb * 4) * 1024) + lane * 16 : 0xfffff000u;
      const int rq = tk >> 2, pp = lane & 3;
#pragma unroll
      for (int c = 0; c < 4; ++c) {
        const u32x2 l2 = __builtin_bit_cast(u32x2, __builtin_amdgcn_ds_read_tr16_b64_v4i16((LAS s16x4*)(t + (8 * q + rq) * 128 + c * 32 + pp * 8)));
        const u32x2 h2 = __builtin_bit_cast(u32x2, __builtin_amdgcn_ds_read_tr16_b64_v4i16((LAS s16x4*)(t + (8 * q + 4 + rq) * 128 + c * 32 + pp * 8)));
        st16(rws, dst + (dopt ? c * 1024 : 0), (u32x4){l2.x, l2.y, h2.x, h2.y});
      }
    }
    if (w == 0) { wait256(rws, ws, WS_FN, tag, lane); }
    bar();
  }
  u32x2 xv[5];
  {
    LANE_IDS
    f32x4 ur[5], ui[5];
#pragma unroll
    for (int i = 0; i < 5; ++i) { ur[i] = (f32x4){0, 0, 0, 0}; ui[i] = (f32x4){0, 0, 0, 0}; }
    f32x4 rsum[2] = {(f32x4){0, 0, 0, 0}, (f32x4){0, 0, 0, 0}};
    bf16x8 ones = __builtin_bit_cast(bf16x8, (u32x4){0x3f803f80u, 0x3f803f80u, 0x3f803f80u, 0x3f803f80u});
    asm volatile("" : "+v"(ones));
    const unsigned wa0 = (unsigned)(w * 4096 + tk * 128 + ((q ^ ((lane >> 1) & 7)) * 16));
    const unsigned xa = 16384 + lane * 16;
    bf16x8 Wa[2], Xa[5], Wb[2], Xb[5];
    auto rdm = [&](bf16x8(&Wn)[2], bf16x8(&Xn)[5], int slot, int kk, bf16x8(&Wf)[2], bf16x8(&Xf)[5]) {
      LAS char* sb = smem + slot * A_SLOT; const unsigned wa = wa0 ^ (kk * 64);
      static_for<0, 12>([&](auto ic) { constexpr int z = decltype(ic)::value, i = z >> 1, t = z & 1;
        if constexpr (z < 10) { if constexpr (t == 0) ur[i] = mfma(Xf[i], Wf[0], ur[i]); else ui[i] = mfma(Xf[i], Wf[1], ui[i]); } else rsum[t] = mfma(ones, Wf[t], rsum[t]);
        if constexpr (z == 0) Wn[0] = *(LAS bf16x8*)(sb + wa);
        if constexpr (z == 1) Xn[0] = *(LAS bf16x8*)(sb + xa + (kk * 5) * 1024);
        if constexpr (z == 2) Wn[1] = *(LAS bf16x8*)(sb + wa + 2048);
        if constexpr (z >= 3 && z < 7) Xn[z - 2] = *(LAS bf16x8*)(sb + xa + (kk * 5 + z - 2) * 1024);
        sched_fence(); });
    };
    auto mmlast = [&](bf16x8(&Wf)[2], bf16x8(&Xf)[5]) {
#pragma unroll
      for (int i = 0; i < 5; ++i) { ur[i] = mfma(Xf[i], Wf[0], ur[i]); ui[i] = mfma(Xf[i], Wf[1], ui[i]); }
      rsum[0] = mfma(ones, Wf[0], rsum[0]); rsum[1] = mfma(ones, Wf[1], rsum[1]);
    };
    int slot = 0;
    auto steps = [&](int n0, int n1, bool hooks) {
#pragma unroll 1
      for (int n = n0; n < n1; ++n) {
        rdm(Wb, Xb, slot, 1, Wa, Xa);
        const int ns = slot + 1 == A_NS ? 0 : slot + 1;
        if (hooks && n == 8) wait_vm<0>();
        wait_lgkm0(); bar(); sched_fence();
        if (hooks && n == 8 && w == 0 && lane == 0) set_flag(ws, WS_F2 + p * 128 + j * 4, tag);
        rdm(Wa, Xa, ns, 0, Wb, Xb);
        slot = ns;
      }
    };
    bar();
    { LAS char* sb = smem; Wa[0] = *(LAS bf16x8*)(sb + wa0); Wa[1] = *(LAS bf16x8*)(sb + wa0 + 2048);
#pragma unroll
      for (int i = 0; i < 5; ++i) Xa[i] = *(LAS bf16x8*)(sb + xa + i * 1024); }
    steps(0, 12, true);
    const u32x4 f2 = peek256(rws, WS_F2, lane);
    steps(12, 18, false);
    if (!flags_ok(f2, tag)) wait256(rws, ws, WS_F2, tag, lane);
    if (b == 0 && w == 0 && lane == 0) set_flag(ws, WS_EPOCH, tag + 15);
#pragma unroll
    for (int i = 0; i < 5; ++i) xv[i] = *(const u32x2*)(P.x + (size_t)token * D + 80 * j + 16 * i + 4 * q);
    steps(18, 31, false);
    { LAS char* sb = smem + slot * A_SLOT; const unsigned wa = wa0 ^ 64;
      Wb[0] = *(LAS bf16x8*)(sb + wa); Wb[1] = *(LAS bf16x8*)(sb + wa + 2048);
#pragma unroll
      for (int i = 0; i < 5; ++i) Xb[i] = *(LAS bf16x8*)(sb + xa + (5 + i) * 1024); }
    mmlast(Wa, Xa);
    wait_lgkm0(); bar();
    mmlast(Wb, Xb);
    const float rs0 = rsum[0][0], rs1 = rsum[1][0];
#pragma unroll
    for (int i = 0; i < 5; ++i) {
      u32x2 a, c;
      const f32x4 sa = *(const LAS f32x4*)(smem + T_MOD + (16 * i + 4 * q) * 4), c1 = *(const LAS f32x4*)(smem + T_MOD + (80 + 16 * i + 4 * q) * 4);
      { const f32x4 u = ur[i] * c1 + rs0 * sa; a.x = pack2(u[0], u[1]); a.y = pack2(u[2], u[3]); }
      { const f32x4 v = -(ui[i] * c1 + rs1 * sa); c.x = pack2(v[0], v[1]); c.y = pack2(v[2], v[3]); }
      const int dt = 5 * j + i, kb = dt >> 1, qq = 2 * (dt & 1) + (q >> 1);
      st16(rws, WS_UV + (unsigned)(((p * 2 + (q & 1)) * 20 + kb) * 4 + w) * 1024 + (qq * 16 + tk) * 16, swap_pair(a, c));
    }
    wait_vm<0>(); bar();
    if (w == 0) { if (lane == 0) set_flag(ws, WS_FU + p * 128 + j * 4, tag); wait_flags8(ws, WS_FU + p * 128, tag, lane); }
    bar();
  }
  u32x2 x1p[5];
  {
    LANE_IDS
    f32x4 acc[5][2];
#pragma unroll
    for (int i = 0; i < 5; ++i) { acc[i][0] = (f32x4){0, 0, 0, 0}; acc[i][1] = (f32x4){0, 0, 0, 0}; }
    const unsigned wa = (unsigned)(tk * 128 + (((q + 4 * kh) ^ ((lane >> 1) & 7)) * 16));
    const unsigned xa = 10240 + (kh * 4 + 2 * mh) * 1024 + lane * 16;
    ksplit_loop<B_NS, 20, 5, 2>(
        [&](bf16x8(&A)[5], bf16x8(&Bf)[2], int slot, int) { LAS char* sb = smem + slot * B_SLOT;
          A[0] = *(LAS bf16x8*)(sb + wa); Bf[0] = *(LAS bf16x8*)(sb + xa); Bf[1] = *(LAS bf16x8*)(sb + xa + 1024);
#pragma unroll
          for (int i = 1; i < 5; ++i) A[i] = *(LAS bf16x8*)(sb + wa + i * 2048); },
        [&](bf16x8(&A)[5], bf16x8(&Bf)[2], bool) {
#pragma unroll
          for (int i = 0; i < 5; ++i) { acc[i][0] = mfma(A[i], Bf[0], acc[i][0]); acc[i][1] = mfma(A[i], Bf[1], acc[i][1]); } });
    f32x4 y[5];
    kreduce<5>(acc, y, smem + B_SCR, w, lane);
    float xs[20]; u32x2 xp[5];
    float s = 0.f;
#pragma unroll
    for (int i = 0; i < 5; ++i) {
      const f32x4 ga = *(const LAS f32x4*)(smem + T_MOD + (160 + 16 * i + 4 * q) * 4), c1b = *(const LAS f32x4*)(smem + T_MOD + 960 + (16 * i + 4 * q) * 4);
      x1p[i].x = pack2(bflo(xv[i].x) + ga[0] * y[i][0], bfhi(xv[i].x) + ga[1] * y[i][1]);
      x1p[i].y = pack2(bflo(xv[i].y) + ga[2] * y[i][2], bfhi(xv[i].y) + ga[3] * y[i][3]);
      asm volatile("" : "+v"(x1p[i].x), "+v"(x1p[i].y));
      xs[4 * i] = bflo(x1p[i].x); xs[4 * i + 1] = bfhi(x1p[i].x); xs[4 * i + 2] = bflo(x1p[i].y); xs[4 * i + 3] = bfhi(x1p[i].y);
      s += (xs[4 * i] + xs[4 * i + 1]) + (xs[4 * i + 2] + xs[4 * i + 3]);
      xp[i].x = pack2(xs[4 * i] * c1b[0], xs[4 * i + 1] * c1b[1]); xp[i].y = pack2(xs[4 * i + 2] * c1b[2], xs[4 * i + 3] * c1b[3]);
    }
    const unsigned img = WS_X1P + (unsigned)p * 20 * 4096 + w * 1024;
#pragma unroll
    for (int i = 0; i < 6; i += 2) {
      const int i2 = i + 1 < 5 ? i + 1 : i;
      const int dt = 5 * j + ((q & 1) ? i2 : i), kb = dt >> 1, qq = 2 * (dt & 1) + (q >> 1);
      st16(rws, img + kb * 4096 + (qq * 16 + tk) * 16, swap_pair(xp[i], xp[i2]));
    }
    const float mean = xsum_q(s) * (1.0f / 80.0f);
    float m2 = 0.f;
#pragma unroll
    for (int e = 0; e < 20; ++e) { const float d = xs[e] - mean; m2 += d * d; }
    m2 = xsum_q(m2);
    if (q == 0) __hip_atomic_store((gu64*)(ws + WS_ST1) + token * 8 + j, ((unsigned long long)__float_as_uint(m2) << 32) | __float_as_uint(mean), RLX_AGENT);
    wait_vm<0>(); bar();
    if (w == 0) { if (lane == 0) set_flag(ws, WS_FX + p * 128 + j * 4, tag); wait_flags8(ws, WS_FX + p * 128, tag, lane); }
    bar();
  }
  u32x2 z1p[7]; float bias;
  {
    LANE_IDS
    u32x4 st[4];
#pragma unroll
    for (int k = 0; k < 4; ++k) st[k] = ld16(rws, WS_ST1 + (unsigned)token * 64 + k * 16);
    bias = bflo(P.bsp[token]);
    const unsigned gtag = tag ^ 0x9e3779b9u;
    for (int i = tid; i < 448; i += 256) ((LAS float*)(smem + T_CV))[i] = 0.f;
    f32x4 acc[7][2], aux[4];
    auto clear = [&]() {
#pragma unroll
      for (int i = 0; i < 7; ++i) { acc[i][0] = (f32x4){0, 0, 0, 0}; acc[i][1] = (f32x4){0, 0, 0, 0}; }
#pragma unroll
      for (int i = 0; i < 4; ++i) aux[i] = (f32x4){0, 0, 0, 0};
    };
    const unsigned xa = C_X + (kh * 4 + 2 * mh) * 1024 + lane * 16, wlo = C_W + kh * 7168 + lane * 16 + mh * 4096, whi = C_W + kh * 7168 + lane * 16 - mh * 3072;
    const int r4 = tk & 3;
    const unsigned ma = T_MODB + (r4 == 0 ? q * 16 + kh * 64 : r4 == 1 ? 1280 + q * 16 + kh * 64 : 2560), mstride = r4 < 2 ? 128 : 0;
    auto rd = [&](bf16x8(&A)[7], bf16x8(&Bf)[3], int slot, int n) {
      LAS char* sl = smem + wlo + slot * C_WSLOT; LAS char* sh = smem + whi + slot * C_WSLOT; LAS char* xb = smem + xa + n * 8192;
      Bf[0] = *(LAS bf16x8*)(xb); A[0] = *(LAS bf16x8*)(sl); Bf[1] = *(LAS bf16x8*)(xb + 1024);
      A[1] = *(LAS bf16x8*)(sl + 1024); A[2] = *(LAS bf16x8*)(sl + 2048);
#pragma unroll
      for (int i = 3; i < 7; ++i) A[i] = *(LAS bf16x8*)(sh + i * 1024);
      Bf[2] = *(LAS bf16x8*)(smem + ma + n * mstride);
    };
    auto auxmm = [&](bf16x8(&A)[7], bf16x8(&Bf)[3]) {
#pragma unroll
      for (int i = 0; i < 4; ++i) aux[i] = mfma(Bf[2], A[i], aux[i]);
    };
    auto mm2 = [&](bf16x8(&A)[7], bf16x8(&Bf)[3], bool) {
#pragma unroll
      for (int i = 0; i < 7; ++i) { acc[i][0] = mfma(Bf[0], A[i], acc[i][0]); acc[i][1] = mfma(Bf[1], A[i], acc[i][1]); }
      auxmm(A, Bf); };
    auto mm1 = [&](bf16x8(&A)[7], bf16x8(&Bf)[3], bool) {
#pragma unroll
      for (int i = 0; i < 7; ++i) { acc[i][0] = mfma(A[i], Bf[0], acc[i][0]); acc[i][1] = mfma(A[i], Bf[1], acc[i][1]); }
      auxmm(A, Bf); };
    auto put_cv = [&](int pass_) {
      LAS float* cv = (LAS float*)(smem + T_CV) + pass_ * 224;
      if (q == 0) {
#pragma unroll
        for (int i = 0; i < 4; ++i) if (mh == 0 || i < 3) { const int t = rot(i);
          __hip_atomic_fetch_add(cv + 16 * t + tk, aux[i][0], __ATOMIC_RELAXED, __HIP_MEMORY_SCOPE_WORKGROUP);
          __hip_atomic_fetch_add(cv + 112 + 16 * t + tk, aux[i][1], __ATOMIC_RELAXED, __HIP_MEMORY_SCOPE_WORKGROUP); }
      }
    };
    float rmean, rrstd;
    { float mu[8], mean = 0.f;
#pragma unroll
      for (int k = 0; k < 4; ++k) { mu[2 * k] = __uint_as_float(st[k].x); mu[2 * k + 1] = __uint_as_float(st[k].z); mean += mu[2 * k] + mu[2 * k + 1]; }
      mean *= 0.125f; float M2 = 0.f;
#pragma unroll
      for (int k = 0; k < 4; ++k) { const float d0 = mu[2 * k] - mean, d1 = mu[2 * k + 1] - mean; M2 += __uint_as_float(st[k].y) + __uint_as_float(st[k].w) + 80.0f * (d0 * d0 + d1 * d1); }
      rmean = mean; rrstd = __builtin_amdgcn_rsqf(M2 * (1.0f / D) + 1e-5f); }
    asm volatile("" : "+v"(rmean), "+v"(rrstd));
    bf16x8 A0[7], B0[3], A1[7], B1[3];
    int slot = 0;
    auto nohook = [](int) {};
    clear();
    wait_lgkm0(); bar();
    rd(A0, B0, 0, 0);
    ksplit_steps<C_NS, 10, 7, 3, 18>(A0, B0, A1, B1, slot, 0, 8, rd, mm2, nohook, nohook);
    ksplit_last<C_NS, 10, 7, 3>(A0, B0, A1, B1, slot, rd, mm2);
    put_cv(0);
    f32x4 zs[7];
    kreduce<7>(acc, zs, smem + C_SCR, w, lane);
    wait_lgkm0(); bar();
    f32x2 mr01, mr23, rr01, rr23;
    mr01 = (f32x2){__shfl(rmean, 4 * q), __shfl(rmean, 4 * q + 1)}; mr23 = (f32x2){__shfl(rmean, 4 * q + 2), __shfl(rmean, 4 * q + 3)};
    rr01 = (f32x2){__shfl(rrstd, 4 * q), __shfl(rrstd, 4 * q + 1)}; rr23 = (f32x2){__shfl(rrstd, 4 * q + 2), __shfl(rrstd, 4 * q + 3)};
    f32x2 z01[7], z23[7];
#pragma unroll
    for (int i = 0; i < 7; ++i) {
      const float c1 = ((const LAS float*)(smem + T_CV))[16 * rot(i) + tk], c2 = ((const LAS float*)(smem + T_CV))[112 + 16 * rot(i) + tk];
      z01[i] = gelu2(rr01 * ((f32x2){zs[i][0], zs[i][1]} - mr01 * c1) + c2);
      z23[i] = gelu2(rr23 * ((f32x2){zs[i][2], zs[i][3]} - mr23 * c1) + c2);
    }
    {
      const float inv = 1.0f / (16.0f * nft);
      const f32x2 zero2 = (f32x2){0.f, 0.f};
      f32x2 s01 = zero2, s23 = zero2;
#pragma unroll
      for (int i = 0; i < 7; ++i) { const bool ok = (i != 2 && i != 6) || rot(i) < nft; s01 += ok ? z01[i] : zero2; s23 += ok ? z23[i] : zero2; }
      const f32x2 m01 = (f32x2){xsum_16(s01[0]), xsum_16(s01[1])} * inv, m23 = (f32x2){xsum_16(s23[0]), xsum_16(s23[1])} * inv;
      f32x2 t01 = zero2, t23 = zero2;
#pragma unroll
      for (int i = 0; i < 7; ++i) { const bool ok = (i != 2 && i != 6) || rot(i) < nft; const f32x2 a = ok ? z01[i] - m01 : zero2, c = ok ? z23[i] - m23 : zero2; t01 += a * a; t23 += c * c; }
      const float q0 = xsum_16(t01[0]), q1 = xsum_16(t01[1]), q2 = xsum_16(t23[0]), q3 = xsum_16(t23[1]);
      if (tk < 8) { const int r = tk & 3; const float a = r == 0 ? m01[0] : r == 1 ? m01[1] : r == 2 ? m23[0] : m23[1], c = r == 0 ? q0 : r == 1 ? q1 : r == 2 ? q2 : q3;
        const int tok = 64 * p + 16 * w + 4 * q + r;
        __hip_atomic_store((gu64*)(ws + WS_ST2) + (tok * 8 + j) * 2 + (tk >> 2), ((unsigned long long)(gtag ^ __float_as_uint(tk < 4 ? c : a)) << 32) | __float_as_uint(tk < 4 ? a : c), RLX_AGENT); }
    }
    clear();
    slot = 0;
    bar();
    rd(A0, B0, 0, 0);
    ksplit_steps<C_NS, 10, 7, 3, 18>(A0, B0, A1, B1, slot, 0, 4, rd, mm1, nohook, nohook);
    {
      u32x4 g0, g1;
      const unsigned go = WS_ST2 + (unsigned)(token * 8 + 2 * q) * 16;
      for (unsigned spins = 0;;) { g0 = ld16(rws, go); g1 = ld16(rws, go + 16);
        if (__all(g0.y == (gtag ^ g0.z) && g0.w == (gtag ^ g0.x) && g1.y == (gtag ^ g1.z) && g1.w == (gtag ^ g1.x))) break; if (spin_fail(spins, ws)) break; }
      const float n0 = 112.0f, n1 = (q & 1) ? 96.0f : 112.0f;
      const float m0 = __uint_as_float(g0.x), m1 = __uint_as_float(g1.x);
      const float mean = xsum_q(n0 * m0 + n1 * m1) * (1.0f / FH);
      const float d0 = m0 - mean, d1 = m1 - mean;
      const float M2 = xsum_q(__uint_as_float(g0.z) + __uint_as_float(g1.z) + n0 * d0 * d0 + n1 * d1 * d1);
      const float rstd = __builtin_amdgcn_rsqf(M2 * (1.0f / FH) + 1e-5f);
      mr01 = (f32x2){__shfl(mean, 4 * q), __shfl(mean, 4 * q + 1)}; mr23 = (f32x2){__shfl(mean, 4 * q + 2), __shfl(mean, 4 * q + 3)};
      rr01 = (f32x2){__shfl(rstd, 4 * q), __shfl(rstd, 4 * q + 1)}; rr23 = (f32x2){__shfl(rstd, 4 * q + 2), __shfl(rstd, 4 * q + 3)};
    }
    {
      u32x2 pk[7];
#pragma unroll
      for (int i = 0; i < 7; ++i) { const f32x2 a = (z01[i] - mr01) * rr01, c = (z23[i] - mr23) * rr23; pk[i].x = pack2(a[0], a[1]); pk[i].y = pack2(c[0], c[1]); }
      const unsigned img = WS_Z2N + (unsigned)(2 * p + (w >> 1)) * 55 * 1024 + ((2 * (w & 1) + (q >> 1)) * 16 + tk) * 16;
#pragma unroll
      for (int i = 0; i < 8; i += 2) {
        const int i2 = i + 1 < 7 ? i + 1 : i;
        const u32x4 ch = swap_pair(pk[i], pk[i2]);
        const int ti = (q & 1) ? rot(i2) : rot(i);
        st16(rws, ti < nft ? img + (ft0 + ti) * 1024 : 0xfffff000u, ch);
      }
    }
    ksplit_steps<C_NS, 10, 7, 3, 18>(A0, B0, A1, B1, slot, 4, 8, rd, mm1,
        [&](int n) { if (n == 5) wait_vm<0>(); },
        [&](int n) { if (n == 5 && w == 0 && lane == 0) set_flag(ws, WS_FZ + (j * 4 + (p >> 3)) * 128 + (p & 7) * 4, tag); });
    ksplit_last<C_NS, 10, 7, 3>(A0, B0, A1, B1, slot, rd, mm1);
    const unsigned fzo = WS_FZ + (j * 4 + ((lane & 7) >> 1)) * 128 + (lane & 1) * 16;
    const u32x4 fz = ld16(rws, fzo);
    put_cv(1);
    kreduce<7>(acc, zs, smem + C_SCR, w, lane);
    if (!flags_ok(fz, tag)) { for (unsigned spins = 0;;) { if (flags_ok(ld16(rws, fzo), tag)) break; if (spin_fail(spins, ws)) break; } }
    wait_lgkm0(); bar();
#pragma unroll
    for (int i = 0; i < 7; ++i) {
      const LAS float* cv = (const LAS float*)(smem + T_CV) + 224;
      const f32x4 c1 = *(const LAS f32x4*)(cv + 16 * rot(i) + 4 * q), c2 = *(const LAS f32x4*)(cv + 112 + 16 * rot(i) + 4 * q);
      const f32x2 a = gelu2(rrstd * ((f32x2){zs[i][0], zs[i][1]} - rmean * (f32x2){c1[0], c1[1]}) + (f32x2){c2[0], c2[1]});
      const f32x2 c = gelu2(rrstd * ((f32x2){zs[i][2], zs[i][3]} - rmean * (f32x2){c1[2], c1[3]}) + (f32x2){c2[2], c2[3]});
      z1p[i] = (u32x2){pack2(a[0], a[1]), pack2(c[0], c[1])};
      asm volatile("" : "+v"(z1p[i].x), "+v"(z1p[i].y));
    }
  }
  {
    LANE_IDS
    f32x4 acc[7][2];
#pragma unroll
    for (int i = 0; i < 7; ++i) { acc[i][0] = (f32x4){0, 0, 0, 0}; acc[i][1] = (f32x4){0, 0, 0, 0}; }
    const unsigned wa = (unsigned)((2 * mh) * 2048 + tk * 128 + (((q + 4 * kh) ^ ((lane >> 1) & 7)) * 16));
    const unsigned xlo = 8192 + kh * 7168 + lane * 16 + mh * 4096, xhi = 8192 + kh * 7168 + lane * 16 - mh * 3072;
    ksplit_loop<D_NS, 32, 7, 2>(
        [&](bf16x8(&A)[7], bf16x8(&Bf)[2], int slot, int) { LAS char* sb = smem + slot * D_SLOT;
          Bf[0] = *(LAS bf16x8*)(sb + wa); A[0] = *(LAS bf16x8*)(sb + xlo); Bf[1] = *(LAS bf16x8*)(sb + wa + 2048);
          A[1] = *(LAS bf16x8*)(sb + xlo + 1024); A[2] = *(LAS bf16x8*)(sb + xlo + 2048);
#pragma unroll
          for (int i = 3; i < 7; ++i) A[i] = *(LAS bf16x8*)(sb + xhi + i * 1024); },
        [&](bf16x8(&A)[7], bf16x8(&Bf)[2], bool) {
#pragma unroll
          for (int i = 0; i < 7; ++i) { acc[i][0] = mfma(A[i], Bf[0], acc[i][0]); acc[i][1] = mfma(A[i], Bf[1], acc[i][1]); } });
    f32x4 g[7];
    kreduce<7>(acc, g, smem + D_SCR, w, lane);
    u32x2 pk[7];
#pragma unroll
    for (int i = 0; i < 7; ++i) {
      pk[i].x = pack2(bflo(z1p[i].x) * (g[i][0] + bias), bfhi(z1p[i].x) * (g[i][1] + bias));
      pk[i].y = pack2(bflo(z1p[i].y) * (g[i][2] + bias), bfhi(z1p[i].y) * (g[i][3] + bias));
    }
    const unsigned img = WS_P + (unsigned)p * 28 * 4096 + w * 1024;
#pragma unroll
    for (int i = 0; i < 8; i += 2) {
      const int i2 = i + 1 < 7 ? i + 1 : i;
      const u32x4 ch = swap_pair(pk[i], pk[i2]);
      const int ti = (q & 1) ? rot(i2) : rot(i), ft = ft0 + ti, kb = ft >> 1, qq = 2 * (ft & 1) + (q >> 1);
      st16(rws, ti < nft ? img + kb * 4096 + (qq * 16 + tk) * 16 : 0xfffff000u, ch);
    }
    wait_vm<0>(); bar();
    if (w == 0) { if (lane == 0) set_flag(ws, WS_FP + p * 128 + j * 4, tag); wait_flags8(ws, WS_FP + p * 128, tag, lane); }
    bar();
  }
  {
    LANE_IDS
    f32x4 acc[5][2];
#pragma unroll
    for (int i = 0; i < 5; ++i) { acc[i][0] = (f32x4){0, 0, 0, 0}; acc[i][1] = (f32x4){0, 0, 0, 0}; }
    const unsigned wa = kh * 5120 + lane * 16, xa = 10240 + (kh * 4 + 2 * mh) * 1024 + lane * 16;
    ksplit_loop<E_NS, 14, 5, 2>(
        [&](bf16x8(&A)[5], bf16x8(&Bf)[2], int slot, int) { LAS char* sb = smem + slot * E_SLOT;
          A[0] = *(LAS bf16x8*)(sb + wa); Bf[0] = *(LAS bf16x8*)(sb + xa); Bf[1] = *(LAS bf16x8*)(sb + xa + 1024);
#pragma unroll
          for (int i = 1; i < 5; ++i) A[i] = *(LAS bf16x8*)(sb + wa + i * 1024); },
        [&](bf16x8(&A)[5], bf16x8(&Bf)[2], bool last) {
          if (!(last && kh)) {
#pragma unroll
            for (int i = 0; i < 5; ++i) { acc[i][0] = mfma(A[i], Bf[0], acc[i][0]); acc[i][1] = mfma(A[i], Bf[1], acc[i][1]); } } });
    f32x4 o[5];
    kreduce<5>(acc, o, smem + E_SCR, w, lane);
    u32x2 pk[5];
#pragma unroll
    for (int i = 0; i < 5; ++i) {
      const f32x4 gb = *(const LAS f32x4*)(smem + T_MOD + 960 + (80 + 16 * i + 4 * q) * 4);
      pk[i].x = pack2(bflo(x1p[i].x) + gb[0] * o[i][0], bfhi(x1p[i].x) + gb[1] * o[i][1]);
      pk[i].y = pack2(bflo(x1p[i].y) + gb[2] * o[i][2], bfhi(x1p[i].y) + gb[3] * o[i][3]);
    }
    bf16_t* orow = P.out + (size_t)token * D + 80 * j;
#pragma unroll
    for (int i = 0; i < 6; i += 2) {
      const int i2 = i + 1 < 5 ? i + 1 : i;
      const u32x4 ch = swap_pair(pk[i], pk[i2]);
      *(u32x4*)(orow + 16 * ((q & 1) ? i2 : i) + 8 * (q >> 1)) = ch;
    }
  }
}

__global__ __launch_bounds__(NT, 2) void fused_block(Params P) {
  extern __shared__ __attribute__((aligned(16))) char smem_[];
  LAS char* smem = (LAS char*)smem_;
  const int tid = threadIdx.x, lane = tid & 63, wid = __builtin_amdgcn_readfirstlane(tid >> 6), b = blockIdx.x;
  const int xcd = b & 7, idx = b >> 3, p = xcd * 4 + (idx >> 3), j = idx & 7;
  if (wid >= 4) loader_main(P, (unsigned)(size_t)smem, smem, wid - 4, lane, b, p, j);
  else consumer_main(P, smem, wid, lane, b, p, j);
}

extern "C" void kernel_launch(void* const* d_in, const int* in_sizes, int n_in, void* d_out, int out_size, void* d_ws, size_t ws_size, hipStream_t stream) {
  Params P;
  P.x = (const bf16_t*)d_in[0]; P.wsr = (const bf16_t*)d_in[1]; P.wsi = (const bf16_t*)d_in[2];
  P.wdr = (const bf16_t*)d_in[3]; P.wdi = (const bf16_t*)d_in[4]; P.win = (const bf16_t*)d_in[6];
  P.wsp = (const bf16_t*)d_in[7]; P.bsp = (const bf16_t*)d_in[8]; P.wout = (const bf16_t*)d_in[9];
  P.c = (const bf16_t*)d_in[10]; P.wmod = (const bf16_t*)d_in[11];
  P.out = (bf16_t*)d_out; P.ws = (char*)d_ws;
  (void)hipFuncSetAttribute((const void*)fused_block, hipFuncAttributeMaxDynamicSharedMemorySize, LDS_BYTES);
  fused_block<<<256, NT, LDS_BYTES, stream>>>(P);
}
```
